```python
import torch

def _softmax(x, axis=-1):
    return torch.softmax(x, dim=axis)
def _gelu(x):
    return torch.nn.functional.gelu(x, approximate="tanh")
def _silu(x):
    return torch.nn.functional.silu(x)
def _sigmoid(x):
    return torch.sigmoid(x)
def _layernorm(x, axis=-1, eps=1e-5):
    m = x.mean(dim=axis, keepdim=True)
    v = x.var(dim=axis, keepdim=True, unbiased=False)
    return (x - m) / torch.sqrt(v + eps)
def _scatter_add(tgt, idx, src, axis=0):
    return tgt.clone().index_add_(axis, idx, src)
def _segment_sum(src, seg, n):
    out = torch.zeros((n,) + src.shape[1:], dtype=src.dtype)
    return out.index_add_(0, seg, src)
def _pad(x, pw):
    flat = tuple(v for pair in reversed(pw) for v in pair)
    return torch.nn.functional.pad(x, flat)

kernel_name = 'parallel_sum__compressed_kv_cla__dense_activation_mish__prompt'

def setup_inputs():

    _w = (lambda a: (lambda t: t.to(torch.bfloat16) if t.is_floating_point() else t)(torch.as_tensor(a)))
    def _A_setup():
        B, H, Sq, Sk, D, M = 1, 32, 2048, 2048, 128, 512
        Dc = 4 * D
        return {
            "x": _w((lambda *s: torch.randn(*s, dtype=torch.bfloat16) * 0.05)(B, Sq, M)),
            "c_kv": _w((lambda *s: torch.randn(*s, dtype=torch.bfloat16) * 0.4)(B, Sk, Dc)),
            "w_q": _w((lambda *s: torch.randn(*s, dtype=torch.bfloat16) * 3.2)(M, H, D) * (M ** -0.5)),
            "w_uk": _w((lambda *s: torch.randn(*s, dtype=torch.bfloat16) * 3.2)(Dc, H, D) * (Dc ** -0.5)),
            "w_uv": _w((lambda *s: torch.randn(*s, dtype=torch.bfloat16) * 0.4)(Dc, H, D) * (Dc ** -0.5)),
            "w_o": _w((lambda *s: torch.randn(*s, dtype=torch.bfloat16) * 0.4)(H, D, M) * ((H * D) ** -0.5)),
        }
    _A_in = _A_setup()

    def _B_setup():
        B, S, D, F = 1, 2048, 512, 1376
        return {
            "x": _w((lambda *s: torch.randn(*s, dtype=torch.bfloat16) * 0.05)(B, S, D)),
            "w1": _w((lambda *s: torch.randn(*s, dtype=torch.bfloat16) * 0.8)(D, F) * (D ** -0.5)),
            "b1": _w((lambda *s: torch.randn(*s, dtype=torch.bfloat16) * 0.2)(F)),
            "w2": _w((lambda *s: torch.randn(*s, dtype=torch.bfloat16) * 0.1)(F, D) * (F ** -0.5)),
        }
    _B_in = _B_setup()

    return {
        "A_x": _A_in["x"],
        "A_c_kv": _A_in["c_kv"],
        "A_w_q": _A_in["w_q"],
        "A_w_uk": _A_in["w_uk"],
        "A_w_uv": _A_in["w_uv"],
        "A_w_o": _A_in["w_o"],
        "B_x": _B_in["x"],
        "B_w1": _B_in["w1"],
        "B_b1": _B_in["b1"],
        "B_w2": _B_in["w2"],
    }

def reference(A_x, A_c_kv, A_w_q, A_w_uk, A_w_uv, A_w_o, B_x, B_w1, B_b1, B_w2):
    def _A_ref(x, c_kv, w_q, w_uk, w_uv, w_o):
        B, Sq, M = x.shape[0], x.shape[1], x.shape[2]
        Sk = c_kv.shape[1]
        H, D = w_q.shape[1], w_q.shape[2]
        q = torch.einsum("bqm,mhd->bhqd", x, w_q)
        k = torch.einsum("bkc,chd->bhkd", c_kv, w_uk)
        v = torch.einsum("bkc,chd->bhkd", c_kv, w_uv)
        scores = torch.einsum("bhqd,bhkd->bhqk", q, k) * (D ** -0.5)
        off = Sk - Sq
        qi = torch.arange(Sq)[:, None]
        kj = torch.arange(Sk)[None, :]
        cmask = kj <= (qi + off)
        scores = torch.where(cmask[None, None], scores, -1e9)
        p = _softmax(scores, axis=-1)
        ctx = torch.einsum("bhqk,bhkd->bhqd", p, v)
        return torch.einsum("bhqd,hdm->bqm", ctx, w_o)

    def _B_ref(x, w1, b1, w2):
        h = torch.einsum("bsd,df->bsf", x, w1) + b1
        sp = torch.log1p(torch.exp(-torch.abs(h))) + torch.where(h > 0, h, torch.zeros_like(h))
        act = h * torch.tanh(sp)
        return torch.einsum("bsf,fd->bsd", act, w2)

    x = A_x
    xn = _layernorm(x, axis=-1)
    return x + _A_ref(xn, A_c_kv, A_w_q, A_w_uk, A_w_uv, A_w_o) + _B_ref(xn, B_w1, B_b1, B_w2)
```

```cpp
#include <hip/hip_runtime.h>
#include <hip/hip_bf16.h>
typedef unsigned short u16;
__device__ inline float b2f(u16 b){ return __uint_as_float(((unsigned)b)<<16); }
__device__ inline u16 f2b(float f){ unsigned u=__float_as_uint(f); u += 0x7fffu + ((u>>16)&1u); return (u16)(u>>16); }
__global__ void ln_k(const u16* x, u16* xn){
  int row=blockIdx.x; int l=threadIdx.x; float v[8]; float s=0;
  for(int j=0;j<8;j++){ v[j]=b2f(x[row*512+l*8+j]); s+=v[j]; }
  for(int o=32;o>=1;o>>=1) s+=__shfl_xor(s,o);
  float m=s/512.f; float q=0; for(int j=0;j<8;j++){ float d=v[j]-m; q+=d*d; }
  for(int o=32;o>=1;o>>=1) q+=__shfl_xor(q,o);
  float r=rsqrtf(q/512.f+1e-5f);
  for(int j=0;j<8;j++) xn[row*512+l*8+j]=f2b((v[j]-m)*r);
}
__global__ void gemm_k(const u16* A,int lda,const u16* B,int ldb,u16* C,int ldc,float* Cf,const u16* bias,int M,int N,int K,int mode){
  __shared__ float As[32][33], Bs[32][33];
  int tx=threadIdx.x, ty=threadIdx.y; int row=blockIdx.y*32+ty, col=blockIdx.x*32+tx; float acc=0;
  for(int k0=0;k0<K;k0+=32){
    As[ty][tx]=(k0+tx<K)?b2f(A[(size_t)row*lda+k0+tx]):0.f;
    Bs[ty][tx]=(k0+ty<K&&col<N)?b2f(B[(size_t)(k0+ty)*ldb+col]):0.f;
    __syncthreads();
    for(int k=0;k<32;k++) acc+=As[ty][k]*Bs[k][tx];
    __syncthreads();
  }
  if(col<N){
    if(mode==0) C[(size_t)row*ldc+col]=f2b(acc);
    else if(mode==1){ float h=acc+b2f(bias[col]); float sp=log1pf(expf(-fabsf(h)))+fmaxf(h,0.f); C[(size_t)row*ldc+col]=f2b(h*tanhf(sp)); }
    else if(mode==2) Cf[(size_t)row*ldc+col]=acc;
    else { float o=Cf[(size_t)row*ldc+col]+acc+b2f(bias[(size_t)row*ldc+col]); C[(size_t)row*ldc+col]=f2b(o);}
  }
}
__global__ void attn_k(const u16* q,const u16* k,const u16* v,u16* ctx){
  int row=blockIdx.x, h=blockIdx.y, t=threadIdx.x;
  __shared__ float qs[128]; __shared__ float p[2048]; __shared__ float red[128];
  qs[t]=b2f(q[(size_t)row*4096+h*128+t]); __syncthreads();
  float mx=-1e30f;
  for(int j=t;j<=row;j+=128){ float s=0; for(int d=0;d<128;d++) s+=qs[d]*b2f(k[(size_t)j*4096+h*128+d]); s*=0.08838834764831845f; p[j]=s; mx=fmaxf(mx,s);}
  red[t]=mx; __syncthreads(); for(int o=64;o>=1;o>>=1){ if(t<o) red[t]=fmaxf(red[t],red[t+o]); __syncthreads(); } mx=red[0]; __syncthreads();
  float sm=0; for(int j=t;j<=row;j+=128){ float e=expf(p[j]-mx); p[j]=e; sm+=e; }
  red[t]=sm; __syncthreads(); for(int o=64;o>=1;o>>=1){ if(t<o) red[t]+=red[t+o]; __syncthreads(); } sm=red[0];
  float o=0; for(int j=0;j<=row;j++) o+=p[j]*b2f(v[(size_t)j*4096+h*128+t]);
  ctx[(size_t)row*4096+h*128+t]=f2b(o/sm);
}
extern "C" void kernel_launch(void* const* d_in, const int* in_sizes, int n_in, void* d_out, int out_size, void* d_ws, size_t ws_size, hipStream_t stream){
  const u16* x=(const u16*)d_in[0]; const u16* ckv=(const u16*)d_in[1]; const u16* wq=(const u16*)d_in[2]; const u16* wuk=(const u16*)d_in[3];
  const u16* wuv=(const u16*)d_in[4]; const u16* wo=(const u16*)d_in[5]; const u16* w1=(const u16*)d_in[7]; const u16* b1=(const u16*)d_in[8]; const u16* w2=(const u16*)d_in[9];
  char* ws=(char*)d_ws; u16* xn=(u16*)ws; u16* q=(u16*)(ws+(4<<20)); u16* k=(u16*)(ws+(24<<20)); u16* v=(u16*)(ws+(44<<20)); u16* ctx=(u16*)(ws+(64<<20)); u16* act=(u16*)(ws+(84<<20)); float* tmp=(float*)(ws+(96<<20));
  ln_k<<<2048,64,0,stream>>>(x,xn);
  dim3 b(32,32);
  gemm_k<<<dim3(128,64),b,0,stream>>>(xn,512,wq,4096,q,4096,0,0,2048,4096,512,0);
  gemm_k<<<dim3(128,64),b,0,stream>>>(ckv,512,wuk,4096,k,4096,0,0,2048,4096,512,0);
  gemm_k<<<dim3(128,64),b,0,stream>>>(ckv,512,wuv,4096,v,4096,0,0,2048,4096,512,0);
  attn_k<<<dim3(2048,32),128,0,stream>>>(q,k,v,ctx);
  gemm_k<<<dim3(43,64),b,0,stream>>>(xn,512,w1,1376,act,1376,0,b1,2048,1376,512,1);
  gemm_k<<<dim3(16,64),b,0,stream>>>(ctx,4096,wo,512,0,512,tmp,0,2048,512,4096,2);
  gemm_k<<<dim3(16,64),b,0,stream>>>(act,1376,w2,512,(u16*)d_out,512,tmp,x,2048,512,1376,3);
}
```

```cpp
#include <hip/hip_runtime.h>
#include <hip/hip_bf16.h>

__global__ __launch_bounds__(256, 1) void fused_mla_ffn(const void* x, const void* ckv, const void* wq, const void* wuk, const void* wuv, const void* wo, const void* w1, const void* b1, const void* w2, void* out, void* ws) {
  const unsigned wid = __builtin_amdgcn_readfirstlane(threadIdx.x >> 6);
  asm volatile(
"s_load_dwordx16 s[40:55], %[ka], 0\n"
"s_load_dwordx4 s[56:59], %[ka], 64\n"
"s_load_dwordx2 s[60:61], %[ka], 0x50\n"
"s_mov_b32 s8, %[wid]\n"
"s_mov_b32 s9, %[blk]\n"
"v_mbcnt_lo_u32_b32 v255, -1, 0\n"
"v_mbcnt_hi_u32_b32 v255, -1, v255\n"
"v_lshlrev_b32 v254, 4, v255\n"
"s_and_b32 s10, s9, 7\n"
"s_lshl_b32 s10, s10, 2\n"
"s_lshr_b32 s12, s9, 6\n"
"s_add_u32 s10, s10, s12\n"
"s_lshr_b32 s11, s9, 3\n"
"s_and_b32 s11, s11, 7\n"
"s_getpc_b64 s[4:5]\n"
"s_lshr_b32 s14, s9, 3\n"
"s_lshl_b32 s14, s14, 2\n"
"s_add_u32 s14, s14, s8\n"
"s_lshl_b32 s14, s14, 10\n"
"s_min_u32 s14, s14, 0x19400\n"
"v_and_b32 v248, 7, v255\n"
"v_lshl_add_u32 v248, v248, 7, s14\n"
"global_load_dword v178, v248, s[4:5]\n"
"v_lshrrev_b32 v160, 3, v255\n"
"v_lshrrev_b32 v161, 4, v255\n"
"v_and_b32 v162, 7, v255\n"
"s_lshl_b32 s12, s11, 8\n"
"s_lshl_b32 s13, s8, 6\n"
"s_add_u32 s12, s12, s13\n"
"v_add_u32 v160, s12, v160\n"
"v_lshlrev_b32 v160, 10, v160\n"
"v_xor_b32 v163, v162, v161\n"
"v_lshl_add_u32 v128, v163, 4, v160\n"
"v_xor_b32 v163, 4, v163\n"
"v_lshl_add_u32 v129, v163, 4, v160\n"
"v_add_u32 v129, 0x1c00, v129\n"
"v_add_u32 v130, 0x3800, v128\n"
"v_add_u32 v131, 0x3800, v129\n"
"v_add_u32 v132, 0x8000, v128\n"
"v_add_u32 v133, 0x8000, v129\n"
"v_add_u32 v134, 0xb800, v128\n"
"v_add_u32 v135, 0xb800, v129\n"
"s_lshl_b32 s12, s8, 4\n"
"v_add_u32 v160, s12, v161\n"
"v_lshlrev_b32 v160, 13, v160\n"
"s_lshl_b32 s12, s10, 8\n"
"v_add_u32 v160, s12, v160\n"
"v_and_b32 v162, 15, v255\n"
"v_lshlrev_b32 v163, 1, v161\n"
"v_xor_b32 v162, v162, v163\n"
"v_lshl_add_u32 v136, v162, 4, v160\n"
"v_add_u32 v137, 0x7c00, v136\n"
"v_xor_b32 v162, 8, v162\n"
"v_lshl_add_u32 v138, v162, 4, v160\n"
"v_add_u32 v139, 0x17400, v138\n"
"v_add_u32 v138, 0xf800, v138\n"
"s_lshl_b32 s70, s8, 13\n"
"s_lshl_b32 s71, s8, 12\n"
"s_add_u32 s71, s71, 0x8000\n"
"s_mov_b32 s68, 0\n"
"s_mov_b32 s69, 0\n"
"s_lshl_b32 s12, s9, 13\n"
"s_lshl_b32 s13, s8, 11\n"
"s_add_u32 s12, s12, s13\n"
"v_add_u32 v238, s12, v254\n"
"s_waitcnt lgkmcnt(0)\n"
"s_load_dwordx2 s[66:67], s[60:61], 0\n"
"s_lshl_b32 s14, s9, 7\n"
"v_mov_b32 v179, s14\n"
"s_mov_b32 s16, s42\n"
"s_and_b32 s17, s43, 0xffff\n"
"s_mov_b32 s18, 0x200000\n"
"s_mov_b32 s19, 0x20000\n"
"s_mov_b32 s20, s46\n"
"s_and_b32 s21, s47, 0xffff\n"
"s_mov_b32 s22, 0x400000\n"
"s_mov_b32 s23, 0x20000\n"
"s_mov_b32 s24, s48\n"
"s_and_b32 s25, s49, 0xffff\n"
"s_mov_b32 s26, 0x400000\n"
"s_mov_b32 s27, 0x20000\n"
"s_add_u32 m0, s70, 0\n"
"s_nop 0\n"
"buffer_load_dwordx4 v128, s[16:19], s68 offen lds\n"
"buffer_load_dwordx4 v129, s[16:19], s68 offen offset:1024 lds\n"
"buffer_load_dwordx4 v130, s[16:19], s68 offen offset:2048 lds\n"
"buffer_load_dwordx4 v131, s[16:19], s68 offen offset:3072 lds\n"
"s_add_u32 m0, s70, 0x1000\n"
"s_nop 0\n"
"buffer_load_dwordx4 v132, s[16:19], s68 offen lds\n"
"buffer_load_dwordx4 v133, s[16:19], s68 offen offset:1024 lds\n"
"buffer_load_dwordx4 v134, s[16:19], s68 offen offset:2048 lds\n"
"buffer_load_dwordx4 v135, s[16:19], s68 offen offset:3072 lds\n"
"s_add_u32 m0, s71, 0\n"
"s_nop 0\n"
"buffer_load_dwordx4 v136, s[20:23], s69 offen lds\n"
"buffer_load_dwordx4 v137, s[20:23], s69 offen offset:1024 lds\n"
"buffer_load_dwordx4 v138, s[20:23], s69 offen offset:2048 lds\n"
"buffer_load_dwordx4 v139, s[20:23], s69 offen offset:3072 lds\n"
"s_add_u32 m0, s71, 0x4000\n"
"s_nop 0\n"
"buffer_load_dwordx4 v136, s[24:27], s69 offen lds\n"
"buffer_load_dwordx4 v137, s[24:27], s69 offen offset:1024 lds\n"
"buffer_load_dwordx4 v138, s[24:27], s69 offen offset:2048 lds\n"
"buffer_load_dwordx4 v139, s[24:27], s69 offen offset:3072 lds\n"
"s_add_u32 s68, s68, 0x80\n"
"s_add_u32 s69, s69, 0x80000\n"
"global_load_dwordx4 v[240:243], v238, s[40:41]\n"
"global_load_dwordx4 v[244:247], v238, s[40:41] offset:1024\n"
"s_lshl_b32 s12, s11, 3\n"
"s_lshl_b32 s13, s8, 1\n"
"s_add_u32 s12, s12, s13\n"
"s_add_u32 s12, s12, 64\n"
"v_and_b32 v183, 1, v255\n"
"v_add_u32 v183, s12, v183\n"
"v_lshlrev_b32 v183, 13, v183\n"
"v_bfe_u32 v184, v255, 1, 1\n"
"v_lshl_add_u32 v183, v184, 7, v183\n"
"s_lshl_b32 s13, s10, 8\n"
"v_add_u32 v183, s13, v183\n"
"s_lshl_b32 s12, s11, 8\n"
"s_and_b32 s13, s10, 3\n"
"s_lshl_b32 s13, s13, 6\n"
"s_add_u32 s12, s12, s13\n"
"s_lshl_b32 s13, s8, 4\n"
"s_add_u32 s12, s12, s13\n"
"v_and_b32 v184, 15, v255\n"
"v_add_u32 v184, s12, v184\n"
"v_lshlrev_b32 v184, 10, v184\n"
"v_add_u32 v184, 0x80, v184\n"
"v_and_b32 v185, 16, v255\n"
"v_cmp_eq_u32 s[62:63], 0, v185\n"
"v_and_b32 v185, 4, v255\n"
"v_cmp_eq_u32 vcc, 4, v185\n"
"v_mov_b32 v180, s46\n"
"v_mov_b32 v181, s47\n"
"v_mov_b32 v182, s48\n"
"v_mov_b32 v185, s49\n"
"v_cndmask_b32 v180, v180, v182, vcc\n"
"v_cndmask_b32 v181, v181, v185, vcc\n"
"v_mov_b32 v182, s42\n"
"v_mov_b32 v185, s43\n"
"v_cndmask_b32 v180, v180, v182, s[62:63]\n"
"v_cndmask_b32 v181, v181, v185, s[62:63]\n"
"v_cndmask_b32 v183, v183, v184, s[62:63]\n"
"v_mov_b32 v182, 0x80000\n"
"v_mov_b32 v185, 0x80\n"
"v_cndmask_b32 v182, v182, v185, s[62:63]\n"
"v_add_co_u32 v180, vcc, v183, v180\n"
"s_nop 1\n"
"v_addc_co_u32 v181, vcc, 0, v181, vcc\n"
"global_load_dword v178, v[180:181], off\n"
"v_add_co_u32 v180, vcc, v182, v180\n"
"s_nop 1\n"
"v_addc_co_u32 v181, vcc, 0, v181, vcc\n"
"global_load_dword v178, v[180:181], off\n"
"v_add_co_u32 v180, vcc, v182, v180\n"
"s_nop 1\n"
"v_addc_co_u32 v181, vcc, 0, v181, vcc\n"
"global_load_dword v178, v179, s[60:61] sc1\n"
"v_lshrrev_b32 v161, 4, v255\n"
"v_and_b32 v164, 15, v255\n"
"s_and_b32 s12, s8, 1\n"
"s_lshl_b32 s12, s12, 14\n"
"v_lshlrev_b32 v160, 7, v164\n"
"v_add_u32 v160, s12, v160\n"
"v_lshrrev_b32 v162, 1, v164\n"
"v_xor_b32 v162, v162, v161\n"
"v_lshl_add_u32 v140, v162, 4, v160\n"
"v_xor_b32 v141, 64, v140\n"
"v_add_u32 v142, 0x10000, v140\n"
"v_add_u32 v143, 0x10000, v141\n"
"v_lshrrev_b32 v165, 2, v164\n"
"v_and_b32 v166, 3, v255\n"
"v_lshl_add_u32 v160, v161, 3, v165\n"
"v_lshlrev_b32 v160, 8, v160\n"
"v_lshl_add_u32 v160, v166, 3, v160\n"
"v_add_u32 v160, 0x8000, v160\n"
"v_and_b32 v162, 1, v161\n"
"v_lshl_or_b32 v162, v162, 2, v165\n"
"s_lshr_b32 s12, s8, 1\n"
"s_lshl_b32 s12, s12, 2\n"
"v_xor_b32 v162, s12, v162\n"
"v_xor_b32 v163, 0, v162\n"
"v_lshl_add_u32 v144, v163, 5, v160\n"
"v_add_u32 v148, 0x10000, v144\n"
"v_xor_b32 v163, 1, v162\n"
"v_lshl_add_u32 v145, v163, 5, v160\n"
"v_add_u32 v149, 0x10000, v145\n"
"v_xor_b32 v163, 2, v162\n"
"v_lshl_add_u32 v146, v163, 5, v160\n"
"v_add_u32 v150, 0x10000, v146\n"
"v_xor_b32 v163, 3, v162\n"
"v_lshl_add_u32 v147, v163, 5, v160\n"
"v_add_u32 v151, 0x10000, v147\n"
"s_lshl_b32 s72, s10, 19\n"
"s_lshl_b32 s12, s11, 16\n"
"s_add_u32 s72, s72, s12\n"
"s_and_b32 s12, s8, 1\n"
"s_lshl_b32 s12, s12, 15\n"
"s_add_u32 s72, s72, s12\n"
"s_lshr_b32 s12, s8, 1\n"
"s_lshl_b32 s13, s12, 12\n"
"s_add_u32 s73, s72, s13\n"
"s_lshl_b32 s13, s12, 11\n"
"s_add_u32 s72, s72, s13\n"
"s_waitcnt vmcnt(5) lgkmcnt(0)\n"
"s_barrier\n"
"ds_read_b128 a[192:195], v140\n"
"ds_read_b128 a[196:199], v141\n"
"ds_read_b128 a[200:203], v140 offset:2048\n"
"ds_read_b128 a[204:207], v141 offset:2048\n"
"ds_read_b64_tr_b16 v[0:1], v144\n"
"ds_read_b64_tr_b16 v[2:3], v144 offset:1024\n"
"ds_read_b64_tr_b16 v[32:33], v144 offset:8192\n"
"ds_read_b64_tr_b16 v[34:35], v144 offset:9216\n"
"ds_read_b64_tr_b16 v[4:5], v145\n"
"ds_read_b64_tr_b16 v[6:7], v145 offset:1024\n"
"ds_read_b64_tr_b16 v[36:37], v145 offset:8192\n"
"ds_read_b64_tr_b16 v[38:39], v145 offset:9216\n"
"ds_read_b64_tr_b16 v[8:9], v146\n"
"ds_read_b64_tr_b16 v[10:11], v146 offset:1024\n"
"ds_read_b64_tr_b16 v[40:41], v146 offset:8192\n"
"ds_read_b64_tr_b16 v[42:43], v146 offset:9216\n"
"ds_read_b64_tr_b16 v[12:13], v147\n"
"ds_read_b64_tr_b16 v[14:15], v147 offset:1024\n"
"ds_read_b64_tr_b16 v[44:45], v147 offset:8192\n"
"ds_read_b64_tr_b16 v[46:47], v147 offset:9216\n"
"ds_read_b64_tr_b16 v[16:17], v144 offset:16384\n"
"ds_read_b64_tr_b16 v[18:19], v144 offset:17408\n"
"ds_read_b64_tr_b16 v[48:49], v144 offset:24576\n"
"ds_read_b64_tr_b16 v[50:51], v144 offset:25600\n"
"ds_read_b64_tr_b16 v[20:21], v145 offset:16384\n"
"ds_read_b64_tr_b16 v[22:23], v145 offset:17408\n"
"ds_read_b64_tr_b16 v[52:53], v145 offset:24576\n"
"ds_read_b64_tr_b16 v[54:55], v145 offset:25600\n"
"ds_read_b64_tr_b16 v[24:25], v146 offset:16384\n"
"ds_read_b64_tr_b16 v[26:27], v146 offset:17408\n"
"ds_read_b64_tr_b16 v[56:57], v146 offset:24576\n"
"ds_read_b64_tr_b16 v[58:59], v146 offset:25600\n"
"ds_read_b64_tr_b16 v[28:29], v147 offset:16384\n"
"ds_read_b64_tr_b16 v[30:31], v147 offset:17408\n"
"ds_read_b64_tr_b16 v[60:61], v147 offset:24576\n"
"ds_read_b64_tr_b16 v[62:63], v147 offset:25600\n"
"s_waitcnt lgkmcnt(15)\n"
"v_mfma_f32_16x16x32_bf16 a[0:3], v[0:3], a[192:195], 0\n"
"v_mfma_f32_16x16x32_bf16 a[0:3], v[32:35], a[196:199], a[0:3]\n"
"s_add_u32 m0, s70, 0x10000\n"
"v_mfma_f32_16x16x32_bf16 a[32:35], v[32:35], a[204:207], 0\n"
"buffer_load_dwordx4 v128, s[16:19], s68 offen lds\n"
"v_mfma_f32_16x16x32_bf16 a[32:35], v[0:3], a[200:203], a[32:35]\n"
"s_lshl_b32 s12, s11, 6\n"
"s_waitcnt lgkmcnt(15)\n"
"v_mfma_f32_16x16x32_bf16 a[4:7], v[4:7], a[192:195], 0\n"
"s_lshl_b32 s13, s8, 4\n"
"v_mfma_f32_16x16x32_bf16 a[4:7], v[36:39], a[196:199], a[4:7]\n"
"buffer_load_dwordx4 v129, s[16:19], s68 offen offset:1024 lds\n"
"v_mfma_f32_16x16x32_bf16 a[36:39], v[36:39], a[204:207], 0\n"
"ds_read_b128 a[208:211], v140 offset:4096\n"
"s_add_u32 s12, s12, s13\n"
"v_mfma_f32_16x16x32_bf16 a[36:39], v[4:7], a[200:203], a[36:39]\n"
"v_bfe_u32 v183, v255, 1, 2\n"
"s_waitcnt lgkmcnt(15)\n"
"v_mfma_f32_16x16x32_bf16 a[8:11], v[8:11], a[192:195], 0\n"
"ds_read_b128 a[212:215], v141 offset:4096\n"
"v_add_u32 v183, s12, v183\n"
"v_mfma_f32_16x16x32_bf16 a[8:11], v[40:43], a[196:199], a[8:11]\n"
"buffer_load_dwordx4 v130, s[16:19], s68 offen offset:2048 lds\n"
"v_mfma_f32_16x16x32_bf16 a[40:43], v[40:43], a[204:207], 0\n"
"v_and_b32 v184, 1, v255\n"
"v_mfma_f32_16x16x32_bf16 a[40:43], v[8:11], a[200:203], a[40:43]\n"
"v_lshlrev_b32 v186, 13, v183\n"
"s_waitcnt lgkmcnt(15)\n"
"v_mfma_f32_16x16x32_bf16 a[12:15], v[12:15], a[192:195], 0\n"
"buffer_load_dwordx4 v131, s[16:19], s68 offen offset:3072 lds\n"
"v_mfma_f32_16x16x32_bf16 a[12:15], v[44:47], a[196:199], a[12:15]\n"
"s_add_u32 m0, s70, 0x11000\n"
"v_lshl_add_u32 v186, v184, 7, v186\n"
"v_mfma_f32_16x16x32_bf16 a[44:47], v[44:47], a[204:207], 0\n"
"buffer_load_dwordx4 v132, s[16:19], s68 offen lds\n"
"v_mfma_f32_16x16x32_bf16 a[44:47], v[12:15], a[200:203], a[44:47]\n"
"s_lshl_b32 s13, s10, 8\n"
"s_waitcnt lgkmcnt(14)\n"
"v_mfma_f32_16x16x32_bf16 a[16:19], a[192:195], v[16:19], 0\n"
"v_add_u32 v186, s13, v186\n"
"v_mfma_f32_16x16x32_bf16 a[16:19], a[196:199], v[48:51], a[16:19]\n"
"buffer_load_dwordx4 v133, s[16:19], s68 offen offset:1024 lds\n"
"v_mfma_f32_16x16x32_bf16 a[48:51], a[204:207], v[48:51], 0\n"
"ds_read_b128 a[216:219], v140 offset:6144\n"
"v_mul_u32_u24 v187, 0xac0, v183\n"
"v_mfma_f32_16x16x32_bf16 a[48:51], a[200:203], v[16:19], a[48:51]\n"
"v_mul_u32_u24 v184, 0x5c, v184\n"
"s_waitcnt lgkmcnt(11)\n"
"v_mfma_f32_16x16x32_bf16 a[20:23], a[192:195], v[20:23], 0\n"
"ds_read_b128 a[220:223], v141 offset:6144\n"
"v_add_u32 v187, v184, v187\n"
"v_mfma_f32_16x16x32_bf16 a[20:23], a[196:199], v[52:55], a[20:23]\n"
"buffer_load_dwordx4 v134, s[16:19], s68 offen offset:2048 lds\n"
"v_mfma_f32_16x16x32_bf16 a[52:55], a[204:207], v[52:55], 0\n"
"s_mul_i32 s13, s10, 0x60\n"
"v_mfma_f32_16x16x32_bf16 a[52:55], a[200:203], v[20:23], a[52:55]\n"
"v_add_u32 v187, s13, v187\n"
"s_waitcnt lgkmcnt(8)\n"
"v_mfma_f32_16x16x32_bf16 a[24:27], a[192:195], v[24:27], 0\n"
"buffer_load_dwordx4 v135, s[16:19], s68 offen offset:3072 lds\n"
"v_mfma_f32_16x16x32_bf16 a[24:27], a[196:199], v[56:59], a[24:27]\n"
"s_add_u32 m0, s71, 0x10000\n"
"v_min_u32 v187, 0x14fefc, v187\n"
"v_mfma_f32_16x16x32_bf16 a[56:59], a[204:207], v[56:59], 0\n"
"buffer_load_dwordx4 v136, s[20:23], s69 offen lds\n"
"v_mfma_f32_16x16x32_bf16 a[56:59], a[200:203], v[24:27], a[56:59]\n"
"s_lshl_b32 s12, s9, 2\n"
"s_waitcnt lgkmcnt(4)\n"
"v_mfma_f32_16x16x32_bf16 a[28:31], a[192:195], v[28:31], 0\n"
"s_add_u32 s12, s12, s8\n"
"v_mfma_f32_16x16x32_bf16 a[28:31], a[196:199], v[60:63], a[28:31]\n"
"buffer_load_dwordx4 v137, s[20:23], s69 offen offset:1024 lds\n"
"v_mfma_f32_16x16x32_bf16 a[60:63], a[204:207], v[60:63], 0\n"
"s_lshl_b32 s13, s12, 12\n"
"v_mfma_f32_16x16x32_bf16 a[60:63], a[200:203], v[28:31], a[60:63]\n"
"v_and_b32 v176, 15, v255\n"
"s_waitcnt lgkmcnt(2)\n"
"v_mfma_f32_16x16x32_bf16 a[64:67], v[0:3], a[208:211], 0\n"
"buffer_load_dwordx4 v138, s[20:23], s69 offen offset:2048 lds\n"
"v_mfma_f32_16x16x32_bf16 a[64:67], v[32:35], a[212:215], a[64:67]\n"
"v_lshl_add_u32 v176, v176, 7, s13\n"
"v_mfma_f32_16x16x32_bf16 a[68:71], v[36:39], a[212:215], 0\n"
"s_mul_i32 s13, s12, 0x560\n"
"v_mfma_f32_16x16x32_bf16 a[68:71], v[4:7], a[208:211], a[68:71]\n"
"buffer_load_dwordx4 v139, s[20:23], s69 offen offset:3072 lds\n"
"v_mfma_f32_16x16x32_bf16 a[72:75], v[8:11], a[208:211], 0\n"
"ds_read_b128 a[192:195], v140 offset:8192\n"
"v_and_b32 v177, 15, v255\n"
"v_mfma_f32_16x16x32_bf16 a[72:75], v[40:43], a[212:215], a[72:75]\n"
"s_add_u32 m0, s71, 0x14000\n"
"v_lshl_add_u32 v177, v177, 7, s13\n"
"v_mfma_f32_16x16x32_bf16 a[76:79], v[44:47], a[212:215], 0\n"
"ds_read_b128 a[196:199], v141 offset:8192\n"
"v_min_u32 v177, 0x157ffc, v177\n"
"v_mfma_f32_16x16x32_bf16 a[76:79], v[12:15], a[208:211], a[76:79]\n"
"buffer_load_dwordx4 v136, s[24:27], s69 offen lds\n"
"v_mfma_f32_16x16x32_bf16 a[80:83], a[208:211], v[16:19], 0\n"
"s_add_u32 s28, s60, 0x400000\n"
"s_addc_u32 s29, s61, 0\n"
"v_mfma_f32_16x16x32_bf16 a[80:83], a[212:215], v[48:51], a[80:83]\n"
"buffer_load_dwordx4 v137, s[24:27], s69 offen offset:1024 lds\n"
"v_mfma_f32_16x16x32_bf16 a[84:87], a[212:215], v[52:55], 0\n"
"s_and_b32 s29, s29, 0xffff\n"
"v_mfma_f32_16x16x32_bf16 a[84:87], a[208:211], v[20:23], a[84:87]\n"
"s_mov_b32 s30, 0x1000000\n"
"v_mfma_f32_16x16x32_bf16 a[88:91], a[208:211], v[24:27], 0\n"
"buffer_load_dwordx4 v138, s[24:27], s69 offen offset:2048 lds\n"
"v_mfma_f32_16x16x32_bf16 a[88:91], a[212:215], v[56:59], a[88:91]\n"
"s_mov_b32 s31, 0x20000\n"
"v_mfma_f32_16x16x32_bf16 a[92:95], a[212:215], v[60:63], 0\n"
"s_add_u32 s96, s60, 0x1400000\n"
"s_addc_u32 s97, s61, 0\n"
"v_mfma_f32_16x16x32_bf16 a[92:95], a[208:211], v[28:31], a[92:95]\n"
"s_and_b32 s97, s97, 0xffff\n"
"s_waitcnt lgkmcnt(2)\n"
"v_mfma_f32_16x16x32_bf16 a[96:99], v[0:3], a[216:219], 0\n"
"ds_read_b128 a[200:203], v140 offset:10240\n"
"s_mov_b32 s98, 0x1000000\n"
"v_mfma_f32_16x16x32_bf16 a[96:99], v[32:35], a[220:223], a[96:99]\n"
"buffer_load_dwordx4 v139, s[24:27], s69 offen offset:3072 lds\n"
"v_mfma_f32_16x16x32_bf16 a[100:103], v[36:39], a[220:223], 0\n"
"ds_read_b128 a[204:207], v141 offset:10240\n"
"s_add_u32 s68, s68, 0x80\n"
"s_add_u32 s69, s69, 0x80000\n"
"v_mfma_f32_16x16x32_bf16 a[100:103], v[4:7], a[216:219], a[100:103]\n"
"global_load_dword v178, v[180:181], off\n"
"v_mfma_f32_16x16x32_bf16 a[104:107], v[8:11], a[216:219], 0\n"
"s_mov_b32 s99, 0x20000\n"
"v_mfma_f32_16x16x32_bf16 a[104:107], v[40:43], a[220:223], a[104:107]\n"
"s_add_u32 s36, s60, 0x2400000\n"
"s_addc_u32 s37, s61, 0\n"
"v_mfma_f32_16x16x32_bf16 a[108:111], v[44:47], a[220:223], 0\n"
"s_and_b32 s37, s37, 0xffff\n"
"v_mfma_f32_16x16x32_bf16 a[108:111], v[12:15], a[216:219], a[108:111]\n"
"s_mov_b32 s38, 0x1560000\n"
"v_mfma_f32_16x16x32_bf16 a[112:115], a[216:219], v[16:19], 0\n"
"s_mov_b32 s39, 0x20000\n"
"v_mfma_f32_16x16x32_bf16 a[112:115], a[220:223], v[48:51], a[112:115]\n"
"v_add_co_u32 v180, vcc, v182, v180\n"
"s_nop 1\n"
"v_addc_co_u32 v181, vcc, 0, v181, vcc\n"
"v_mfma_f32_16x16x32_bf16 a[116:119], a[220:223], v[52:55], 0\n"
"s_mov_b32 s92, s60\n"
"v_mfma_f32_16x16x32_bf16 a[116:119], a[216:219], v[20:23], a[116:119]\n"
"s_and_b32 s93, s61, 0xffff\n"
"v_mfma_f32_16x16x32_bf16 a[120:123], a[216:219], v[24:27], 0\n"
"ds_read_b128 a[208:211], v140 offset:12288\n"
"s_mov_b32 s94, 0x8000\n"
"v_mfma_f32_16x16x32_bf16 a[120:123], a[220:223], v[56:59], a[120:123]\n"
"s_mov_b32 s95, 0x20000\n"
"v_mfma_f32_16x16x32_bf16 a[124:127], a[220:223], v[60:63], 0\n"
"ds_read_b128 a[212:215], v141 offset:12288\n"
"v_mfma_f32_16x16x32_bf16 a[124:127], a[216:219], v[28:31], a[124:127]\n"
"s_waitcnt lgkmcnt(4)\n"
"v_mfma_f32_16x16x32_bf16 a[128:131], v[0:3], a[192:195], 0\n"
"s_add_u32 s66, s66, 1\n"
"s_addc_u32 s67, s67, 0\n"
"v_mfma_f32_16x16x32_bf16 a[128:131], v[32:35], a[196:199], a[128:131]\n"
"v_mov_b32 v250, s66\n"
"v_mfma_f32_16x16x32_bf16 a[132:135], v[36:39], a[196:199], 0\n"
"v_mov_b32 v251, s67\n"
"v_mfma_f32_16x16x32_bf16 a[132:135], v[4:7], a[192:195], a[132:135]\n"
"v_mov_b32 v249, 0\n"
"v_mfma_f32_16x16x32_bf16 a[136:139], v[8:11], a[192:195], 0\n"
"s_cmp_eq_u32 s8, 0\n"
"s_cselect_b32 s34, 1, 0\n"
"v_mfma_f32_16x16x32_bf16 a[136:139], v[40:43], a[196:199], a[136:139]\n"
"s_mov_b32 s35, 0\n"
"v_mfma_f32_16x16x32_bf16 a[140:143], v[44:47], a[196:199], 0\n"
"ds_read_b128 a[216:219], v140 offset:14336\n"
"s_waitcnt vmcnt(20)\n"
"v_mfma_f32_16x16x32_bf16 a[140:143], v[12:15], a[192:195], a[140:143]\n"
"s_add_u32 s64, s60, 0x100000\n"
"s_addc_u32 s65, s61, 0\n"
"v_mfma_f32_16x16x32_bf16 a[144:147], a[192:195], v[16:19], 0\n"
"ds_read_b128 a[220:223], v141 offset:14336\n"
"s_and_b32 s65, s65, 0xffff\n"
"v_mfma_f32_16x16x32_bf16 a[144:147], a[196:199], v[48:51], a[144:147]\n"
"s_mov_b32 s66, 0x200000\n"
"v_mfma_f32_16x16x32_bf16 a[148:151], a[196:199], v[52:55], 0\n"
"s_mov_b32 s67, 0x20000\n"
"v_mfma_f32_16x16x32_bf16 a[148:151], a[192:195], v[20:23], a[148:151]\n"
"v_lshlrev_b32 v208, 16, v240\n"
"v_mfma_f32_16x16x32_bf16 a[152:155], a[192:195], v[24:27], 0\n"
"v_and_b32 v209, 0xffff0000, v240\n"
"v_mfma_f32_16x16x32_bf16 a[152:155], a[196:199], v[56:59], a[152:155]\n"
"v_lshlrev_b32 v210, 16, v241\n"
"v_mfma_f32_16x16x32_bf16 a[156:159], a[196:199], v[60:63], 0\n"
"v_and_b32 v211, 0xffff0000, v241\n"
"v_mfma_f32_16x16x32_bf16 a[156:159], a[192:195], v[28:31], a[156:159]\n"
"v_lshlrev_b32 v212, 16, v242\n"
"s_waitcnt lgkmcnt(4)\n"
"v_mfma_f32_16x16x32_bf16 a[160:163], v[0:3], a[200:203], 0\n"
"v_and_b32 v213, 0xffff0000, v242\n"
"v_mfma_f32_16x16x32_bf16 a[160:163], v[32:35], a[204:207], a[160:163]\n"
"v_lshlrev_b32 v214, 16, v243\n"
"v_mfma_f32_16x16x32_bf16 a[164:167], v[36:39], a[204:207], 0\n"
"v_and_b32 v215, 0xffff0000, v243\n"
"v_mfma_f32_16x16x32_bf16 a[164:167], v[4:7], a[200:203], a[164:167]\n"
"v_lshlrev_b32 v216, 16, v244\n"
"v_mfma_f32_16x16x32_bf16 a[168:171], v[8:11], a[200:203], 0\n"
"v_and_b32 v217, 0xffff0000, v244\n"
"v_mfma_f32_16x16x32_bf16 a[168:171], v[40:43], a[204:207], a[168:171]\n"
"v_lshlrev_b32 v218, 16, v245\n"
"v_mfma_f32_16x16x32_bf16 a[172:175], v[44:47], a[204:207], 0\n"
"v_and_b32 v219, 0xffff0000, v245\n"
"v_mfma_f32_16x16x32_bf16 a[172:175], v[12:15], a[200:203], a[172:175]\n"
"v_lshlrev_b32 v220, 16, v246\n"
"v_mfma_f32_16x16x32_bf16 a[176:179], a[200:203], v[16:19], 0\n"
"v_and_b32 v221, 0xffff0000, v246\n"
"v_mfma_f32_16x16x32_bf16 a[176:179], a[204:207], v[48:51], a[176:179]\n"
"v_lshlrev_b32 v222, 16, v247\n"
"v_mfma_f32_16x16x32_bf16 a[180:183], a[204:207], v[52:55], 0\n"
"v_and_b32 v223, 0xffff0000, v247\n"
"v_mfma_f32_16x16x32_bf16 a[180:183], a[200:203], v[20:23], a[180:183]\n"
"v_mfma_f32_16x16x32_bf16 a[184:187], a[200:203], v[24:27], 0\n"
"s_waitcnt vmcnt(1) lgkmcnt(0)\n"
"s_barrier\n"
"v_mfma_f32_16x16x32_bf16 a[184:187], a[204:207], v[56:59], a[184:187]\n"
"ds_read_b128 a[192:195], v142\n"
"v_pk_add_f32 v[232:233], v[208:209], v[210:211]\n"
"v_mfma_f32_16x16x32_bf16 a[188:191], a[204:207], v[60:63], 0\n"
"ds_read_b128 a[196:199], v143\n"
"v_pk_add_f32 v[236:237], v[216:217], v[218:219]\n"
"v_mfma_f32_16x16x32_bf16 a[188:191], a[200:203], v[28:31], a[188:191]\n"
"s_add_u32 m0, s70, 0\n"
"v_pk_add_f32 v[234:235], v[212:213], v[214:215]\n"
"v_mfma_f32_16x16x32_bf16 v[64:67], v[0:3], a[208:211], 0\n"
"ds_read_b128 a[200:203], v142 offset:2048\n"
"v_pk_add_f32 v[238:239], v[220:221], v[222:223]\n"
"v_mfma_f32_16x16x32_bf16 v[64:67], v[32:35], a[212:215], v[64:67]\n"
"buffer_load_dwordx4 v128, s[16:19], s68 offen lds\n"
"v_mfma_f32_16x16x32_bf16 v[96:99], v[32:35], a[220:223], 0\n"
"ds_read_b128 a[204:207], v143 offset:2048\n"
"v_pk_add_f32 v[232:233], v[232:233], v[234:235]\n"
"v_mfma_f32_16x16x32_bf16 v[96:99], v[0:3], a[216:219], v[96:99]\n"
"v_pk_add_f32 v[236:237], v[236:237], v[238:239]\n"
"v_mfma_f32_16x16x32_bf16 v[68:71], v[4:7], a[208:211], 0\n"
"ds_read_b64_tr_b16 v[0:1], v148\n"
"ds_read_b64_tr_b16 v[2:3], v148 offset:1024\n"
"v_mfma_f32_16x16x32_bf16 v[68:71], v[36:39], a[212:215], v[68:71]\n"
"buffer_load_dwordx4 v129, s[16:19], s68 offen offset:1024 lds\n"
"v_mfma_f32_16x16x32_bf16 v[100:103], v[36:39], a[220:223], 0\n"
"ds_read_b64_tr_b16 v[32:33], v148 offset:8192\n"
"ds_read_b64_tr_b16 v[34:35], v148 offset:9216\n"
"v_mfma_f32_16x16x32_bf16 v[100:103], v[4:7], a[216:219], v[100:103]\n"
"v_add_f32 v224, v232, v233\n"
"v_mfma_f32_16x16x32_bf16 v[72:75], v[8:11], a[208:211], 0\n"
"ds_read_b64_tr_b16 v[4:5], v149\n"
"ds_read_b64_tr_b16 v[6:7], v149 offset:1024\n"
"v_mfma_f32_16x16x32_bf16 v[72:75], v[40:43], a[212:215], v[72:75]\n"
"buffer_load_dwordx4 v130, s[16:19], s68 offen offset:2048 lds\n"
"v_mfma_f32_16x16x32_bf16 v[104:107], v[40:43], a[220:223], 0\n"
"ds_read_b64_tr_b16 v[36:37], v149 offset:8192\n"
"ds_read_b64_tr_b16 v[38:39], v149 offset:9216\n"
"v_mfma_f32_16x16x32_bf16 v[104:107], v[8:11], a[216:219], v[104:107]\n"
"v_add_f32 v225, v236, v237\n"
"v_mfma_f32_16x16x32_bf16 v[76:79], v[12:15], a[208:211], 0\n"
"ds_read_b64_tr_b16 v[8:9], v150\n"
"ds_read_b64_tr_b16 v[10:11], v150 offset:1024\n"
"v_mfma_f32_16x16x32_bf16 v[76:79], v[44:47], a[212:215], v[76:79]\n"
"buffer_load_dwordx4 v131, s[16:19], s68 offen offset:3072 lds\n"
"v_mfma_f32_16x16x32_bf16 v[108:111], v[44:47], a[220:223], 0\n"
"ds_read_b64_tr_b16 v[40:41], v150 offset:8192\n"
"ds_read_b64_tr_b16 v[42:43], v150 offset:9216\n"
"v_mfma_f32_16x16x32_bf16 v[108:111], v[12:15], a[216:219], v[108:111]\n"
"s_add_u32 m0, s70, 0x1000\n"
"s_nop 0\n"
"v_mfma_f32_16x16x32_bf16 v[80:83], a[208:211], v[16:19], 0\n"
"ds_read_b64_tr_b16 v[12:13], v151\n"
"ds_read_b64_tr_b16 v[14:15], v151 offset:1024\n"
"v_mfma_f32_16x16x32_bf16 v[80:83], a[212:215], v[48:51], v[80:83]\n"
"buffer_load_dwordx4 v132, s[16:19], s68 offen lds\n"
"v_mfma_f32_16x16x32_bf16 v[112:115], a[220:223], v[48:51], 0\n"
"ds_read_b64_tr_b16 v[44:45], v151 offset:8192\n"
"ds_read_b64_tr_b16 v[46:47], v151 offset:9216\n"
"v_mfma_f32_16x16x32_bf16 v[112:115], a[216:219], v[16:19], v[112:115]\n"
"v_add_f32_dpp v224, v224, v224 quad_perm:[1,0,3,2] row_mask:0xf bank_mask:0xf\n"
"v_mfma_f32_16x16x32_bf16 v[84:87], a[208:211], v[20:23], 0\n"
"ds_read_b64_tr_b16 v[16:17], v148 offset:16384\n"
"ds_read_b64_tr_b16 v[18:19], v148 offset:17408\n"
"v_mfma_f32_16x16x32_bf16 v[84:87], a[212:215], v[52:55], v[84:87]\n"
"buffer_load_dwordx4 v133, s[16:19], s68 offen offset:1024 lds\n"
"v_mfma_f32_16x16x32_bf16 v[116:119], a[220:223], v[52:55], 0\n"
"ds_read_b64_tr_b16 v[48:49], v148 offset:24576\n"
"ds_read_b64_tr_b16 v[50:51], v148 offset:25600\n"
"v_mfma_f32_16x16x32_bf16 v[116:119], a[216:219], v[20:23], v[116:119]\n"
"v_add_f32_dpp v225, v225, v225 quad_perm:[1,0,3,2] row_mask:0xf bank_mask:0xf\n"
"v_mfma_f32_16x16x32_bf16 v[88:91], a[208:211], v[24:27], 0\n"
"ds_read_b64_tr_b16 v[20:21], v149 offset:16384\n"
"ds_read_b64_tr_b16 v[22:23], v149 offset:17408\n"
"v_mfma_f32_16x16x32_bf16 v[88:91], a[212:215], v[56:59], v[88:91]\n"
"buffer_load_dwordx4 v134, s[16:19], s68 offen offset:2048 lds\n"
"v_mfma_f32_16x16x32_bf16 v[120:123], a[220:223], v[56:59], 0\n"
"ds_read_b64_tr_b16 v[52:53], v149 offset:24576\n"
"ds_read_b64_tr_b16 v[54:55], v149 offset:25600\n"
"v_mfma_f32_16x16x32_bf16 v[120:123], a[216:219], v[24:27], v[120:123]\n"
"s_nop 0\n"
"v_mfma_f32_16x16x32_bf16 v[92:95], a[208:211], v[28:31], 0\n"
"ds_read_b64_tr_b16 v[24:25], v150 offset:16384\n"
"ds_read_b64_tr_b16 v[26:27], v150 offset:17408\n"
"v_mfma_f32_16x16x32_bf16 v[92:95], a[212:215], v[60:63], v[92:95]\n"
"buffer_load_dwordx4 v135, s[16:19], s68 offen offset:3072 lds\n"
"v_mfma_f32_16x16x32_bf16 v[124:127], a[220:223], v[60:63], 0\n"
"ds_read_b64_tr_b16 v[56:57], v150 offset:24576\n"
"ds_read_b64_tr_b16 v[58:59], v150 offset:25600\n"
"v_mfma_f32_16x16x32_bf16 v[124:127], a[216:219], v[28:31], v[124:127]\n"
"s_add_u32 m0, s71, 0\n"
"s_waitcnt lgkmcnt(15)\n"
"v_mfma_f32_16x16x32_bf16 a[0:3], v[0:3], a[192:195], a[0:3]\n"
"ds_read_b64_tr_b16 v[28:29], v151 offset:16384\n"
"ds_read_b64_tr_b16 v[30:31], v151 offset:17408\n"
"v_mfma_f32_16x16x32_bf16 a[0:3], v[32:35], a[196:199], a[0:3]\n"
"buffer_load_dwordx4 v136, s[20:23], s69 offen lds\n"
"v_mfma_f32_16x16x32_bf16 a[32:35], v[32:35], a[204:207], a[32:35]\n"
"ds_read_b64_tr_b16 v[60:61], v151 offset:24576\n"
"ds_read_b64_tr_b16 v[62:63], v151 offset:25600\n"
"v_mfma_f32_16x16x32_bf16 a[32:35], v[0:3], a[200:203], a[32:35]\n"
"v_add_f32_dpp v224, v224, v224 quad_perm:[2,3,0,1] row_mask:0xf bank_mask:0xf\n"
"v_mfma_f32_16x16x32_bf16 a[4:7], v[4:7], a[192:195], a[4:7]\n"
"buffer_load_dwordx4 v137, s[20:23], s69 offen offset:1024 lds\n"
"v_mfma_f32_16x16x32_bf16 a[4:7], v[36:39], a[196:199], a[4:7]\n"
"v_add_f32_dpp v225, v225, v225 quad_perm:[2,3,0,1] row_mask:0xf bank_mask:0xf\n"
"v_mfma_f32_16x16x32_bf16 a[36:39], v[36:39], a[204:207], a[36:39]\n"
"ds_read_b128 a[208:211], v142 offset:4096\n"
"s_nop 0\n"
"v_mfma_f32_16x16x32_bf16 a[36:39], v[4:7], a[200:203], a[36:39]\n"
"v_add_f32_dpp v224, v224, v224 row_half_mirror row_mask:0xf bank_mask:0xf\n"
"s_waitcnt lgkmcnt(15)\n"
"v_mfma_f32_16x16x32_bf16 a[8:11], v[8:11], a[192:195], a[8:11]\n"
"ds_read_b128 a[212:215], v143 offset:4096\n"
"v_add_f32_dpp v225, v225, v225 row_half_mirror row_mask:0xf bank_mask:0xf\n"
"v_mfma_f32_16x16x32_bf16 a[8:11], v[40:43], a[196:199], a[8:11]\n"
"buffer_load_dwordx4 v138, s[20:23], s69 offen offset:2048 lds\n"
"v_mfma_f32_16x16x32_bf16 a[40:43], v[40:43], a[204:207], a[40:43]\n"
"s_nop 0\n"
"v_mfma_f32_16x16x32_bf16 a[40:43], v[8:11], a[200:203], a[40:43]\n"
"v_add_f32_dpp v224, v224, v224 row_mirror row_mask:0xf bank_mask:0xf\n"
"v_mfma_f32_16x16x32_bf16 a[12:15], v[12:15], a[192:195], a[12:15]\n"
"buffer_load_dwordx4 v139, s[20:23], s69 offen offset:3072 lds\n"
"v_mfma_f32_16x16x32_bf16 a[12:15], v[44:47], a[196:199], a[12:15]\n"
"v_add_f32_dpp v225, v225, v225 row_mirror row_mask:0xf bank_mask:0xf\n"
"v_mfma_f32_16x16x32_bf16 a[44:47], v[44:47], a[204:207], a[44:47]\n"
"v_mov_b32 v226, v224\n"
"v_mfma_f32_16x16x32_bf16 a[44:47], v[12:15], a[200:203], a[44:47]\n"
"s_add_u32 m0, s71, 0x4000\n"
"s_waitcnt lgkmcnt(10)\n"
"v_mfma_f32_16x16x32_bf16 a[16:19], a[192:195], v[16:19], a[16:19]\n"
"buffer_load_dwordx4 v136, s[24:27], s69 offen lds\n"
"v_mfma_f32_16x16x32_bf16 a[16:19], a[196:199], v[48:51], a[16:19]\n"
"v_mov_b32 v227, v225\n"
"v_mfma_f32_16x16x32_bf16 a[48:51], a[204:207], v[48:51], a[48:51]\n"
"ds_read_b128 a[216:219], v142 offset:6144\n"
"s_nop 0\n"
"v_mfma_f32_16x16x32_bf16 a[48:51], a[200:203], v[16:19], a[48:51]\n"
"v_permlane16_swap_b32 v224, v226\n"
"v_mfma_f32_16x16x32_bf16 a[20:23], a[192:195], v[20:23], a[20:23]\n"
"ds_read_b128 a[220:223], v143 offset:6144\n"
"v_permlane16_swap_b32 v225, v227\n"
"v_mfma_f32_16x16x32_bf16 a[20:23], a[196:199], v[52:55], a[20:23]\n"
"buffer_load_dwordx4 v137, s[24:27], s69 offen offset:1024 lds\n"
"v_mfma_f32_16x16x32_bf16 a[52:55], a[204:207], v[52:55], a[52:55]\n"
"v_add_f32 v224, v224, v226\n"
"v_mfma_f32_16x16x32_bf16 a[52:55], a[200:203], v[20:23], a[52:55]\n"
"v_add_f32 v225, v225, v227\n"
"s_waitcnt lgkmcnt(4)\n"
"v_mfma_f32_16x16x32_bf16 a[24:27], a[192:195], v[24:27], a[24:27]\n"
"buffer_load_dwordx4 v138, s[24:27], s69 offen offset:2048 lds\n"
"v_mfma_f32_16x16x32_bf16 a[24:27], a[196:199], v[56:59], a[24:27]\n"
"v_mov_b32 v226, v224\n"
"v_mfma_f32_16x16x32_bf16 a[56:59], a[204:207], v[56:59], a[56:59]\n"
"v_mov_b32 v227, v225\n"
"v_mfma_f32_16x16x32_bf16 a[56:59], a[200:203], v[24:27], a[56:59]\n"
"s_nop 0\n"
"v_mfma_f32_16x16x32_bf16 a[28:31], a[192:195], v[28:31], a[28:31]\n"
"buffer_load_dwordx4 v139, s[24:27], s69 offen offset:3072 lds\n"
"v_mfma_f32_16x16x32_bf16 a[28:31], a[196:199], v[60:63], a[28:31]\n"
"v_permlane32_swap_b32 v224, v226\n"
"v_mfma_f32_16x16x32_bf16 a[60:63], a[204:207], v[60:63], a[60:63]\n"
"v_permlane32_swap_b32 v225, v227\n"
"v_mfma_f32_16x16x32_bf16 a[60:63], a[200:203], v[28:31], a[60:63]\n"
"s_add_u32 s68, s68, 0x80\n"
"s_waitcnt lgkmcnt(2)\n"
"v_mfma_f32_16x16x32_bf16 a[64:67], v[0:3], a[208:211], a[64:67]\n"
"s_add_u32 s69, s69, 0x80000\n"
"v_add_f32 v224, v224, v226\n"
"v_mfma_f32_16x16x32_bf16 a[64:67], v[32:35], a[212:215], a[64:67]\n"
"v_add_f32 v225, v225, v227\n"
"v_mfma_f32_16x16x32_bf16 a[68:71], v[36:39], a[212:215], a[68:71]\n"
"v_mul_f32 v228, 0xbb000000, v224\n"
"v_mfma_f32_16x16x32_bf16 a[68:71], v[4:7], a[208:211], a[68:71]\n"
"v_mul_f32 v230, 0xbb000000, v225\n"
"v_mfma_f32_16x16x32_bf16 a[72:75], v[8:11], a[208:211], a[72:75]\n"
"ds_read_b128 a[192:195], v142 offset:8192\n"
"v_pk_add_f32 v[208:209], v[208:209], v[228:229] op_sel_hi:[1,0]\n"
"v_mfma_f32_16x16x32_bf16 a[72:75], v[40:43], a[212:215], a[72:75]\n"
"global_load_dword v178, v[180:181], off\n"
"v_mfma_f32_16x16x32_bf16 a[76:79], v[44:47], a[212:215], a[76:79]\n"
"ds_read_b128 a[196:199], v143 offset:8192\n"
"v_pk_add_f32 v[216:217], v[216:217], v[230:231] op_sel_hi:[1,0]\n"
"v_mfma_f32_16x16x32_bf16 a[76:79], v[12:15], a[208:211], a[76:79]\n"
"v_pk_add_f32 v[210:211], v[210:211], v[228:229] op_sel_hi:[1,0]\n"
"v_mfma_f32_16x16x32_bf16 a[80:83], a[208:211], v[16:19], a[80:83]\n"
"v_pk_add_f32 v[218:219], v[218:219], v[230:231] op_sel_hi:[1,0]\n"
"v_mfma_f32_16x16x32_bf16 a[80:83], a[212:215], v[48:51], a[80:83]\n"
"v_pk_add_f32 v[212:213], v[212:213], v[228:229] op_sel_hi:[1,0]\n"
"v_mfma_f32_16x16x32_bf16 a[84:87], a[212:215], v[52:55], a[84:87]\n"
"global_load_dword v178, v186, s[44:45]\n"
"v_mfma_f32_16x16x32_bf16 a[84:87], a[208:211], v[20:23], a[84:87]\n"
"v_pk_add_f32 v[220:221], v[220:221], v[230:231] op_sel_hi:[1,0]\n"
"v_mfma_f32_16x16x32_bf16 a[88:91], a[208:211], v[24:27], a[88:91]\n"
"v_pk_add_f32 v[214:215], v[214:215], v[228:229] op_sel_hi:[1,0]\n"
"v_mfma_f32_16x16x32_bf16 a[88:91], a[212:215], v[56:59], a[88:91]\n"
"v_pk_add_f32 v[222:223], v[222:223], v[230:231] op_sel_hi:[1,0]\n"
"v_mfma_f32_16x16x32_bf16 a[92:95], a[212:215], v[60:63], a[92:95]\n"
"v_pk_mul_f32 v[232:233], v[208:209], v[208:209]\n"
"v_mfma_f32_16x16x32_bf16 a[92:95], a[208:211], v[28:31], a[92:95]\n"
"v_pk_mul_f32 v[236:237], v[216:217], v[216:217]\n"
"s_waitcnt lgkmcnt(2)\n"
"v_mfma_f32_16x16x32_bf16 a[96:99], v[0:3], a[216:219], a[96:99]\n"
"ds_read_b128 a[200:203], v142 offset:10240\n"
"v_pk_fma_f32 v[232:233], v[210:211], v[210:211], v[232:233]\n"
"v_mfma_f32_16x16x32_bf16 a[96:99], v[32:35], a[220:223], a[96:99]\n"
"global_load_dword v178, v187, s[52:53]\n"
"v_mfma_f32_16x16x32_bf16 a[100:103], v[36:39], a[220:223], a[100:103]\n"
"ds_read_b128 a[204:207], v143 offset:10240\n"
"v_pk_fma_f32 v[236:237], v[218:219], v[218:219], v[236:237]\n"
"v_mfma_f32_16x16x32_bf16 a[100:103], v[4:7], a[216:219], a[100:103]\n"
"v_pk_fma_f32 v[232:233], v[212:213], v[212:213], v[232:233]\n"
"v_mfma_f32_16x16x32_bf16 a[104:107], v[8:11], a[216:219], a[104:107]\n"
"v_pk_fma_f32 v[236:237], v[220:221], v[220:221], v[236:237]\n"
"v_mfma_f32_16x16x32_bf16 a[104:107], v[40:43], a[220:223], a[104:107]\n"
"v_pk_fma_f32 v[232:233], v[214:215], v[214:215], v[232:233]\n"
"v_mfma_f32_16x16x32_bf16 a[108:111], v[44:47], a[220:223], a[108:111]\n"
"v_pk_fma_f32 v[236:237], v[222:223], v[222:223], v[236:237]\n"
"v_mfma_f32_16x16x32_bf16 a[108:111], v[12:15], a[216:219], a[108:111]\n"
"v_add_co_u32 v180, vcc, v182, v180\n"
"s_nop 1\n"
"v_addc_co_u32 v181, vcc, 0, v181, vcc\n"
"v_mfma_f32_16x16x32_bf16 a[112:115], a[216:219], v[16:19], a[112:115]\n"
"v_add_f32 v224, v232, v233\n"
"v_mfma_f32_16x16x32_bf16 a[112:115], a[220:223], v[48:51], a[112:115]\n"
"v_add_u32 v186, 0x8000, v186\n"
"v_add_u32 v187, 0x2b00, v187\n"
"v_mfma_f32_16x16x32_bf16 a[116:119], a[220:223], v[52:55], a[116:119]\n"
"v_add_f32 v225, v236, v237\n"
"v_mfma_f32_16x16x32_bf16 a[116:119], a[216:219], v[20:23], a[116:119]\n"
"s_nop 0\n"
"v_mfma_f32_16x16x32_bf16 a[120:123], a[216:219], v[24:27], a[120:123]\n"
"ds_read_b128 a[208:211], v142 offset:12288\n"
"v_add_f32_dpp v224, v224, v224 quad_perm:[1,0,3,2] row_mask:0xf bank_mask:0xf\n"
"v_mfma_f32_16x16x32_bf16 a[120:123], a[220:223], v[56:59], a[120:123]\n"
"v_add_f32_dpp v225, v225, v225 quad_perm:[1,0,3,2] row_mask:0xf bank_mask:0xf\n"
"v_mfma_f32_16x16x32_bf16 a[124:127], a[220:223], v[60:63], a[124:127]\n"
"ds_read_b128 a[212:215], v143 offset:12288\n"
"s_nop 0\n"
"v_mfma_f32_16x16x32_bf16 a[124:127], a[216:219], v[28:31], a[124:127]\n"
"v_add_f32_dpp v224, v224, v224 quad_perm:[2,3,0,1] row_mask:0xf bank_mask:0xf\n"
"s_waitcnt lgkmcnt(4)\n"
"v_mfma_f32_16x16x32_bf16 a[128:131], v[0:3], a[192:195], a[128:131]\n"
"v_add_f32_dpp v225, v225, v225 quad_perm:[2,3,0,1] row_mask:0xf bank_mask:0xf\n"
"v_mfma_f32_16x16x32_bf16 a[128:131], v[32:35], a[196:199], a[128:131]\n"
"s_nop 0\n"
"v_mfma_f32_16x16x32_bf16 a[132:135], v[36:39], a[196:199], a[132:135]\n"
"v_add_f32_dpp v224, v224, v224 row_half_mirror row_mask:0xf bank_mask:0xf\n"
"v_mfma_f32_16x16x32_bf16 a[132:135], v[4:7], a[192:195], a[132:135]\n"
"v_add_f32_dpp v225, v225, v225 row_half_mirror row_mask:0xf bank_mask:0xf\n"
"v_mfma_f32_16x16x32_bf16 a[136:139], v[8:11], a[192:195], a[136:139]\n"
"s_nop 0\n"
"v_mfma_f32_16x16x32_bf16 a[136:139], v[40:43], a[196:199], a[136:139]\n"
"v_add_f32_dpp v224, v224, v224 row_mirror row_mask:0xf bank_mask:0xf\n"
"v_mfma_f32_16x16x32_bf16 a[140:143], v[44:47], a[196:199], a[140:143]\n"
"ds_read_b128 a[216:219], v142 offset:14336\n"
"v_add_f32_dpp v225, v225, v225 row_mirror row_mask:0xf bank_mask:0xf\n"
"v_mfma_f32_16x16x32_bf16 a[140:143], v[12:15], a[192:195], a[140:143]\n"
"v_mov_b32 v226, v224\n"
"v_mfma_f32_16x16x32_bf16 a[144:147], a[192:195], v[16:19], a[144:147]\n"
"ds_read_b128 a[220:223], v143 offset:14336\n"
"v_mov_b32 v227, v225\n"
"v_mfma_f32_16x16x32_bf16 a[144:147], a[196:199], v[48:51], a[144:147]\n"
"s_nop 0\n"
"v_mfma_f32_16x16x32_bf16 a[148:151], a[196:199], v[52:55], a[148:151]\n"
"v_permlane16_swap_b32 v224, v226\n"
"v_mfma_f32_16x16x32_bf16 a[148:151], a[192:195], v[20:23], a[148:151]\n"
"v_permlane16_swap_b32 v225, v227\n"
"v_mfma_f32_16x16x32_bf16 a[152:155], a[192:195], v[24:27], a[152:155]\n"
"v_add_f32 v224, v224, v226\n"
"v_mfma_f32_16x16x32_bf16 a[152:155], a[196:199], v[56:59], a[152:155]\n"
"v_add_f32 v225, v225, v227\n"
"v_mfma_f32_16x16x32_bf16 a[156:159], a[196:199], v[60:63], a[156:159]\n"
"v_mov_b32 v226, v224\n"
"v_mfma_f32_16x16x32_bf16 a[156:159], a[192:195], v[28:31], a[156:159]\n"
"v_mov_b32 v227, v225\n"
"s_waitcnt lgkmcnt(4)\n"
"v_mfma_f32_16x16x32_bf16 a[160:163], v[0:3], a[200:203], a[160:163]\n"
"s_nop 0\n"
"v_mfma_f32_16x16x32_bf16 a[160:163], v[32:35], a[204:207], a[160:163]\n"
"v_permlane32_swap_b32 v224, v226\n"
"v_mfma_f32_16x16x32_bf16 a[164:167], v[36:39], a[204:207], a[164:167]\n"
"v_permlane32_swap_b32 v225, v227\n"
"v_mfma_f32_16x16x32_bf16 a[164:167], v[4:7], a[200:203], a[164:167]\n"
"v_add_f32 v224, v224, v226\n"
"v_mfma_f32_16x16x32_bf16 a[168:171], v[8:11], a[200:203], a[168:171]\n"
"v_add_f32 v225, v225, v227\n"
"v_mfma_f32_16x16x32_bf16 a[168:171], v[40:43], a[204:207], a[168:171]\n"
"s_mov_b32 s12, 0x3df5fdf0\n"
"v_mfma_f32_16x16x32_bf16 a[172:175], v[44:47], a[204:207], a[172:175]\n"
"v_mov_b32 v229, 0x3a21369a\n"
"v_mfma_f32_16x16x32_bf16 a[172:175], v[12:15], a[200:203], a[172:175]\n"
"v_fma_f32 v224, v224, s12, v229\n"
"v_mfma_f32_16x16x32_bf16 a[176:179], a[200:203], v[16:19], a[176:179]\n"
"v_fma_f32 v225, v225, s12, v229\n"
"v_mfma_f32_16x16x32_bf16 a[176:179], a[204:207], v[48:51], a[176:179]\n"
"v_rsq_f32 v228, v224\n"
"v_mfma_f32_16x16x32_bf16 a[180:183], a[204:207], v[52:55], a[180:183]\n"
"v_rsq_f32 v230, v225\n"
"v_mfma_f32_16x16x32_bf16 a[180:183], a[200:203], v[20:23], a[180:183]\n"
"v_mfma_f32_16x16x32_bf16 a[184:187], a[200:203], v[24:27], a[184:187]\n"
"s_waitcnt vmcnt(3) lgkmcnt(0)\n"
"s_barrier\n"
"v_mfma_f32_16x16x32_bf16 a[184:187], a[204:207], v[56:59], a[184:187]\n"
"ds_read_b128 a[192:195], v140\n"
"v_pk_mul_f32 v[208:209], v[208:209], v[228:229] op_sel_hi:[1,0]\n"
"v_mfma_f32_16x16x32_bf16 a[188:191], a[204:207], v[60:63], a[188:191]\n"
"ds_read_b128 a[196:199], v141\n"
"v_pk_mul_f32 v[216:217], v[216:217], v[230:231] op_sel_hi:[1,0]\n"
"v_mfma_f32_16x16x32_bf16 a[188:191], a[200:203], v[28:31], a[188:191]\n"
"s_add_u32 m0, s70, 0x10000\n"
"v_pk_mul_f32 v[210:211], v[210:211], v[228:229] op_sel_hi:[1,0]\n"
"v_mfma_f32_16x16x32_bf16 v[64:67], v[0:3], a[208:211], v[64:67]\n"
"ds_read_b128 a[200:203], v140 offset:2048\n"
"v_pk_mul_f32 v[218:219], v[218:219], v[230:231] op_sel_hi:[1,0]\n"
"v_mfma_f32_16x16x32_bf16 v[64:67], v[32:35], a[212:215], v[64:67]\n"
"buffer_load_dwordx4 v128, s[16:19], s68 offen lds\n"
"v_mfma_f32_16x16x32_bf16 v[96:99], v[32:35], a[220:223], v[96:99]\n"
"ds_read_b128 a[204:207], v141 offset:2048\n"
"v_pk_mul_f32 v[212:213], v[212:213], v[228:229] op_sel_hi:[1,0]\n"
"v_mfma_f32_16x16x32_bf16 v[96:99], v[0:3], a[216:219], v[96:99]\n"
"v_pk_mul_f32 v[220:221], v[220:221], v[230:231] op_sel_hi:[1,0]\n"
"v_mfma_f32_16x16x32_bf16 v[68:71], v[4:7], a[208:211], v[68:71]\n"
"ds_read_b64_tr_b16 v[0:1], v144\n"
"ds_read_b64_tr_b16 v[2:3], v144 offset:1024\n"
"v_mfma_f32_16x16x32_bf16 v[68:71], v[36:39], a[212:215], v[68:71]\n"
"buffer_load_dwordx4 v129, s[16:19], s68 offen offset:1024 lds\n"
"v_mfma_f32_16x16x32_bf16 v[100:103], v[36:39], a[220:223], v[100:103]\n"
"ds_read_b64_tr_b16 v[32:33], v144 offset:8192\n"
"ds_read_b64_tr_b16 v[34:35], v144 offset:9216\n"
"v_mfma_f32_16x16x32_bf16 v[100:103], v[4:7], a[216:219], v[100:103]\n"
"v_pk_mul_f32 v[214:215], v[214:215], v[228:229] op_sel_hi:[1,0]\n"
"v_mfma_f32_16x16x32_bf16 v[72:75], v[8:11], a[208:211], v[72:75]\n"
"ds_read_b64_tr_b16 v[4:5], v145\n"
"ds_read_b64_tr_b16 v[6:7], v145 offset:1024\n"
"v_mfma_f32_16x16x32_bf16 v[72:75], v[40:43], a[212:215], v[72:75]\n"
"buffer_load_dwordx4 v130, s[16:19], s68 offen offset:2048 lds\n"
"v_mfma_f32_16x16x32_bf16 v[104:107], v[40:43], a[220:223], v[104:107]\n"
"ds_read_b64_tr_b16 v[36:37], v145 offset:8192\n"
"ds_read_b64_tr_b16 v[38:39], v145 offset:9216\n"
"v_mfma_f32_16x16x32_bf16 v[104:107], v[8:11], a[216:219], v[104:107]\n"
"v_pk_mul_f32 v[222:223], v[222:223], v[230:231] op_sel_hi:[1,0]\n"
"v_mfma_f32_16x16x32_bf16 v[76:79], v[12:15], a[208:211], v[76:79]\n"
"ds_read_b64_tr_b16 v[8:9], v146\n"
"ds_read_b64_tr_b16 v[10:11], v146 offset:1024\n"
"v_mfma_f32_16x16x32_bf16 v[76:79], v[44:47], a[212:215], v[76:79]\n"
"buffer_load_dwordx4 v131, s[16:19], s68 offen offset:3072 lds\n"
"v_mfma_f32_16x16x32_bf16 v[108:111], v[44:47], a[220:223], v[108:111]\n"
"ds_read_b64_tr_b16 v[40:41], v146 offset:8192\n"
"ds_read_b64_tr_b16 v[42:43], v146 offset:9216\n"
"v_mfma_f32_16x16x32_bf16 v[108:111], v[12:15], a[216:219], v[108:111]\n"
"s_add_u32 m0, s70, 0x11000\n"
"v_cvt_pk_bf16_f32 v232, v208, v209\n"
"v_mfma_f32_16x16x32_bf16 v[80:83], a[208:211], v[16:19], v[80:83]\n"
"ds_read_b64_tr_b16 v[12:13], v147\n"
"ds_read_b64_tr_b16 v[14:15], v147 offset:1024\n"
"v_mfma_f32_16x16x32_bf16 v[80:83], a[212:215], v[48:51], v[80:83]\n"
"buffer_load_dwordx4 v132, s[16:19], s68 offen lds\n"
"v_mfma_f32_16x16x32_bf16 v[112:115], a[220:223], v[48:51], v[112:115]\n"
"ds_read_b64_tr_b16 v[44:45], v147 offset:8192\n"
"ds_read_b64_tr_b16 v[46:47], v147 offset:9216\n"
"v_mfma_f32_16x16x32_bf16 v[112:115], a[216:219], v[16:19], v[112:115]\n"
"v_cvt_pk_bf16_f32 v236, v216, v217\n"
"v_mfma_f32_16x16x32_bf16 v[84:87], a[208:211], v[20:23], v[84:87]\n"
"ds_read_b64_tr_b16 v[16:17], v144 offset:16384\n"
"ds_read_b64_tr_b16 v[18:19], v144 offset:17408\n"
"v_mfma_f32_16x16x32_bf16 v[84:87], a[212:215], v[52:55], v[84:87]\n"
"buffer_load_dwordx4 v133, s[16:19], s68 offen offset:1024 lds\n"
"v_mfma_f32_16x16x32_bf16 v[116:119], a[220:223], v[52:55], v[116:119]\n"
"ds_read_b64_tr_b16 v[48:49], v144 offset:24576\n"
"ds_read_b64_tr_b16 v[50:51], v144 offset:25600\n"
"v_mfma_f32_16x16x32_bf16 v[116:119], a[216:219], v[20:23], v[116:119]\n"
"v_cvt_pk_bf16_f32 v233, v210, v211\n"
"v_mfma_f32_16x16x32_bf16 v[88:91], a[208:211], v[24:27], v[88:91]\n"
"ds_read_b64_tr_b16 v[20:21], v145 offset:16384\n"
"ds_read_b64_tr_b16 v[22:23], v145 offset:17408\n"
"v_mfma_f32_16x16x32_bf16 v[88:91], a[212:215], v[56:59], v[88:91]\n"
"buffer_load_dwordx4 v134, s[16:19], s68 offen offset:2048 lds\n"
"v_mfma_f32_16x16x32_bf16 v[120:123], a[220:223], v[56:59], v[120:123]\n"
"ds_read_b64_tr_b16 v[52:53], v145 offset:24576\n"
"ds_read_b64_tr_b16 v[54:55], v145 offset:25600\n"
"v_mfma_f32_16x16x32_bf16 v[120:123], a[216:219], v[24:27], v[120:123]\n"
"v_cvt_pk_bf16_f32 v237, v218, v219\n"
"v_mfma_f32_16x16x32_bf16 v[92:95], a[208:211], v[28:31], v[92:95]\n"
"ds_read_b64_tr_b16 v[24:25], v146 offset:16384\n"
"ds_read_b64_tr_b16 v[26:27], v146 offset:17408\n"
"v_mfma_f32_16x16x32_bf16 v[92:95], a[212:215], v[60:63], v[92:95]\n"
"buffer_load_dwordx4 v135, s[16:19], s68 offen offset:3072 lds\n"
"v_mfma_f32_16x16x32_bf16 v[124:127], a[220:223], v[60:63], v[124:127]\n"
"ds_read_b64_tr_b16 v[56:57], v146 offset:24576\n"
"ds_read_b64_tr_b16 v[58:59], v146 offset:25600\n"
"v_mfma_f32_16x16x32_bf16 v[124:127], a[216:219], v[28:31], v[124:127]\n"
"s_add_u32 m0, s71, 0x10000\n"
"s_waitcnt lgkmcnt(15)\n"
"v_mfma_f32_16x16x32_bf16 a[0:3], v[0:3], a[192:195], a[0:3]\n"
"ds_read_b64_tr_b16 v[28:29], v147 offset:16384\n"
"ds_read_b64_tr_b16 v[30:31], v147 offset:17408\n"
"v_mfma_f32_16x16x32_bf16 a[0:3], v[32:35], a[196:199], a[0:3]\n"
"buffer_load_dwordx4 v136, s[20:23], s69 offen lds\n"
"v_mfma_f32_16x16x32_bf16 a[32:35], v[32:35], a[204:207], a[32:35]\n"
"ds_read_b64_tr_b16 v[60:61], v147 offset:24576\n"
"ds_read_b64_tr_b16 v[62:63], v147 offset:25600\n"
"v_mfma_f32_16x16x32_bf16 a[32:35], v[0:3], a[200:203], a[32:35]\n"
"v_cvt_pk_bf16_f32 v234, v212, v213\n"
"v_mfma_f32_16x16x32_bf16 a[4:7], v[4:7], a[192:195], a[4:7]\n"
"buffer_load_dwordx4 v137, s[20:23], s69 offen offset:1024 lds\n"
"v_mfma_f32_16x16x32_bf16 a[4:7], v[36:39], a[196:199], a[4:7]\n"
"v_cvt_pk_bf16_f32 v238, v220, v221\n"
"v_mfma_f32_16x16x32_bf16 a[36:39], v[36:39], a[204:207], a[36:39]\n"
"ds_read_b128 a[208:211], v140 offset:4096\n"
"v_cvt_pk_bf16_f32 v235, v214, v215\n"
"v_mfma_f32_16x16x32_bf16 a[36:39], v[4:7], a[200:203], a[36:39]\n"
"v_cvt_pk_bf16_f32 v239, v222, v223\n"
"s_waitcnt lgkmcnt(15)\n"
"v_mfma_f32_16x16x32_bf16 a[8:11], v[8:11], a[192:195], a[8:11]\n"
"ds_read_b128 a[212:215], v141 offset:4096\n"
"s_lshl_b32 s12, s8, 5\n"
"v_mfma_f32_16x16x32_bf16 a[8:11], v[40:43], a[196:199], a[8:11]\n"
"buffer_load_dwordx4 v138, s[20:23], s69 offen offset:2048 lds\n"
"v_mfma_f32_16x16x32_bf16 a[40:43], v[40:43], a[204:207], a[40:43]\n"
"s_add_u32 s12, s12, 0x20000\n"
"v_mfma_f32_16x16x32_bf16 a[40:43], v[8:11], a[200:203], a[40:43]\n"
"v_lshl_add_u32 v229, v255, 7, s12\n"
"v_mfma_f32_16x16x32_bf16 a[12:15], v[12:15], a[192:195], a[12:15]\n"
"buffer_load_dwordx4 v139, s[20:23], s69 offen offset:3072 lds\n"
"v_mfma_f32_16x16x32_bf16 a[12:15], v[44:47], a[196:199], a[12:15]\n"
"ds_write_b128 v229, v[232:235]\n"
"v_mfma_f32_16x16x32_bf16 a[44:47], v[44:47], a[204:207], a[44:47]\n"
"ds_write_b128 v229, v[236:239] offset:16\n"
"v_mfma_f32_16x16x32_bf16 a[44:47], v[12:15], a[200:203], a[44:47]\n"
"s_add_u32 m0, s71, 0x14000\n"
"s_waitcnt lgkmcnt(12)\n"
"v_mfma_f32_16x16x32_bf16 a[16:19], a[192:195], v[16:19], a[16:19]\n"
"buffer_load_dwordx4 v136, s[24:27], s69 offen lds\n"
"v_mfma_f32_16x16x32_bf16 a[16:19], a[196:199], v[48:51], a[16:19]\n"
"s_lshl_b32 s12, s8, 11\n"
"v_mfma_f32_16x16x32_bf16 a[48:51], a[204:207], v[48:51], a[48:51]\n"
"ds_read_b128 a[216:219], v140 offset:6144\n"
"s_add_u32 s12, s12, 0x20000\n"
"v_mfma_f32_16x16x32_bf16 a[48:51], a[200:203], v[16:19], a[48:51]\n"
"v_add_u32 v230, s12, v254\n"
"v_mfma_f32_16x16x32_bf16 a[20:23], a[192:195], v[20:23], a[20:23]\n"
"ds_read_b128 a[220:223], v141 offset:6144\n"
"v_lshrrev_b32 v229, 5, v255\n"
"v_mfma_f32_16x16x32_bf16 a[20:23], a[196:199], v[52:55], a[20:23]\n"
"buffer_load_dwordx4 v137, s[24:27], s69 offen offset:1024 lds\n"
"v_mfma_f32_16x16x32_bf16 a[52:55], a[204:207], v[52:55], a[52:55]\n"
"v_lshlrev_b32 v229, 17, v229\n"
"v_mfma_f32_16x16x32_bf16 a[52:55], a[200:203], v[20:23], a[52:55]\n"
"v_bfe_u32 v231, v255, 3, 2\n"
"s_waitcnt lgkmcnt(6)\n"
"v_mfma_f32_16x16x32_bf16 a[24:27], a[192:195], v[24:27], a[24:27]\n"
"buffer_load_dwordx4 v138, s[24:27], s69 offen offset:2048 lds\n"
"v_mfma_f32_16x16x32_bf16 a[24:27], a[196:199], v[56:59], a[24:27]\n"
"v_lshl_add_u32 v229, v231, 8, v229\n"
"v_mfma_f32_16x16x32_bf16 a[56:59], a[204:207], v[56:59], a[56:59]\n"
"v_and_b32 v231, 7, v255\n"
"v_mfma_f32_16x16x32_bf16 a[56:59], a[200:203], v[24:27], a[56:59]\n"
"v_lshl_add_u32 v229, v231, 4, v229\n"
"v_mfma_f32_16x16x32_bf16 a[28:31], a[192:195], v[28:31], a[28:31]\n"
"buffer_load_dwordx4 v139, s[24:27], s69 offen offset:3072 lds\n"
"v_mfma_f32_16x16x32_bf16 a[28:31], a[196:199], v[60:63], a[28:31]\n"
"v_add_u32 v228, 0x40000, v229\n"
"v_mfma_f32_16x16x32_bf16 a[60:63], a[204:207], v[60:63], a[60:63]\n"
"s_lshr_b32 s12, s9, 1\n"
"v_mfma_f32_16x16x32_bf16 a[60:63], a[200:203], v[28:31], a[60:63]\n"
"s_add_u32 s68, s68, 0x80\n"
"s_waitcnt lgkmcnt(4)\n"
"v_mfma_f32_16x16x32_bf16 a[64:67], v[0:3], a[208:211], a[64:67]\n"
"s_add_u32 s69, s69, 0x80000\n"
"s_lshl_b32 s12, s12, 10\n"
"v_mfma_f32_16x16x32_bf16 a[64:67], v[32:35], a[212:215], a[64:67]\n"
"s_and_b32 s13, s9, 1\n"
"v_mfma_f32_16x16x32_bf16 a[68:71], v[36:39], a[212:215], a[68:71]\n"
"s_lshl_b32 s13, s13, 7\n"
"v_mfma_f32_16x16x32_bf16 a[68:71], v[4:7], a[208:211], a[68:71]\n"
"s_add_u32 s12, s12, s13\n"
"v_mfma_f32_16x16x32_bf16 a[72:75], v[8:11], a[208:211], a[72:75]\n"
"ds_read_b128 a[192:195], v140 offset:8192\n"
"s_lshl_b32 s13, s8, 19\n"
"v_mfma_f32_16x16x32_bf16 a[72:75], v[40:43], a[212:215], a[72:75]\n"
"global_load_dword v178, v[180:181], off\n"
"v_mfma_f32_16x16x32_bf16 a[76:79], v[44:47], a[212:215], a[76:79]\n"
"ds_read_b128 a[196:199], v141 offset:8192\n"
"s_add_u32 s63, s12, s13\n"
"v_mfma_f32_16x16x32_bf16 a[76:79], v[12:15], a[208:211], a[76:79]\n"
"v_mfma_f32_16x16x32_bf16 a[80:83], a[208:211], v[16:19], a[80:83]\n"
"v_mfma_f32_16x16x32_bf16 a[80:83], a[212:215], v[48:51], a[80:83]\n"
"v_mfma_f32_16x16x32_bf16 a[84:87], a[212:215], v[52:55], a[84:87]\n"
"global_load_dword v178, v186, s[44:45]\n"
"v_mfma_f32_16x16x32_bf16 a[84:87], a[208:211], v[20:23], a[84:87]\n"
"v_mfma_f32_16x16x32_bf16 a[88:91], a[208:211], v[24:27], a[88:91]\n"
"v_mfma_f32_16x16x32_bf16 a[88:91], a[212:215], v[56:59], a[88:91]\n"
"v_mfma_f32_16x16x32_bf16 a[92:95], a[212:215], v[60:63], a[92:95]\n"
"v_mfma_f32_16x16x32_bf16 a[92:95], a[208:211], v[28:31], a[92:95]\n"
"s_waitcnt lgkmcnt(2)\n"
"v_mfma_f32_16x16x32_bf16 a[96:99], v[0:3], a[216:219], a[96:99]\n"
"ds_read_b128 a[200:203], v140 offset:10240\n"
"v_mfma_f32_16x16x32_bf16 a[96:99], v[32:35], a[220:223], a[96:99]\n"
"global_load_dword v178, v187, s[52:53]\n"
"v_mfma_f32_16x16x32_bf16 a[100:103], v[36:39], a[220:223], a[100:103]\n"
"ds_read_b128 a[204:207], v141 offset:10240\n"
"v_mfma_f32_16x16x32_bf16 a[100:103], v[4:7], a[216:219], a[100:103]\n"
"v_mfma_f32_16x16x32_bf16 a[104:107], v[8:11], a[216:219], a[104:107]\n"
"v_mfma_f32_16x16x32_bf16 a[104:107], v[40:43], a[220:223], a[104:107]\n"
"v_mfma_f32_16x16x32_bf16 a[108:111], v[44:47], a[220:223], a[108:111]\n"
"v_mfma_f32_16x16x32_bf16 a[108:111], v[12:15], a[216:219], a[108:111]\n"
"v_add_co_u32 v180, vcc, v182, v180\n"
"s_nop 1\n"
"v_addc_co_u32 v181, vcc, 0, v181, vcc\n"
"v_mfma_f32_16x16x32_bf16 a[112:115], a[216:219], v[16:19], a[112:115]\n"
"v_mfma_f32_16x16x32_bf16 a[112:115], a[220:223], v[48:51], a[112:115]\n"
"v_add_u32 v186, 0x8000, v186\n"
"v_add_u32 v187, 0x2b00, v187\n"
"v_mfma_f32_16x16x32_bf16 a[116:119], a[220:223], v[52:55], a[116:119]\n"
"v_mfma_f32_16x16x32_bf16 a[116:119], a[216:219], v[20:23], a[116:119]\n"
"v_mfma_f32_16x16x32_bf16 a[120:123], a[216:219], v[24:27], a[120:123]\n"
"ds_read_b128 a[208:211], v140 offset:12288\n"
"v_mfma_f32_16x16x32_bf16 a[120:123], a[220:223], v[56:59], a[120:123]\n"
"v_mfma_f32_16x16x32_bf16 a[124:127], a[220:223], v[60:63], a[124:127]\n"
"ds_read_b128 a[212:215], v141 offset:12288\n"
"v_mfma_f32_16x16x32_bf16 a[124:127], a[216:219], v[28:31], a[124:127]\n"
"s_waitcnt lgkmcnt(4)\n"
"v_mfma_f32_16x16x32_bf16 a[128:131], v[0:3], a[192:195], a[128:131]\n"
"v_mfma_f32_16x16x32_bf16 a[128:131], v[32:35], a[196:199], a[128:131]\n"
"v_mfma_f32_16x16x32_bf16 a[132:135], v[36:39], a[196:199], a[132:135]\n"
"v_mfma_f32_16x16x32_bf16 a[132:135], v[4:7], a[192:195], a[132:135]\n"
"v_mfma_f32_16x16x32_bf16 a[136:139], v[8:11], a[192:195], a[136:139]\n"
"v_mfma_f32_16x16x32_bf16 a[136:139], v[40:43], a[196:199], a[136:139]\n"
"v_mfma_f32_16x16x32_bf16 a[140:143], v[44:47], a[196:199], a[140:143]\n"
"ds_read_b128 a[216:219], v140 offset:14336\n"
"v_mfma_f32_16x16x32_bf16 a[140:143], v[12:15], a[192:195], a[140:143]\n"
"v_mfma_f32_16x16x32_bf16 a[144:147], a[192:195], v[16:19], a[144:147]\n"
"ds_read_b128 a[220:223], v141 offset:14336\n"
"v_mfma_f32_16x16x32_bf16 a[144:147], a[196:199], v[48:51], a[144:147]\n"
"v_mfma_f32_16x16x32_bf16 a[148:151], a[196:199], v[52:55], a[148:151]\n"
"v_mfma_f32_16x16x32_bf16 a[148:151], a[192:195], v[20:23], a[148:151]\n"
"v_mfma_f32_16x16x32_bf16 a[152:155], a[192:195], v[24:27], a[152:155]\n"
"v_mfma_f32_16x16x32_bf16 a[152:155], a[196:199], v[56:59], a[152:155]\n"
"v_mfma_f32_16x16x32_bf16 a[156:159], a[196:199], v[60:63], a[156:159]\n"
"v_mfma_f32_16x16x32_bf16 a[156:159], a[192:195], v[28:31], a[156:159]\n"
"s_waitcnt lgkmcnt(4)\n"
"v_mfma_f32_16x16x32_bf16 a[160:163], v[0:3], a[200:203], a[160:163]\n"
"v_mfma_f32_16x16x32_bf16 a[160:163], v[32:35], a[204:207], a[160:163]\n"
"v_mfma_f32_16x16x32_bf16 a[164:167], v[36:39], a[204:207], a[164:167]\n"
"v_mfma_f32_16x16x32_bf16 a[164:167], v[4:7], a[200:203], a[164:167]\n"
"v_mfma_f32_16x16x32_bf16 a[168:171], v[8:11], a[200:203], a[168:171]\n"
"v_mfma_f32_16x16x32_bf16 a[168:171], v[40:43], a[204:207], a[168:171]\n"
"v_mfma_f32_16x16x32_bf16 a[172:175], v[44:47], a[204:207], a[172:175]\n"
"v_mfma_f32_16x16x32_bf16 a[172:175], v[12:15], a[200:203], a[172:175]\n"
"v_mfma_f32_16x16x32_bf16 a[176:179], a[200:203], v[16:19], a[176:179]\n"
"v_mfma_f32_16x16x32_bf16 a[176:179], a[204:207], v[48:51], a[176:179]\n"
"v_mfma_f32_16x16x32_bf16 a[180:183], a[204:207], v[52:55], a[180:183]\n"
"v_mfma_f32_16x16x32_bf16 a[180:183], a[200:203], v[20:23], a[180:183]\n"
"v_mfma_f32_16x16x32_bf16 a[184:187], a[200:203], v[24:27], a[184:187]\n"
"s_waitcnt vmcnt(3) lgkmcnt(0)\n"
"s_barrier\n"
"v_mfma_f32_16x16x32_bf16 a[184:187], a[204:207], v[56:59], a[184:187]\n"
"ds_read_b128 a[192:195], v142\n"
"v_mfma_f32_16x16x32_bf16 a[188:191], a[204:207], v[60:63], a[188:191]\n"
"ds_read_b128 a[196:199], v143\n"
"v_mfma_f32_16x16x32_bf16 a[188:191], a[200:203], v[28:31], a[188:191]\n"
"s_add_u32 m0, s70, 0\n"
"v_mfma_f32_16x16x32_bf16 v[64:67], v[0:3], a[208:211], v[64:67]\n"
"ds_read_b128 a[200:203], v142 offset:2048\n"
"v_mfma_f32_16x16x32_bf16 v[64:67], v[32:35], a[212:215], v[64:67]\n"
"buffer_load_dwordx4 v128, s[16:19], s68 offen lds\n"
"v_mfma_f32_16x16x32_bf16 v[96:99], v[32:35], a[220:223], v[96:99]\n"
"ds_read_b128 a[204:207], v143 offset:2048\n"
"v_mfma_f32_16x16x32_bf16 v[96:99], v[0:3], a[216:219], v[96:99]\n"
"v_mfma_f32_16x16x32_bf16 v[68:71], v[4:7], a[208:211], v[68:71]\n"
"ds_read_b64_tr_b16 v[0:1], v148\n"
"ds_read_b64_tr_b16 v[2:3], v148 offset:1024\n"
"v_mfma_f32_16x16x32_bf16 v[68:71], v[36:39], a[212:215], v[68:71]\n"
"buffer_load_dwordx4 v129, s[16:19], s68 offen offset:1024 lds\n"
"v_mfma_f32_16x16x32_bf16 v[100:103], v[36:39], a[220:223], v[100:103]\n"
"ds_read_b64_tr_b16 v[32:33], v148 offset:8192\n"
"ds_read_b64_tr_b16 v[34:35], v148 offset:9216\n"
"v_mfma_f32_16x16x32_bf16 v[100:103], v[4:7], a[216:219], v[100:103]\n"
"v_mfma_f32_16x16x32_bf16 v[72:75], v[8:11], a[208:211], v[72:75]\n"
"ds_read_b64_tr_b16 v[4:5], v149\n"
"ds_read_b64_tr_b16 v[6:7], v149 offset:1024\n"
"v_mfma_f32_16x16x32_bf16 v[72:75], v[40:43], a[212:215], v[72:75]\n"
"buffer_load_dwordx4 v130, s[16:19], s68 offen offset:2048 lds\n"
"v_mfma_f32_16x16x32_bf16 v[104:107], v[40:43], a[220:223], v[104:107]\n"
"ds_read_b64_tr_b16 v[36:37], v149 offset:8192\n"
"ds_read_b64_tr_b16 v[38:39], v149 offset:9216\n"
"v_mfma_f32_16x16x32_bf16 v[104:107], v[8:11], a[216:219], v[104:107]\n"
"v_mfma_f32_16x16x32_bf16 v[76:79], v[12:15], a[208:211], v[76:79]\n"
"ds_read_b64_tr_b16 v[8:9], v150\n"
"ds_read_b64_tr_b16 v[10:11], v150 offset:1024\n"
"v_mfma_f32_16x16x32_bf16 v[76:79], v[44:47], a[212:215], v[76:79]\n"
"buffer_load_dwordx4 v131, s[16:19], s68 offen offset:3072 lds\n"
"v_mfma_f32_16x16x32_bf16 v[108:111], v[44:47], a[220:223], v[108:111]\n"
"ds_read_b64_tr_b16 v[40:41], v150 offset:8192\n"
"ds_read_b64_tr_b16 v[42:43], v150 offset:9216\n"
"v_mfma_f32_16x16x32_bf16 v[108:111], v[12:15], a[216:219], v[108:111]\n"
"s_add_u32 m0, s70, 0x1000\n"
"v_mfma_f32_16x16x32_bf16 v[80:83], a[208:211], v[16:19], v[80:83]\n"
"ds_read_b64_tr_b16 v[12:13], v151\n"
"ds_read_b64_tr_b16 v[14:15], v151 offset:1024\n"
"v_mfma_f32_16x16x32_bf16 v[80:83], a[212:215], v[48:51], v[80:83]\n"
"buffer_load_dwordx4 v132, s[16:19], s68 offen lds\n"
"v_mfma_f32_16x16x32_bf16 v[112:115], a[220:223], v[48:51], v[112:115]\n"
"ds_read_b64_tr_b16 v[44:45], v151 offset:8192\n"
"ds_read_b64_tr_b16 v[46:47], v151 offset:9216\n"
"v_mfma_f32_16x16x32_bf16 v[112:115], a[216:219], v[16:19], v[112:115]\n"
"v_mfma_f32_16x16x32_bf16 v[84:87], a[208:211], v[20:23], v[84:87]\n"
"ds_read_b64_tr_b16 v[16:17], v148 offset:16384\n"
"ds_read_b64_tr_b16 v[18:19], v148 offset:17408\n"
"v_mfma_f32_16x16x32_bf16 v[84:87], a[212:215], v[52:55], v[84:87]\n"
"buffer_load_dwordx4 v133, s[16:19], s68 offen offset:1024 lds\n"
"v_mfma_f32_16x16x32_bf16 v[116:119], a[220:223], v[52:55], v[116:119]\n"
"ds_read_b64_tr_b16 v[48:49], v148 offset:24576\n"
"ds_read_b64_tr_b16 v[50:51], v148 offset:25600\n"
"v_mfma_f32_16x16x32_bf16 v[116:119], a[216:219], v[20:23], v[116:119]\n"
"v_mfma_f32_16x16x32_bf16 v[88:91], a[208:211], v[24:27], v[88:91]\n"
"ds_read_b64_tr_b16 v[20:21], v149 offset:16384\n"
"ds_read_b64_tr_b16 v[22:23], v149 offset:17408\n"
"v_mfma_f32_16x16x32_bf16 v[88:91], a[212:215], v[56:59], v[88:91]\n"
"buffer_load_dwordx4 v134, s[16:19], s68 offen offset:2048 lds\n"
"v_mfma_f32_16x16x32_bf16 v[120:123], a[220:223], v[56:59], v[120:123]\n"
"ds_read_b64_tr_b16 v[52:53], v149 offset:24576\n"
"ds_read_b64_tr_b16 v[54:55], v149 offset:25600\n"
"v_mfma_f32_16x16x32_bf16 v[120:123], a[216:219], v[24:27], v[120:123]\n"
"v_mfma_f32_16x16x32_bf16 v[92:95], a[208:211], v[28:31], v[92:95]\n"
"ds_read_b64_tr_b16 v[24:25], v150 offset:16384\n"
"ds_read_b64_tr_b16 v[26:27], v150 offset:17408\n"
"v_mfma_f32_16x16x32_bf16 v[92:95], a[212:215], v[60:63], v[92:95]\n"
"buffer_load_dwordx4 v135, s[16:19], s68 offen offset:3072 lds\n"
"v_mfma_f32_16x16x32_bf16 v[124:127], a[220:223], v[60:63], v[124:127]\n"
"ds_read_b64_tr_b16 v[56:57], v150 offset:24576\n"
"ds_read_b64_tr_b16 v[58:59], v150 offset:25600\n"
"v_mfma_f32_16x16x32_bf16 v[124:127], a[216:219], v[28:31], v[124:127]\n"
"s_add_u32 m0, s71, 0\n"
"s_waitcnt lgkmcnt(15)\n"
"v_mfma_f32_16x16x32_bf16 a[0:3], v[0:3], a[192:195], a[0:3]\n"
"ds_read_b64_tr_b16 v[28:29], v151 offset:16384\n"
"ds_read_b64_tr_b16 v[30:31], v151 offset:17408\n"
"v_mfma_f32_16x16x32_bf16 a[0:3], v[32:35], a[196:199], a[0:3]\n"
"buffer_load_dwordx4 v136, s[20:23], s69 offen lds\n"
"v_mfma_f32_16x16x32_bf16 a[32:35], v[32:35], a[204:207], a[32:35]\n"
"ds_read_b64_tr_b16 v[60:61], v151 offset:24576\n"
"ds_read_b64_tr_b16 v[62:63], v151 offset:25600\n"
"v_mfma_f32_16x16x32_bf16 a[32:35], v[0:3], a[200:203], a[32:35]\n"
"v_mfma_f32_16x16x32_bf16 a[4:7], v[4:7], a[192:195], a[4:7]\n"
"buffer_load_dwordx4 v137, s[20:23], s69 offen offset:1024 lds\n"
"v_mfma_f32_16x16x32_bf16 a[4:7], v[36:39], a[196:199], a[4:7]\n"
"s_lshl_b32 s12, s11, 3\n"
"v_mfma_f32_16x16x32_bf16 a[36:39], v[36:39], a[204:207], a[36:39]\n"
"ds_read_b128 a[208:211], v142 offset:4096\n"
"s_sub_u32 s13, 0x78, s12\n"
"v_mfma_f32_16x16x32_bf16 a[36:39], v[4:7], a[200:203], a[36:39]\n"
"s_waitcnt lgkmcnt(15)\n"
"v_mfma_f32_16x16x32_bf16 a[8:11], v[8:11], a[192:195], a[8:11]\n"
"ds_read_b128 a[212:215], v143 offset:4096\n"
"v_mfma_f32_16x16x32_bf16 a[8:11], v[40:43], a[196:199], a[8:11]\n"
"buffer_load_dwordx4 v138, s[20:23], s69 offen offset:2048 lds\n"
"v_mfma_f32_16x16x32_bf16 a[40:43], v[40:43], a[204:207], a[40:43]\n"
"ds_read_b128 v[232:235], v230\n"
"ds_read_b128 v[236:239], v230 offset:1024\n"
"v_mfma_f32_16x16x32_bf16 a[40:43], v[8:11], a[200:203], a[40:43]\n"
"s_cmp_lt_u32 s8, 2\n"
"s_cselect_b32 s77, s12, s13\n"
"v_mfma_f32_16x16x32_bf16 a[12:15], v[12:15], a[192:195], a[12:15]\n"
"buffer_load_dwordx4 v139, s[20:23], s69 offen offset:3072 lds\n"
"v_mfma_f32_16x16x32_bf16 a[12:15], v[44:47], a[196:199], a[12:15]\n"
"s_and_b32 s12, s8, 1\n"
"v_mfma_f32_16x16x32_bf16 a[44:47], v[44:47], a[204:207], a[44:47]\n"
"s_lshl_b32 s12, s12, 2\n"
"v_mfma_f32_16x16x32_bf16 a[44:47], v[12:15], a[200:203], a[44:47]\n"
"s_add_u32 m0, s71, 0x4000\n"
"s_waitcnt lgkmcnt(12)\n"
"v_mfma_f32_16x16x32_bf16 a[16:19], a[192:195], v[16:19], a[16:19]\n"
"buffer_load_dwordx4 v136, s[24:27], s69 offen lds\n"
"v_mfma_f32_16x16x32_bf16 a[16:19], a[196:199], v[48:51], a[16:19]\n"
"s_add_u32 s77, s77, s12\n"
"v_mfma_f32_16x16x32_bf16 a[48:51], a[204:207], v[48:51], a[48:51]\n"
"ds_read_b128 a[216:219], v142 offset:6144\n"
"s_lshl_b32 s80, s77, 10\n"
"v_mfma_f32_16x16x32_bf16 a[48:51], a[200:203], v[16:19], a[48:51]\n"
"v_lshrrev_b32 v212, 4, v255\n"
"v_mfma_f32_16x16x32_bf16 a[20:23], a[192:195], v[20:23], a[20:23]\n"
"ds_read_b128 a[220:223], v143 offset:6144\n"
"v_and_b32 v213, 15, v255\n"
"v_mfma_f32_16x16x32_bf16 a[20:23], a[196:199], v[52:55], a[20:23]\n"
"buffer_load_dwordx4 v137, s[24:27], s69 offen offset:1024 lds\n"
"v_mfma_f32_16x16x32_bf16 a[52:55], a[204:207], v[52:55], a[52:55]\n"
"v_lshrrev_b32 v214, 2, v213\n"
"v_mfma_f32_16x16x32_bf16 a[52:55], a[200:203], v[20:23], a[52:55]\n"
"v_and_b32 v215, 3, v255\n"
"s_waitcnt lgkmcnt(6)\n"
"v_mfma_f32_16x16x32_bf16 a[24:27], a[192:195], v[24:27], a[24:27]\n"
"s_waitcnt lgkmcnt(0)\n"
"s_lshl_b32 s12, s8, 5\n"
"v_mfma_f32_16x16x32_bf16 a[24:27], a[196:199], v[56:59], a[24:27]\n"
"buffer_load_dwordx4 v138, s[24:27], s69 offen offset:2048 lds\n"
"v_mfma_f32_16x16x32_bf16 a[56:59], a[204:207], v[56:59], a[56:59]\n"
"v_add_u32 v208, s12, v212\n"
"v_mfma_f32_16x16x32_bf16 a[56:59], a[200:203], v[24:27], a[56:59]\n"
"v_lshlrev_b32 v208, 13, v208\n"
"v_mfma_f32_16x16x32_bf16 a[28:31], a[192:195], v[28:31], a[28:31]\n"
"buffer_load_dwordx4 v139, s[24:27], s69 offen offset:3072 lds\n"
"v_mfma_f32_16x16x32_bf16 a[28:31], a[196:199], v[60:63], a[28:31]\n"
"s_lshl_b32 s13, s10, 8\n"
"v_mfma_f32_16x16x32_bf16 a[60:63], a[204:207], v[60:63], a[60:63]\n"
"v_add_u32 v208, s13, v208\n"
"v_mfma_f32_16x16x32_bf16 a[60:63], a[200:203], v[28:31], a[60:63]\n"
"s_add_u32 s68, s68, 0x80\n"
"s_waitcnt lgkmcnt(4)\n"
"v_mfma_f32_16x16x32_bf16 a[64:67], v[0:3], a[208:211], a[64:67]\n"
"s_add_u32 s69, s69, 0x80000\n"
"v_lshlrev_b32 v211, 1, v212\n"
"v_mfma_f32_16x16x32_bf16 a[64:67], v[32:35], a[212:215], a[64:67]\n"
"v_xor_b32 v210, v213, v211\n"
"v_mfma_f32_16x16x32_bf16 a[68:71], v[36:39], a[212:215], a[68:71]\n"
"v_lshl_add_u32 v188, v210, 4, v208\n"
"v_mfma_f32_16x16x32_bf16 a[68:71], v[4:7], a[208:211], a[68:71]\n"
"v_add_u32 v189, 0x7c00, v188\n"
"v_mfma_f32_16x16x32_bf16 a[72:75], v[8:11], a[208:211], a[72:75]\n"
"ds_read_b128 a[192:195], v142 offset:8192\n"
"v_xor_b32 v210, 8, v210\n"
"v_mfma_f32_16x16x32_bf16 a[72:75], v[40:43], a[212:215], a[72:75]\n"
"buffer_store_dwordx4 v[232:235], v229, s[64:67], s63 offen sc1\n"
"v_mfma_f32_16x16x32_bf16 a[76:79], v[44:47], a[212:215], a[76:79]\n"
"ds_read_b128 a[196:199], v143 offset:8192\n"
"v_lshl_add_u32 v190, v210, 4, v208\n"
"v_mfma_f32_16x16x32_bf16 a[76:79], v[12:15], a[208:211], a[76:79]\n"
"v_add_u32 v191, 0x17400, v190\n"
"v_mfma_f32_16x16x32_bf16 a[80:83], a[208:211], v[16:19], a[80:83]\n"
"v_add_u32 v190, 0xf800, v190\n"
"v_mfma_f32_16x16x32_bf16 a[80:83], a[212:215], v[48:51], a[80:83]\n"
"v_lshrrev_b32 v208, 3, v255\n"
"v_mfma_f32_16x16x32_bf16 a[84:87], a[212:215], v[52:55], a[84:87]\n"
"v_add_u32 v208, s12, v208\n"
"v_mfma_f32_16x16x32_bf16 a[84:87], a[208:211], v[20:23], a[84:87]\n"
"v_mul_u32_u24 v208, 0xac0, v208\n"
"v_mfma_f32_16x16x32_bf16 a[88:91], a[208:211], v[24:27], a[88:91]\n"
"buffer_store_dwordx4 v[236:239], v228, s[64:67], s63 offen sc1\n"
"v_mfma_f32_16x16x32_bf16 a[88:91], a[212:215], v[56:59], a[88:91]\n"
"s_mul_i32 s13, s10, 0x60\n"
"v_mfma_f32_16x16x32_bf16 a[92:95], a[212:215], v[60:63], a[92:95]\n"
"v_add_u32 v208, s13, v208\n"
"v_mfma_f32_16x16x32_bf16 a[92:95], a[208:211], v[28:31], a[92:95]\n"
"v_and_b32 v210, 7, v255\n"
"s_waitcnt lgkmcnt(2)\n"
"v_mfma_f32_16x16x32_bf16 a[96:99], v[0:3], a[216:219], a[96:99]\n"
"ds_read_b128 a[200:203], v142 offset:10240\n"
"v_and_b32 v211, 1, v212\n"
"v_mfma_f32_16x16x32_bf16 a[96:99], v[32:35], a[220:223], a[96:99]\n"
"v_lshlrev_b32 v211, 1, v211\n"
"v_mfma_f32_16x16x32_bf16 a[100:103], v[36:39], a[220:223], a[100:103]\n"
"ds_read_b128 a[204:207], v143 offset:10240\n"
"v_xor_b32 v210, v210, v211\n"
"v_mfma_f32_16x16x32_bf16 a[100:103], v[4:7], a[216:219], a[100:103]\n"
"v_lshl_add_u32 v192, v210, 4, v208\n"
"v_mfma_f32_16x16x32_bf16 a[104:107], v[8:11], a[216:219], a[104:107]\n"
"global_load_dword v178, v[180:181], off\n"
"v_mfma_f32_16x16x32_bf16 a[104:107], v[40:43], a[220:223], a[104:107]\n"
"v_add_u32 v209, 2, v210\n"
"v_mfma_f32_16x16x32_bf16 a[108:111], v[44:47], a[220:223], a[108:111]\n"
"v_and_b32 v209, 8, v209\n"
"v_mfma_f32_16x16x32_bf16 a[108:111], v[12:15], a[216:219], a[108:111]\n"
"v_lshl_add_u32 v192, v209, 27, v192\n"
"v_mfma_f32_16x16x32_bf16 a[112:115], a[216:219], v[16:19], a[112:115]\n"
"v_xor_b32 v210, 4, v210\n"
"v_mfma_f32_16x16x32_bf16 a[112:115], a[220:223], v[48:51], a[112:115]\n"
"v_lshl_add_u32 v193, v210, 4, v208\n"
"v_mfma_f32_16x16x32_bf16 a[116:119], a[220:223], v[52:55], a[116:119]\n"
"global_load_dword v178, v186, s[44:45]\n"
"v_mfma_f32_16x16x32_bf16 a[116:119], a[216:219], v[20:23], a[116:119]\n"
"v_add_u32 v193, 0x5200, v193\n"
"v_mfma_f32_16x16x32_bf16 a[120:123], a[216:219], v[24:27], a[120:123]\n"
"ds_read_b128 a[208:211], v142 offset:12288\n"
"v_add_u32 v209, 2, v210\n"
"v_mfma_f32_16x16x32_bf16 a[120:123], a[220:223], v[56:59], a[120:123]\n"
"v_and_b32 v209, 8, v209\n"
"v_mfma_f32_16x16x32_bf16 a[124:127], a[220:223], v[60:63], a[124:127]\n"
"ds_read_b128 a[212:215], v143 offset:12288\n"
"v_lshl_add_u32 v193, v209, 27, v193\n"
"v_mfma_f32_16x16x32_bf16 a[124:127], a[216:219], v[28:31], a[124:127]\n"
"v_add_u32 v194, 0xa400, v192\n"
"s_waitcnt lgkmcnt(4)\n"
"v_mfma_f32_16x16x32_bf16 a[128:131], v[0:3], a[192:195], a[128:131]\n"
"global_load_dword v178, v187, s[52:53]\n"
"v_mfma_f32_16x16x32_bf16 a[128:131], v[32:35], a[196:199], a[128:131]\n"
"v_add_u32 v195, 0xa400, v193\n"
"v_mfma_f32_16x16x32_bf16 a[132:135], v[36:39], a[196:199], a[132:135]\n"
"s_mov_b32 s74, 0\n"
"v_mfma_f32_16x16x32_bf16 a[132:135], v[4:7], a[192:195], a[132:135]\n"
"s_mov_b32 s75, 0x20000\n"
"v_mfma_f32_16x16x32_bf16 a[136:139], v[8:11], a[192:195], a[136:139]\n"
"s_mov_b32 s76, 0\n"
"v_mfma_f32_16x16x32_bf16 a[136:139], v[40:43], a[196:199], a[136:139]\n"
"s_lshl_b32 s78, s8, 13\n"
"v_mfma_f32_16x16x32_bf16 a[140:143], v[44:47], a[196:199], a[140:143]\n"
"ds_read_b128 a[216:219], v142 offset:14336\n"
"s_lshl_b32 s79, s8, 12\n"
"v_mfma_f32_16x16x32_bf16 a[140:143], v[12:15], a[192:195], a[140:143]\n"
"v_add_co_u32 v180, vcc, v182, v180\n"
"s_nop 1\n"
"v_addc_co_u32 v181, vcc, 0, v181, vcc\n"
"v_mfma_f32_16x16x32_bf16 a[144:147], a[192:195], v[16:19], a[144:147]\n"
"ds_read_b128 a[220:223], v143 offset:14336\n"
"s_add_u32 s79, s79, 0x20000\n"
"v_mfma_f32_16x16x32_bf16 a[144:147], a[196:199], v[48:51], a[144:147]\n"
"v_add_u32 v186, 0x8000, v186\n"
"v_add_u32 v187, 0x2b00, v187\n"
"v_mfma_f32_16x16x32_bf16 a[148:151], a[196:199], v[52:55], a[148:151]\n"
"v_lshl_add_u32 v208, v212, 3, v214\n"
"v_mfma_f32_16x16x32_bf16 a[148:151], a[192:195], v[20:23], a[148:151]\n"
"v_lshlrev_b32 v209, 8, v208\n"
"v_mfma_f32_16x16x32_bf16 a[152:155], a[192:195], v[24:27], a[152:155]\n"
"v_lshl_add_u32 v209, v215, 3, v209\n"
"v_mfma_f32_16x16x32_bf16 a[152:155], a[196:199], v[56:59], a[152:155]\n"
"v_and_b32 v210, 1, v212\n"
"v_mfma_f32_16x16x32_bf16 a[156:159], a[196:199], v[60:63], a[156:159]\n"
"v_lshl_or_b32 v210, v210, 2, v214\n"
"v_mfma_f32_16x16x32_bf16 a[156:159], a[192:195], v[28:31], a[156:159]\n"
"v_xor_b32 v211, 0, v210\n"
"s_waitcnt lgkmcnt(4)\n"
"v_mfma_f32_16x16x32_bf16 a[160:163], v[0:3], a[200:203], a[160:163]\n"
"v_lshl_add_u32 v196, v211, 5, v209\n"
"v_mfma_f32_16x16x32_bf16 a[160:163], v[32:35], a[204:207], a[160:163]\n"
"v_xor_b32 v211, 1, v210\n"
"v_mfma_f32_16x16x32_bf16 a[164:167], v[36:39], a[204:207], a[164:167]\n"
"v_lshl_add_u32 v197, v211, 5, v209\n"
"v_mfma_f32_16x16x32_bf16 a[164:167], v[4:7], a[200:203], a[164:167]\n"
"v_xor_b32 v211, 2, v210\n"
"v_mfma_f32_16x16x32_bf16 a[168:171], v[8:11], a[200:203], a[168:171]\n"
"v_lshl_add_u32 v198, v211, 5, v209\n"
"v_mfma_f32_16x16x32_bf16 a[168:171], v[40:43], a[204:207], a[168:171]\n"
"v_xor_b32 v211, 3, v210\n"
"v_mfma_f32_16x16x32_bf16 a[172:175], v[44:47], a[204:207], a[172:175]\n"
"v_lshl_add_u32 v199, v211, 5, v209\n"
"v_mfma_f32_16x16x32_bf16 a[172:175], v[12:15], a[200:203], a[172:175]\n"
"v_xor_b32 v211, 4, v210\n"
"v_mfma_f32_16x16x32_bf16 a[176:179], a[200:203], v[16:19], a[176:179]\n"
"v_lshl_add_u32 v200, v211, 5, v209\n"
"v_mfma_f32_16x16x32_bf16 a[176:179], a[204:207], v[48:51], a[176:179]\n"
"v_xor_b32 v211, 5, v210\n"
"v_mfma_f32_16x16x32_bf16 a[180:183], a[204:207], v[52:55], a[180:183]\n"
"v_lshl_add_u32 v201, v211, 5, v209\n"
"v_mfma_f32_16x16x32_bf16 a[180:183], a[200:203], v[20:23], a[180:183]\n"
"v_mfma_f32_16x16x32_bf16 a[184:187], a[200:203], v[24:27], a[184:187]\n"
"s_waitcnt vmcnt(5) lgkmcnt(0)\n"
"s_barrier\n"
"v_mfma_f32_16x16x32_bf16 a[184:187], a[204:207], v[56:59], a[184:187]\n"
"ds_read_b128 a[192:195], v140\n"
"v_xor_b32 v211, 6, v210\n"
"v_mfma_f32_16x16x32_bf16 a[188:191], a[204:207], v[60:63], a[188:191]\n"
"ds_read_b128 a[196:199], v141\n"
"v_lshl_add_u32 v202, v211, 5, v209\n"
"v_mfma_f32_16x16x32_bf16 a[188:191], a[200:203], v[28:31], a[188:191]\n"
"s_add_u32 m0, s70, 0x10000\n"
"v_xor_b32 v211, 7, v210\n"
"v_mfma_f32_16x16x32_bf16 v[64:67], v[0:3], a[208:211], v[64:67]\n"
"ds_read_b128 a[200:203], v140 offset:2048\n"
"v_lshl_add_u32 v203, v211, 5, v209\n"
"v_mfma_f32_16x16x32_bf16 v[64:67], v[32:35], a[212:215], v[64:67]\n"
"buffer_load_dwordx4 v128, s[16:19], s68 offen lds\n"
"v_mfma_f32_16x16x32_bf16 v[96:99], v[32:35], a[220:223], v[96:99]\n"
"ds_read_b128 a[204:207], v141 offset:2048\n"
"v_lshlrev_b32 v209, 7, v208\n"
"v_mfma_f32_16x16x32_bf16 v[96:99], v[0:3], a[216:219], v[96:99]\n"
"v_lshl_add_u32 v209, v215, 3, v209\n"
"v_mfma_f32_16x16x32_bf16 v[68:71], v[4:7], a[208:211], v[68:71]\n"
"ds_read_b64_tr_b16 v[0:1], v144\n"
"ds_read_b64_tr_b16 v[2:3], v144 offset:1024\n"
"v_mfma_f32_16x16x32_bf16 v[68:71], v[36:39], a[212:215], v[68:71]\n"
"buffer_load_dwordx4 v129, s[16:19], s68 offen offset:1024 lds\n"
"v_mfma_f32_16x16x32_bf16 v[100:103], v[36:39], a[220:223], v[100:103]\n"
"ds_read_b64_tr_b16 v[32:33], v144 offset:8192\n"
"ds_read_b64_tr_b16 v[34:35], v144 offset:9216\n"
"v_mfma_f32_16x16x32_bf16 v[100:103], v[4:7], a[216:219], v[100:103]\n"
"v_add_u32 v209, 0x20000, v209\n"
"v_mfma_f32_16x16x32_bf16 v[72:75], v[8:11], a[208:211], v[72:75]\n"
"ds_read_b64_tr_b16 v[4:5], v145\n"
"ds_read_b64_tr_b16 v[6:7], v145 offset:1024\n"
"v_mfma_f32_16x16x32_bf16 v[72:75], v[40:43], a[212:215], v[72:75]\n"
"buffer_load_dwordx4 v130, s[16:19], s68 offen offset:2048 lds\n"
"v_mfma_f32_16x16x32_bf16 v[104:107], v[40:43], a[220:223], v[104:107]\n"
"ds_read_b64_tr_b16 v[36:37], v145 offset:8192\n"
"ds_read_b64_tr_b16 v[38:39], v145 offset:9216\n"
"v_mfma_f32_16x16x32_bf16 v[104:107], v[8:11], a[216:219], v[104:107]\n"
"v_and_b32 v210, 1, v212\n"
"v_mfma_f32_16x16x32_bf16 v[76:79], v[12:15], a[208:211], v[76:79]\n"
"ds_read_b64_tr_b16 v[8:9], v146\n"
"ds_read_b64_tr_b16 v[10:11], v146 offset:1024\n"
"v_mfma_f32_16x16x32_bf16 v[76:79], v[44:47], a[212:215], v[76:79]\n"
"buffer_load_dwordx4 v131, s[16:19], s68 offen offset:3072 lds\n"
"v_mfma_f32_16x16x32_bf16 v[108:111], v[44:47], a[220:223], v[108:111]\n"
"ds_read_b64_tr_b16 v[40:41], v146 offset:8192\n"
"ds_read_b64_tr_b16 v[42:43], v146 offset:9216\n"
"v_mfma_f32_16x16x32_bf16 v[108:111], v[12:15], a[216:219], v[108:111]\n"
"s_add_u32 m0, s70, 0x11000\n"
"v_lshlrev_b32 v210, 1, v210\n"
"v_mfma_f32_16x16x32_bf16 v[80:83], a[208:211], v[16:19], v[80:83]\n"
"ds_read_b64_tr_b16 v[12:13], v147\n"
"ds_read_b64_tr_b16 v[14:15], v147 offset:1024\n"
"v_mfma_f32_16x16x32_bf16 v[80:83], a[212:215], v[48:51], v[80:83]\n"
"buffer_load_dwordx4 v132, s[16:19], s68 offen lds\n"
"v_mfma_f32_16x16x32_bf16 v[112:115], a[220:223], v[48:51], v[112:115]\n"
"ds_read_b64_tr_b16 v[44:45], v147 offset:8192\n"
"ds_read_b64_tr_b16 v[46:47], v147 offset:9216\n"
"v_mfma_f32_16x16x32_bf16 v[112:115], a[216:219], v[16:19], v[112:115]\n"
"v_lshrrev_b32 v211, 1, v214\n"
"v_mfma_f32_16x16x32_bf16 v[84:87], a[208:211], v[20:23], v[84:87]\n"
"ds_read_b64_tr_b16 v[16:17], v144 offset:16384\n"
"ds_read_b64_tr_b16 v[18:19], v144 offset:17408\n"
"v_mfma_f32_16x16x32_bf16 v[84:87], a[212:215], v[52:55], v[84:87]\n"
"buffer_load_dwordx4 v133, s[16:19], s68 offen offset:1024 lds\n"
"v_mfma_f32_16x16x32_bf16 v[116:119], a[220:223], v[52:55], v[116:119]\n"
"ds_read_b64_tr_b16 v[48:49], v144 offset:24576\n"
"ds_read_b64_tr_b16 v[50:51], v144 offset:25600\n"
"v_mfma_f32_16x16x32_bf16 v[116:119], a[216:219], v[20:23], v[116:119]\n"
"v_or_b32 v210, v210, v211\n"
"v_mfma_f32_16x16x32_bf16 v[88:91], a[208:211], v[24:27], v[88:91]\n"
"ds_read_b64_tr_b16 v[20:21], v145 offset:16384\n"
"ds_read_b64_tr_b16 v[22:23], v145 offset:17408\n"
"v_mfma_f32_16x16x32_bf16 v[88:91], a[212:215], v[56:59], v[88:91]\n"
"buffer_load_dwordx4 v134, s[16:19], s68 offen offset:2048 lds\n"
"v_mfma_f32_16x16x32_bf16 v[120:123], a[220:223], v[56:59], v[120:123]\n"
"ds_read_b64_tr_b16 v[52:53], v145 offset:24576\n"
"ds_read_b64_tr_b16 v[54:55], v145 offset:25600\n"
"v_mfma_f32_16x16x32_bf16 v[120:123], a[216:219], v[24:27], v[120:123]\n"
"v_xor_b32 v211, 0, v210\n"
"v_mfma_f32_16x16x32_bf16 v[92:95], a[208:211], v[28:31], v[92:95]\n"
"ds_read_b64_tr_b16 v[24:25], v146 offset:16384\n"
"ds_read_b64_tr_b16 v[26:27], v146 offset:17408\n"
"v_mfma_f32_16x16x32_bf16 v[92:95], a[212:215], v[60:63], v[92:95]\n"
"buffer_load_dwordx4 v135, s[16:19], s68 offen offset:3072 lds\n"
"v_mfma_f32_16x16x32_bf16 v[124:127], a[220:223], v[60:63], v[124:127]\n"
"ds_read_b64_tr_b16 v[56:57], v146 offset:24576\n"
"ds_read_b64_tr_b16 v[58:59], v146 offset:25600\n"
"v_mfma_f32_16x16x32_bf16 v[124:127], a[216:219], v[28:31], v[124:127]\n"
"s_add_u32 m0, s71, 0x10000\n"
"s_waitcnt lgkmcnt(15)\n"
"v_mfma_f32_16x16x32_bf16 a[0:3], v[0:3], a[192:195], a[0:3]\n"
"ds_read_b64_tr_b16 v[28:29], v147 offset:16384\n"
"ds_read_b64_tr_b16 v[30:31], v147 offset:17408\n"
"v_mfma_f32_16x16x32_bf16 a[0:3], v[32:35], a[196:199], a[0:3]\n"
"buffer_load_dwordx4 v136, s[20:23], s69 offen lds\n"
"v_mfma_f32_16x16x32_bf16 a[32:35], v[32:35], a[204:207], a[32:35]\n"
"ds_read_b64_tr_b16 v[60:61], v147 offset:24576\n"
"ds_read_b64_tr_b16 v[62:63], v147 offset:25600\n"
"v_mfma_f32_16x16x32_bf16 a[32:35], v[0:3], a[200:203], a[32:35]\n"
"v_lshl_add_u32 v204, v211, 5, v209\n"
"v_mfma_f32_16x16x32_bf16 a[4:7], v[4:7], a[192:195], a[4:7]\n"
"buffer_load_dwordx4 v137, s[20:23], s69 offen offset:1024 lds\n"
"v_mfma_f32_16x16x32_bf16 a[4:7], v[36:39], a[196:199], a[4:7]\n"
"v_xor_b32 v211, 1, v210\n"
"v_mfma_f32_16x16x32_bf16 a[36:39], v[36:39], a[204:207], a[36:39]\n"
"ds_read_b128 a[208:211], v140 offset:4096\n"
"v_lshl_add_u32 v205, v211, 5, v209\n"
"v_mfma_f32_16x16x32_bf16 a[36:39], v[4:7], a[200:203], a[36:39]\n"
"v_xor_b32 v211, 2, v210\n"
"s_waitcnt lgkmcnt(15)\n"
"v_mfma_f32_16x16x32_bf16 a[8:11], v[8:11], a[192:195], a[8:11]\n"
"ds_read_b128 a[212:215], v141 offset:4096\n"
"v_lshl_add_u32 v206, v211, 5, v209\n"
"v_mfma_f32_16x16x32_bf16 a[8:11], v[40:43], a[196:199], a[8:11]\n"
"buffer_load_dwordx4 v138, s[20:23], s69 offen offset:2048 lds\n"
"v_mfma_f32_16x16x32_bf16 a[40:43], v[40:43], a[204:207], a[40:43]\n"
"v_and_b32 v207, 7, v255\n"
"v_mfma_f32_16x16x32_bf16 a[40:43], v[8:11], a[200:203], a[40:43]\n"
"v_lshlrev_b32 v207, 3, v207\n"
"v_mfma_f32_16x16x32_bf16 a[12:15], v[12:15], a[192:195], a[12:15]\n"
"buffer_load_dwordx4 v139, s[20:23], s69 offen offset:3072 lds\n"
"v_mfma_f32_16x16x32_bf16 a[12:15], v[44:47], a[196:199], a[12:15]\n"
"v_mfma_f32_16x16x32_bf16 a[44:47], v[44:47], a[204:207], a[44:47]\n"
"v_mfma_f32_16x16x32_bf16 a[44:47], v[12:15], a[200:203], a[44:47]\n"
"s_add_u32 m0, s71, 0x14000\n"
"s_waitcnt lgkmcnt(10)\n"
"v_mfma_f32_16x16x32_bf16 a[16:19], a[192:195], v[16:19], a[16:19]\n"
"buffer_load_dwordx4 v136, s[24:27], s69 offen lds\n"
"v_mfma_f32_16x16x32_bf16 a[16:19], a[196:199], v[48:51], a[16:19]\n"
"v_mfma_f32_16x16x32_bf16 a[48:51], a[204:207], v[48:51], a[48:51]\n"
"ds_read_b128 a[216:219], v140 offset:6144\n"
"v_mfma_f32_16x16x32_bf16 a[48:51], a[200:203], v[16:19], a[48:51]\n"
"v_mfma_f32_16x16x32_bf16 a[20:23], a[192:195], v[20:23], a[20:23]\n"
"ds_read_b128 a[220:223], v141 offset:6144\n"
"v_mfma_f32_16x16x32_bf16 a[20:23], a[196:199], v[52:55], a[20:23]\n"
"buffer_load_dwordx4 v137, s[24:27], s69 offen offset:1024 lds\n"
"v_mfma_f32_16x16x32_bf16 a[52:55], a[204:207], v[52:55], a[52:55]\n"
"v_mfma_f32_16x16x32_bf16 a[52:55], a[200:203], v[20:23], a[52:55]\n"
"s_waitcnt lgkmcnt(4)\n"
"v_mfma_f32_16x16x32_bf16 a[24:27], a[192:195], v[24:27], a[24:27]\n"
"buffer_load_dwordx4 v138, s[24:27], s69 offen offset:2048 lds\n"
"v_mfma_f32_16x16x32_bf16 a[24:27], a[196:199], v[56:59], a[24:27]\n"
"v_mfma_f32_16x16x32_bf16 a[56:59], a[204:207], v[56:59], a[56:59]\n"
"v_mfma_f32_16x16x32_bf16 a[56:59], a[200:203], v[24:27], a[56:59]\n"
"v_mfma_f32_16x16x32_bf16 a[28:31], a[192:195], v[28:31], a[28:31]\n"
"buffer_load_dwordx4 v139, s[24:27], s69 offen offset:3072 lds\n"
"v_mfma_f32_16x16x32_bf16 a[28:31], a[196:199], v[60:63], a[28:31]\n"
"v_mfma_f32_16x16x32_bf16 a[60:63], a[204:207], v[60:63], a[60:63]\n"
"v_mfma_f32_16x16x32_bf16 a[60:63], a[200:203], v[28:31], a[60:63]\n"
"s_add_u32 s68, s68, 0x80\n"
"s_waitcnt lgkmcnt(2)\n"
"v_mfma_f32_16x16x32_bf16 a[64:67], v[0:3], a[208:211], a[64:67]\n"
"s_add_u32 s69, s69, 0x80000\n"
"v_mfma_f32_16x16x32_bf16 a[64:67], v[32:35], a[212:215], a[64:67]\n"
"v_mfma_f32_16x16x32_bf16 a[68:71], v[36:39], a[212:215], a[68:71]\n"
"v_mfma_f32_16x16x32_bf16 a[68:71], v[4:7], a[208:211], a[68:71]\n"
"v_mfma_f32_16x16x32_bf16 a[72:75], v[8:11], a[208:211], a[72:75]\n"
"ds_read_b128 a[192:195], v140 offset:8192\n"
"v_mfma_f32_16x16x32_bf16 a[72:75], v[40:43], a[212:215], a[72:75]\n"
"global_load_dword v178, v[180:181], off\n"
"v_mfma_f32_16x16x32_bf16 a[76:79], v[44:47], a[212:215], a[76:79]\n"
"ds_read_b128 a[196:199], v141 offset:8192\n"
"v_mfma_f32_16x16x32_bf16 a[76:79], v[12:15], a[208:211], a[76:79]\n"
"v_mfma_f32_16x16x32_bf16 a[80:83], a[208:211], v[16:19], a[80:83]\n"
"v_mfma_f32_16x16x32_bf16 a[80:83], a[212:215], v[48:51], a[80:83]\n"
"v_mfma_f32_16x16x32_bf16 a[84:87], a[212:215], v[52:55], a[84:87]\n"
"global_load_dword v178, v186, s[44:45]\n"
"v_mfma_f32_16x16x32_bf16 a[84:87], a[208:211], v[20:23], a[84:87]\n"
"v_mfma_f32_16x16x32_bf16 a[88:91], a[208:211], v[24:27], a[88:91]\n"
"v_mfma_f32_16x16x32_bf16 a[88:91], a[212:215], v[56:59], a[88:91]\n"
"v_mfma_f32_16x16x32_bf16 a[92:95], a[212:215], v[60:63], a[92:95]\n"
"v_mfma_f32_16x16x32_bf16 a[92:95], a[208:211], v[28:31], a[92:95]\n"
"s_waitcnt lgkmcnt(2)\n"
"v_mfma_f32_16x16x32_bf16 a[96:99], v[0:3], a[216:219], a[96:99]\n"
"ds_read_b128 a[200:203], v140 offset:10240\n"
"v_mfma_f32_16x16x32_bf16 a[96:99], v[32:35], a[220:223], a[96:99]\n"
"global_load_dword v178, v187, s[52:53]\n"
"v_mfma_f32_16x16x32_bf16 a[100:103], v[36:39], a[220:223], a[100:103]\n"
"ds_read_b128 a[204:207], v141 offset:10240\n"
"v_mfma_f32_16x16x32_bf16 a[100:103], v[4:7], a[216:219], a[100:103]\n"
"v_mfma_f32_16x16x32_bf16 a[104:107], v[8:11], a[216:219], a[104:107]\n"
"v_mfma_f32_16x16x32_bf16 a[104:107], v[40:43], a[220:223], a[104:107]\n"
"v_mfma_f32_16x16x32_bf16 a[108:111], v[44:47], a[220:223], a[108:111]\n"
"v_mfma_f32_16x16x32_bf16 a[108:111], v[12:15], a[216:219], a[108:111]\n"
"v_mfma_f32_16x16x32_bf16 a[112:115], a[216:219], v[16:19], a[112:115]\n"
"v_mfma_f32_16x16x32_bf16 a[112:115], a[220:223], v[48:51], a[112:115]\n"
"v_mfma_f32_16x16x32_bf16 a[116:119], a[220:223], v[52:55], a[116:119]\n"
"v_mfma_f32_16x16x32_bf16 a[116:119], a[216:219], v[20:23], a[116:119]\n"
"v_mfma_f32_16x16x32_bf16 a[120:123], a[216:219], v[24:27], a[120:123]\n"
"ds_read_b128 a[208:211], v140 offset:12288\n"
"v_mfma_f32_16x16x32_bf16 a[120:123], a[220:223], v[56:59], a[120:123]\n"
"v_mfma_f32_16x16x32_bf16 a[124:127], a[220:223], v[60:63], a[124:127]\n"
"ds_read_b128 a[212:215], v141 offset:12288\n"
"v_mfma_f32_16x16x32_bf16 a[124:127], a[216:219], v[28:31], a[124:127]\n"
"s_waitcnt lgkmcnt(4)\n"
"v_mfma_f32_16x16x32_bf16 a[128:131], v[0:3], a[192:195], a[128:131]\n"
"v_lshrrev_b32 v241, 4, v255\n"
"v_lshlrev_b32 v241, 3, v241\n"
"v_mfma_f32_16x16x32_bf16 a[128:131], v[32:35], a[196:199], a[128:131]\n"
"v_mfma_f32_16x16x32_bf16 a[132:135], v[36:39], a[196:199], a[132:135]\n"
"v_mfma_f32_16x16x32_bf16 a[132:135], v[4:7], a[192:195], a[132:135]\n"
"v_mfma_f32_16x16x32_bf16 a[136:139], v[8:11], a[192:195], a[136:139]\n"
"v_mfma_f32_16x16x32_bf16 a[136:139], v[40:43], a[196:199], a[136:139]\n"
"v_mfma_f32_16x16x32_bf16 a[140:143], v[44:47], a[196:199], a[140:143]\n"
"ds_read_b128 a[216:219], v140 offset:14336\n"
"v_mfma_f32_16x16x32_bf16 a[140:143], v[12:15], a[192:195], a[140:143]\n"
"v_mfma_f32_16x16x32_bf16 a[144:147], a[192:195], v[16:19], a[144:147]\n"
"ds_read_b128 a[220:223], v141 offset:14336\n"
"v_mfma_f32_16x16x32_bf16 a[144:147], a[196:199], v[48:51], a[144:147]\n"
"v_mfma_f32_16x16x32_bf16 a[148:151], a[196:199], v[52:55], a[148:151]\n"
"v_mfma_f32_16x16x32_bf16 a[148:151], a[192:195], v[20:23], a[148:151]\n"
"v_mfma_f32_16x16x32_bf16 a[152:155], a[192:195], v[24:27], a[152:155]\n"
"v_mfma_f32_16x16x32_bf16 a[152:155], a[196:199], v[56:59], a[152:155]\n"
"v_mfma_f32_16x16x32_bf16 a[156:159], a[196:199], v[60:63], a[156:159]\n"
"v_mfma_f32_16x16x32_bf16 a[156:159], a[192:195], v[28:31], a[156:159]\n"
"s_waitcnt lgkmcnt(4)\n"
"v_mfma_f32_16x16x32_bf16 a[160:163], v[0:3], a[200:203], a[160:163]\n"
"v_mfma_f32_16x16x32_bf16 a[160:163], v[32:35], a[204:207], a[160:163]\n"
"v_mfma_f32_16x16x32_bf16 a[164:167], v[36:39], a[204:207], a[164:167]\n"
"v_mfma_f32_16x16x32_bf16 a[164:167], v[4:7], a[200:203], a[164:167]\n"
"v_mfma_f32_16x16x32_bf16 a[168:171], v[8:11], a[200:203], a[168:171]\n"
"v_mfma_f32_16x16x32_bf16 a[168:171], v[40:43], a[204:207], a[168:171]\n"
"v_mfma_f32_16x16x32_bf16 a[172:175], v[44:47], a[204:207], a[172:175]\n"
"v_mfma_f32_16x16x32_bf16 a[172:175], v[12:15], a[200:203], a[172:175]\n"
"v_mfma_f32_16x16x32_bf16 a[176:179], a[200:203], v[16:19], a[176:179]\n"
"v_mfma_f32_16x16x32_bf16 a[176:179], a[204:207], v[48:51], a[176:179]\n"
"v_mfma_f32_16x16x32_bf16 a[180:183], a[204:207], v[52:55], a[180:183]\n"
"v_mfma_f32_16x16x32_bf16 a[180:183], a[200:203], v[20:23], a[180:183]\n"
"v_mfma_f32_16x16x32_bf16 a[184:187], a[200:203], v[24:27], a[184:187]\n"
"s_waitcnt vmcnt(3) lgkmcnt(0)\n"
"s_barrier\n"
"v_mfma_f32_16x16x32_bf16 a[184:187], a[204:207], v[56:59], a[184:187]\n"
"ds_read_b128 a[192:195], v142\n"
"v_mfma_f32_16x16x32_bf16 a[188:191], a[204:207], v[60:63], a[188:191]\n"
"ds_read_b128 a[196:199], v143\n"
"v_mfma_f32_16x16x32_bf16 a[188:191], a[200:203], v[28:31], a[188:191]\n"
"s_add_u32 m0, s70, 0\n"
"v_mfma_f32_16x16x32_bf16 v[64:67], v[0:3], a[208:211], v[64:67]\n"
"ds_read_b128 a[200:203], v142 offset:2048\n"
"v_mfma_f32_16x16x32_bf16 v[64:67], v[32:35], a[212:215], v[64:67]\n"
"buffer_load_dwordx4 v128, s[16:19], s68 offen lds\n"
"v_mfma_f32_16x16x32_bf16 v[96:99], v[32:35], a[220:223], v[96:99]\n"
"ds_read_b128 a[204:207], v143 offset:2048\n"
"v_mfma_f32_16x16x32_bf16 v[96:99], v[0:3], a[216:219], v[96:99]\n"
"v_mfma_f32_16x16x32_bf16 v[68:71], v[4:7], a[208:211], v[68:71]\n"
"ds_read_b64_tr_b16 v[0:1], v148\n"
"ds_read_b64_tr_b16 v[2:3], v148 offset:1024\n"
"v_mfma_f32_16x16x32_bf16 v[68:71], v[36:39], a[212:215], v[68:71]\n"
"buffer_load_dwordx4 v129, s[16:19], s68 offen offset:1024 lds\n"
"v_mfma_f32_16x16x32_bf16 v[100:103], v[36:39], a[220:223], v[100:103]\n"
"ds_read_b64_tr_b16 v[32:33], v148 offset:8192\n"
"ds_read_b64_tr_b16 v[34:35], v148 offset:9216\n"
"v_mfma_f32_16x16x32_bf16 v[100:103], v[4:7], a[216:219], v[100:103]\n"
"v_mfma_f32_16x16x32_bf16 v[72:75], v[8:11], a[208:211], v[72:75]\n"
"ds_read_b64_tr_b16 v[4:5], v149\n"
"ds_read_b64_tr_b16 v[6:7], v149 offset:1024\n"
"v_mfma_f32_16x16x32_bf16 v[72:75], v[40:43], a[212:215], v[72:75]\n"
"buffer_load_dwordx4 v130, s[16:19], s68 offen offset:2048 lds\n"
"v_mfma_f32_16x16x32_bf16 v[104:107], v[40:43], a[220:223], v[104:107]\n"
"ds_read_b64_tr_b16 v[36:37], v149 offset:8192\n"
"ds_read_b64_tr_b16 v[38:39], v149 offset:9216\n"
"v_mfma_f32_16x16x32_bf16 v[104:107], v[8:11], a[216:219], v[104:107]\n"
"v_mfma_f32_16x16x32_bf16 v[76:79], v[12:15], a[208:211], v[76:79]\n"
"ds_read_b64_tr_b16 v[8:9], v150\n"
"ds_read_b64_tr_b16 v[10:11], v150 offset:1024\n"
"v_mfma_f32_16x16x32_bf16 v[76:79], v[44:47], a[212:215], v[76:79]\n"
"buffer_load_dwordx4 v131, s[16:19], s68 offen offset:3072 lds\n"
"v_mfma_f32_16x16x32_bf16 v[108:111], v[44:47], a[220:223], v[108:111]\n"
"ds_read_b64_tr_b16 v[40:41], v150 offset:8192\n"
"ds_read_b64_tr_b16 v[42:43], v150 offset:9216\n"
"v_mfma_f32_16x16x32_bf16 v[108:111], v[12:15], a[216:219], v[108:111]\n"
"s_add_u32 m0, s70, 0x1000\n"
"v_mfma_f32_16x16x32_bf16 v[80:83], a[208:211], v[16:19], v[80:83]\n"
"ds_read_b64_tr_b16 v[12:13], v151\n"
"ds_read_b64_tr_b16 v[14:15], v151 offset:1024\n"
"v_mfma_f32_16x16x32_bf16 v[80:83], a[212:215], v[48:51], v[80:83]\n"
"buffer_load_dwordx4 v132, s[16:19], s68 offen lds\n"
"v_mfma_f32_16x16x32_bf16 v[112:115], a[220:223], v[48:51], v[112:115]\n"
"ds_read_b64_tr_b16 v[44:45], v151 offset:8192\n"
"ds_read_b64_tr_b16 v[46:47], v151 offset:9216\n"
"v_mfma_f32_16x16x32_bf16 v[112:115], a[216:219], v[16:19], v[112:115]\n"
"v_mfma_f32_16x16x32_bf16 v[84:87], a[208:211], v[20:23], v[84:87]\n"
"ds_read_b64_tr_b16 v[16:17], v148 offset:16384\n"
"ds_read_b64_tr_b16 v[18:19], v148 offset:17408\n"
"v_mfma_f32_16x16x32_bf16 v[84:87], a[212:215], v[52:55], v[84:87]\n"
"buffer_load_dwordx4 v133, s[16:19], s68 offen offset:1024 lds\n"
"v_mfma_f32_16x16x32_bf16 v[116:119], a[220:223], v[52:55], v[116:119]\n"
"ds_read_b64_tr_b16 v[48:49], v148 offset:24576\n"
"ds_read_b64_tr_b16 v[50:51], v148 offset:25600\n"
"v_mfma_f32_16x16x32_bf16 v[116:119], a[216:219], v[20:23], v[116:119]\n"
"v_mfma_f32_16x16x32_bf16 v[88:91], a[208:211], v[24:27], v[88:91]\n"
"ds_read_b64_tr_b16 v[20:21], v149 offset:16384\n"
"ds_read_b64_tr_b16 v[22:23], v149 offset:17408\n"
"v_mfma_f32_16x16x32_bf16 v[88:91], a[212:215], v[56:59], v[88:91]\n"
"buffer_load_dwordx4 v134, s[16:19], s68 offen offset:2048 lds\n"
"v_mfma_f32_16x16x32_bf16 v[120:123], a[220:223], v[56:59], v[120:123]\n"
"ds_read_b64_tr_b16 v[52:53], v149 offset:24576\n"
"ds_read_b64_tr_b16 v[54:55], v149 offset:25600\n"
"v_mfma_f32_16x16x32_bf16 v[120:123], a[216:219], v[24:27], v[120:123]\n"
"v_mfma_f32_16x16x32_bf16 v[92:95], a[208:211], v[28:31], v[92:95]\n"
"ds_read_b64_tr_b16 v[24:25], v150 offset:16384\n"
"ds_read_b64_tr_b16 v[26:27], v150 offset:17408\n"
"v_mfma_f32_16x16x32_bf16 v[92:95], a[212:215], v[60:63], v[92:95]\n"
"buffer_load_dwordx4 v135, s[16:19], s68 offen offset:3072 lds\n"
"v_mfma_f32_16x16x32_bf16 v[124:127], a[220:223], v[60:63], v[124:127]\n"
"ds_read_b64_tr_b16 v[56:57], v150 offset:24576\n"
"ds_read_b64_tr_b16 v[58:59], v150 offset:25600\n"
"v_mfma_f32_16x16x32_bf16 v[124:127], a[216:219], v[28:31], v[124:127]\n"
"s_add_u32 m0, s71, 0\n"
"s_waitcnt lgkmcnt(15)\n"
"v_mfma_f32_16x16x32_bf16 a[0:3], v[0:3], a[192:195], a[0:3]\n"
"ds_read_b64_tr_b16 v[28:29], v151 offset:16384\n"
"ds_read_b64_tr_b16 v[30:31], v151 offset:17408\n"
"v_mfma_f32_16x16x32_bf16 a[0:3], v[32:35], a[196:199], a[0:3]\n"
"buffer_load_dwordx4 v136, s[20:23], s69 offen lds\n"
"v_mfma_f32_16x16x32_bf16 a[32:35], v[32:35], a[204:207], a[32:35]\n"
"ds_read_b64_tr_b16 v[60:61], v151 offset:24576\n"
"ds_read_b64_tr_b16 v[62:63], v151 offset:25600\n"
"v_mfma_f32_16x16x32_bf16 a[32:35], v[0:3], a[200:203], a[32:35]\n"
"v_mfma_f32_16x16x32_bf16 a[4:7], v[4:7], a[192:195], a[4:7]\n"
"buffer_load_dwordx4 v137, s[20:23], s69 offen offset:1024 lds\n"
"v_mfma_f32_16x16x32_bf16 a[4:7], v[36:39], a[196:199], a[4:7]\n"
"v_mfma_f32_16x16x32_bf16 a[36:39], v[36:39], a[204:207], a[36:39]\n"
"ds_read_b128 a[208:211], v142 offset:4096\n"
"v_mfma_f32_16x16x32_bf16 a[36:39], v[4:7], a[200:203], a[36:39]\n"
"s_waitcnt lgkmcnt(15)\n"
"v_mfma_f32_16x16x32_bf16 a[8:11], v[8:11], a[192:195], a[8:11]\n"
"ds_read_b128 a[212:215], v143 offset:4096\n"
"v_mfma_f32_16x16x32_bf16 a[8:11], v[40:43], a[196:199], a[8:11]\n"
"buffer_load_dwordx4 v138, s[20:23], s69 offen offset:2048 lds\n"
"v_mfma_f32_16x16x32_bf16 a[40:43], v[40:43], a[204:207], a[40:43]\n"
"v_mfma_f32_16x16x32_bf16 a[40:43], v[8:11], a[200:203], a[40:43]\n"
"v_mfma_f32_16x16x32_bf16 a[12:15], v[12:15], a[192:195], a[12:15]\n"
"buffer_load_dwordx4 v139, s[20:23], s69 offen offset:3072 lds\n"
"v_mfma_f32_16x16x32_bf16 a[12:15], v[44:47], a[196:199], a[12:15]\n"
"v_mfma_f32_16x16x32_bf16 a[44:47], v[44:47], a[204:207], a[44:47]\n"
"v_mfma_f32_16x16x32_bf16 a[44:47], v[12:15], a[200:203], a[44:47]\n"
"s_add_u32 m0, s71, 0x4000\n"
"s_waitcnt lgkmcnt(10)\n"
"v_mfma_f32_16x16x32_bf16 a[16:19], a[192:195], v[16:19], a[16:19]\n"
"buffer_load_dwordx4 v136, s[24:27], s69 offen lds\n"
"v_mfma_f32_16x16x32_bf16 a[16:19], a[196:199], v[48:51], a[16:19]\n"
"v_mfma_f32_16x16x32_bf16 a[48:51], a[204:207], v[48:51], a[48:51]\n"
"ds_read_b128 a[216:219], v142 offset:6144\n"
"v_mfma_f32_16x16x32_bf16 a[48:51], a[200:203], v[16:19], a[48:51]\n"
"v_mfma_f32_16x16x32_bf16 a[20:23], a[192:195], v[20:23], a[20:23]\n"
"ds_read_b128 a[220:223], v143 offset:6144\n"
"v_mfma_f32_16x16x32_bf16 a[20:23], a[196:199], v[52:55], a[20:23]\n"
"buffer_load_dwordx4 v137, s[24:27], s69 offen offset:1024 lds\n"
"v_mfma_f32_16x16x32_bf16 a[52:55], a[204:207], v[52:55], a[52:55]\n"
"v_mfma_f32_16x16x32_bf16 a[52:55], a[200:203], v[20:23], a[52:55]\n"
"s_waitcnt lgkmcnt(4)\n"
"v_mfma_f32_16x16x32_bf16 a[24:27], a[192:195], v[24:27], a[24:27]\n"
"buffer_load_dwordx4 v138, s[24:27], s69 offen offset:2048 lds\n"
"v_mfma_f32_16x16x32_bf16 a[24:27], a[196:199], v[56:59], a[24:27]\n"
"v_mfma_f32_16x16x32_bf16 a[56:59], a[204:207], v[56:59], a[56:59]\n"
"v_mfma_f32_16x16x32_bf16 a[56:59], a[200:203], v[24:27], a[56:59]\n"
"v_mfma_f32_16x16x32_bf16 a[28:31], a[192:195], v[28:31], a[28:31]\n"
"buffer_load_dwordx4 v139, s[24:27], s69 offen offset:3072 lds\n"
"v_mfma_f32_16x16x32_bf16 a[28:31], a[196:199], v[60:63], a[28:31]\n"
"v_mfma_f32_16x16x32_bf16 a[60:63], a[204:207], v[60:63], a[60:63]\n"
"v_mfma_f32_16x16x32_bf16 a[60:63], a[200:203], v[28:31], a[60:63]\n"
"s_add_u32 s68, s68, 0x80\n"
"s_waitcnt lgkmcnt(2)\n"
"v_mfma_f32_16x16x32_bf16 a[64:67], v[0:3], a[208:211], a[64:67]\n"
"s_add_u32 s69, s69, 0x80000\n"
"v_mfma_f32_16x16x32_bf16 a[64:67], v[32:35], a[212:215], a[64:67]\n"
"v_mfma_f32_16x16x32_bf16 a[68:71], v[36:39], a[212:215], a[68:71]\n"
"v_mfma_f32_16x16x32_bf16 a[68:71], v[4:7], a[208:211], a[68:71]\n"
"v_mfma_f32_16x16x32_bf16 a[72:75], v[8:11], a[208:211], a[72:75]\n"
"ds_read_b128 a[192:195], v142 offset:8192\n"
"s_lshl_b32 s12, s9, 3\n"
"s_add_u32 s12, s12, 0x800\n"
"s_mov_b64 exec, s[34:35]\n"
"buffer_store_dwordx2 v[250:251], v249, s[92:95], s12 offen sc1\n"
"s_mov_b64 exec, -1\n"
"v_mfma_f32_16x16x32_bf16 a[72:75], v[40:43], a[212:215], a[72:75]\n"
"v_mfma_f32_16x16x32_bf16 a[76:79], v[44:47], a[212:215], a[76:79]\n"
"ds_read_b128 a[196:199], v143 offset:8192\n"
"v_mfma_f32_16x16x32_bf16 a[76:79], v[12:15], a[208:211], a[76:79]\n"
"v_mfma_f32_16x16x32_bf16 a[80:83], a[208:211], v[16:19], a[80:83]\n"
"v_mfma_f32_16x16x32_bf16 a[80:83], a[212:215], v[48:51], a[80:83]\n"
"v_mfma_f32_16x16x32_bf16 a[84:87], a[212:215], v[52:55], a[84:87]\n"
"v_mfma_f32_16x16x32_bf16 a[84:87], a[208:211], v[20:23], a[84:87]\n"
"v_mfma_f32_16x16x32_bf16 a[88:91], a[208:211], v[24:27], a[88:91]\n"
"v_mfma_f32_16x16x32_bf16 a[88:91], a[212:215], v[56:59], a[88:91]\n"
"v_mfma_f32_16x16x32_bf16 a[92:95], a[212:215], v[60:63], a[92:95]\n"
"v_mfma_f32_16x16x32_bf16 a[92:95], a[208:211], v[28:31], a[92:95]\n"
"s_waitcnt lgkmcnt(2)\n"
"v_mfma_f32_16x16x32_bf16 a[96:99], v[0:3], a[216:219], a[96:99]\n"
"ds_read_b128 a[200:203], v142 offset:10240\n"
"v_mfma_f32_16x16x32_bf16 a[96:99], v[32:35], a[220:223], a[96:99]\n"
"v_mfma_f32_16x16x32_bf16 a[100:103], v[36:39], a[220:223], a[100:103]\n"
"ds_read_b128 a[204:207], v143 offset:10240\n"
"v_mfma_f32_16x16x32_bf16 a[100:103], v[4:7], a[216:219], a[100:103]\n"
"v_mfma_f32_16x16x32_bf16 a[104:107], v[8:11], a[216:219], a[104:107]\n"
"v_mfma_f32_16x16x32_bf16 a[104:107], v[40:43], a[220:223], a[104:107]\n"
"v_mfma_f32_16x16x32_bf16 a[108:111], v[44:47], a[220:223], a[108:111]\n"
"v_mfma_f32_16x16x32_bf16 a[108:111], v[12:15], a[216:219], a[108:111]\n"
"v_mfma_f32_16x16x32_bf16 a[112:115], a[216:219], v[16:19], a[112:115]\n"
"v_mfma_f32_16x16x32_bf16 a[112:115], a[220:223], v[48:51], a[112:115]\n"
"v_mfma_f32_16x16x32_bf16 a[116:119], a[220:223], v[52:55], a[116:119]\n"
"v_mfma_f32_16x16x32_bf16 a[116:119], a[216:219], v[20:23], a[116:119]\n"
"v_mfma_f32_16x16x32_bf16 a[120:123], a[216:219], v[24:27], a[120:123]\n"
"ds_read_b128 a[208:211], v142 offset:12288\n"
"v_mfma_f32_16x16x32_bf16 a[120:123], a[220:223], v[56:59], a[120:123]\n"
"v_mfma_f32_16x16x32_bf16 a[124:127], a[220:223], v[60:63], a[124:127]\n"
"ds_read_b128 a[212:215], v143 offset:12288\n"
"v_mfma_f32_16x16x32_bf16 a[124:127], a[216:219], v[28:31], a[124:127]\n"
"s_waitcnt lgkmcnt(4)\n"
"v_mfma_f32_16x16x32_bf16 a[128:131], v[0:3], a[192:195], a[128:131]\n"
"v_mfma_f32_16x16x32_bf16 a[128:131], v[32:35], a[196:199], a[128:131]\n"
"v_mfma_f32_16x16x32_bf16 a[132:135], v[36:39], a[196:199], a[132:135]\n"
"v_mfma_f32_16x16x32_bf16 a[132:135], v[4:7], a[192:195], a[132:135]\n"
"v_mfma_f32_16x16x32_bf16 a[136:139], v[8:11], a[192:195], a[136:139]\n"
"v_mfma_f32_16x16x32_bf16 a[136:139], v[40:43], a[196:199], a[136:139]\n"
"v_mfma_f32_16x16x32_bf16 a[140:143], v[44:47], a[196:199], a[140:143]\n"
"ds_read_b128 a[216:219], v142 offset:14336\n"
"v_mfma_f32_16x16x32_bf16 a[140:143], v[12:15], a[192:195], a[140:143]\n"
"v_mfma_f32_16x16x32_bf16 a[144:147], a[192:195], v[16:19], a[144:147]\n"
"ds_read_b128 a[220:223], v143 offset:14336\n"
"v_mfma_f32_16x16x32_bf16 a[144:147], a[196:199], v[48:51], a[144:147]\n"
"v_mfma_f32_16x16x32_bf16 a[148:151], a[196:199], v[52:55], a[148:151]\n"
"v_mfma_f32_16x16x32_bf16 a[148:151], a[192:195], v[20:23], a[148:151]\n"
"v_mfma_f32_16x16x32_bf16 a[152:155], a[192:195], v[24:27], a[152:155]\n"
"v_mfma_f32_16x16x32_bf16 a[152:155], a[196:199], v[56:59], a[152:155]\n"
"v_mfma_f32_16x16x32_bf16 a[156:159], a[196:199], v[60:63], a[156:159]\n"
"v_mfma_f32_16x16x32_bf16 a[156:159], a[192:195], v[28:31], a[156:159]\n"
"s_waitcnt lgkmcnt(4)\n"
"v_mfma_f32_16x16x32_bf16 a[160:163], v[0:3], a[200:203], a[160:163]\n"
"v_mfma_f32_16x16x32_bf16 a[160:163], v[32:35], a[204:207], a[160:163]\n"
"v_mfma_f32_16x16x32_bf16 a[164:167], v[36:39], a[204:207], a[164:167]\n"
"v_mfma_f32_16x16x32_bf16 a[164:167], v[4:7], a[200:203], a[164:167]\n"
"v_mfma_f32_16x16x32_bf16 a[168:171], v[8:11], a[200:203], a[168:171]\n"
"v_mfma_f32_16x16x32_bf16 a[168:171], v[40:43], a[204:207], a[168:171]\n"
"v_mfma_f32_16x16x32_bf16 a[172:175], v[44:47], a[204:207], a[172:175]\n"
"v_mfma_f32_16x16x32_bf16 a[172:175], v[12:15], a[200:203], a[172:175]\n"
"v_mfma_f32_16x16x32_bf16 a[176:179], a[200:203], v[16:19], a[176:179]\n"
"v_mfma_f32_16x16x32_bf16 a[176:179], a[204:207], v[48:51], a[176:179]\n"
"v_mfma_f32_16x16x32_bf16 a[180:183], a[204:207], v[52:55], a[180:183]\n"
"v_mfma_f32_16x16x32_bf16 a[180:183], a[200:203], v[20:23], a[180:183]\n"
"v_mfma_f32_16x16x32_bf16 a[184:187], a[200:203], v[24:27], a[184:187]\n"
"s_waitcnt vmcnt(1) lgkmcnt(0)\n"
"s_barrier\n"
"v_mfma_f32_16x16x32_bf16 a[184:187], a[204:207], v[56:59], a[184:187]\n"
"ds_read_b128 a[192:195], v140\n"
"v_mfma_f32_16x16x32_bf16 a[188:191], a[204:207], v[60:63], a[188:191]\n"
"ds_read_b128 a[196:199], v141\n"
"v_mfma_f32_16x16x32_bf16 a[188:191], a[200:203], v[28:31], a[188:191]\n"
"s_add_u32 m0, s70, 0x10000\n"
"v_mfma_f32_16x16x32_bf16 v[64:67], v[0:3], a[208:211], v[64:67]\n"
"ds_read_b128 a[200:203], v140 offset:2048\n"
"v_mfma_f32_16x16x32_bf16 v[64:67], v[32:35], a[212:215], v[64:67]\n"
"buffer_load_dwordx4 v128, s[16:19], s68 offen lds\n"
"v_mfma_f32_16x16x32_bf16 v[96:99], v[32:35], a[220:223], v[96:99]\n"
"ds_read_b128 a[204:207], v141 offset:2048\n"
"v_mfma_f32_16x16x32_bf16 v[96:99], v[0:3], a[216:219], v[96:99]\n"
"v_mfma_f32_16x16x32_bf16 v[68:71], v[4:7], a[208:211], v[68:71]\n"
"ds_read_b64_tr_b16 v[0:1], v144\n"
"ds_read_b64_tr_b16 v[2:3], v144 offset:1024\n"
"v_mfma_f32_16x16x32_bf16 v[68:71], v[36:39], a[212:215], v[68:71]\n"
"buffer_load_dwordx4 v129, s[16:19], s68 offen offset:1024 lds\n"
"v_mfma_f32_16x16x32_bf16 v[100:103], v[36:39], a[220:223], v[100:103]\n"
"ds_read_b64_tr_b16 v[32:33], v144 offset:8192\n"
"ds_read_b64_tr_b16 v[34:35], v144 offset:9216\n"
"v_mfma_f32_16x16x32_bf16 v[100:103], v[4:7], a[216:219], v[100:103]\n"
"v_mfma_f32_16x16x32_bf16 v[72:75], v[8:11], a[208:211], v[72:75]\n"
"ds_read_b64_tr_b16 v[4:5], v145\n"
"ds_read_b64_tr_b16 v[6:7], v145 offset:1024\n"
"v_mfma_f32_16x16x32_bf16 v[72:75], v[40:43], a[212:215], v[72:75]\n"
"buffer_load_dwordx4 v130, s[16:19], s68 offen offset:2048 lds\n"
"v_mfma_f32_16x16x32_bf16 v[104:107], v[40:43], a[220:223], v[104:107]\n"
"ds_read_b64_tr_b16 v[36:37], v145 offset:8192\n"
"ds_read_b64_tr_b16 v[38:39], v145 offset:9216\n"
"v_mfma_f32_16x16x32_bf16 v[104:107], v[8:11], a[216:219], v[104:107]\n"
"v_mfma_f32_16x16x32_bf16 v[76:79], v[12:15], a[208:211], v[76:79]\n"
"ds_read_b64_tr_b16 v[8:9], v146\n"
"ds_read_b64_tr_b16 v[10:11], v146 offset:1024\n"
"v_mfma_f32_16x16x32_bf16 v[76:79], v[44:47], a[212:215], v[76:79]\n"
"buffer_load_dwordx4 v131, s[16:19], s68 offen offset:3072 lds\n"
"v_mfma_f32_16x16x32_bf16 v[108:111], v[44:47], a[220:223], v[108:111]\n"
"ds_read_b64_tr_b16 v[40:41], v146 offset:8192\n"
"ds_read_b64_tr_b16 v[42:43], v146 offset:9216\n"
"v_mfma_f32_16x16x32_bf16 v[108:111], v[12:15], a[216:219], v[108:111]\n"
"s_add_u32 m0, s70, 0x11000\n"
"v_mfma_f32_16x16x32_bf16 v[80:83], a[208:211], v[16:19], v[80:83]\n"
"ds_read_b64_tr_b16 v[12:13], v147\n"
"ds_read_b64_tr_b16 v[14:15], v147 offset:1024\n"
"v_mfma_f32_16x16x32_bf16 v[80:83], a[212:215], v[48:51], v[80:83]\n"
"buffer_load_dwordx4 v132, s[16:19], s68 offen lds\n"
"v_mfma_f32_16x16x32_bf16 v[112:115], a[220:223], v[48:51], v[112:115]\n"
"ds_read_b64_tr_b16 v[44:45], v147 offset:8192\n"
"ds_read_b64_tr_b16 v[46:47], v147 offset:9216\n"
"v_mfma_f32_16x16x32_bf16 v[112:115], a[216:219], v[16:19], v[112:115]\n"
"v_mfma_f32_16x16x32_bf16 v[84:87], a[208:211], v[20:23], v[84:87]\n"
"ds_read_b64_tr_b16 v[16:17], v144 offset:16384\n"
"ds_read_b64_tr_b16 v[18:19], v144 offset:17408\n"
"v_mfma_f32_16x16x32_bf16 v[84:87], a[212:215], v[52:55], v[84:87]\n"
"buffer_load_dwordx4 v133, s[16:19], s68 offen offset:1024 lds\n"
"v_mfma_f32_16x16x32_bf16 v[116:119], a[220:223], v[52:55], v[116:119]\n"
"ds_read_b64_tr_b16 v[48:49], v144 offset:24576\n"
"ds_read_b64_tr_b16 v[50:51], v144 offset:25600\n"
"v_mfma_f32_16x16x32_bf16 v[116:119], a[216:219], v[20:23], v[116:119]\n"
"v_mfma_f32_16x16x32_bf16 v[88:91], a[208:211], v[24:27], v[88:91]\n"
"ds_read_b64_tr_b16 v[20:21], v145 offset:16384\n"
"ds_read_b64_tr_b16 v[22:23], v145 offset:17408\n"
"v_mfma_f32_16x16x32_bf16 v[88:91], a[212:215], v[56:59], v[88:91]\n"
"buffer_load_dwordx4 v134, s[16:19], s68 offen offset:2048 lds\n"
"v_mfma_f32_16x16x32_bf16 v[120:123], a[220:223], v[56:59], v[120:123]\n"
"ds_read_b64_tr_b16 v[52:53], v145 offset:24576\n"
"ds_read_b64_tr_b16 v[54:55], v145 offset:25600\n"
"v_mfma_f32_16x16x32_bf16 v[120:123], a[216:219], v[24:27], v[120:123]\n"
"v_mfma_f32_16x16x32_bf16 v[92:95], a[208:211], v[28:31], v[92:95]\n"
"ds_read_b64_tr_b16 v[24:25], v146 offset:16384\n"
"ds_read_b64_tr_b16 v[26:27], v146 offset:17408\n"
"v_mfma_f32_16x16x32_bf16 v[92:95], a[212:215], v[60:63], v[92:95]\n"
"buffer_load_dwordx4 v135, s[16:19], s68 offen offset:3072 lds\n"
"v_mfma_f32_16x16x32_bf16 v[124:127], a[220:223], v[60:63], v[124:127]\n"
"ds_read_b64_tr_b16 v[56:57], v146 offset:24576\n"
"ds_read_b64_tr_b16 v[58:59], v146 offset:25600\n"
"v_mfma_f32_16x16x32_bf16 v[124:127], a[216:219], v[28:31], v[124:127]\n"
"s_add_u32 m0, s71, 0x10000\n"
"s_waitcnt lgkmcnt(15)\n"
"v_mfma_f32_16x16x32_bf16 a[0:3], v[0:3], a[192:195], a[0:3]\n"
"ds_read_b64_tr_b16 v[28:29], v147 offset:16384\n"
"ds_read_b64_tr_b16 v[30:31], v147 offset:17408\n"
"s_lshl_b32 s13, s77, 4\n"
"s_add_u32 s62, s13, 0x800\n"
"v_mfma_f32_16x16x32_bf16 a[0:3], v[32:35], a[196:199], a[0:3]\n"
"buffer_load_dwordx4 v136, s[20:23], s69 offen lds\n"
"v_mfma_f32_16x16x32_bf16 a[32:35], v[32:35], a[204:207], a[32:35]\n"
"ds_read_b64_tr_b16 v[60:61], v147 offset:24576\n"
"ds_read_b64_tr_b16 v[62:63], v147 offset:25600\n"
"v_mfma_f32_16x16x32_bf16 a[32:35], v[0:3], a[200:203], a[32:35]\n"
"buffer_load_dwordx2 v[238:239], v207, s[92:95], s62 offen sc1\n"
"v_mfma_f32_16x16x32_bf16 a[4:7], v[4:7], a[192:195], a[4:7]\n"
"buffer_load_dwordx4 v137, s[20:23], s69 offen offset:1024 lds\n"
"v_mfma_f32_16x16x32_bf16 a[4:7], v[36:39], a[196:199], a[4:7]\n"
"v_mfma_f32_16x16x32_bf16 a[36:39], v[36:39], a[204:207], a[36:39]\n"
"ds_read_b128 a[208:211], v140 offset:4096\n"
"v_mfma_f32_16x16x32_bf16 a[36:39], v[4:7], a[200:203], a[36:39]\n"
"s_waitcnt lgkmcnt(15)\n"
"v_mfma_f32_16x16x32_bf16 a[8:11], v[8:11], a[192:195], a[8:11]\n"
"ds_read_b128 a[212:215], v141 offset:4096\n"
"v_mfma_f32_16x16x32_bf16 a[8:11], v[40:43], a[196:199], a[8:11]\n"
"buffer_load_dwordx4 v138, s[20:23], s69 offen offset:2048 lds\n"
"v_mfma_f32_16x16x32_bf16 a[40:43], v[40:43], a[204:207], a[40:43]\n"
"v_mfma_f32_16x16x32_bf16 a[40:43], v[8:11], a[200:203], a[40:43]\n"
"v_mfma_f32_16x16x32_bf16 a[12:15], v[12:15], a[192:195], a[12:15]\n"
"buffer_load_dwordx4 v139, s[20:23], s69 offen offset:3072 lds\n"
"v_mfma_f32_16x16x32_bf16 a[12:15], v[44:47], a[196:199], a[12:15]\n"
"v_mfma_f32_16x16x32_bf16 a[44:47], v[44:47], a[204:207], a[44:47]\n"
"v_mfma_f32_16x16x32_bf16 a[44:47], v[12:15], a[200:203], a[44:47]\n"
"s_add_u32 m0, s71, 0x14000\n"
"s_waitcnt lgkmcnt(10)\n"
"v_mfma_f32_16x16x32_bf16 a[16:19], a[192:195], v[16:19], a[16:19]\n"
"buffer_load_dwordx4 v136, s[24:27], s69 offen lds\n"
"v_mfma_f32_16x16x32_bf16 a[16:19], a[196:199], v[48:51], a[16:19]\n"
"v_mfma_f32_16x16x32_bf16 a[48:51], a[204:207], v[48:51], a[48:51]\n"
"ds_read_b128 a[216:219], v140 offset:6144\n"
"v_mfma_f32_16x16x32_bf16 a[48:51], a[200:203], v[16:19], a[48:51]\n"
"v_mfma_f32_16x16x32_bf16 a[20:23], a[192:195], v[20:23], a[20:23]\n"
"ds_read_b128 a[220:223], v141 offset:6144\n"
"v_mfma_f32_16x16x32_bf16 a[20:23], a[196:199], v[52:55], a[20:23]\n"
"buffer_load_dwordx4 v137, s[24:27], s69 offen offset:1024 lds\n"
"v_mfma_f32_16x16x32_bf16 a[52:55], a[204:207], v[52:55], a[52:55]\n"
"v_mfma_f32_16x16x32_bf16 a[52:55], a[200:203], v[20:23], a[52:55]\n"
"s_waitcnt lgkmcnt(4)\n"
"v_mfma_f32_16x16x32_bf16 a[24:27], a[192:195], v[24:27], a[24:27]\n"
"buffer_load_dwordx4 v138, s[24:27], s69 offen offset:2048 lds\n"
"v_mfma_f32_16x16x32_bf16 a[24:27], a[196:199], v[56:59], a[24:27]\n"
"v_mfma_f32_16x16x32_bf16 a[56:59], a[204:207], v[56:59], a[56:59]\n"
"v_mfma_f32_16x16x32_bf16 a[56:59], a[200:203], v[24:27], a[56:59]\n"
"v_mfma_f32_16x16x32_bf16 a[28:31], a[192:195], v[28:31], a[28:31]\n"
"buffer_load_dwordx4 v139, s[24:27], s69 offen offset:3072 lds\n"
"v_mfma_f32_16x16x32_bf16 a[28:31], a[196:199], v[60:63], a[28:31]\n"
"v_mfma_f32_16x16x32_bf16 a[60:63], a[204:207], v[60:63], a[60:63]\n"
"v_mfma_f32_16x16x32_bf16 a[60:63], a[200:203], v[28:31], a[60:63]\n"
"s_add_u32 s68, s68, 0x80\n"
"s_waitcnt lgkmcnt(2)\n"
"v_mfma_f32_16x16x32_bf16 a[64:67], v[0:3], a[208:211], a[64:67]\n"
"s_add_u32 s69, s69, 0x80000\n"
"v_mfma_f32_16x16x32_bf16 a[64:67], v[32:35], a[212:215], a[64:67]\n"
"v_mfma_f32_16x16x32_bf16 a[68:71], v[36:39], a[212:215], a[68:71]\n"
"v_mfma_f32_16x16x32_bf16 a[68:71], v[4:7], a[208:211], a[68:71]\n"
"v_mfma_f32_16x16x32_bf16 a[72:75], v[8:11], a[208:211], a[72:75]\n"
"ds_read_b128 a[192:195], v140 offset:8192\n"
"v_mfma_f32_16x16x32_bf16 a[72:75], v[40:43], a[212:215], a[72:75]\n"
"global_load_dword v178, v248, s[4:5]\n"
"v_mfma_f32_16x16x32_bf16 a[76:79], v[44:47], a[212:215], a[76:79]\n"
"ds_read_b128 a[196:199], v141 offset:8192\n"
"v_mfma_f32_16x16x32_bf16 a[76:79], v[12:15], a[208:211], a[76:79]\n"
"v_mfma_f32_16x16x32_bf16 a[80:83], a[208:211], v[16:19], a[80:83]\n"
"v_mfma_f32_16x16x32_bf16 a[80:83], a[212:215], v[48:51], a[80:83]\n"
"v_mfma_f32_16x16x32_bf16 a[84:87], a[212:215], v[52:55], a[84:87]\n"
"v_mfma_f32_16x16x32_bf16 a[84:87], a[208:211], v[20:23], a[84:87]\n"
"v_mfma_f32_16x16x32_bf16 a[88:91], a[208:211], v[24:27], a[88:91]\n"
"v_mfma_f32_16x16x32_bf16 a[88:91], a[212:215], v[56:59], a[88:91]\n"
"v_mfma_f32_16x16x32_bf16 a[92:95], a[212:215], v[60:63], a[92:95]\n"
"v_mfma_f32_16x16x32_bf16 a[92:95], a[208:211], v[28:31], a[92:95]\n"
"s_waitcnt lgkmcnt(2)\n"
"v_mfma_f32_16x16x32_bf16 a[96:99], v[0:3], a[216:219], a[96:99]\n"
"ds_read_b128 a[200:203], v140 offset:10240\n"
"v_mfma_f32_16x16x32_bf16 a[96:99], v[32:35], a[220:223], a[96:99]\n"
"v_mfma_f32_16x16x32_bf16 a[100:103], v[36:39], a[220:223], a[100:103]\n"
"ds_read_b128 a[204:207], v141 offset:10240\n"
"v_mfma_f32_16x16x32_bf16 a[100:103], v[4:7], a[216:219], a[100:103]\n"
"v_mfma_f32_16x16x32_bf16 a[104:107], v[8:11], a[216:219], a[104:107]\n"
"v_mfma_f32_16x16x32_bf16 a[104:107], v[40:43], a[220:223], a[104:107]\n"
"s_mov_b32 s16, s44\n"
"s_and_b32 s17, s45, 0xffff\n"
"s_mov_b32 s18, 0x400000\n"
"s_mov_b32 s19, 0x20000\n"
"s_mov_b32 s20, s52\n"
"s_and_b32 s21, s53, 0xffff\n"
"s_mov_b32 s22, 0x158000\n"
"s_mov_b32 s23, 0x20000\n"
"s_add_u32 s24, s60, 0x100000\n"
"s_addc_u32 s25, s61, 0\n"
"s_and_b32 s25, s25, 0xffff\n"
"s_mov_b32 s26, 0x200000\n"
"s_mov_b32 s27, 0x20000\n"
"s_mov_b32 s64, s54\n"
"s_and_b32 s65, s55, 0xffff\n"
"s_mov_b32 s66, 0xac0\n"
"s_mov_b32 s67, 0x20000\n"
"s_mul_i32 s12, s10, 0x60\n"
"v_mfma_f32_16x16x32_bf16 a[108:111], v[44:47], a[220:223], a[108:111]\n"
"buffer_load_dwordx2 v[242:243], v241, s[64:67], s12 offen\n"
"v_mfma_f32_16x16x32_bf16 a[108:111], v[12:15], a[216:219], a[108:111]\n"
"buffer_load_dwordx2 v[244:245], v241, s[64:67], s12 offen offset:32\n"
"v_mfma_f32_16x16x32_bf16 a[112:115], a[216:219], v[16:19], a[112:115]\n"
"v_mfma_f32_16x16x32_bf16 a[112:115], a[220:223], v[48:51], a[112:115]\n"
"buffer_load_dwordx2 v[246:247], v241, s[64:67], s12 offen offset:64\n"
"v_mfma_f32_16x16x32_bf16 a[116:119], a[220:223], v[52:55], a[116:119]\n"
"v_mfma_f32_16x16x32_bf16 a[116:119], a[216:219], v[20:23], a[116:119]\n"
"v_mfma_f32_16x16x32_bf16 a[120:123], a[216:219], v[24:27], a[120:123]\n"
"ds_read_b128 a[208:211], v140 offset:12288\n"
"v_mfma_f32_16x16x32_bf16 a[120:123], a[220:223], v[56:59], a[120:123]\n"
"v_mfma_f32_16x16x32_bf16 a[124:127], a[220:223], v[60:63], a[124:127]\n"
"ds_read_b128 a[212:215], v141 offset:12288\n"
"v_mfma_f32_16x16x32_bf16 a[124:127], a[216:219], v[28:31], a[124:127]\n"
"s_waitcnt lgkmcnt(4)\n"
"v_mfma_f32_16x16x32_bf16 a[128:131], v[0:3], a[192:195], a[128:131]\n"
"v_mfma_f32_16x16x32_bf16 a[128:131], v[32:35], a[196:199], a[128:131]\n"
"v_mfma_f32_16x16x32_bf16 a[132:135], v[36:39], a[196:199], a[132:135]\n"
"v_mfma_f32_16x16x32_bf16 a[132:135], v[4:7], a[192:195], a[132:135]\n"
"v_mfma_f32_16x16x32_bf16 a[136:139], v[8:11], a[192:195], a[136:139]\n"
"v_mfma_f32_16x16x32_bf16 a[136:139], v[40:43], a[196:199], a[136:139]\n"
"v_mfma_f32_16x16x32_bf16 a[140:143], v[44:47], a[196:199], a[140:143]\n"
"ds_read_b128 a[216:219], v140 offset:14336\n"
"v_mfma_f32_16x16x32_bf16 a[140:143], v[12:15], a[192:195], a[140:143]\n"
"v_mfma_f32_16x16x32_bf16 a[144:147], a[192:195], v[16:19], a[144:147]\n"
"ds_read_b128 a[220:223], v141 offset:14336\n"
"v_mfma_f32_16x16x32_bf16 a[144:147], a[196:199], v[48:51], a[144:147]\n"
"v_mfma_f32_16x16x32_bf16 a[148:151], a[196:199], v[52:55], a[148:151]\n"
"v_mfma_f32_16x16x32_bf16 a[148:151], a[192:195], v[20:23], a[148:151]\n"
"v_mfma_f32_16x16x32_bf16 a[152:155], a[192:195], v[24:27], a[152:155]\n"
"v_mfma_f32_16x16x32_bf16 a[152:155], a[196:199], v[56:59], a[152:155]\n"
"v_mfma_f32_16x16x32_bf16 a[156:159], a[196:199], v[60:63], a[156:159]\n"
"v_mfma_f32_16x16x32_bf16 a[156:159], a[192:195], v[28:31], a[156:159]\n"
"s_waitcnt lgkmcnt(4)\n"
"v_mfma_f32_16x16x32_bf16 a[160:163], v[0:3], a[200:203], a[160:163]\n"
"v_mfma_f32_16x16x32_bf16 a[160:163], v[32:35], a[204:207], a[160:163]\n"
"v_mfma_f32_16x16x32_bf16 a[164:167], v[36:39], a[204:207], a[164:167]\n"
"v_mfma_f32_16x16x32_bf16 a[164:167], v[4:7], a[200:203], a[164:167]\n"
"v_mfma_f32_16x16x32_bf16 a[168:171], v[8:11], a[200:203], a[168:171]\n"
"v_mfma_f32_16x16x32_bf16 a[168:171], v[40:43], a[204:207], a[168:171]\n"
"v_mfma_f32_16x16x32_bf16 a[172:175], v[44:47], a[204:207], a[172:175]\n"
"v_mfma_f32_16x16x32_bf16 a[172:175], v[12:15], a[200:203], a[172:175]\n"
"v_mfma_f32_16x16x32_bf16 a[176:179], a[200:203], v[16:19], a[176:179]\n"
"v_mfma_f32_16x16x32_bf16 a[176:179], a[204:207], v[48:51], a[176:179]\n"
"v_mfma_f32_16x16x32_bf16 a[180:183], a[204:207], v[52:55], a[180:183]\n"
"v_mfma_f32_16x16x32_bf16 a[180:183], a[200:203], v[20:23], a[180:183]\n"
"v_mfma_f32_16x16x32_bf16 a[184:187], a[200:203], v[24:27], a[184:187]\n"
"s_waitcnt vmcnt(4) lgkmcnt(0)\n"
"s_barrier\n"
"v_mfma_f32_16x16x32_bf16 a[184:187], a[204:207], v[56:59], a[184:187]\n"
"ds_read_b128 a[192:195], v142\n"
"v_mfma_f32_16x16x32_bf16 a[188:191], a[204:207], v[60:63], a[188:191]\n"
"ds_read_b128 a[196:199], v143\n"
"s_add_u32 m0, s78, 0\n"
"v_mfma_f32_16x16x32_bf16 a[188:191], a[200:203], v[28:31], a[188:191]\n"
"buffer_load_dwordx4 v188, s[16:19], s74 offen lds\n"
"v_mfma_f32_16x16x32_bf16 v[64:67], v[0:3], a[208:211], v[64:67]\n"
"ds_read_b128 a[200:203], v142 offset:2048\n"
"v_mfma_f32_16x16x32_bf16 v[64:67], v[32:35], a[212:215], v[64:67]\n"
"v_mfma_f32_16x16x32_bf16 v[96:99], v[32:35], a[220:223], v[96:99]\n"
"ds_read_b128 a[204:207], v143 offset:2048\n"
"v_mfma_f32_16x16x32_bf16 v[96:99], v[0:3], a[216:219], v[96:99]\n"
"buffer_load_dwordx4 v189, s[16:19], s74 offen offset:1024 lds\n"
"v_mfma_f32_16x16x32_bf16 v[68:71], v[4:7], a[208:211], v[68:71]\n"
"ds_read_b64_tr_b16 v[0:1], v148\n"
"ds_read_b64_tr_b16 v[2:3], v148 offset:1024\n"
"v_mfma_f32_16x16x32_bf16 v[68:71], v[36:39], a[212:215], v[68:71]\n"
"v_mfma_f32_16x16x32_bf16 v[100:103], v[36:39], a[220:223], v[100:103]\n"
"ds_read_b64_tr_b16 v[32:33], v148 offset:8192\n"
"ds_read_b64_tr_b16 v[34:35], v148 offset:9216\n"
"v_mfma_f32_16x16x32_bf16 v[100:103], v[4:7], a[216:219], v[100:103]\n"
"buffer_load_dwordx4 v190, s[16:19], s74 offen offset:2048 lds\n"
"v_mfma_f32_16x16x32_bf16 v[72:75], v[8:11], a[208:211], v[72:75]\n"
"ds_read_b64_tr_b16 v[4:5], v149\n"
"ds_read_b64_tr_b16 v[6:7], v149 offset:1024\n"
"v_mfma_f32_16x16x32_bf16 v[72:75], v[40:43], a[212:215], v[72:75]\n"
"buffer_load_dwordx4 v191, s[16:19], s74 offen offset:3072 lds\n"
"v_mfma_f32_16x16x32_bf16 v[104:107], v[40:43], a[220:223], v[104:107]\n"
"ds_read_b64_tr_b16 v[36:37], v149 offset:8192\n"
"ds_read_b64_tr_b16 v[38:39], v149 offset:9216\n"
"v_mfma_f32_16x16x32_bf16 v[104:107], v[8:11], a[216:219], v[104:107]\n"
"v_mfma_f32_16x16x32_bf16 v[76:79], v[12:15], a[208:211], v[76:79]\n"
"ds_read_b64_tr_b16 v[8:9], v150\n"
"ds_read_b64_tr_b16 v[10:11], v150 offset:1024\n"
"v_mfma_f32_16x16x32_bf16 v[76:79], v[44:47], a[212:215], v[76:79]\n"
"s_add_u32 m0, s78, 0x1000\n"
"v_mfma_f32_16x16x32_bf16 v[108:111], v[44:47], a[220:223], v[108:111]\n"
"ds_read_b64_tr_b16 v[40:41], v150 offset:8192\n"
"ds_read_b64_tr_b16 v[42:43], v150 offset:9216\n"
"v_mfma_f32_16x16x32_bf16 v[108:111], v[12:15], a[216:219], v[108:111]\n"
"buffer_load_dwordx4 v188, s[16:19], s75 offen lds\n"
"v_mfma_f32_16x16x32_bf16 v[80:83], a[208:211], v[16:19], v[80:83]\n"
"ds_read_b64_tr_b16 v[12:13], v151\n"
"ds_read_b64_tr_b16 v[14:15], v151 offset:1024\n"
"v_mfma_f32_16x16x32_bf16 v[80:83], a[212:215], v[48:51], v[80:83]\n"
"buffer_load_dwordx4 v189, s[16:19], s75 offen offset:1024 lds\n"
"v_mfma_f32_16x16x32_bf16 v[112:115], a[220:223], v[48:51], v[112:115]\n"
"ds_read_b64_tr_b16 v[44:45], v151 offset:8192\n"
"ds_read_b64_tr_b16 v[46:47], v151 offset:9216\n"
"v_mfma_f32_16x16x32_bf16 v[112:115], a[216:219], v[16:19], v[112:115]\n"
"v_mfma_f32_16x16x32_bf16 v[84:87], a[208:211], v[20:23], v[84:87]\n"
"ds_read_b64_tr_b16 v[16:17], v148 offset:16384\n"
"ds_read_b64_tr_b16 v[18:19], v148 offset:17408\n"
"v_mfma_f32_16x16x32_bf16 v[84:87], a[212:215], v[52:55], v[84:87]\n"
"s_waitcnt vmcnt(17)\n"
"v_cmp_eq_u64 vcc, v[238:239], v[250:251]\n"
"s_nop 1\n"
"s_cmp_eq_u64 vcc, exec\n"
"s_cbranch_scc1 .Lpok_1_%=\n"
"s_mov_b32 s15, 0\n"
".Lploop_2_%=:\n"
"s_sleep 4\n"
"buffer_load_dwordx2 v[238:239], v207, s[92:95], s62 offen sc1\n"
"s_waitcnt vmcnt(0)\n"
"v_cmp_eq_u64 vcc, v[238:239], v[250:251]\n"
"s_add_u32 s15, s15, 1\n"
"s_nop 0\n"
"s_cmp_eq_u64 vcc, exec\n"
"s_cbranch_scc1 .Lpok_1_%=\n"
"s_cmp_lt_u32 s15, 0x20000\n"
"s_cbranch_scc1 .Lploop_2_%=\n"
"s_mov_b32 s15, 0x80\n"
"s_mov_b64 exec, 1\n"
"buffer_store_dwordx2 v[250:251], v249, s[92:95], s15 offen sc1\n"
"s_mov_b64 exec, -1\n"
".Lpok_1_%=:\n"
"buffer_load_dwordx4 v190, s[16:19], s75 offen offset:2048 lds\n"
"v_mfma_f32_16x16x32_bf16 v[116:119], a[220:223], v[52:55], v[116:119]\n"
"ds_read_b64_tr_b16 v[48:49], v148 offset:24576\n"
"ds_read_b64_tr_b16 v[50:51], v148 offset:25600\n"
"v_mfma_f32_16x16x32_bf16 v[116:119], a[216:219], v[20:23], v[116:119]\n"
"v_mfma_f32_16x16x32_bf16 v[88:91], a[208:211], v[24:27], v[88:91]\n"
"ds_read_b64_tr_b16 v[20:21], v149 offset:16384\n"
"ds_read_b64_tr_b16 v[22:23], v149 offset:17408\n"
"v_mfma_f32_16x16x32_bf16 v[88:91], a[212:215], v[56:59], v[88:91]\n"
"buffer_load_dwordx4 v191, s[16:19], s75 offen offset:3072 lds\n"
"v_mfma_f32_16x16x32_bf16 v[120:123], a[220:223], v[56:59], v[120:123]\n"
"ds_read_b64_tr_b16 v[52:53], v149 offset:24576\n"
"ds_read_b64_tr_b16 v[54:55], v149 offset:25600\n"
"v_mfma_f32_16x16x32_bf16 v[120:123], a[216:219], v[24:27], v[120:123]\n"
"s_add_u32 m0, s79, 0\n"
"v_mfma_f32_16x16x32_bf16 v[92:95], a[208:211], v[28:31], v[92:95]\n"
"ds_read_b64_tr_b16 v[24:25], v150 offset:16384\n"
"ds_read_b64_tr_b16 v[26:27], v150 offset:17408\n"
"v_mfma_f32_16x16x32_bf16 v[92:95], a[212:215], v[60:63], v[92:95]\n"
"buffer_load_dwordx4 v192, s[20:23], s76 offen lds\n"
"v_mfma_f32_16x16x32_bf16 v[124:127], a[220:223], v[60:63], v[124:127]\n"
"ds_read_b64_tr_b16 v[56:57], v150 offset:24576\n"
"ds_read_b64_tr_b16 v[58:59], v150 offset:25600\n"
"v_mfma_f32_16x16x32_bf16 v[124:127], a[216:219], v[28:31], v[124:127]\n"
"s_waitcnt lgkmcnt(15)\n"
"v_mfma_f32_16x16x32_bf16 a[0:3], v[0:3], a[192:195], a[0:3]\n"
"ds_read_b64_tr_b16 v[28:29], v151 offset:16384\n"
"ds_read_b64_tr_b16 v[30:31], v151 offset:17408\n"
"s_lshl_b32 s12, s8, 14\n"
"s_add_u32 s12, s12, 0x10000\n"
"v_mfma_f32_16x16x32_bf16 a[0:3], v[32:35], a[196:199], a[0:3]\n"
"buffer_load_dwordx4 v193, s[20:23], s76 offen offset:1024 lds\n"
"v_add_u32 v237, s12, v254\n"
"v_mfma_f32_16x16x32_bf16 a[32:35], v[32:35], a[204:207], a[32:35]\n"
"ds_read_b64_tr_b16 v[60:61], v151 offset:24576\n"
"ds_read_b64_tr_b16 v[62:63], v151 offset:25600\n"
"v_mfma_f32_16x16x32_bf16 a[32:35], v[0:3], a[200:203], a[32:35]\n"
"buffer_load_dwordx4 v194, s[20:23], s76 offen offset:2048 lds\n"
"v_mfma_f32_16x16x32_bf16 a[4:7], v[4:7], a[192:195], a[4:7]\n"
"v_mfma_f32_16x16x32_bf16 a[4:7], v[36:39], a[196:199], a[4:7]\n"
"v_mfma_f32_16x16x32_bf16 a[36:39], v[36:39], a[204:207], a[36:39]\n"
"ds_read_b128 a[208:211], v142 offset:4096\n"
"v_mfma_f32_16x16x32_bf16 a[36:39], v[4:7], a[200:203], a[36:39]\n"
"s_waitcnt lgkmcnt(15)\n"
"v_mfma_f32_16x16x32_bf16 a[8:11], v[8:11], a[192:195], a[8:11]\n"
"ds_read_b128 a[212:215], v143 offset:4096\n"
"s_waitcnt vmcnt(11)\n"
"v_mfma_f32_16x16x32_bf16 a[8:11], v[40:43], a[196:199], a[8:11]\n"
"buffer_load_dwordx4 v195, s[20:23], s76 offen offset:3072 lds\n"
"v_mfma_f32_16x16x32_bf16 a[40:43], v[40:43], a[204:207], a[40:43]\n"
"s_add_u32 s74, s74, 0x100000\n"
"s_add_u32 s75, s75, 0x100000\n"
"s_add_u32 s76, s76, 0x56000\n"
"s_add_u32 m0, s78, 0x8000\n"
"v_mfma_f32_16x16x32_bf16 a[40:43], v[8:11], a[200:203], a[40:43]\n"
"buffer_load_dwordx4 v188, s[16:19], s74 offen lds\n"
"v_mfma_f32_16x16x32_bf16 a[12:15], v[12:15], a[192:195], a[12:15]\n"
"v_lshlrev_b32 v176, 16, v242\n"
"v_and_b32 v177, 0xffff0000, v242\n"
"v_mfma_f32_16x16x32_bf16 a[12:15], v[44:47], a[196:199], a[12:15]\n"
"v_lshlrev_b32 v178, 16, v243\n"
"v_and_b32 v179, 0xffff0000, v243\n"
"v_mfma_f32_16x16x32_bf16 a[44:47], v[44:47], a[204:207], a[44:47]\n"
"buffer_load_dwordx4 v189, s[16:19], s74 offen offset:1024 lds\n"
"v_mfma_f32_16x16x32_bf16 a[44:47], v[12:15], a[200:203], a[44:47]\n"
"v_lshlrev_b32 v180, 16, v244\n"
"s_waitcnt lgkmcnt(10)\n"
"v_mfma_f32_16x16x32_bf16 a[16:19], a[192:195], v[16:19], a[16:19]\n"
"v_and_b32 v181, 0xffff0000, v244\n"
"v_lshlrev_b32 v182, 16, v245\n"
"v_mfma_f32_16x16x32_bf16 a[16:19], a[196:199], v[48:51], a[16:19]\n"
"v_and_b32 v183, 0xffff0000, v245\n"
"v_lshlrev_b32 v184, 16, v246\n"
"v_mfma_f32_16x16x32_bf16 a[48:51], a[204:207], v[48:51], a[48:51]\n"
"ds_read_b128 a[216:219], v142 offset:6144\n"
"v_and_b32 v185, 0xffff0000, v246\n"
"v_mfma_f32_16x16x32_bf16 a[48:51], a[200:203], v[16:19], a[48:51]\n"
"buffer_load_dwordx4 v190, s[16:19], s74 offen offset:2048 lds\n"
"v_mfma_f32_16x16x32_bf16 a[20:23], a[192:195], v[20:23], a[20:23]\n"
"ds_read_b128 a[220:223], v143 offset:6144\n"
"v_lshlrev_b32 v186, 16, v247\n"
"v_mfma_f32_16x16x32_bf16 a[20:23], a[196:199], v[52:55], a[20:23]\n"
"buffer_load_dwordx4 v191, s[16:19], s74 offen offset:3072 lds\n"
"v_mfma_f32_16x16x32_bf16 a[52:55], a[204:207], v[52:55], a[52:55]\n"
"v_and_b32 v187, 0xffff0000, v247\n"
"s_mov_b32 s15, 0x3e0293ee\n"
"v_mfma_f32_16x16x32_bf16 a[52:55], a[200:203], v[20:23], a[52:55]\n"
"v_mul_f32 v176, s15, v176\n"
"s_waitcnt lgkmcnt(4)\n"
"v_mfma_f32_16x16x32_bf16 a[24:27], a[192:195], v[24:27], a[24:27]\n"
"s_add_u32 m0, s78, 0x9000\n"
"v_mul_f32 v177, s15, v177\n"
"v_mfma_f32_16x16x32_bf16 a[24:27], a[196:199], v[56:59], a[24:27]\n"
"buffer_load_dwordx4 v188, s[16:19], s75 offen lds\n"
"v_mfma_f32_16x16x32_bf16 a[56:59], a[204:207], v[56:59], a[56:59]\n"
"v_mul_f32 v178, s15, v178\n"
"v_mul_f32 v179, s15, v179\n"
"v_mfma_f32_16x16x32_bf16 a[56:59], a[200:203], v[24:27], a[56:59]\n"
"v_mul_f32 v180, s15, v180\n"
"v_mul_f32 v181, s15, v181\n"
"v_mfma_f32_16x16x32_bf16 a[28:31], a[192:195], v[28:31], a[28:31]\n"
"buffer_load_dwordx4 v189, s[16:19], s75 offen offset:1024 lds\n"
"v_mfma_f32_16x16x32_bf16 a[28:31], a[196:199], v[60:63], a[28:31]\n"
"v_mul_f32 v182, s15, v182\n"
"v_mul_f32 v183, s15, v183\n"
"v_mfma_f32_16x16x32_bf16 a[60:63], a[204:207], v[60:63], a[60:63]\n"
"v_mul_f32 v184, s15, v184\n"
"v_mul_f32 v185, s15, v185\n"
"v_mfma_f32_16x16x32_bf16 a[60:63], a[200:203], v[28:31], a[60:63]\n"
"v_mul_f32 v186, s15, v186\n"
"s_waitcnt lgkmcnt(2)\n"
"v_mfma_f32_16x16x32_bf16 a[64:67], v[0:3], a[208:211], a[64:67]\n"
"buffer_load_dwordx4 v190, s[16:19], s75 offen offset:2048 lds\n"
"v_mfma_f32_16x16x32_bf16 a[64:67], v[32:35], a[212:215], a[64:67]\n"
"v_mul_f32 v187, s15, v187\n"
"v_mfma_f32_16x16x32_bf16 a[68:71], v[36:39], a[212:215], a[68:71]\n"
"v_mfma_f32_16x16x32_bf16 a[68:71], v[4:7], a[208:211], a[68:71]\n"
"buffer_load_dwordx4 v191, s[16:19], s75 offen offset:3072 lds\n"
"v_mfma_f32_16x16x32_bf16 a[72:75], v[8:11], a[208:211], a[72:75]\n"
"ds_read_b128 a[192:195], v142 offset:8192\n"
"v_accvgpr_read_b32 v152, a0\n"
"v_mfma_f32_16x16x32_bf16 a[72:75], v[40:43], a[212:215], a[72:75]\n"
"v_accvgpr_read_b32 v153, a1\n"
"v_accvgpr_read_b32 v154, a2\n"
"v_mfma_f32_16x16x32_bf16 a[76:79], v[44:47], a[212:215], a[76:79]\n"
"ds_read_b128 a[196:199], v143 offset:8192\n"
"s_add_u32 m0, s79, 0x4000\n"
"v_mfma_f32_16x16x32_bf16 a[76:79], v[12:15], a[208:211], a[76:79]\n"
"buffer_load_dwordx4 v192, s[20:23], s76 offen lds\n"
"v_mfma_f32_16x16x32_bf16 a[80:83], a[208:211], v[16:19], a[80:83]\n"
"v_accvgpr_read_b32 v155, a3\n"
"v_accvgpr_read_b32 v156, a4\n"
"v_mfma_f32_16x16x32_bf16 a[80:83], a[212:215], v[48:51], a[80:83]\n"
"v_accvgpr_read_b32 v157, a5\n"
"v_accvgpr_read_b32 v158, a6\n"
"v_mfma_f32_16x16x32_bf16 a[84:87], a[212:215], v[52:55], a[84:87]\n"
"buffer_load_dwordx4 v193, s[20:23], s76 offen offset:1024 lds\n"
"v_mfma_f32_16x16x32_bf16 a[84:87], a[208:211], v[20:23], a[84:87]\n"
"v_accvgpr_read_b32 v159, a7\n"
"v_cvt_pk_bf16_f32 v128, v152, v153\n"
"v_mfma_f32_16x16x32_bf16 a[88:91], a[208:211], v[24:27], a[88:91]\n"
"v_cvt_pk_bf16_f32 v129, v154, v155\n"
"v_cvt_pk_bf16_f32 v130, v156, v157\n"
"v_mfma_f32_16x16x32_bf16 a[88:91], a[212:215], v[56:59], a[88:91]\n"
"v_cvt_pk_bf16_f32 v131, v158, v159\n"
"s_add_u32 s14, s72, 0\n"
"v_mfma_f32_16x16x32_bf16 a[92:95], a[212:215], v[60:63], a[92:95]\n"
"buffer_load_dwordx4 v194, s[20:23], s76 offen offset:2048 lds\n"
"v_mfma_f32_16x16x32_bf16 a[92:95], a[208:211], v[28:31], a[92:95]\n"
"s_waitcnt lgkmcnt(2)\n"
"v_mfma_f32_16x16x32_bf16 a[96:99], v[0:3], a[216:219], a[96:99]\n"
"ds_read_b128 a[200:203], v142 offset:10240\n"
"v_mfma_f32_16x16x32_bf16 a[96:99], v[32:35], a[220:223], a[96:99]\n"
"buffer_load_dwordx4 v195, s[20:23], s76 offen offset:3072 lds\n"
"v_mfma_f32_16x16x32_bf16 a[100:103], v[36:39], a[220:223], a[100:103]\n"
"ds_read_b128 a[204:207], v143 offset:10240\n"
"v_mfma_f32_16x16x32_bf16 a[100:103], v[4:7], a[216:219], a[100:103]\n"
"buffer_store_dwordx4 v[128:131], v254, s[28:31], s14 offen sc1\n"
"v_mfma_f32_16x16x32_bf16 a[104:107], v[8:11], a[216:219], a[104:107]\n"
"s_add_u32 s74, s74, 0x100000\n"
"s_add_u32 s75, s75, 0x100000\n"
"v_mfma_f32_16x16x32_bf16 a[104:107], v[40:43], a[220:223], a[104:107]\n"
"s_add_u32 s76, s76, 0x56000\n"
"v_accvgpr_read_b32 v160, a8\n"
"v_mfma_f32_16x16x32_bf16 a[108:111], v[44:47], a[220:223], a[108:111]\n"
"v_accvgpr_read_b32 v161, a9\n"
"v_accvgpr_read_b32 v162, a10\n"
"v_mfma_f32_16x16x32_bf16 a[108:111], v[12:15], a[216:219], a[108:111]\n"
"v_accvgpr_read_b32 v163, a11\n"
"v_accvgpr_read_b32 v164, a12\n"
"v_mfma_f32_16x16x32_bf16 a[112:115], a[216:219], v[16:19], a[112:115]\n"
"v_accvgpr_read_b32 v165, a13\n"
"v_accvgpr_read_b32 v166, a14\n"
"v_mfma_f32_16x16x32_bf16 a[112:115], a[220:223], v[48:51], a[112:115]\n"
"v_accvgpr_read_b32 v167, a15\n"
"v_cvt_pk_bf16_f32 v132, v160, v161\n"
"v_mfma_f32_16x16x32_bf16 a[116:119], a[220:223], v[52:55], a[116:119]\n"
"v_cvt_pk_bf16_f32 v133, v162, v163\n"
"v_cvt_pk_bf16_f32 v134, v164, v165\n"
"v_mfma_f32_16x16x32_bf16 a[116:119], a[216:219], v[20:23], a[116:119]\n"
"v_cvt_pk_bf16_f32 v135, v166, v167\n"
"s_add_u32 s14, s72, 0\n"
"v_mfma_f32_16x16x32_bf16 a[120:123], a[216:219], v[24:27], a[120:123]\n"
"ds_read_b128 a[208:211], v142 offset:12288\n"
"v_mfma_f32_16x16x32_bf16 a[120:123], a[220:223], v[56:59], a[120:123]\n"
"buffer_store_dwordx4 v[132:135], v254, s[28:31], s14 offen offset:1024 sc1\n"
"v_mfma_f32_16x16x32_bf16 a[124:127], a[220:223], v[60:63], a[124:127]\n"
"ds_read_b128 a[212:215], v143 offset:12288\n"
"v_accvgpr_read_b32 v168, a32\n"
"v_mfma_f32_16x16x32_bf16 a[124:127], a[216:219], v[28:31], a[124:127]\n"
"v_accvgpr_read_b32 v169, a33\n"
"s_waitcnt lgkmcnt(4)\n"
"v_mfma_f32_16x16x32_bf16 a[128:131], v[0:3], a[192:195], a[128:131]\n"
"v_accvgpr_read_b32 v170, a34\n"
"v_accvgpr_read_b32 v171, a35\n"
"v_mfma_f32_16x16x32_bf16 a[128:131], v[32:35], a[196:199], a[128:131]\n"
"v_accvgpr_read_b32 v172, a36\n"
"v_accvgpr_read_b32 v173, a37\n"
"v_mfma_f32_16x16x32_bf16 a[132:135], v[36:39], a[196:199], a[132:135]\n"
"v_accvgpr_read_b32 v174, a38\n"
"v_accvgpr_read_b32 v175, a39\n"
"v_mfma_f32_16x16x32_bf16 a[132:135], v[4:7], a[192:195], a[132:135]\n"
"v_cvt_pk_bf16_f32 v136, v168, v169\n"
"v_cvt_pk_bf16_f32 v137, v170, v171\n"
"v_mfma_f32_16x16x32_bf16 a[136:139], v[8:11], a[192:195], a[136:139]\n"
"v_cvt_pk_bf16_f32 v138, v172, v173\n"
"v_cvt_pk_bf16_f32 v139, v174, v175\n"
"v_mfma_f32_16x16x32_bf16 a[136:139], v[40:43], a[196:199], a[136:139]\n"
"s_add_u32 s14, s72, 0x1000\n"
"v_mfma_f32_16x16x32_bf16 a[140:143], v[44:47], a[196:199], a[140:143]\n"
"ds_read_b128 a[216:219], v142 offset:14336\n"
"v_mfma_f32_16x16x32_bf16 a[140:143], v[12:15], a[192:195], a[140:143]\n"
"buffer_store_dwordx4 v[136:139], v254, s[28:31], s14 offen sc1\n"
"v_mfma_f32_16x16x32_bf16 a[144:147], a[192:195], v[16:19], a[144:147]\n"
"ds_read_b128 a[220:223], v143 offset:14336\n"
"v_accvgpr_read_b32 v152, a40\n"
"v_mfma_f32_16x16x32_bf16 a[144:147], a[196:199], v[48:51], a[144:147]\n"
"v_accvgpr_read_b32 v153, a41\n"
"v_accvgpr_read_b32 v154, a42\n"
"v_mfma_f32_16x16x32_bf16 a[148:151], a[196:199], v[52:55], a[148:151]\n"
"v_accvgpr_read_b32 v155, a43\n"
"v_accvgpr_read_b32 v156, a44\n"
"v_mfma_f32_16x16x32_bf16 a[148:151], a[192:195], v[20:23], a[148:151]\n"
"v_accvgpr_read_b32 v157, a45\n"
"v_accvgpr_read_b32 v158, a46\n"
"v_mfma_f32_16x16x32_bf16 a[152:155], a[192:195], v[24:27], a[152:155]\n"
"v_accvgpr_read_b32 v159, a47\n"
"v_cvt_pk_bf16_f32 v208, v152, v153\n"
"v_mfma_f32_16x16x32_bf16 a[152:155], a[196:199], v[56:59], a[152:155]\n"
"v_cvt_pk_bf16_f32 v209, v154, v155\n"
"v_cvt_pk_bf16_f32 v210, v156, v157\n"
"v_mfma_f32_16x16x32_bf16 a[156:159], a[196:199], v[60:63], a[156:159]\n"
"v_cvt_pk_bf16_f32 v211, v158, v159\n"
"v_accvgpr_read_b32 v160, a16\n"
"v_mfma_f32_16x16x32_bf16 a[156:159], a[192:195], v[28:31], a[156:159]\n"
"v_accvgpr_read_b32 v161, a17\n"
"s_waitcnt lgkmcnt(4)\n"
"v_mfma_f32_16x16x32_bf16 a[160:163], v[0:3], a[200:203], a[160:163]\n"
"v_accvgpr_read_b32 v162, a18\n"
"v_accvgpr_read_b32 v163, a19\n"
"v_mfma_f32_16x16x32_bf16 a[160:163], v[32:35], a[204:207], a[160:163]\n"
"v_accvgpr_read_b32 v164, a48\n"
"v_accvgpr_read_b32 v165, a49\n"
"v_mfma_f32_16x16x32_bf16 a[164:167], v[36:39], a[204:207], a[164:167]\n"
"v_accvgpr_read_b32 v166, a50\n"
"v_accvgpr_read_b32 v167, a51\n"
"v_mfma_f32_16x16x32_bf16 a[164:167], v[4:7], a[200:203], a[164:167]\n"
"v_cvt_pk_bf16_f32 v212, v160, v161\n"
"v_cvt_pk_bf16_f32 v213, v162, v163\n"
"v_mfma_f32_16x16x32_bf16 a[168:171], v[8:11], a[200:203], a[168:171]\n"
"v_cvt_pk_bf16_f32 v214, v164, v165\n"
"v_cvt_pk_bf16_f32 v215, v166, v167\n"
"v_mfma_f32_16x16x32_bf16 a[168:171], v[40:43], a[204:207], a[168:171]\n"
"v_accvgpr_read_b32 v168, a20\n"
"v_accvgpr_read_b32 v169, a21\n"
"v_mfma_f32_16x16x32_bf16 a[172:175], v[44:47], a[204:207], a[172:175]\n"
"v_accvgpr_read_b32 v170, a22\n"
"v_accvgpr_read_b32 v171, a23\n"
"v_mfma_f32_16x16x32_bf16 a[172:175], v[12:15], a[200:203], a[172:175]\n"
"v_accvgpr_read_b32 v172, a52\n"
"v_accvgpr_read_b32 v173, a53\n"
"v_mfma_f32_16x16x32_bf16 a[176:179], a[200:203], v[16:19], a[176:179]\n"
"v_accvgpr_read_b32 v174, a54\n"
"v_accvgpr_read_b32 v175, a55\n"
"v_mfma_f32_16x16x32_bf16 a[176:179], a[204:207], v[48:51], a[176:179]\n"
"v_mfma_f32_16x16x32_bf16 a[180:183], a[204:207], v[52:55], a[180:183]\n"
"v_mfma_f32_16x16x32_bf16 a[180:183], a[200:203], v[20:23], a[180:183]\n"
"v_mfma_f32_16x16x32_bf16 a[184:187], a[200:203], v[24:27], a[184:187]\n"
"s_waitcnt vmcnt(15) lgkmcnt(0)\n"
"s_barrier\n"
"v_mfma_f32_16x16x32_bf16 a[184:187], a[204:207], v[56:59], a[184:187]\n"
"v_cvt_pk_bf16_f32 v216, v168, v169\n"
"v_cvt_pk_bf16_f32 v217, v170, v171\n"
"v_mfma_f32_16x16x32_bf16 a[188:191], a[204:207], v[60:63], a[188:191]\n"
"v_cvt_pk_bf16_f32 v218, v172, v173\n"
"v_cvt_pk_bf16_f32 v219, v174, v175\n"
"v_mfma_f32_16x16x32_bf16 a[188:191], a[200:203], v[28:31], a[188:191]\n"
"v_accvgpr_read_b32 v152, a24\n"
"v_accvgpr_read_b32 v153, a25\n"
"v_mfma_f32_16x16x32_bf16 v[64:67], v[0:3], a[208:211], v[64:67]\n"
"v_accvgpr_read_b32 v154, a26\n"
"v_accvgpr_read_b32 v155, a27\n"
"v_mfma_f32_16x16x32_bf16 v[64:67], v[32:35], a[212:215], v[64:67]\n"
"v_accvgpr_read_b32 v156, a56\n"
"v_accvgpr_read_b32 v157, a57\n"
"v_mfma_f32_16x16x32_bf16 v[96:99], v[32:35], a[220:223], v[96:99]\n"
"v_accvgpr_read_b32 v158, a58\n"
"v_accvgpr_read_b32 v159, a59\n"
"v_mfma_f32_16x16x32_bf16 v[96:99], v[0:3], a[216:219], v[96:99]\n"
"v_cvt_pk_bf16_f32 v220, v152, v153\n"
"v_cvt_pk_bf16_f32 v221, v154, v155\n"
"v_mfma_f32_16x16x32_bf16 v[68:71], v[4:7], a[208:211], v[68:71]\n"
"v_cvt_pk_bf16_f32 v222, v156, v157\n"
"v_cvt_pk_bf16_f32 v223, v158, v159\n"
"v_mfma_f32_16x16x32_bf16 v[68:71], v[36:39], a[212:215], v[68:71]\n"
"v_accvgpr_read_b32 v160, a28\n"
"v_accvgpr_read_b32 v161, a29\n"
"v_mfma_f32_16x16x32_bf16 v[100:103], v[36:39], a[220:223], v[100:103]\n"
"v_accvgpr_read_b32 v162, a30\n"
"v_accvgpr_read_b32 v163, a31\n"
"v_mfma_f32_16x16x32_bf16 v[100:103], v[4:7], a[216:219], v[100:103]\n"
"v_accvgpr_read_b32 v164, a60\n"
"v_accvgpr_read_b32 v165, a61\n"
"v_mfma_f32_16x16x32_bf16 v[72:75], v[8:11], a[208:211], v[72:75]\n"
"v_accvgpr_read_b32 v166, a62\n"
"v_accvgpr_read_b32 v167, a63\n"
"v_mfma_f32_16x16x32_bf16 v[72:75], v[40:43], a[212:215], v[72:75]\n"
"v_cvt_pk_bf16_f32 v224, v160, v161\n"
"v_cvt_pk_bf16_f32 v225, v162, v163\n"
"v_mfma_f32_16x16x32_bf16 v[104:107], v[40:43], a[220:223], v[104:107]\n"
"v_cvt_pk_bf16_f32 v226, v164, v165\n"
"v_cvt_pk_bf16_f32 v227, v166, v167\n"
"v_mfma_f32_16x16x32_bf16 v[104:107], v[8:11], a[216:219], v[104:107]\n"
"buffer_load_dwordx4 a[0:3], v254, s[24:27], s80 offen sc1\n"
"v_mfma_f32_16x16x32_bf16 v[76:79], v[12:15], a[208:211], v[76:79]\n"
"v_accvgpr_read_b32 v168, a64\n"
"v_accvgpr_read_b32 v169, a65\n"
"v_mfma_f32_16x16x32_bf16 v[76:79], v[44:47], a[212:215], v[76:79]\n"
"v_accvgpr_read_b32 v170, a66\n"
"v_accvgpr_read_b32 v171, a67\n"
"v_mfma_f32_16x16x32_bf16 v[108:111], v[44:47], a[220:223], v[108:111]\n"
"buffer_load_dwordx4 a[4:7], v254, s[24:27], s80 offen offset:1024 sc1\n"
"v_mfma_f32_16x16x32_bf16 v[108:111], v[12:15], a[216:219], v[108:111]\n"
"v_accvgpr_read_b32 v172, a68\n"
"v_accvgpr_read_b32 v173, a69\n"
"v_mfma_f32_16x16x32_bf16 v[80:83], a[208:211], v[16:19], v[80:83]\n"
"v_accvgpr_read_b32 v174, a70\n"
"v_accvgpr_read_b32 v175, a71\n"
"v_mfma_f32_16x16x32_bf16 v[80:83], a[212:215], v[48:51], v[80:83]\n"
"buffer_load_dwordx4 a[8:11], v254, s[24:27], s80 offen offset:2048 sc1\n"
"v_mfma_f32_16x16x32_bf16 v[112:115], a[220:223], v[48:51], v[112:115]\n"
"v_cvt_pk_bf16_f32 v228, v168, v169\n"
"v_cvt_pk_bf16_f32 v229, v170, v171\n"
"v_mfma_f32_16x16x32_bf16 v[112:115], a[216:219], v[16:19], v[112:115]\n"
"v_cvt_pk_bf16_f32 v230, v172, v173\n"
"v_cvt_pk_bf16_f32 v231, v174, v175\n"
"v_mfma_f32_16x16x32_bf16 v[84:87], a[208:211], v[20:23], v[84:87]\n"
"buffer_load_dwordx4 a[12:15], v254, s[24:27], s80 offen offset:3072 sc1\n"
"v_mfma_f32_16x16x32_bf16 v[84:87], a[212:215], v[52:55], v[84:87]\n"
"v_accvgpr_read_b32 v152, a72\n"
"v_accvgpr_read_b32 v153, a73\n"
"v_mfma_f32_16x16x32_bf16 v[116:119], a[220:223], v[52:55], v[116:119]\n"
"s_add_u32 s80, s80, 0x20000\n"
"v_accvgpr_read_b32 v154, a74\n"
"v_mfma_f32_16x16x32_bf16 v[116:119], a[216:219], v[20:23], v[116:119]\n"
"buffer_load_dwordx4 a[16:19], v254, s[24:27], s80 offen sc1\n"
"v_mfma_f32_16x16x32_bf16 v[88:91], a[208:211], v[24:27], v[88:91]\n"
"v_accvgpr_read_b32 v155, a75\n"
"v_accvgpr_read_b32 v156, a76\n"
"v_mfma_f32_16x16x32_bf16 v[88:91], a[212:215], v[56:59], v[88:91]\n"
"v_accvgpr_read_b32 v157, a77\n"
"v_accvgpr_read_b32 v158, a78\n"
"v_mfma_f32_16x16x32_bf16 v[120:123], a[220:223], v[56:59], v[120:123]\n"
"buffer_load_dwordx4 a[20:23], v254, s[24:27], s80 offen offset:1024 sc1\n"
"v_mfma_f32_16x16x32_bf16 v[120:123], a[216:219], v[24:27], v[120:123]\n"
"v_accvgpr_read_b32 v159, a79\n"
"v_cvt_pk_bf16_f32 v232, v152, v153\n"
"v_mfma_f32_16x16x32_bf16 v[92:95], a[208:211], v[28:31], v[92:95]\n"
"v_cvt_pk_bf16_f32 v233, v154, v155\n"
"v_cvt_pk_bf16_f32 v234, v156, v157\n"
"v_mfma_f32_16x16x32_bf16 v[92:95], a[212:215], v[60:63], v[92:95]\n"
"v_cvt_pk_bf16_f32 v235, v158, v159\n"
"v_accvgpr_read_b32 v160, a96\n"
"v_mfma_f32_16x16x32_bf16 v[124:127], a[220:223], v[60:63], v[124:127]\n"
"v_accvgpr_read_b32 v161, a97\n"
"v_accvgpr_read_b32 v162, a98\n"
"v_mfma_f32_16x16x32_bf16 v[124:127], a[216:219], v[28:31], v[124:127]\n"
"v_accvgpr_read_b32 v163, a99\n"
"v_accvgpr_read_b32 v164, a100\n"
"buffer_load_dwordx4 a[24:27], v254, s[24:27], s80 offen offset:2048 sc1\n"
"v_accvgpr_read_b32 v165, a101\n"
"v_accvgpr_read_b32 v166, a102\n"
"v_accvgpr_read_b32 v167, a103\n"
"v_cvt_pk_bf16_f32 v132, v160, v161\n"
"v_cvt_pk_bf16_f32 v133, v162, v163\n"
"v_cvt_pk_bf16_f32 v134, v164, v165\n"
"v_cvt_pk_bf16_f32 v135, v166, v167\n"
"v_accvgpr_write_b32 a252, v132\n"
"v_accvgpr_write_b32 a253, v133\n"
"v_accvgpr_write_b32 a254, v134\n"
"v_accvgpr_write_b32 a255, v135\n"
"v_accvgpr_read_b32 v168, a104\n"
"buffer_load_dwordx4 a[28:31], v254, s[24:27], s80 offen offset:3072 sc1\n"
"s_add_u32 s80, s80, 0x20000\n"
"v_accvgpr_read_b32 v169, a105\n"
"v_accvgpr_read_b32 v170, a106\n"
"v_accvgpr_read_b32 v171, a107\n"
"v_accvgpr_read_b32 v172, a108\n"
"v_accvgpr_read_b32 v173, a109\n"
"v_accvgpr_read_b32 v174, a110\n"
"v_accvgpr_read_b32 v175, a111\n"
"v_cvt_pk_bf16_f32 v136, v168, v169\n"
"v_cvt_pk_bf16_f32 v137, v170, v171\n"
"v_cvt_pk_bf16_f32 v138, v172, v173\n"
"v_cvt_pk_bf16_f32 v139, v174, v175\n"
"v_accvgpr_write_b32 a248, v136\n"
"buffer_load_dwordx4 a[32:35], v254, s[24:27], s80 offen sc1\n"
"v_accvgpr_write_b32 a249, v137\n"
"v_accvgpr_write_b32 a250, v138\n"
"v_accvgpr_write_b32 a251, v139\n"
"v_accvgpr_read_b32 v152, a80\n"
"v_accvgpr_read_b32 v153, a81\n"
"v_accvgpr_read_b32 v154, a82\n"
"v_accvgpr_read_b32 v155, a83\n"
"v_accvgpr_read_b32 v156, a112\n"
"v_accvgpr_read_b32 v157, a113\n"
"v_accvgpr_read_b32 v158, a114\n"
"v_accvgpr_read_b32 v159, a115\n"
"v_cvt_pk_bf16_f32 v128, v152, v153\n"
"buffer_load_dwordx4 a[36:39], v254, s[24:27], s80 offen offset:1024 sc1\n"
"v_cvt_pk_bf16_f32 v129, v154, v155\n"
"v_cvt_pk_bf16_f32 v130, v156, v157\n"
"v_cvt_pk_bf16_f32 v131, v158, v159\n"
"v_accvgpr_write_b32 a244, v128\n"
"v_accvgpr_write_b32 a245, v129\n"
"v_accvgpr_write_b32 a246, v130\n"
"v_accvgpr_write_b32 a247, v131\n"
"v_accvgpr_read_b32 v160, a84\n"
"v_accvgpr_read_b32 v161, a85\n"
"v_accvgpr_read_b32 v162, a86\n"
"v_accvgpr_read_b32 v163, a87\n"
"v_accvgpr_read_b32 v164, a116\n"
"buffer_load_dwordx4 a[40:43], v254, s[24:27], s80 offen offset:2048 sc1\n"
"v_accvgpr_read_b32 v165, a117\n"
"v_accvgpr_read_b32 v166, a118\n"
"v_accvgpr_read_b32 v167, a119\n"
"v_cvt_pk_bf16_f32 v132, v160, v161\n"
"v_cvt_pk_bf16_f32 v133, v162, v163\n"
"v_cvt_pk_bf16_f32 v134, v164, v165\n"
"v_cvt_pk_bf16_f32 v135, v166, v167\n"
"v_accvgpr_write_b32 a240, v132\n"
"v_accvgpr_write_b32 a241, v133\n"
"v_accvgpr_write_b32 a242, v134\n"
"v_accvgpr_write_b32 a243, v135\n"
"v_accvgpr_read_b32 v168, a88\n"
"buffer_load_dwordx4 a[44:47], v254, s[24:27], s80 offen offset:3072 sc1\n"
"s_add_u32 s80, s80, 0x20000\n"
"v_accvgpr_read_b32 v169, a89\n"
"v_accvgpr_read_b32 v170, a90\n"
"v_accvgpr_read_b32 v171, a91\n"
"v_accvgpr_read_b32 v172, a120\n"
"v_accvgpr_read_b32 v173, a121\n"
"v_accvgpr_read_b32 v174, a122\n"
"v_accvgpr_read_b32 v175, a123\n"
"v_cvt_pk_bf16_f32 v136, v168, v169\n"
"v_cvt_pk_bf16_f32 v137, v170, v171\n"
"v_cvt_pk_bf16_f32 v138, v172, v173\n"
"v_cvt_pk_bf16_f32 v139, v174, v175\n"
"v_accvgpr_write_b32 a236, v136\n"
"buffer_load_dwordx4 a[48:51], v254, s[24:27], s80 offen sc1\n"
"v_accvgpr_write_b32 a237, v137\n"
"v_accvgpr_write_b32 a238, v138\n"
"v_accvgpr_write_b32 a239, v139\n"
"v_accvgpr_read_b32 v152, a92\n"
"v_accvgpr_read_b32 v153, a93\n"
"v_accvgpr_read_b32 v154, a94\n"
"v_accvgpr_read_b32 v155, a95\n"
"v_accvgpr_read_b32 v156, a124\n"
"v_accvgpr_read_b32 v157, a125\n"
"v_accvgpr_read_b32 v158, a126\n"
"v_accvgpr_read_b32 v159, a127\n"
"v_cvt_pk_bf16_f32 v128, v152, v153\n"
"buffer_load_dwordx4 a[52:55], v254, s[24:27], s80 offen offset:1024 sc1\n"
"v_cvt_pk_bf16_f32 v129, v154, v155\n"
"v_cvt_pk_bf16_f32 v130, v156, v157\n"
"v_cvt_pk_bf16_f32 v131, v158, v159\n"
"v_accvgpr_write_b32 a232, v128\n"
"v_accvgpr_write_b32 a233, v129\n"
"v_accvgpr_write_b32 a234, v130\n"
"v_accvgpr_write_b32 a235, v131\n"
"buffer_load_dwordx4 a[56:59], v254, s[24:27], s80 offen offset:2048 sc1\n"
"v_accvgpr_read_b32 v160, a128\n"
"v_accvgpr_read_b32 v161, a129\n"
"v_accvgpr_read_b32 v162, a130\n"
"v_accvgpr_read_b32 v163, a131\n"
"v_accvgpr_read_b32 v164, a132\n"
"v_accvgpr_read_b32 v165, a133\n"
"v_accvgpr_read_b32 v166, a134\n"
"v_accvgpr_read_b32 v167, a135\n"
"v_cvt_pk_bf16_f32 v144, v160, v161\n"
"v_cvt_pk_bf16_f32 v145, v162, v163\n"
"v_cvt_pk_bf16_f32 v146, v164, v165\n"
"v_cvt_pk_bf16_f32 v147, v166, v167\n"
"buffer_load_dwordx4 a[60:63], v254, s[24:27], s80 offen offset:3072 sc1\n"
"s_add_u32 s80, s80, 0x20000\n"
"ds_write_b128 v237, v[144:147]\n"
"v_accvgpr_read_b32 v168, a136\n"
"v_accvgpr_read_b32 v169, a137\n"
"v_accvgpr_read_b32 v170, a138\n"
"v_accvgpr_read_b32 v171, a139\n"
"v_accvgpr_read_b32 v172, a140\n"
"v_accvgpr_read_b32 v173, a141\n"
"v_accvgpr_read_b32 v174, a142\n"
"v_accvgpr_read_b32 v175, a143\n"
"v_cvt_pk_bf16_f32 v148, v168, v169\n"
"v_cvt_pk_bf16_f32 v149, v170, v171\n"
"v_cvt_pk_bf16_f32 v150, v172, v173\n"
"buffer_load_dwordx4 a[64:67], v254, s[24:27], s80 offen sc1\n"
"v_cvt_pk_bf16_f32 v151, v174, v175\n"
"ds_write_b128 v237, v[148:151] offset:1024\n"
"v_accvgpr_read_b32 v152, a160\n"
"v_accvgpr_read_b32 v153, a161\n"
"v_accvgpr_read_b32 v154, a162\n"
"v_accvgpr_read_b32 v155, a163\n"
"v_accvgpr_read_b32 v156, a164\n"
"v_accvgpr_read_b32 v157, a165\n"
"v_accvgpr_read_b32 v158, a166\n"
"v_accvgpr_read_b32 v159, a167\n"
"v_cvt_pk_bf16_f32 v128, v152, v153\n"
"v_cvt_pk_bf16_f32 v129, v154, v155\n"
"buffer_load_dwordx4 a[68:71], v254, s[24:27], s80 offen offset:1024 sc1\n"
"v_cvt_pk_bf16_f32 v130, v156, v157\n"
"v_cvt_pk_bf16_f32 v131, v158, v159\n"
"ds_write_b128 v237, v[128:131] offset:2048\n"
"v_accvgpr_read_b32 v160, a168\n"
"v_accvgpr_read_b32 v161, a169\n"
"v_accvgpr_read_b32 v162, a170\n"
"v_accvgpr_read_b32 v163, a171\n"
"v_accvgpr_read_b32 v164, a172\n"
"v_accvgpr_read_b32 v165, a173\n"
"v_accvgpr_read_b32 v166, a174\n"
"v_accvgpr_read_b32 v167, a175\n"
"v_cvt_pk_bf16_f32 v132, v160, v161\n"
"buffer_load_dwordx4 a[72:75], v254, s[24:27], s80 offen offset:2048 sc1\n"
"v_cvt_pk_bf16_f32 v133, v162, v163\n"
"v_cvt_pk_bf16_f32 v134, v164, v165\n"
"v_cvt_pk_bf16_f32 v135, v166, v167\n"
"ds_write_b128 v237, v[132:135] offset:3072\n"
"v_accvgpr_read_b32 v168, a144\n"
"v_accvgpr_read_b32 v169, a145\n"
"v_accvgpr_read_b32 v170, a146\n"
"v_accvgpr_read_b32 v171, a147\n"
"v_accvgpr_read_b32 v172, a176\n"
"v_accvgpr_read_b32 v173, a177\n"
"v_accvgpr_read_b32 v174, a178\n"
"v_accvgpr_read_b32 v175, a179\n"
"buffer_load_dwordx4 a[76:79], v254, s[24:27], s80 offen offset:3072 sc1\n"
"s_add_u32 s80, s80, 0x20000\n"
"v_cvt_pk_bf16_f32 v136, v168, v169\n"
"v_cvt_pk_bf16_f32 v137, v170, v171\n"
"v_cvt_pk_bf16_f32 v138, v172, v173\n"
"v_cvt_pk_bf16_f32 v139, v174, v175\n"
"ds_write_b128 v237, v[136:139] offset:4096\n"
"v_accvgpr_read_b32 v152, a148\n"
"v_accvgpr_read_b32 v153, a149\n"
"v_accvgpr_read_b32 v154, a150\n"
"v_accvgpr_read_b32 v155, a151\n"
"v_accvgpr_read_b32 v156, a180\n"
"v_accvgpr_read_b32 v157, a181\n"
"v_accvgpr_read_b32 v158, a182\n"
"buffer_load_dwordx4 a[80:83], v254, s[24:27], s80 offen sc1\n"
"v_accvgpr_read_b32 v159, a183\n"
"v_cvt_pk_bf16_f32 v140, v152, v153\n"
"v_cvt_pk_bf16_f32 v141, v154, v155\n"
"v_cvt_pk_bf16_f32 v142, v156, v157\n"
"v_cvt_pk_bf16_f32 v143, v158, v159\n"
"ds_write_b128 v237, v[140:143] offset:5120\n"
"v_accvgpr_read_b32 v160, a152\n"
"v_accvgpr_read_b32 v161, a153\n"
"v_accvgpr_read_b32 v162, a154\n"
"v_accvgpr_read_b32 v163, a155\n"
"v_accvgpr_read_b32 v164, a184\n"
"v_accvgpr_read_b32 v165, a185\n"
"buffer_load_dwordx4 a[84:87], v254, s[24:27], s80 offen offset:1024 sc1\n"
"v_accvgpr_read_b32 v166, a186\n"
"v_accvgpr_read_b32 v167, a187\n"
"v_cvt_pk_bf16_f32 v144, v160, v161\n"
"v_cvt_pk_bf16_f32 v145, v162, v163\n"
"v_cvt_pk_bf16_f32 v146, v164, v165\n"
"v_cvt_pk_bf16_f32 v147, v166, v167\n"
"ds_write_b128 v237, v[144:147] offset:6144\n"
"v_accvgpr_read_b32 v168, a156\n"
"v_accvgpr_read_b32 v169, a157\n"
"v_accvgpr_read_b32 v170, a158\n"
"v_accvgpr_read_b32 v171, a159\n"
"v_accvgpr_read_b32 v172, a188\n"
"buffer_load_dwordx4 a[88:91], v254, s[24:27], s80 offen offset:2048 sc1\n"
"v_accvgpr_read_b32 v173, a189\n"
"v_accvgpr_read_b32 v174, a190\n"
"v_accvgpr_read_b32 v175, a191\n"
"v_cvt_pk_bf16_f32 v148, v168, v169\n"
"v_cvt_pk_bf16_f32 v149, v170, v171\n"
"v_cvt_pk_bf16_f32 v150, v172, v173\n"
"v_cvt_pk_bf16_f32 v151, v174, v175\n"
"ds_write_b128 v237, v[148:151] offset:7168\n"
"buffer_load_dwordx4 a[92:95], v254, s[24:27], s80 offen offset:3072 sc1\n"
"s_add_u32 s80, s80, 0x20000\n"
"ds_read_b64_tr_b16 a[128:129], v196\n"
"v_cvt_pk_bf16_f32 v64, v64, v65\n"
"v_cvt_pk_bf16_f32 v65, v66, v67\n"
"v_cvt_pk_bf16_f32 v66, v68, v69\n"
"ds_read_b64_tr_b16 a[130:131], v196 offset:1024\n"
"v_cvt_pk_bf16_f32 v67, v70, v71\n"
"ds_write_b128 v237, v[64:67] offset:8192\n"
"v_cvt_pk_bf16_f32 v72, v72, v73\n"
"ds_read_b64_tr_b16 a[172:173], v196 offset:8192\n"
"v_cvt_pk_bf16_f32 v73, v74, v75\n"
"v_cvt_pk_bf16_f32 v74, v76, v77\n"
"v_cvt_pk_bf16_f32 v75, v78, v79\n"
"ds_read_b64_tr_b16 a[174:175], v196 offset:9216\n"
"ds_write_b128 v237, v[72:75] offset:9216\n"
"v_cvt_pk_bf16_f32 v96, v96, v97\n"
"v_cvt_pk_bf16_f32 v97, v98, v99\n"
"buffer_load_dwordx4 a[96:99], v254, s[24:27], s80 offen sc1\n"
"ds_read_b64_tr_b16 a[132:133], v197\n"
"v_cvt_pk_bf16_f32 v98, v100, v101\n"
"v_cvt_pk_bf16_f32 v99, v102, v103\n"
"ds_write_b128 v237, v[96:99] offset:10240\n"
"ds_read_b64_tr_b16 a[134:135], v197 offset:1024\n"
"v_cvt_pk_bf16_f32 v104, v104, v105\n"
"v_cvt_pk_bf16_f32 v105, v106, v107\n"
"v_cvt_pk_bf16_f32 v106, v108, v109\n"
"ds_read_b64_tr_b16 a[176:177], v197 offset:8192\n"
"v_cvt_pk_bf16_f32 v107, v110, v111\n"
"ds_write_b128 v237, v[104:107] offset:11264\n"
"v_cvt_pk_bf16_f32 v80, v80, v81\n"
"ds_read_b64_tr_b16 a[178:179], v197 offset:9216\n"
"v_cvt_pk_bf16_f32 v81, v82, v83\n"
"v_cvt_pk_bf16_f32 v82, v112, v113\n"
"v_cvt_pk_bf16_f32 v83, v114, v115\n"
"buffer_load_dwordx4 a[100:103], v254, s[24:27], s80 offen offset:1024 sc1\n"
"ds_read_b64_tr_b16 a[136:137], v198\n"
"v_accvgpr_write_b32 a228, v80\n"
"v_accvgpr_write_b32 a229, v81\n"
"v_accvgpr_write_b32 a230, v82\n"
"ds_read_b64_tr_b16 a[138:139], v198 offset:1024\n"
"v_accvgpr_write_b32 a231, v83\n"
"v_cvt_pk_bf16_f32 v84, v84, v85\n"
"v_cvt_pk_bf16_f32 v85, v86, v87\n"
"ds_read_b64_tr_b16 a[180:181], v198 offset:8192\n"
"v_cvt_pk_bf16_f32 v86, v116, v117\n"
"v_cvt_pk_bf16_f32 v87, v118, v119\n"
"v_accvgpr_write_b32 a224, v84\n"
"ds_read_b64_tr_b16 a[182:183], v198 offset:9216\n"
"v_accvgpr_write_b32 a225, v85\n"
"v_accvgpr_write_b32 a226, v86\n"
"v_accvgpr_write_b32 a227, v87\n"
"buffer_load_dwordx4 a[104:107], v254, s[24:27], s80 offen offset:2048 sc1\n"
"ds_read_b64_tr_b16 a[140:141], v199\n"
"v_cvt_pk_bf16_f32 v88, v88, v89\n"
"v_cvt_pk_bf16_f32 v89, v90, v91\n"
"v_cvt_pk_bf16_f32 v90, v120, v121\n"
"ds_read_b64_tr_b16 a[142:143], v199 offset:1024\n"
"v_cvt_pk_bf16_f32 v91, v122, v123\n"
"v_accvgpr_write_b32 a220, v88\n"
"v_accvgpr_write_b32 a221, v89\n"
"ds_read_b64_tr_b16 a[184:185], v199 offset:8192\n"
"v_accvgpr_write_b32 a222, v90\n"
"v_accvgpr_write_b32 a223, v91\n"
"v_cvt_pk_bf16_f32 v92, v92, v93\n"
"ds_read_b64_tr_b16 a[186:187], v199 offset:9216\n"
"v_cvt_pk_bf16_f32 v93, v94, v95\n"
"v_cvt_pk_bf16_f32 v94, v124, v125\n"
"v_cvt_pk_bf16_f32 v95, v126, v127\n"
"buffer_load_dwordx4 a[108:111], v254, s[24:27], s80 offen offset:3072 sc1\n"
"s_add_u32 s80, s80, 0x20000\n"
"v_accvgpr_write_b32 a216, v92\n"
"v_accvgpr_write_b32 a217, v93\n"
"v_accvgpr_write_b32 a218, v94\n"
"v_accvgpr_write_b32 a219, v95\n"
"v_lshlrev_b32 v247, 3, v255\n"
"v_mov_b32 v240, 0xc17af233\n"
"v_mov_b32 v242, 0x40faf233\n"
"s_waitcnt vmcnt(20) lgkmcnt(0)\n"
"v_mfma_f32_16x16x32_bf16 v[0:3], a[128:131], a[0:3], 0\n"
"buffer_load_dwordx4 a[112:115], v254, s[24:27], s80 offen sc1\n"
"v_mfma_f32_16x16x32_bf16 v[0:3], a[172:175], a[16:19], v[0:3]\n"
"v_mfma_f32_16x16x32_bf16 v[8:11], a[172:175], a[20:23], 0\n"
"ds_read_b64_tr_b16 a[144:145], v200\n"
"v_mfma_f32_16x16x32_bf16 v[8:11], a[128:131], a[4:7], v[8:11]\n"
"ds_read_b64_tr_b16 a[146:147], v200 offset:1024\n"
"v_mfma_f32_16x16x32_bf16 v[16:19], a[128:131], a[8:11], 0\n"
"ds_read_b64_tr_b16 a[188:189], v200 offset:8192\n"
"v_mfma_f32_16x16x32_bf16 v[16:19], a[172:175], a[24:27], v[16:19]\n"
"ds_read_b64_tr_b16 a[190:191], v200 offset:9216\n"
"v_mfma_f32_16x16x32_bf16 v[24:27], a[172:175], a[28:31], 0\n"
"buffer_load_dwordx4 a[116:119], v254, s[24:27], s80 offen offset:1024 sc1\n"
"v_mfma_f32_16x16x32_bf16 v[24:27], a[128:131], a[12:15], v[24:27]\n"
"v_mfma_f32_16x16x32_bf16 v[4:7], a[132:135], a[0:3], 0\n"
"buffer_load_dwordx4 a[120:123], v254, s[24:27], s80 offen offset:2048 sc1\n"
"v_mfma_f32_16x16x32_bf16 v[4:7], a[176:179], a[16:19], v[4:7]\n"
"v_mfma_f32_16x16x32_bf16 v[12:15], a[176:179], a[20:23], 0\n"
"ds_read_b64_tr_b16 a[148:149], v201\n"
"v_mfma_f32_16x16x32_bf16 v[12:15], a[132:135], a[4:7], v[12:15]\n"
"ds_read_b64_tr_b16 a[150:151], v201 offset:1024\n"
"v_mfma_f32_16x16x32_bf16 v[20:23], a[132:135], a[8:11], 0\n"
"ds_read_b64_tr_b16 a[192:193], v201 offset:8192\n"
"v_mfma_f32_16x16x32_bf16 v[20:23], a[176:179], a[24:27], v[20:23]\n"
"ds_read_b64_tr_b16 a[194:195], v201 offset:9216\n"
"v_mfma_f32_16x16x32_bf16 v[28:31], a[176:179], a[28:31], 0\n"
"buffer_load_dwordx4 a[124:127], v254, s[24:27], s80 offen offset:3072 sc1\n"
"v_mfma_f32_16x16x32_bf16 v[28:31], a[132:135], a[12:15], v[28:31]\n"
"s_add_u32 s80, s80, 0x20000\n"
"v_mfma_f32_16x16x32_bf16 v[32:35], a[136:139], a[0:3], 0\n"
"v_mfma_f32_16x16x32_bf16 v[32:35], a[180:183], a[16:19], v[32:35]\n"
"v_mfma_f32_16x16x32_bf16 v[40:43], a[180:183], a[20:23], 0\n"
"ds_read_b64_tr_b16 a[152:153], v202\n"
"v_mfma_f32_16x16x32_bf16 v[40:43], a[136:139], a[4:7], v[40:43]\n"
"ds_read_b64_tr_b16 a[154:155], v202 offset:1024\n"
"v_mfma_f32_16x16x32_bf16 v[48:51], a[136:139], a[8:11], 0\n"
"ds_read_b64_tr_b16 a[196:197], v202 offset:8192\n"
"v_mfma_f32_16x16x32_bf16 v[48:51], a[180:183], a[24:27], v[48:51]\n"
"ds_read_b64_tr_b16 a[198:199], v202 offset:9216\n"
"v_mfma_f32_16x16x32_bf16 v[56:59], a[180:183], a[28:31], 0\n"
"v_mfma_f32_16x16x32_bf16 v[56:59], a[136:139], a[12:15], v[56:59]\n"
"v_mfma_f32_16x16x32_bf16 v[36:39], a[140:143], a[0:3], 0\n"
"v_mfma_f32_16x16x32_bf16 v[36:39], a[184:187], a[16:19], v[36:39]\n"
"v_mfma_f32_16x16x32_bf16 v[44:47], a[184:187], a[20:23], 0\n"
"ds_read_b64_tr_b16 a[156:157], v203\n"
"v_mfma_f32_16x16x32_bf16 v[44:47], a[140:143], a[4:7], v[44:47]\n"
"ds_read_b64_tr_b16 a[158:159], v203 offset:1024\n"
"v_mfma_f32_16x16x32_bf16 v[52:55], a[140:143], a[8:11], 0\n"
"ds_read_b64_tr_b16 a[200:201], v203 offset:8192\n"
"v_mfma_f32_16x16x32_bf16 v[52:55], a[184:187], a[24:27], v[52:55]\n"
"ds_read_b64_tr_b16 a[202:203], v203 offset:9216\n"
"v_mfma_f32_16x16x32_bf16 v[60:63], a[184:187], a[28:31], 0\n"
"s_add_u32 s14, s72, 0x1000\n"
"v_mfma_f32_16x16x32_bf16 v[60:63], a[140:143], a[12:15], v[60:63]\n"
"s_waitcnt lgkmcnt(8)\n"
"v_mfma_f32_16x16x32_bf16 v[64:67], a[144:147], a[0:3], 0\n"
"buffer_store_dwordx4 v[208:211], v254, s[28:31], s14 offen offset:1024 sc1\n"
"v_mfma_f32_16x16x32_bf16 v[64:67], a[188:191], a[16:19], v[64:67]\n"
"v_mfma_f32_16x16x32_bf16 v[72:75], a[188:191], a[20:23], 0\n"
"ds_read_b64_tr_b16 a[160:161], v204\n"
"v_mfma_f32_16x16x32_bf16 v[72:75], a[144:147], a[4:7], v[72:75]\n"
"ds_read_b64_tr_b16 a[162:163], v204 offset:512\n"
"v_mfma_f32_16x16x32_bf16 v[80:83], a[144:147], a[8:11], 0\n"
"ds_read_b64_tr_b16 a[204:205], v204 offset:4096\n"
"v_mfma_f32_16x16x32_bf16 v[80:83], a[188:191], a[24:27], v[80:83]\n"
"ds_read_b64_tr_b16 a[206:207], v204 offset:4608\n"
"v_mfma_f32_16x16x32_bf16 v[88:91], a[188:191], a[28:31], 0\n"
"v_mfma_f32_16x16x32_bf16 v[88:91], a[144:147], a[12:15], v[88:91]\n"
"v_mfma_f32_16x16x32_bf16 v[68:71], a[148:151], a[0:3], 0\n"
"v_mfma_f32_16x16x32_bf16 v[68:71], a[192:195], a[16:19], v[68:71]\n"
"v_mfma_f32_16x16x32_bf16 v[76:79], a[192:195], a[20:23], 0\n"
"ds_read_b64_tr_b16 a[164:165], v205\n"
"s_add_u32 s14, s73, 0\n"
"v_mfma_f32_16x16x32_bf16 v[76:79], a[148:151], a[4:7], v[76:79]\n"
"ds_read_b64_tr_b16 a[166:167], v205 offset:512\n"
"v_mfma_f32_16x16x32_bf16 v[84:87], a[148:151], a[8:11], 0\n"
"ds_read_b64_tr_b16 a[208:209], v205 offset:4096\n"
"v_mfma_f32_16x16x32_bf16 v[84:87], a[192:195], a[24:27], v[84:87]\n"
"ds_read_b64_tr_b16 a[210:211], v205 offset:4608\n"
"v_mfma_f32_16x16x32_bf16 v[92:95], a[192:195], a[28:31], 0\n"
"buffer_store_dwordx4 v[212:215], v254, s[96:99], s14 offen sc1\n"
"v_mfma_f32_16x16x32_bf16 v[92:95], a[148:151], a[12:15], v[92:95]\n"
"s_waitcnt lgkmcnt(8)\n"
"v_mfma_f32_16x16x32_bf16 v[96:99], a[152:155], a[0:3], 0\n"
"v_mfma_f32_16x16x32_bf16 v[96:99], a[196:199], a[16:19], v[96:99]\n"
"v_mfma_f32_16x16x32_bf16 v[104:107], a[196:199], a[20:23], 0\n"
"ds_read_b64_tr_b16 a[168:169], v206\n"
"v_mfma_f32_16x16x32_bf16 v[104:107], a[152:155], a[4:7], v[104:107]\n"
"ds_read_b64_tr_b16 a[170:171], v206 offset:512\n"
"v_mfma_f32_16x16x32_bf16 v[112:115], a[152:155], a[8:11], 0\n"
"ds_read_b64_tr_b16 a[212:213], v206 offset:4096\n"
"v_mfma_f32_16x16x32_bf16 v[112:115], a[196:199], a[24:27], v[112:115]\n"
"ds_read_b64_tr_b16 a[214:215], v206 offset:4608\n"
"v_mfma_f32_16x16x32_bf16 v[120:123], a[196:199], a[28:31], 0\n"
"s_add_u32 s14, s73, 0\n"
"v_mfma_f32_16x16x32_bf16 v[120:123], a[152:155], a[12:15], v[120:123]\n"
"buffer_store_dwordx4 v[216:219], v254, s[96:99], s14 offen offset:1024 sc1\n"
"v_mfma_f32_16x16x32_bf16 v[100:103], a[156:159], a[0:3], 0\n"
"v_mfma_f32_16x16x32_bf16 v[100:103], a[200:203], a[16:19], v[100:103]\n"
"v_mfma_f32_16x16x32_bf16 v[108:111], a[200:203], a[20:23], 0\n"
"ds_read_b64_tr_b16 a[128:129], v196 offset:16384\n"
"v_mfma_f32_16x16x32_bf16 v[108:111], a[156:159], a[4:7], v[108:111]\n"
"ds_read_b64_tr_b16 a[130:131], v196 offset:17408\n"
"v_mfma_f32_16x16x32_bf16 v[116:119], a[156:159], a[8:11], 0\n"
"ds_read_b64_tr_b16 a[172:173], v196 offset:24576\n"
"v_mfma_f32_16x16x32_bf16 v[116:119], a[200:203], a[24:27], v[116:119]\n"
"ds_read_b64_tr_b16 a[174:175], v196 offset:25600\n"
"v_mfma_f32_16x16x32_bf16 v[124:127], a[200:203], a[28:31], 0\n"
"v_mfma_f32_16x16x32_bf16 v[124:127], a[156:159], a[12:15], v[124:127]\n"
"s_waitcnt lgkmcnt(8)\n"
"v_mfma_f32_16x16x32_bf16 v[128:131], a[160:163], a[0:3], v[176:179]\n"
"v_mfma_f32_16x16x32_bf16 v[128:131], a[204:207], a[16:19], v[128:131]\n"
"v_mfma_f32_16x16x32_bf16 v[132:135], a[204:207], a[20:23], v[176:179]\n"
"ds_read_b64_tr_b16 a[132:133], v197 offset:16384\n"
"s_add_u32 s14, s73, 0\n"
"v_mfma_f32_16x16x32_bf16 v[132:135], a[160:163], a[4:7], v[132:135]\n"
"ds_read_b64_tr_b16 a[134:135], v197 offset:17408\n"
"v_mfma_f32_16x16x32_bf16 v[136:139], a[160:163], a[8:11], v[176:179]\n"
"ds_read_b64_tr_b16 a[176:177], v197 offset:24576\n"
"v_mfma_f32_16x16x32_bf16 v[136:139], a[204:207], a[24:27], v[136:139]\n"
"ds_read_b64_tr_b16 a[178:179], v197 offset:25600\n"
"v_mfma_f32_16x16x32_bf16 v[140:143], a[204:207], a[28:31], v[176:179]\n"
"buffer_store_dwordx4 v[220:223], v254, s[96:99], s14 offen offset:2048 sc1\n"
"v_mfma_f32_16x16x32_bf16 v[140:143], a[160:163], a[12:15], v[140:143]\n"
"v_mfma_f32_16x16x32_bf16 v[144:147], a[164:167], a[0:3], v[180:183]\n"
"v_mfma_f32_16x16x32_bf16 v[144:147], a[208:211], a[16:19], v[144:147]\n"
"v_mfma_f32_16x16x32_bf16 v[148:151], a[208:211], a[20:23], v[180:183]\n"
"ds_read_b64_tr_b16 a[136:137], v198 offset:16384\n"
"v_mfma_f32_16x16x32_bf16 v[148:151], a[164:167], a[4:7], v[148:151]\n"
"ds_read_b64_tr_b16 a[138:139], v198 offset:17408\n"
"v_mfma_f32_16x16x32_bf16 v[152:155], a[164:167], a[8:11], v[180:183]\n"
"ds_read_b64_tr_b16 a[180:181], v198 offset:24576\n"
"v_mfma_f32_16x16x32_bf16 v[152:155], a[208:211], a[24:27], v[152:155]\n"
"ds_read_b64_tr_b16 a[182:183], v198 offset:25600\n"
"v_mfma_f32_16x16x32_bf16 v[156:159], a[208:211], a[28:31], v[180:183]\n"
"s_add_u32 s14, s73, 0\n"
"v_mfma_f32_16x16x32_bf16 v[156:159], a[164:167], a[12:15], v[156:159]\n"
"s_waitcnt lgkmcnt(12)\n"
"v_mfma_f32_16x16x32_bf16 v[160:163], a[168:171], a[0:3], v[184:187]\n"
"buffer_store_dwordx4 v[224:227], v254, s[96:99], s14 offen offset:3072 sc1\n"
"v_mfma_f32_16x16x32_bf16 v[160:163], a[212:215], a[16:19], v[160:163]\n"
"v_mfma_f32_16x16x32_bf16 v[164:167], a[212:215], a[20:23], v[184:187]\n"
"ds_read_b64_tr_b16 a[140:141], v199 offset:16384\n"
"v_mfma_f32_16x16x32_bf16 v[164:167], a[168:171], a[4:7], v[164:167]\n"
"ds_read_b64_tr_b16 a[142:143], v199 offset:17408\n"
"v_mfma_f32_16x16x32_bf16 v[168:171], a[168:171], a[8:11], v[184:187]\n"
"ds_read_b64_tr_b16 a[184:185], v199 offset:24576\n"
"v_mfma_f32_16x16x32_bf16 v[168:171], a[212:215], a[24:27], v[168:171]\n"
"ds_read_b64_tr_b16 a[186:187], v199 offset:25600\n"
"v_mfma_f32_16x16x32_bf16 v[172:175], a[212:215], a[28:31], v[184:187]\n"
"v_mfma_f32_16x16x32_bf16 v[172:175], a[168:171], a[12:15], v[172:175]\n"
"s_waitcnt vmcnt(21) lgkmcnt(8)\n"
"v_mfma_f32_16x16x32_bf16 v[0:3], a[128:131], a[32:35], v[0:3]\n"
"v_mfma_f32_16x16x32_bf16 v[0:3], a[172:175], a[48:51], v[0:3]\n"
"buffer_load_dwordx4 a[0:3], v254, s[24:27], s80 offen sc1\n"
"v_mfma_f32_16x16x32_bf16 v[8:11], a[172:175], a[52:55], v[8:11]\n"
"ds_read_b64_tr_b16 a[144:145], v200 offset:16384\n"
"v_mfma_f32_16x16x32_bf16 v[8:11], a[128:131], a[36:39], v[8:11]\n"
"ds_read_b64_tr_b16 a[146:147], v200 offset:17408\n"
"v_mfma_f32_16x16x32_bf16 v[16:19], a[128:131], a[40:43], v[16:19]\n"
"ds_read_b64_tr_b16 a[188:189], v200 offset:24576\n"
"v_mfma_f32_16x16x32_bf16 v[16:19], a[172:175], a[56:59], v[16:19]\n"
"ds_read_b64_tr_b16 a[190:191], v200 offset:25600\n"
"v_mfma_f32_16x16x32_bf16 v[24:27], a[172:175], a[60:63], v[24:27]\n"
"buffer_load_dwordx4 a[4:7], v254, s[24:27], s80 offen offset:1024 sc1\n"
"v_mfma_f32_16x16x32_bf16 v[24:27], a[128:131], a[44:47], v[24:27]\n"
"v_mfma_f32_16x16x32_bf16 v[4:7], a[132:135], a[32:35], v[4:7]\n"
"v_mfma_f32_16x16x32_bf16 v[4:7], a[176:179], a[48:51], v[4:7]\n"
"buffer_load_dwordx4 a[8:11], v254, s[24:27], s80 offen offset:2048 sc1\n"
"v_mfma_f32_16x16x32_bf16 v[12:15], a[176:179], a[52:55], v[12:15]\n"
"ds_read_b64_tr_b16 a[148:149], v201 offset:16384\n"
"v_mfma_f32_16x16x32_bf16 v[12:15], a[132:135], a[36:39], v[12:15]\n"
"ds_read_b64_tr_b16 a[150:151], v201 offset:17408\n"
"v_mfma_f32_16x16x32_bf16 v[20:23], a[132:135], a[40:43], v[20:23]\n"
"ds_read_b64_tr_b16 a[192:193], v201 offset:24576\n"
"v_mfma_f32_16x16x32_bf16 v[20:23], a[176:179], a[56:59], v[20:23]\n"
"ds_read_b64_tr_b16 a[194:195], v201 offset:25600\n"
"v_mfma_f32_16x16x32_bf16 v[28:31], a[176:179], a[60:63], v[28:31]\n"
"buffer_load_dwordx4 a[12:15], v254, s[24:27], s80 offen offset:3072 sc1\n"
"v_mfma_f32_16x16x32_bf16 v[28:31], a[132:135], a[44:47], v[28:31]\n"
"s_waitcnt lgkmcnt(8)\n"
"v_mfma_f32_16x16x32_bf16 v[32:35], a[136:139], a[32:35], v[32:35]\n"
"s_add_u32 s80, s80, 0x20000\n"
"v_mfma_f32_16x16x32_bf16 v[32:35], a[180:183], a[48:51], v[32:35]\n"
"s_add_u32 s14, s72, 0x2000\n"
"v_mfma_f32_16x16x32_bf16 v[40:43], a[180:183], a[52:55], v[40:43]\n"
"ds_read_b64_tr_b16 a[152:153], v202 offset:16384\n"
"v_mfma_f32_16x16x32_bf16 v[40:43], a[136:139], a[36:39], v[40:43]\n"
"ds_read_b64_tr_b16 a[154:155], v202 offset:17408\n"
"v_mfma_f32_16x16x32_bf16 v[48:51], a[136:139], a[40:43], v[48:51]\n"
"ds_read_b64_tr_b16 a[196:197], v202 offset:24576\n"
"v_mfma_f32_16x16x32_bf16 v[48:51], a[180:183], a[56:59], v[48:51]\n"
"ds_read_b64_tr_b16 a[198:199], v202 offset:25600\n"
"v_mfma_f32_16x16x32_bf16 v[56:59], a[180:183], a[60:63], v[56:59]\n"
"buffer_store_dwordx4 v[228:231], v254, s[28:31], s14 offen sc1\n"
"v_mfma_f32_16x16x32_bf16 v[56:59], a[136:139], a[44:47], v[56:59]\n"
"s_add_u32 s14, s72, 0x2000\n"
"v_mfma_f32_16x16x32_bf16 v[36:39], a[140:143], a[32:35], v[36:39]\n"
"buffer_store_dwordx4 v[232:235], v254, s[28:31], s14 offen offset:1024 sc1\n"
"v_mfma_f32_16x16x32_bf16 v[36:39], a[184:187], a[48:51], v[36:39]\n"
"v_mfma_f32_16x16x32_bf16 v[44:47], a[184:187], a[52:55], v[44:47]\n"
"ds_read_b64_tr_b16 a[156:157], v203 offset:16384\n"
"v_mfma_f32_16x16x32_bf16 v[44:47], a[140:143], a[36:39], v[44:47]\n"
"ds_read_b64_tr_b16 a[158:159], v203 offset:17408\n"
"s_add_u32 s14, s73, 0x6000\n"
"v_mfma_f32_16x16x32_bf16 v[52:55], a[140:143], a[40:43], v[52:55]\n"
"ds_read_b64_tr_b16 a[200:201], v203 offset:24576\n"
"v_mfma_f32_16x16x32_bf16 v[52:55], a[184:187], a[56:59], v[52:55]\n"
"ds_read_b64_tr_b16 a[202:203], v203 offset:25600\n"
"v_mfma_f32_16x16x32_bf16 v[60:63], a[184:187], a[60:63], v[60:63]\n"
"buffer_store_dwordx4 a[216:219], v254, s[96:99], s14 offen offset:3072 sc1\n"
"v_mfma_f32_16x16x32_bf16 v[60:63], a[140:143], a[44:47], v[60:63]\n"
"s_waitcnt lgkmcnt(8)\n"
"v_mfma_f32_16x16x32_bf16 v[64:67], a[144:147], a[32:35], v[64:67]\n"
"buffer_load_dwordx4 a[16:19], v254, s[24:27], s80 offen sc1\n"
"v_mfma_f32_16x16x32_bf16 v[64:67], a[188:191], a[48:51], v[64:67]\n"
"v_mfma_f32_16x16x32_bf16 v[72:75], a[188:191], a[52:55], v[72:75]\n"
"ds_read_b64_tr_b16 a[160:161], v204 offset:8192\n"
"v_mfma_f32_16x16x32_bf16 v[72:75], a[144:147], a[36:39], v[72:75]\n"
"ds_read_b64_tr_b16 a[162:163], v204 offset:8704\n"
"v_mfma_f32_16x16x32_bf16 v[80:83], a[144:147], a[40:43], v[80:83]\n"
"ds_read_b64_tr_b16 a[204:205], v204 offset:12288\n"
"v_mfma_f32_16x16x32_bf16 v[80:83], a[188:191], a[56:59], v[80:83]\n"
"ds_read_b64_tr_b16 a[206:207], v204 offset:12800\n"
"v_mfma_f32_16x16x32_bf16 v[88:91], a[188:191], a[60:63], v[88:91]\n"
"buffer_load_dwordx4 a[20:23], v254, s[24:27], s80 offen offset:1024 sc1\n"
"v_mfma_f32_16x16x32_bf16 v[88:91], a[144:147], a[44:47], v[88:91]\n"
"buffer_load_dwordx4 a[24:27], v254, s[24:27], s80 offen offset:2048 sc1\n"
"v_mfma_f32_16x16x32_bf16 v[68:71], a[148:151], a[32:35], v[68:71]\n"
"v_mfma_f32_16x16x32_bf16 v[68:71], a[192:195], a[48:51], v[68:71]\n"
"v_mfma_f32_16x16x32_bf16 v[76:79], a[192:195], a[52:55], v[76:79]\n"
"ds_read_b64_tr_b16 a[164:165], v205 offset:8192\n"
"v_mfma_f32_16x16x32_bf16 v[76:79], a[148:151], a[36:39], v[76:79]\n"
"ds_read_b64_tr_b16 a[166:167], v205 offset:8704\n"
"v_mfma_f32_16x16x32_bf16 v[84:87], a[148:151], a[40:43], v[84:87]\n"
"ds_read_b64_tr_b16 a[208:209], v205 offset:12288\n"
"v_mfma_f32_16x16x32_bf16 v[84:87], a[192:195], a[56:59], v[84:87]\n"
"ds_read_b64_tr_b16 a[210:211], v205 offset:12800\n"
"v_mfma_f32_16x16x32_bf16 v[92:95], a[192:195], a[60:63], v[92:95]\n"
"buffer_load_dwordx4 a[28:31], v254, s[24:27], s80 offen offset:3072 sc1\n"
"v_mfma_f32_16x16x32_bf16 v[92:95], a[148:151], a[44:47], v[92:95]\n"
"s_add_u32 s80, s80, 0x20000\n"
"s_waitcnt lgkmcnt(8)\n"
"v_mfma_f32_16x16x32_bf16 v[96:99], a[152:155], a[32:35], v[96:99]\n"
"v_mfma_f32_16x16x32_bf16 v[96:99], a[196:199], a[48:51], v[96:99]\n"
"v_mfma_f32_16x16x32_bf16 v[104:107], a[196:199], a[52:55], v[104:107]\n"
"ds_read_b64_tr_b16 a[168:169], v206 offset:8192\n"
"v_mfma_f32_16x16x32_bf16 v[104:107], a[152:155], a[36:39], v[104:107]\n"
"ds_read_b64_tr_b16 a[170:171], v206 offset:8704\n"
"v_mfma_f32_16x16x32_bf16 v[112:115], a[152:155], a[40:43], v[112:115]\n"
"ds_read_b64_tr_b16 a[212:213], v206 offset:12288\n"
"v_mfma_f32_16x16x32_bf16 v[112:115], a[196:199], a[56:59], v[112:115]\n"
"ds_read_b64_tr_b16 a[214:215], v206 offset:12800\n"
"v_mfma_f32_16x16x32_bf16 v[120:123], a[196:199], a[60:63], v[120:123]\n"
"v_mfma_f32_16x16x32_bf16 v[120:123], a[152:155], a[44:47], v[120:123]\n"
"v_mfma_f32_16x16x32_bf16 v[100:103], a[156:159], a[32:35], v[100:103]\n"
"v_mfma_f32_16x16x32_bf16 v[100:103], a[200:203], a[48:51], v[100:103]\n"
"s_waitcnt vmcnt(51) lgkmcnt(0)\n"
"s_barrier\n"
"v_mfma_f32_16x16x32_bf16 v[108:111], a[200:203], a[52:55], v[108:111]\n"
"ds_read_b64_tr_b16 a[128:129], v196 offset:32768\n"
"s_add_u32 m0, s78, 0\n"
"v_mfma_f32_16x16x32_bf16 v[108:111], a[156:159], a[36:39], v[108:111]\n"
"ds_read_b64_tr_b16 a[130:131], v196 offset:33792\n"
"v_mfma_f32_16x16x32_bf16 v[116:119], a[156:159], a[40:43], v[116:119]\n"
"ds_read_b64_tr_b16 a[172:173], v196 offset:40960\n"
"v_mfma_f32_16x16x32_bf16 v[116:119], a[200:203], a[56:59], v[116:119]\n"
"ds_read_b64_tr_b16 a[174:175], v196 offset:41984\n"
"v_mfma_f32_16x16x32_bf16 v[124:127], a[200:203], a[60:63], v[124:127]\n"
"buffer_load_dwordx4 v188, s[16:19], s74 offen lds\n"
"v_mfma_f32_16x16x32_bf16 v[124:127], a[156:159], a[44:47], v[124:127]\n"
"s_waitcnt lgkmcnt(8)\n"
"v_mfma_f32_16x16x32_bf16 v[128:131], a[160:163], a[32:35], v[128:131]\n"
"buffer_load_dwordx4 v189, s[16:19], s74 offen offset:1024 lds\n"
"v_mfma_f32_16x16x32_bf16 v[128:131], a[204:207], a[48:51], v[128:131]\n"
"v_mfma_f32_16x16x32_bf16 v[132:135], a[204:207], a[52:55], v[132:135]\n"
"ds_read_b64_tr_b16 a[132:133], v197 offset:32768\n"
"v_mfma_f32_16x16x32_bf16 v[132:135], a[160:163], a[36:39], v[132:135]\n"
"ds_read_b64_tr_b16 a[134:135], v197 offset:33792\n"
"v_mfma_f32_16x16x32_bf16 v[136:139], a[160:163], a[40:43], v[136:139]\n"
"ds_read_b64_tr_b16 a[176:177], v197 offset:40960\n"
"v_mfma_f32_16x16x32_bf16 v[136:139], a[204:207], a[56:59], v[136:139]\n"
"ds_read_b64_tr_b16 a[178:179], v197 offset:41984\n"
"v_mfma_f32_16x16x32_bf16 v[140:143], a[204:207], a[60:63], v[140:143]\n"
"buffer_load_dwordx4 v190, s[16:19], s74 offen offset:2048 lds\n"
"v_mfma_f32_16x16x32_bf16 v[140:143], a[160:163], a[44:47], v[140:143]\n"
"buffer_load_dwordx4 v191, s[16:19], s74 offen offset:3072 lds\n"
"v_mfma_f32_16x16x32_bf16 v[144:147], a[164:167], a[32:35], v[144:147]\n"
"v_mfma_f32_16x16x32_bf16 v[144:147], a[208:211], a[48:51], v[144:147]\n"
"v_mfma_f32_16x16x32_bf16 v[148:151], a[208:211], a[52:55], v[148:151]\n"
"ds_read_b64_tr_b16 a[136:137], v198 offset:32768\n"
"s_add_u32 m0, s78, 0x1000\n"
"v_mfma_f32_16x16x32_bf16 v[148:151], a[164:167], a[36:39], v[148:151]\n"
"ds_read_b64_tr_b16 a[138:139], v198 offset:33792\n"
"v_mfma_f32_16x16x32_bf16 v[152:155], a[164:167], a[40:43], v[152:155]\n"
"ds_read_b64_tr_b16 a[180:181], v198 offset:40960\n"
"v_mfma_f32_16x16x32_bf16 v[152:155], a[208:211], a[56:59], v[152:155]\n"
"ds_read_b64_tr_b16 a[182:183], v198 offset:41984\n"
"v_mfma_f32_16x16x32_bf16 v[156:159], a[208:211], a[60:63], v[156:159]\n"
"buffer_load_dwordx4 v188, s[16:19], s75 offen lds\n"
"v_mfma_f32_16x16x32_bf16 v[156:159], a[164:167], a[44:47], v[156:159]\n"
"s_waitcnt lgkmcnt(12)\n"
"v_mfma_f32_16x16x32_bf16 v[160:163], a[168:171], a[32:35], v[160:163]\n"
"buffer_load_dwordx4 v189, s[16:19], s75 offen offset:1024 lds\n"
"v_mfma_f32_16x16x32_bf16 v[160:163], a[212:215], a[48:51], v[160:163]\n"
"v_mfma_f32_16x16x32_bf16 v[164:167], a[212:215], a[52:55], v[164:167]\n"
"ds_read_b64_tr_b16 a[140:141], v199 offset:32768\n"
"v_mfma_f32_16x16x32_bf16 v[164:167], a[168:171], a[36:39], v[164:167]\n"
"ds_read_b64_tr_b16 a[142:143], v199 offset:33792\n"
"v_mfma_f32_16x16x32_bf16 v[168:171], a[168:171], a[40:43], v[168:171]\n"
"ds_read_b64_tr_b16 a[184:185], v199 offset:40960\n"
"v_mfma_f32_16x16x32_bf16 v[168:171], a[212:215], a[56:59], v[168:171]\n"
"ds_read_b64_tr_b16 a[186:187], v199 offset:41984\n"
"v_mfma_f32_16x16x32_bf16 v[172:175], a[212:215], a[60:63], v[172:175]\n"
"buffer_load_dwordx4 v190, s[16:19], s75 offen offset:2048 lds\n"
"v_mfma_f32_16x16x32_bf16 v[172:175], a[168:171], a[44:47], v[172:175]\n"
"s_waitcnt vmcnt(31) lgkmcnt(8)\n"
"v_mfma_f32_16x16x32_bf16 v[0:3], a[128:131], a[64:67], v[0:3]\n"
"buffer_load_dwordx4 v191, s[16:19], s75 offen offset:3072 lds\n"
"v_mfma_f32_16x16x32_bf16 v[0:3], a[172:175], a[80:83], v[0:3]\n"
"v_mfma_f32_16x16x32_bf16 v[8:11], a[172:175], a[84:87], v[8:11]\n"
"ds_read_b64_tr_b16 a[144:145], v200 offset:32768\n"
"s_add_u32 m0, s79, 0\n"
"v_mfma_f32_16x16x32_bf16 v[8:11], a[128:131], a[68:71], v[8:11]\n"
"ds_read_b64_tr_b16 a[146:147], v200 offset:33792\n"
"v_mfma_f32_16x16x32_bf16 v[16:19], a[128:131], a[72:75], v[16:19]\n"
"ds_read_b64_tr_b16 a[188:189], v200 offset:40960\n"
"v_mfma_f32_16x16x32_bf16 v[16:19], a[172:175], a[88:91], v[16:19]\n"
"ds_read_b64_tr_b16 a[190:191], v200 offset:41984\n"
"v_mfma_f32_16x16x32_bf16 v[24:27], a[172:175], a[92:95], v[24:27]\n"
"buffer_load_dwordx4 v192, s[20:23], s76 offen lds\n"
"v_mfma_f32_16x16x32_bf16 v[24:27], a[128:131], a[76:79], v[24:27]\n"
"buffer_load_dwordx4 v193, s[20:23], s76 offen offset:1024 lds\n"
"v_mfma_f32_16x16x32_bf16 v[4:7], a[132:135], a[64:67], v[4:7]\n"
"v_mfma_f32_16x16x32_bf16 v[4:7], a[176:179], a[80:83], v[4:7]\n"
"v_mfma_f32_16x16x32_bf16 v[12:15], a[176:179], a[84:87], v[12:15]\n"
"ds_read_b64_tr_b16 a[148:149], v201 offset:32768\n"
"v_mfma_f32_16x16x32_bf16 v[12:15], a[132:135], a[68:71], v[12:15]\n"
"ds_read_b64_tr_b16 a[150:151], v201 offset:33792\n"
"v_mfma_f32_16x16x32_bf16 v[20:23], a[132:135], a[72:75], v[20:23]\n"
"ds_read_b64_tr_b16 a[192:193], v201 offset:40960\n"
"v_mfma_f32_16x16x32_bf16 v[20:23], a[176:179], a[88:91], v[20:23]\n"
"ds_read_b64_tr_b16 a[194:195], v201 offset:41984\n"
"v_mfma_f32_16x16x32_bf16 v[28:31], a[176:179], a[92:95], v[28:31]\n"
"buffer_load_dwordx4 v194, s[20:23], s76 offen offset:2048 lds\n"
"v_mfma_f32_16x16x32_bf16 v[28:31], a[132:135], a[76:79], v[28:31]\n"
"s_waitcnt lgkmcnt(8)\n"
"v_mfma_f32_16x16x32_bf16 v[32:35], a[136:139], a[64:67], v[32:35]\n"
"buffer_load_dwordx4 v195, s[20:23], s76 offen offset:3072 lds\n"
"v_mfma_f32_16x16x32_bf16 v[32:35], a[180:183], a[80:83], v[32:35]\n"
"v_mfma_f32_16x16x32_bf16 v[40:43], a[180:183], a[84:87], v[40:43]\n"
"ds_read_b64_tr_b16 a[152:153], v202 offset:32768\n"
"s_add_u32 s74, s74, 0x100000\n"
"v_mfma_f32_16x16x32_bf16 v[40:43], a[136:139], a[68:71], v[40:43]\n"
"ds_read_b64_tr_b16 a[154:155], v202 offset:33792\n"
"s_add_u32 s75, s75, 0x100000\n"
"s_add_u32 s76, s76, 0x56000\n"
"v_mfma_f32_16x16x32_bf16 v[48:51], a[136:139], a[72:75], v[48:51]\n"
"ds_read_b64_tr_b16 a[196:197], v202 offset:40960\n"
"v_mfma_f32_16x16x32_bf16 v[48:51], a[180:183], a[88:91], v[48:51]\n"
"ds_read_b64_tr_b16 a[198:199], v202 offset:41984\n"
"v_mfma_f32_16x16x32_bf16 v[56:59], a[180:183], a[92:95], v[56:59]\n"
"buffer_load_dwordx4 a[32:35], v254, s[24:27], s80 offen sc1\n"
"v_mfma_f32_16x16x32_bf16 v[56:59], a[136:139], a[76:79], v[56:59]\n"
"buffer_load_dwordx4 a[36:39], v254, s[24:27], s80 offen offset:1024 sc1\n"
"v_mfma_f32_16x16x32_bf16 v[36:39], a[140:143], a[64:67], v[36:39]\n"
"v_mfma_f32_16x16x32_bf16 v[36:39], a[184:187], a[80:83], v[36:39]\n"
"v_mfma_f32_16x16x32_bf16 v[44:47], a[184:187], a[84:87], v[44:47]\n"
"ds_read_b64_tr_b16 a[156:157], v203 offset:32768\n"
"v_mfma_f32_16x16x32_bf16 v[44:47], a[140:143], a[68:71], v[44:47]\n"
"ds_read_b64_tr_b16 a[158:159], v203 offset:33792\n"
"v_mfma_f32_16x16x32_bf16 v[52:55], a[140:143], a[72:75], v[52:55]\n"
"ds_read_b64_tr_b16 a[200:201], v203 offset:40960\n"
"v_mfma_f32_16x16x32_bf16 v[52:55], a[184:187], a[88:91], v[52:55]\n"
"ds_read_b64_tr_b16 a[202:203], v203 offset:41984\n"
"v_mfma_f32_16x16x32_bf16 v[60:63], a[184:187], a[92:95], v[60:63]\n"
"buffer_load_dwordx4 a[40:43], v254, s[24:27], s80 offen offset:2048 sc1\n"
"v_mfma_f32_16x16x32_bf16 v[60:63], a[140:143], a[76:79], v[60:63]\n"
"s_waitcnt lgkmcnt(8)\n"
"v_mfma_f32_16x16x32_bf16 v[64:67], a[144:147], a[64:67], v[64:67]\n"
"buffer_load_dwordx4 a[44:47], v254, s[24:27], s80 offen offset:3072 sc1\n"
"v_mfma_f32_16x16x32_bf16 v[64:67], a[188:191], a[80:83], v[64:67]\n"
"v_mfma_f32_16x16x32_bf16 v[72:75], a[188:191], a[84:87], v[72:75]\n"
"ds_read_b64_tr_b16 a[160:161], v204 offset:16384\n"
"s_add_u32 s80, s80, 0x20000\n"
"v_mfma_f32_16x16x32_bf16 v[72:75], a[144:147], a[68:71], v[72:75]\n"
"ds_read_b64_tr_b16 a[162:163], v204 offset:16896\n"
"s_add_u32 s14, s73, 0x6000\n"
"v_mfma_f32_16x16x32_bf16 v[80:83], a[144:147], a[72:75], v[80:83]\n"
"ds_read_b64_tr_b16 a[204:205], v204 offset:20480\n"
"v_mfma_f32_16x16x32_bf16 v[80:83], a[188:191], a[88:91], v[80:83]\n"
"ds_read_b64_tr_b16 a[206:207], v204 offset:20992\n"
"v_mfma_f32_16x16x32_bf16 v[88:91], a[188:191], a[92:95], v[88:91]\n"
"buffer_store_dwordx4 a[220:223], v254, s[96:99], s14 offen offset:2048 sc1\n"
"v_mfma_f32_16x16x32_bf16 v[88:91], a[144:147], a[76:79], v[88:91]\n"
"buffer_load_dwordx4 a[48:51], v254, s[24:27], s80 offen sc1\n"
"v_mfma_f32_16x16x32_bf16 v[68:71], a[148:151], a[64:67], v[68:71]\n"
"v_mfma_f32_16x16x32_bf16 v[68:71], a[192:195], a[80:83], v[68:71]\n"
"v_mfma_f32_16x16x32_bf16 v[76:79], a[192:195], a[84:87], v[76:79]\n"
"ds_read_b64_tr_b16 a[164:165], v205 offset:16384\n"
"v_mfma_f32_16x16x32_bf16 v[76:79], a[148:151], a[68:71], v[76:79]\n"
"ds_read_b64_tr_b16 a[166:167], v205 offset:16896\n"
"v_mfma_f32_16x16x32_bf16 v[84:87], a[148:151], a[72:75], v[84:87]\n"
"ds_read_b64_tr_b16 a[208:209], v205 offset:20480\n"
"v_mfma_f32_16x16x32_bf16 v[84:87], a[192:195], a[88:91], v[84:87]\n"
"ds_read_b64_tr_b16 a[210:211], v205 offset:20992\n"
"v_mfma_f32_16x16x32_bf16 v[92:95], a[192:195], a[92:95], v[92:95]\n"
"buffer_load_dwordx4 a[52:55], v254, s[24:27], s80 offen offset:1024 sc1\n"
"v_mfma_f32_16x16x32_bf16 v[92:95], a[148:151], a[76:79], v[92:95]\n"
"s_waitcnt lgkmcnt(8)\n"
"v_mfma_f32_16x16x32_bf16 v[96:99], a[152:155], a[64:67], v[96:99]\n"
"buffer_load_dwordx4 a[56:59], v254, s[24:27], s80 offen offset:2048 sc1\n"
"v_mfma_f32_16x16x32_bf16 v[96:99], a[196:199], a[80:83], v[96:99]\n"
"v_mfma_f32_16x16x32_bf16 v[104:107], a[196:199], a[84:87], v[104:107]\n"
"ds_read_b64_tr_b16 a[168:169], v206 offset:16384\n"
"v_mfma_f32_16x16x32_bf16 v[104:107], a[152:155], a[68:71], v[104:107]\n"
"ds_read_b64_tr_b16 a[170:171], v206 offset:16896\n"
"v_mfma_f32_16x16x32_bf16 v[112:115], a[152:155], a[72:75], v[112:115]\n"
"ds_read_b64_tr_b16 a[212:213], v206 offset:20480\n"
"v_mfma_f32_16x16x32_bf16 v[112:115], a[196:199], a[88:91], v[112:115]\n"
"ds_read_b64_tr_b16 a[214:215], v206 offset:20992\n"
"v_mfma_f32_16x16x32_bf16 v[120:123], a[196:199], a[92:95], v[120:123]\n"
"buffer_load_dwordx4 a[60:63], v254, s[24:27], s80 offen offset:3072 sc1\n"
"v_mfma_f32_16x16x32_bf16 v[120:123], a[152:155], a[76:79], v[120:123]\n"
"s_add_u32 s80, s80, 0x20000\n"
"s_add_u32 s14, s73, 0x6000\n"
"v_mfma_f32_16x16x32_bf16 v[100:103], a[156:159], a[64:67], v[100:103]\n"
"buffer_store_dwordx4 a[224:227], v254, s[96:99], s14 offen offset:1024 sc1\n"
"v_mfma_f32_16x16x32_bf16 v[100:103], a[200:203], a[80:83], v[100:103]\n"
"v_mfma_f32_16x16x32_bf16 v[108:111], a[200:203], a[84:87], v[108:111]\n"
"ds_read_b64_tr_b16 a[128:129], v196 offset:49152\n"
"v_mfma_f32_16x16x32_bf16 v[108:111], a[156:159], a[68:71], v[108:111]\n"
"ds_read_b64_tr_b16 a[130:131], v196 offset:50176\n"
"s_add_u32 s14, s73, 0x6000\n"
"v_mfma_f32_16x16x32_bf16 v[116:119], a[156:159], a[72:75], v[116:119]\n"
"ds_read_b64_tr_b16 a[172:173], v196 offset:57344\n"
"v_mfma_f32_16x16x32_bf16 v[116:119], a[200:203], a[88:91], v[116:119]\n"
"ds_read_b64_tr_b16 a[174:175], v196 offset:58368\n"
"v_mfma_f32_16x16x32_bf16 v[124:127], a[200:203], a[92:95], v[124:127]\n"
"buffer_store_dwordx4 a[228:231], v254, s[96:99], s14 offen sc1\n"
"v_mfma_f32_16x16x32_bf16 v[124:127], a[156:159], a[76:79], v[124:127]\n"
"s_add_u32 s14, s73, 0x2000\n"
"s_waitcnt lgkmcnt(8)\n"
"v_mfma_f32_16x16x32_bf16 v[128:131], a[160:163], a[64:67], v[128:131]\n"
"buffer_store_dwordx4 a[232:235], v254, s[96:99], s14 offen offset:3072 sc1\n"
"v_mfma_f32_16x16x32_bf16 v[128:131], a[204:207], a[80:83], v[128:131]\n"
"v_mfma_f32_16x16x32_bf16 v[132:135], a[204:207], a[84:87], v[132:135]\n"
"ds_read_b64_tr_b16 a[132:133], v197 offset:49152\n"
"v_mfma_f32_16x16x32_bf16 v[132:135], a[160:163], a[68:71], v[132:135]\n"
"ds_read_b64_tr_b16 a[134:135], v197 offset:50176\n"
"v_mfma_f32_16x16x32_bf16 v[136:139], a[160:163], a[72:75], v[136:139]\n"
"ds_read_b64_tr_b16 a[176:177], v197 offset:57344\n"
"v_mfma_f32_16x16x32_bf16 v[136:139], a[204:207], a[88:91], v[136:139]\n"
"ds_read_b64_tr_b16 a[178:179], v197 offset:58368\n"
"v_mfma_f32_16x16x32_bf16 v[140:143], a[204:207], a[92:95], v[140:143]\n"
"v_mfma_f32_16x16x32_bf16 v[140:143], a[160:163], a[76:79], v[140:143]\n"
"v_mfma_f32_16x16x32_bf16 v[144:147], a[164:167], a[64:67], v[144:147]\n"
"v_mfma_f32_16x16x32_bf16 v[144:147], a[208:211], a[80:83], v[144:147]\n"
"v_mfma_f32_16x16x32_bf16 v[148:151], a[208:211], a[84:87], v[148:151]\n"
"ds_read_b64_tr_b16 a[136:137], v198 offset:49152\n"
"s_add_u32 s14, s73, 0x2000\n"
"v_mfma_f32_16x16x32_bf16 v[148:151], a[164:167], a[68:71], v[148:151]\n"
"ds_read_b64_tr_b16 a[138:139], v198 offset:50176\n"
"v_mfma_f32_16x16x32_bf16 v[152:155], a[164:167], a[72:75], v[152:155]\n"
"ds_read_b64_tr_b16 a[180:181], v198 offset:57344\n"
"v_mfma_f32_16x16x32_bf16 v[152:155], a[208:211], a[88:91], v[152:155]\n"
"ds_read_b64_tr_b16 a[182:183], v198 offset:58368\n"
"v_mfma_f32_16x16x32_bf16 v[156:159], a[208:211], a[92:95], v[156:159]\n"
"buffer_store_dwordx4 a[236:239], v254, s[96:99], s14 offen offset:2048 sc1\n"
"v_mfma_f32_16x16x32_bf16 v[156:159], a[164:167], a[76:79], v[156:159]\n"
"s_waitcnt lgkmcnt(12)\n"
"v_mfma_f32_16x16x32_bf16 v[160:163], a[168:171], a[64:67], v[160:163]\n"
"v_mfma_f32_16x16x32_bf16 v[160:163], a[212:215], a[80:83], v[160:163]\n"
"v_mfma_f32_16x16x32_bf16 v[164:167], a[212:215], a[84:87], v[164:167]\n"
"ds_read_b64_tr_b16 a[140:141], v199 offset:49152\n"
"v_mfma_f32_16x16x32_bf16 v[164:167], a[168:171], a[68:71], v[164:167]\n"
"ds_read_b64_tr_b16 a[142:143], v199 offset:50176\n"
"v_mfma_f32_16x16x32_bf16 v[168:171], a[168:171], a[72:75], v[168:171]\n"
"ds_read_b64_tr_b16 a[184:185], v199 offset:57344\n"
"v_mfma_f32_16x16x32_bf16 v[168:171], a[212:215], a[88:91], v[168:171]\n"
"ds_read_b64_tr_b16 a[186:187], v199 offset:58368\n"
"v_mfma_f32_16x16x32_bf16 v[172:175], a[212:215], a[92:95], v[172:175]\n"
"s_add_u32 s14, s73, 0x2000\n"
"v_mfma_f32_16x16x32_bf16 v[172:175], a[168:171], a[76:79], v[172:175]\n"
"s_waitcnt vmcnt(41) lgkmcnt(8)\n"
"v_mfma_f32_16x16x32_bf16 v[0:3], a[128:131], a[96:99], v[0:3]\n"
"buffer_store_dwordx4 a[240:243], v254, s[96:99], s14 offen offset:1024 sc1\n"
"v_mfma_f32_16x16x32_bf16 v[0:3], a[172:175], a[112:115], v[0:3]\n"
"v_mfma_f32_16x16x32_bf16 v[8:11], a[172:175], a[116:119], v[8:11]\n"
"ds_read_b64_tr_b16 a[144:145], v200 offset:49152\n"
"v_mfma_f32_16x16x32_bf16 v[8:11], a[128:131], a[100:103], v[8:11]\n"
"ds_read_b64_tr_b16 a[146:147], v200 offset:50176\n"
"v_mfma_f32_16x16x32_bf16 v[16:19], a[128:131], a[104:107], v[16:19]\n"
"ds_read_b64_tr_b16 a[188:189], v200 offset:57344\n"
"v_mfma_f32_16x16x32_bf16 v[16:19], a[172:175], a[120:123], v[16:19]\n"
"ds_read_b64_tr_b16 a[190:191], v200 offset:58368\n"
"v_mfma_f32_16x16x32_bf16 v[24:27], a[172:175], a[124:127], v[24:27]\n"
"buffer_load_dwordx4 a[64:67], v254, s[24:27], s80 offen sc1\n"
"v_mfma_f32_16x16x32_bf16 v[24:27], a[128:131], a[108:111], v[24:27]\n"
"buffer_load_dwordx4 a[68:71], v254, s[24:27], s80 offen offset:1024 sc1\n"
"v_mfma_f32_16x16x32_bf16 v[4:7], a[132:135], a[96:99], v[4:7]\n"
"v_mfma_f32_16x16x32_bf16 v[4:7], a[176:179], a[112:115], v[4:7]\n"
"v_mfma_f32_16x16x32_bf16 v[12:15], a[176:179], a[116:119], v[12:15]\n"
"ds_read_b64_tr_b16 a[148:149], v201 offset:49152\n"
"v_mfma_f32_16x16x32_bf16 v[12:15], a[132:135], a[100:103], v[12:15]\n"
"ds_read_b64_tr_b16 a[150:151], v201 offset:50176\n"
"v_mfma_f32_16x16x32_bf16 v[20:23], a[132:135], a[104:107], v[20:23]\n"
"ds_read_b64_tr_b16 a[192:193], v201 offset:57344\n"
"v_mfma_f32_16x16x32_bf16 v[20:23], a[176:179], a[120:123], v[20:23]\n"
"ds_read_b64_tr_b16 a[194:195], v201 offset:58368\n"
"v_mfma_f32_16x16x32_bf16 v[28:31], a[176:179], a[124:127], v[28:31]\n"
"buffer_load_dwordx4 a[72:75], v254, s[24:27], s80 offen offset:2048 sc1\n"
"v_mfma_f32_16x16x32_bf16 v[28:31], a[132:135], a[108:111], v[28:31]\n"
"s_waitcnt lgkmcnt(8)\n"
"v_mfma_f32_16x16x32_bf16 v[32:35], a[136:139], a[96:99], v[32:35]\n"
"buffer_load_dwordx4 a[76:79], v254, s[24:27], s80 offen offset:3072 sc1\n"
"v_mfma_f32_16x16x32_bf16 v[32:35], a[180:183], a[112:115], v[32:35]\n"
"v_mfma_f32_16x16x32_bf16 v[40:43], a[180:183], a[116:119], v[40:43]\n"
"ds_read_b64_tr_b16 a[152:153], v202 offset:49152\n"
"s_add_u32 s80, s80, 0x20000\n"
"v_mfma_f32_16x16x32_bf16 v[40:43], a[136:139], a[100:103], v[40:43]\n"
"ds_read_b64_tr_b16 a[154:155], v202 offset:50176\n"
"s_add_u32 s14, s73, 0x2000\n"
"v_mfma_f32_16x16x32_bf16 v[48:51], a[136:139], a[104:107], v[48:51]\n"
"ds_read_b64_tr_b16 a[196:197], v202 offset:57344\n"
"v_mfma_f32_16x16x32_bf16 v[48:51], a[180:183], a[120:123], v[48:51]\n"
"ds_read_b64_tr_b16 a[198:199], v202 offset:58368\n"
"v_mfma_f32_16x16x32_bf16 v[56:59], a[180:183], a[124:127], v[56:59]\n"
"buffer_store_dwordx4 a[244:247], v254, s[96:99], s14 offen sc1\n"
"v_mfma_f32_16x16x32_bf16 v[56:59], a[136:139], a[108:111], v[56:59]\n"
"s_add_u32 s14, s72, 0x3000\n"
"v_mfma_f32_16x16x32_bf16 v[36:39], a[140:143], a[96:99], v[36:39]\n"
"buffer_store_dwordx4 a[248:251], v254, s[28:31], s14 offen offset:1024 sc1\n"
"v_mfma_f32_16x16x32_bf16 v[36:39], a[184:187], a[112:115], v[36:39]\n"
"v_mfma_f32_16x16x32_bf16 v[44:47], a[184:187], a[116:119], v[44:47]\n"
"ds_read_b64_tr_b16 a[156:157], v203 offset:49152\n"
"v_mfma_f32_16x16x32_bf16 v[44:47], a[140:143], a[100:103], v[44:47]\n"
"ds_read_b64_tr_b16 a[158:159], v203 offset:50176\n"
"v_mfma_f32_16x16x32_bf16 v[52:55], a[140:143], a[104:107], v[52:55]\n"
"ds_read_b64_tr_b16 a[200:201], v203 offset:57344\n"
"v_mfma_f32_16x16x32_bf16 v[52:55], a[184:187], a[120:123], v[52:55]\n"
"ds_read_b64_tr_b16 a[202:203], v203 offset:58368\n"
"v_mfma_f32_16x16x32_bf16 v[60:63], a[184:187], a[124:127], v[60:63]\n"
"v_mfma_f32_16x16x32_bf16 v[60:63], a[140:143], a[108:111], v[60:63]\n"
"s_waitcnt lgkmcnt(8)\n"
"v_mfma_f32_16x16x32_bf16 v[64:67], a[144:147], a[96:99], v[64:67]\n"
"buffer_load_dwordx4 a[80:83], v254, s[24:27], s80 offen sc1\n"
"v_mfma_f32_16x16x32_bf16 v[64:67], a[188:191], a[112:115], v[64:67]\n"
"v_mfma_f32_16x16x32_bf16 v[72:75], a[188:191], a[116:119], v[72:75]\n"
"ds_read_b64_tr_b16 a[160:161], v204 offset:24576\n"
"v_mfma_f32_16x16x32_bf16 v[72:75], a[144:147], a[100:103], v[72:75]\n"
"ds_read_b64_tr_b16 a[162:163], v204 offset:25088\n"
"v_mfma_f32_16x16x32_bf16 v[80:83], a[144:147], a[104:107], v[80:83]\n"
"ds_read_b64_tr_b16 a[204:205], v204 offset:28672\n"
"v_mfma_f32_16x16x32_bf16 v[80:83], a[188:191], a[120:123], v[80:83]\n"
"ds_read_b64_tr_b16 a[206:207], v204 offset:29184\n"
"v_mfma_f32_16x16x32_bf16 v[88:91], a[188:191], a[124:127], v[88:91]\n"
"buffer_load_dwordx4 a[84:87], v254, s[24:27], s80 offen offset:1024 sc1\n"
"v_mfma_f32_16x16x32_bf16 v[88:91], a[144:147], a[108:111], v[88:91]\n"
"buffer_load_dwordx4 a[88:91], v254, s[24:27], s80 offen offset:2048 sc1\n"
"v_mfma_f32_16x16x32_bf16 v[68:71], a[148:151], a[96:99], v[68:71]\n"
"v_mfma_f32_16x16x32_bf16 v[68:71], a[192:195], a[112:115], v[68:71]\n"
"v_mfma_f32_16x16x32_bf16 v[76:79], a[192:195], a[116:119], v[76:79]\n"
"ds_read_b64_tr_b16 a[164:165], v205 offset:24576\n"
"v_mfma_f32_16x16x32_bf16 v[76:79], a[148:151], a[100:103], v[76:79]\n"
"ds_read_b64_tr_b16 a[166:167], v205 offset:25088\n"
"v_mfma_f32_16x16x32_bf16 v[84:87], a[148:151], a[104:107], v[84:87]\n"
"ds_read_b64_tr_b16 a[208:209], v205 offset:28672\n"
"v_mfma_f32_16x16x32_bf16 v[84:87], a[192:195], a[120:123], v[84:87]\n"
"ds_read_b64_tr_b16 a[210:211], v205 offset:29184\n"
"v_mfma_f32_16x16x32_bf16 v[92:95], a[192:195], a[124:127], v[92:95]\n"
"buffer_load_dwordx4 a[92:95], v254, s[24:27], s80 offen offset:3072 sc1\n"
"v_mfma_f32_16x16x32_bf16 v[92:95], a[148:151], a[108:111], v[92:95]\n"
"s_add_u32 s80, s80, 0x20000\n"
"s_add_u32 s14, s72, 0x3000\n"
"s_waitcnt lgkmcnt(8)\n"
"v_mfma_f32_16x16x32_bf16 v[96:99], a[152:155], a[96:99], v[96:99]\n"
"buffer_store_dwordx4 a[252:255], v254, s[28:31], s14 offen sc1\n"
"v_mfma_f32_16x16x32_bf16 v[96:99], a[196:199], a[112:115], v[96:99]\n"
"v_mfma_f32_16x16x32_bf16 v[104:107], a[196:199], a[116:119], v[104:107]\n"
"ds_read_b64_tr_b16 a[168:169], v206 offset:24576\n"
"v_mfma_f32_16x16x32_bf16 v[104:107], a[152:155], a[100:103], v[104:107]\n"
"ds_read_b64_tr_b16 a[170:171], v206 offset:25088\n"
"v_mfma_f32_16x16x32_bf16 v[112:115], a[152:155], a[104:107], v[112:115]\n"
"ds_read_b64_tr_b16 a[212:213], v206 offset:28672\n"
"v_mfma_f32_16x16x32_bf16 v[112:115], a[196:199], a[120:123], v[112:115]\n"
"ds_read_b64_tr_b16 a[214:215], v206 offset:29184\n"
"v_mfma_f32_16x16x32_bf16 v[120:123], a[196:199], a[124:127], v[120:123]\n"
"v_mfma_f32_16x16x32_bf16 v[120:123], a[152:155], a[108:111], v[120:123]\n"
"v_mfma_f32_16x16x32_bf16 v[100:103], a[156:159], a[96:99], v[100:103]\n"
"v_mfma_f32_16x16x32_bf16 v[100:103], a[200:203], a[112:115], v[100:103]\n"
"s_waitcnt vmcnt(25) lgkmcnt(0)\n"
"s_barrier\n"
"v_mfma_f32_16x16x32_bf16 v[108:111], a[200:203], a[116:119], v[108:111]\n"
"ds_read_b64_tr_b16 a[128:129], v196\n"
"s_add_u32 m0, s78, 0x8000\n"
"v_mfma_f32_16x16x32_bf16 v[108:111], a[156:159], a[100:103], v[108:111]\n"
"ds_read_b64_tr_b16 a[130:131], v196 offset:1024\n"
"v_mfma_f32_16x16x32_bf16 v[116:119], a[156:159], a[104:107], v[116:119]\n"
"ds_read_b64_tr_b16 a[172:173], v196 offset:8192\n"
"v_mfma_f32_16x16x32_bf16 v[116:119], a[200:203], a[120:123], v[116:119]\n"
"ds_read_b64_tr_b16 a[174:175], v196 offset:9216\n"
"v_mfma_f32_16x16x32_bf16 v[124:127], a[200:203], a[124:127], v[124:127]\n"
"buffer_load_dwordx4 v188, s[16:19], s74 offen lds\n"
"v_mfma_f32_16x16x32_bf16 v[124:127], a[156:159], a[108:111], v[124:127]\n"
"s_waitcnt lgkmcnt(8)\n"
"v_mfma_f32_16x16x32_bf16 v[128:131], a[160:163], a[96:99], v[128:131]\n"
"buffer_load_dwordx4 v189, s[16:19], s74 offen offset:1024 lds\n"
"v_mfma_f32_16x16x32_bf16 v[128:131], a[204:207], a[112:115], v[128:131]\n"
"v_mfma_f32_16x16x32_bf16 v[132:135], a[204:207], a[116:119], v[132:135]\n"
"ds_read_b64_tr_b16 a[132:133], v197\n"
"v_mfma_f32_16x16x32_bf16 v[132:135], a[160:163], a[100:103], v[132:135]\n"
"ds_read_b64_tr_b16 a[134:135], v197 offset:1024\n"
"v_mfma_f32_16x16x32_bf16 v[136:139], a[160:163], a[104:107], v[136:139]\n"
"ds_read_b64_tr_b16 a[176:177], v197 offset:8192\n"
"v_mfma_f32_16x16x32_bf16 v[136:139], a[204:207], a[120:123], v[136:139]\n"
"ds_read_b64_tr_b16 a[178:179], v197 offset:9216\n"
"v_mfma_f32_16x16x32_bf16 v[140:143], a[204:207], a[124:127], v[140:143]\n"
"buffer_load_dwordx4 v190, s[16:19], s74 offen offset:2048 lds\n"
"v_mfma_f32_16x16x32_bf16 v[140:143], a[160:163], a[108:111], v[140:143]\n"
"buffer_load_dwordx4 v191, s[16:19], s74 offen offset:3072 lds\n"
"v_mfma_f32_16x16x32_bf16 v[144:147], a[164:167], a[96:99], v[144:147]\n"
"v_mfma_f32_16x16x32_bf16 v[144:147], a[208:211], a[112:115], v[144:147]\n"
"v_mfma_f32_16x16x32_bf16 v[148:151], a[208:211], a[116:119], v[148:151]\n"
"ds_read_b64_tr_b16 a[136:137], v198\n"
"s_add_u32 m0, s78, 0x9000\n"
"v_mfma_f32_16x16x32_bf16 v[148:151], a[164:167], a[100:103], v[148:151]\n"
"ds_read_b64_tr_b16 a[138:139], v198 offset:1024\n"
"v_mfma_f32_16x16x32_bf16 v[152:155], a[164:167], a[104:107], v[152:155]\n"
"ds_read_b64_tr_b16 a[180:181], v198 offset:8192\n"
"v_mfma_f32_16x16x32_bf16 v[152:155], a[208:211], a[120:123], v[152:155]\n"
"ds_read_b64_tr_b16 a[182:183], v198 offset:9216\n"
"v_mfma_f32_16x16x32_bf16 v[156:159], a[208:211], a[124:127], v[156:159]\n"
"buffer_load_dwordx4 v188, s[16:19], s75 offen lds\n"
"v_mfma_f32_16x16x32_bf16 v[156:159], a[164:167], a[108:111], v[156:159]\n"
"s_waitcnt lgkmcnt(12)\n"
"v_mfma_f32_16x16x32_bf16 v[160:163], a[168:171], a[96:99], v[160:163]\n"
"buffer_load_dwordx4 v189, s[16:19], s75 offen offset:1024 lds\n"
"v_mfma_f32_16x16x32_bf16 v[160:163], a[212:215], a[112:115], v[160:163]\n"
"v_mfma_f32_16x16x32_bf16 v[164:167], a[212:215], a[116:119], v[164:167]\n"
"ds_read_b64_tr_b16 a[140:141], v199\n"
"v_mfma_f32_16x16x32_bf16 v[164:167], a[168:171], a[100:103], v[164:167]\n"
"ds_read_b64_tr_b16 a[142:143], v199 offset:1024\n"
"v_mfma_f32_16x16x32_bf16 v[168:171], a[168:171], a[104:107], v[168:171]\n"
"ds_read_b64_tr_b16 a[184:185], v199 offset:8192\n"
"v_mfma_f32_16x16x32_bf16 v[168:171], a[212:215], a[120:123], v[168:171]\n"
"ds_read_b64_tr_b16 a[186:187], v199 offset:9216\n"
"v_mfma_f32_16x16x32_bf16 v[172:175], a[212:215], a[124:127], v[172:175]\n"
"buffer_load_dwordx4 v190, s[16:19], s75 offen offset:2048 lds\n"
"v_mfma_f32_16x16x32_bf16 v[172:175], a[168:171], a[108:111], v[172:175]\n"
"s_waitcnt vmcnt(44) lgkmcnt(8)\n"
"v_mfma_f32_16x16x32_bf16 v[0:3], a[128:131], a[0:3], v[0:3]\n"
"buffer_load_dwordx4 v191, s[16:19], s75 offen offset:3072 lds\n"
"v_mfma_f32_16x16x32_bf16 v[0:3], a[172:175], a[16:19], v[0:3]\n"
"v_mfma_f32_16x16x32_bf16 v[8:11], a[172:175], a[20:23], v[8:11]\n"
"ds_read_b64_tr_b16 a[144:145], v200\n"
"s_add_u32 m0, s79, 0x4000\n"
"v_mfma_f32_16x16x32_bf16 v[8:11], a[128:131], a[4:7], v[8:11]\n"
"ds_read_b64_tr_b16 a[146:147], v200 offset:1024\n"
"v_mfma_f32_16x16x32_bf16 v[16:19], a[128:131], a[8:11], v[16:19]\n"
"ds_read_b64_tr_b16 a[188:189], v200 offset:8192\n"
"v_mfma_f32_16x16x32_bf16 v[16:19], a[172:175], a[24:27], v[16:19]\n"
"ds_read_b64_tr_b16 a[190:191], v200 offset:9216\n"
"v_mfma_f32_16x16x32_bf16 v[24:27], a[172:175], a[28:31], v[24:27]\n"
"buffer_load_dwordx4 v192, s[20:23], s76 offen lds\n"
"v_mfma_f32_16x16x32_bf16 v[24:27], a[128:131], a[12:15], v[24:27]\n"
"buffer_load_dwordx4 v193, s[20:23], s76 offen offset:1024 lds\n"
"v_mfma_f32_16x16x32_bf16 v[4:7], a[132:135], a[0:3], v[4:7]\n"
"v_mfma_f32_16x16x32_bf16 v[4:7], a[176:179], a[16:19], v[4:7]\n"
"v_mfma_f32_16x16x32_bf16 v[12:15], a[176:179], a[20:23], v[12:15]\n"
"ds_read_b64_tr_b16 a[148:149], v201\n"
"v_mfma_f32_16x16x32_bf16 v[12:15], a[132:135], a[4:7], v[12:15]\n"
"ds_read_b64_tr_b16 a[150:151], v201 offset:1024\n"
"v_mfma_f32_16x16x32_bf16 v[20:23], a[132:135], a[8:11], v[20:23]\n"
"ds_read_b64_tr_b16 a[192:193], v201 offset:8192\n"
"v_mfma_f32_16x16x32_bf16 v[20:23], a[176:179], a[24:27], v[20:23]\n"
"ds_read_b64_tr_b16 a[194:195], v201 offset:9216\n"
"v_mfma_f32_16x16x32_bf16 v[28:31], a[176:179], a[28:31], v[28:31]\n"
"buffer_load_dwordx4 v194, s[20:23], s76 offen offset:2048 lds\n"
"v_mfma_f32_16x16x32_bf16 v[28:31], a[132:135], a[12:15], v[28:31]\n"
"s_waitcnt lgkmcnt(8)\n"
"v_mfma_f32_16x16x32_bf16 v[32:35], a[136:139], a[0:3], v[32:35]\n"
"buffer_load_dwordx4 v195, s[20:23], s76 offen offset:3072 lds\n"
"v_mfma_f32_16x16x32_bf16 v[32:35], a[180:183], a[16:19], v[32:35]\n"
"v_mfma_f32_16x16x32_bf16 v[40:43], a[180:183], a[20:23], v[40:43]\n"
"ds_read_b64_tr_b16 a[152:153], v202\n"
"s_add_u32 s74, s74, 0x100000\n"
"v_mfma_f32_16x16x32_bf16 v[40:43], a[136:139], a[4:7], v[40:43]\n"
"ds_read_b64_tr_b16 a[154:155], v202 offset:1024\n"
"s_add_u32 s75, s75, 0x100000\n"
"s_add_u32 s76, s76, 0x56000\n"
"v_mfma_f32_16x16x32_bf16 v[48:51], a[136:139], a[8:11], v[48:51]\n"
"ds_read_b64_tr_b16 a[196:197], v202 offset:8192\n"
"v_mfma_f32_16x16x32_bf16 v[48:51], a[180:183], a[24:27], v[48:51]\n"
"ds_read_b64_tr_b16 a[198:199], v202 offset:9216\n"
"v_mfma_f32_16x16x32_bf16 v[56:59], a[180:183], a[28:31], v[56:59]\n"
"buffer_load_dwordx4 a[96:99], v254, s[24:27], s80 offen sc1\n"
"v_mfma_f32_16x16x32_bf16 v[56:59], a[136:139], a[12:15], v[56:59]\n"
"ds_read_b128 v[212:215], v237\n"
"v_mfma_f32_16x16x32_bf16 v[36:39], a[140:143], a[0:3], v[36:39]\n"
"buffer_load_dwordx4 a[100:103], v254, s[24:27], s80 offen offset:1024 sc1\n"
"v_mfma_f32_16x16x32_bf16 v[36:39], a[184:187], a[16:19], v[36:39]\n"
"v_mfma_f32_16x16x32_bf16 v[44:47], a[184:187], a[20:23], v[44:47]\n"
"ds_read_b64_tr_b16 a[156:157], v203\n"
"v_mfma_f32_16x16x32_bf16 v[44:47], a[140:143], a[4:7], v[44:47]\n"
"ds_read_b64_tr_b16 a[158:159], v203 offset:1024\n"
"v_mfma_f32_16x16x32_bf16 v[52:55], a[140:143], a[8:11], v[52:55]\n"
"ds_read_b64_tr_b16 a[200:201], v203 offset:8192\n"
"v_mfma_f32_16x16x32_bf16 v[52:55], a[184:187], a[24:27], v[52:55]\n"
"ds_read_b64_tr_b16 a[202:203], v203 offset:9216\n"
"v_mfma_f32_16x16x32_bf16 v[60:63], a[184:187], a[28:31], v[60:63]\n"
"buffer_load_dwordx4 a[104:107], v254, s[24:27], s80 offen offset:2048 sc1\n"
"v_mfma_f32_16x16x32_bf16 v[60:63], a[140:143], a[12:15], v[60:63]\n"
"s_waitcnt lgkmcnt(9)\n"
"v_mfma_f32_16x16x32_bf16 v[64:67], a[144:147], a[0:3], v[64:67]\n"
"buffer_load_dwordx4 a[108:111], v254, s[24:27], s80 offen offset:3072 sc1\n"
"v_mfma_f32_16x16x32_bf16 v[64:67], a[188:191], a[16:19], v[64:67]\n"
"v_mfma_f32_16x16x32_bf16 v[72:75], a[188:191], a[20:23], v[72:75]\n"
"ds_read_b64_tr_b16 a[160:161], v204\n"
"s_add_u32 s80, s80, 0x20000\n"
"v_mfma_f32_16x16x32_bf16 v[72:75], a[144:147], a[4:7], v[72:75]\n"
"ds_read_b64_tr_b16 a[162:163], v204 offset:512\n"
"ds_read_b128 v[216:219], v237 offset:1024\n"
"v_mfma_f32_16x16x32_bf16 v[80:83], a[144:147], a[8:11], v[80:83]\n"
"ds_read_b64_tr_b16 a[204:205], v204 offset:4096\n"
"v_mfma_f32_16x16x32_bf16 v[80:83], a[188:191], a[24:27], v[80:83]\n"
"ds_read_b64_tr_b16 a[206:207], v204 offset:4608\n"
"v_mfma_f32_16x16x32_bf16 v[88:91], a[188:191], a[28:31], v[88:91]\n"
"buffer_load_dwordx4 a[112:115], v254, s[24:27], s80 offen sc1\n"
"v_mfma_f32_16x16x32_bf16 v[88:91], a[144:147], a[12:15], v[88:91]\n"
"buffer_load_dwordx4 a[116:119], v254, s[24:27], s80 offen offset:1024 sc1\n"
"v_mfma_f32_16x16x32_bf16 v[68:71], a[148:151], a[0:3], v[68:71]\n"
"v_mfma_f32_16x16x32_bf16 v[68:71], a[192:195], a[16:19], v[68:71]\n"
"v_mfma_f32_16x16x32_bf16 v[76:79], a[192:195], a[20:23], v[76:79]\n"
"ds_read_b64_tr_b16 a[164:165], v205\n"
"v_mfma_f32_16x16x32_bf16 v[76:79], a[148:151], a[4:7], v[76:79]\n"
"ds_read_b64_tr_b16 a[166:167], v205 offset:512\n"
"v_mfma_f32_16x16x32_bf16 v[84:87], a[148:151], a[8:11], v[84:87]\n"
"ds_read_b64_tr_b16 a[208:209], v205 offset:4096\n"
"v_mfma_f32_16x16x32_bf16 v[84:87], a[192:195], a[24:27], v[84:87]\n"
"ds_read_b64_tr_b16 a[210:211], v205 offset:4608\n"
"v_mfma_f32_16x16x32_bf16 v[92:95], a[192:195], a[28:31], v[92:95]\n"
"buffer_load_dwordx4 a[120:123], v254, s[24:27], s80 offen offset:2048 sc1\n"
"v_mfma_f32_16x16x32_bf16 v[92:95], a[148:151], a[12:15], v[92:95]\n"
"ds_read_b128 v[220:223], v237 offset:2048\n"
"s_waitcnt lgkmcnt(10)\n"
"v_mfma_f32_16x16x32_bf16 v[96:99], a[152:155], a[0:3], v[96:99]\n"
"buffer_load_dwordx4 a[124:127], v254, s[24:27], s80 offen offset:3072 sc1\n"
"v_mfma_f32_16x16x32_bf16 v[96:99], a[196:199], a[16:19], v[96:99]\n"
"v_mfma_f32_16x16x32_bf16 v[104:107], a[196:199], a[20:23], v[104:107]\n"
"ds_read_b64_tr_b16 a[168:169], v206\n"
"s_add_u32 s80, s80, 0x20000\n"
"v_mfma_f32_16x16x32_bf16 v[104:107], a[152:155], a[4:7], v[104:107]\n"
"ds_read_b64_tr_b16 a[170:171], v206 offset:512\n"
"s_waitcnt lgkmcnt(15)\n"
"s_add_u32 s14, s72, 0x4000\n"
"v_mfma_f32_16x16x32_bf16 v[112:115], a[152:155], a[8:11], v[112:115]\n"
"ds_read_b64_tr_b16 a[212:213], v206 offset:4096\n"
"v_mfma_f32_16x16x32_bf16 v[112:115], a[196:199], a[24:27], v[112:115]\n"
"ds_read_b64_tr_b16 a[214:215], v206 offset:4608\n"
"v_mfma_f32_16x16x32_bf16 v[120:123], a[196:199], a[28:31], v[120:123]\n"
"buffer_store_dwordx4 v[212:215], v254, s[28:31], s14 offen sc1\n"
"v_mfma_f32_16x16x32_bf16 v[120:123], a[152:155], a[12:15], v[120:123]\n"
"s_waitcnt lgkmcnt(11)\n"
"s_add_u32 s14, s72, 0x4000\n"
"v_mfma_f32_16x16x32_bf16 v[100:103], a[156:159], a[0:3], v[100:103]\n"
"buffer_store_dwordx4 v[216:219], v254, s[28:31], s14 offen offset:1024 sc1\n"
"v_mfma_f32_16x16x32_bf16 v[100:103], a[200:203], a[16:19], v[100:103]\n"
"v_mfma_f32_16x16x32_bf16 v[108:111], a[200:203], a[20:23], v[108:111]\n"
"ds_read_b64_tr_b16 a[128:129], v196 offset:16384\n"
"v_mfma_f32_16x16x32_bf16 v[108:111], a[156:159], a[4:7], v[108:111]\n"
"ds_read_b64_tr_b16 a[130:131], v196 offset:17408\n"
"ds_read_b128 v[208:211], v237 offset:3072\n"
"s_waitcnt lgkmcnt(7)\n"
"s_add_u32 s14, s72, 0x5000\n"
"v_mfma_f32_16x16x32_bf16 v[116:119], a[156:159], a[8:11], v[116:119]\n"
"ds_read_b64_tr_b16 a[172:173], v196 offset:24576\n"
"v_mfma_f32_16x16x32_bf16 v[116:119], a[200:203], a[24:27], v[116:119]\n"
"ds_read_b64_tr_b16 a[174:175], v196 offset:25600\n"
"v_mfma_f32_16x16x32_bf16 v[124:127], a[200:203], a[28:31], v[124:127]\n"
"buffer_store_dwordx4 v[220:223], v254, s[28:31], s14 offen sc1\n"
"v_mfma_f32_16x16x32_bf16 v[124:127], a[156:159], a[12:15], v[124:127]\n"
"s_waitcnt lgkmcnt(10)\n"
"v_mfma_f32_16x16x32_bf16 v[128:131], a[160:163], a[0:3], v[128:131]\n"
"v_mfma_f32_16x16x32_bf16 v[128:131], a[204:207], a[16:19], v[128:131]\n"
"v_mfma_f32_16x16x32_bf16 v[132:135], a[204:207], a[20:23], v[132:135]\n"
"ds_read_b64_tr_b16 a[132:133], v197 offset:16384\n"
"v_mfma_f32_16x16x32_bf16 v[132:135], a[160:163], a[4:7], v[132:135]\n"
"ds_read_b64_tr_b16 a[134:135], v197 offset:17408\n"
"v_mfma_f32_16x16x32_bf16 v[136:139], a[160:163], a[8:11], v[136:139]\n"
"ds_read_b64_tr_b16 a[176:177], v197 offset:24576\n"
"v_mfma_f32_16x16x32_bf16 v[136:139], a[204:207], a[24:27], v[136:139]\n"
"ds_read_b64_tr_b16 a[178:179], v197 offset:25600\n"
"s_waitcnt lgkmcnt(6)\n"
"s_add_u32 s14, s72, 0x5000\n"
"v_mfma_f32_16x16x32_bf16 v[140:143], a[204:207], a[28:31], v[140:143]\n"
"buffer_store_dwordx4 v[208:211], v254, s[28:31], s14 offen offset:1024 sc1\n"
"v_mfma_f32_16x16x32_bf16 v[140:143], a[160:163], a[12:15], v[140:143]\n"
"ds_read_b128 v[212:215], v237 offset:4096\n"
"v_mfma_f32_16x16x32_bf16 v[144:147], a[164:167], a[0:3], v[144:147]\n"
"v_mfma_f32_16x16x32_bf16 v[144:147], a[208:211], a[16:19], v[144:147]\n"
"v_mfma_f32_16x16x32_bf16 v[148:151], a[208:211], a[20:23], v[148:151]\n"
"ds_read_b64_tr_b16 a[136:137], v198 offset:16384\n"
"v_mfma_f32_16x16x32_bf16 v[148:151], a[164:167], a[4:7], v[148:151]\n"
"ds_read_b64_tr_b16 a[138:139], v198 offset:17408\n"
"v_mfma_f32_16x16x32_bf16 v[152:155], a[164:167], a[8:11], v[152:155]\n"
"ds_read_b64_tr_b16 a[180:181], v198 offset:24576\n"
"v_mfma_f32_16x16x32_bf16 v[152:155], a[208:211], a[24:27], v[152:155]\n"
"ds_read_b64_tr_b16 a[182:183], v198 offset:25600\n"
"v_mfma_f32_16x16x32_bf16 v[156:159], a[208:211], a[28:31], v[156:159]\n"
"v_mfma_f32_16x16x32_bf16 v[156:159], a[164:167], a[12:15], v[156:159]\n"
"s_waitcnt lgkmcnt(14)\n"
"v_mfma_f32_16x16x32_bf16 v[160:163], a[168:171], a[0:3], v[160:163]\n"
"v_mfma_f32_16x16x32_bf16 v[160:163], a[212:215], a[16:19], v[160:163]\n"
"s_waitcnt lgkmcnt(4)\n"
"s_add_u32 s14, s73, 0x4000\n"
"v_mfma_f32_16x16x32_bf16 v[164:167], a[212:215], a[20:23], v[164:167]\n"
"ds_read_b64_tr_b16 a[140:141], v199 offset:16384\n"
"v_mfma_f32_16x16x32_bf16 v[164:167], a[168:171], a[4:7], v[164:167]\n"
"ds_read_b64_tr_b16 a[142:143], v199 offset:17408\n"
"ds_read_b128 v[216:219], v237 offset:5120\n"
"v_mfma_f32_16x16x32_bf16 v[168:171], a[168:171], a[8:11], v[168:171]\n"
"ds_read_b64_tr_b16 a[184:185], v199 offset:24576\n"
"v_mfma_f32_16x16x32_bf16 v[168:171], a[212:215], a[24:27], v[168:171]\n"
"ds_read_b64_tr_b16 a[186:187], v199 offset:25600\n"
"v_mfma_f32_16x16x32_bf16 v[172:175], a[212:215], a[28:31], v[172:175]\n"
"buffer_store_dwordx4 v[212:215], v254, s[96:99], s14 offen sc1\n"
"v_mfma_f32_16x16x32_bf16 v[172:175], a[168:171], a[12:15], v[172:175]\n"
"s_waitcnt vmcnt(41) lgkmcnt(10)\n"
"v_mfma_f32_16x16x32_bf16 v[0:3], a[128:131], a[32:35], v[0:3]\n"
"s_lshl_b32 s12, s10, 6\n"
"s_add_u32 s54, s12, 0x1000\n"
"s_lshl_b32 s13, s11, 3\n"
"s_add_u32 s89, s13, s54\n"
"s_mov_b32 s80, 0x413504f3\n"
"s_mov_b32 s81, 0x413504f3\n"
"s_mul_i32 s12, s10, 3\n"
"s_add_u32 s12, s12, 0x100\n"
"s_lshl_b32 s82, s12, 16\n"
"s_lshl_b32 s13, s77, 9\n"
"s_add_u32 s82, s82, s13\n"
"s_cmp_ge_u32 s12, 0x156\n"
"s_cselect_b32 s82, 0x40000000, s82\n"
"s_mul_i32 s12, s10, 3\n"
"s_add_u32 s12, s12, 0x101\n"
"s_lshl_b32 s83, s12, 16\n"
"s_lshl_b32 s13, s77, 9\n"
"s_add_u32 s83, s83, s13\n"
"s_cmp_ge_u32 s12, 0x156\n"
"s_cselect_b32 s83, 0x40000000, s83\n"
"s_mul_i32 s12, s10, 3\n"
"s_add_u32 s12, s12, 0x102\n"
"s_lshl_b32 s55, s12, 16\n"
"s_lshl_b32 s13, s77, 9\n"
"s_add_u32 s55, s55, s13\n"
"s_cmp_ge_u32 s12, 0x156\n"
"s_cselect_b32 s55, 0x40000000, s55\n"
"s_lshl_b32 s53, s11, 8\n"
"s_lshl_b32 s12, s10, 3\n"
"s_add_u32 s53, s53, s12\n"
"s_add_u32 s53, s53, 0x1800\n"
"v_mfma_f32_16x16x32_bf16 v[0:3], a[172:175], a[48:51], v[0:3]\n"
"v_mfma_f32_16x16x32_bf16 v[8:11], a[172:175], a[52:55], v[8:11]\n"
"ds_read_b64_tr_b16 a[144:145], v200 offset:16384\n"
"v_mfma_f32_16x16x32_bf16 v[8:11], a[128:131], a[36:39], v[8:11]\n"
"ds_read_b64_tr_b16 a[146:147], v200 offset:17408\n"
"v_mfma_f32_16x16x32_bf16 v[16:19], a[128:131], a[40:43], v[16:19]\n"
"ds_read_b64_tr_b16 a[188:189], v200 offset:24576\n"
"v_mfma_f32_16x16x32_bf16 v[16:19], a[172:175], a[56:59], v[16:19]\n"
"ds_read_b64_tr_b16 a[190:191], v200 offset:25600\n"
"s_waitcnt lgkmcnt(6)\n"
"s_add_u32 s14, s73, 0x4000\n"
"v_mfma_f32_16x16x32_bf16 v[24:27], a[172:175], a[60:63], v[24:27]\n"
"buffer_store_dwordx4 v[216:219], v254, s[96:99], s14 offen offset:1024 sc1\n"
"v_mfma_f32_16x16x32_bf16 v[24:27], a[128:131], a[44:47], v[24:27]\n"
"v_mfma_f32_16x16x32_bf16 v[4:7], a[132:135], a[32:35], v[4:7]\n"
"ds_read_b128 v[220:223], v237 offset:6144\n"
"v_mfma_f32_16x16x32_bf16 v[4:7], a[176:179], a[48:51], v[4:7]\n"
"v_mfma_f32_16x16x32_bf16 v[12:15], a[176:179], a[52:55], v[12:15]\n"
"ds_read_b64_tr_b16 a[148:149], v201 offset:16384\n"
"v_mfma_f32_16x16x32_bf16 v[12:15], a[132:135], a[36:39], v[12:15]\n"
"ds_read_b64_tr_b16 a[150:151], v201 offset:17408\n"
"v_mfma_f32_16x16x32_bf16 v[20:23], a[132:135], a[40:43], v[20:23]\n"
"ds_read_b64_tr_b16 a[192:193], v201 offset:24576\n"
"v_mfma_f32_16x16x32_bf16 v[20:23], a[176:179], a[56:59], v[20:23]\n"
"ds_read_b64_tr_b16 a[194:195], v201 offset:25600\n"
"v_mfma_f32_16x16x32_bf16 v[28:31], a[176:179], a[60:63], v[28:31]\n"
"v_mfma_f32_16x16x32_bf16 v[28:31], a[132:135], a[44:47], v[28:31]\n"
"s_waitcnt lgkmcnt(9)\n"
"v_mfma_f32_16x16x32_bf16 v[32:35], a[136:139], a[32:35], v[32:35]\n"
"v_mfma_f32_16x16x32_bf16 v[32:35], a[180:183], a[48:51], v[32:35]\n"
"v_mfma_f32_16x16x32_bf16 v[40:43], a[180:183], a[52:55], v[40:43]\n"
"ds_read_b64_tr_b16 a[152:153], v202 offset:16384\n"
"s_waitcnt lgkmcnt(5)\n"
"s_add_u32 s14, s73, 0x4000\n"
"v_mfma_f32_16x16x32_bf16 v[40:43], a[136:139], a[36:39], v[40:43]\n"
"ds_read_b64_tr_b16 a[154:155], v202 offset:17408\n"
"v_mfma_f32_16x16x32_bf16 v[48:51], a[136:139], a[40:43], v[48:51]\n"
"ds_read_b64_tr_b16 a[196:197], v202 offset:24576\n"
"ds_read_b128 v[208:211], v237 offset:7168\n"
"v_mfma_f32_16x16x32_bf16 v[48:51], a[180:183], a[56:59], v[48:51]\n"
"ds_read_b64_tr_b16 a[198:199], v202 offset:25600\n"
"v_mfma_f32_16x16x32_bf16 v[56:59], a[180:183], a[60:63], v[56:59]\n"
"buffer_store_dwordx4 v[220:223], v254, s[96:99], s14 offen offset:2048 sc1\n"
"v_mfma_f32_16x16x32_bf16 v[56:59], a[136:139], a[44:47], v[56:59]\n"
"v_mfma_f32_16x16x32_bf16 v[36:39], a[140:143], a[32:35], v[36:39]\n"
"v_mfma_f32_16x16x32_bf16 v[36:39], a[184:187], a[48:51], v[36:39]\n"
"v_mfma_f32_16x16x32_bf16 v[44:47], a[184:187], a[52:55], v[44:47]\n"
"ds_read_b64_tr_b16 a[156:157], v203 offset:16384\n"
"v_mfma_f32_16x16x32_bf16 v[44:47], a[140:143], a[36:39], v[44:47]\n"
"ds_read_b64_tr_b16 a[158:159], v203 offset:17408\n"
"v_mfma_f32_16x16x32_bf16 v[52:55], a[140:143], a[40:43], v[52:55]\n"
"ds_read_b64_tr_b16 a[200:201], v203 offset:24576\n"
"v_mfma_f32_16x16x32_bf16 v[52:55], a[184:187], a[56:59], v[52:55]\n"
"ds_read_b64_tr_b16 a[202:203], v203 offset:25600\n"
"v_mfma_f32_16x16x32_bf16 v[60:63], a[184:187], a[60:63], v[60:63]\n"
"s_waitcnt lgkmcnt(5)\n"
"s_add_u32 s14, s73, 0x4000\n"
"v_mfma_f32_16x16x32_bf16 v[60:63], a[140:143], a[44:47], v[60:63]\n"
"s_waitcnt lgkmcnt(9)\n"
"v_mfma_f32_16x16x32_bf16 v[64:67], a[144:147], a[32:35], v[64:67]\n"
"ds_read_b128 v[212:215], v237 offset:8192\n"
"v_mfma_f32_16x16x32_bf16 v[64:67], a[188:191], a[48:51], v[64:67]\n"
"buffer_store_dwordx4 v[208:211], v254, s[96:99], s14 offen offset:3072 sc1\n"
"v_mfma_f32_16x16x32_bf16 v[72:75], a[188:191], a[52:55], v[72:75]\n"
"ds_read_b64_tr_b16 a[160:161], v204 offset:8192\n"
"v_mfma_f32_16x16x32_bf16 v[72:75], a[144:147], a[36:39], v[72:75]\n"
"ds_read_b64_tr_b16 a[162:163], v204 offset:8704\n"
"v_mfma_f32_16x16x32_bf16 v[80:83], a[144:147], a[40:43], v[80:83]\n"
"ds_read_b64_tr_b16 a[204:205], v204 offset:12288\n"
"v_mfma_f32_16x16x32_bf16 v[80:83], a[188:191], a[56:59], v[80:83]\n"
"ds_read_b64_tr_b16 a[206:207], v204 offset:12800\n"
"v_mfma_f32_16x16x32_bf16 v[88:91], a[188:191], a[60:63], v[88:91]\n"
"v_mfma_f32_16x16x32_bf16 v[88:91], a[144:147], a[44:47], v[88:91]\n"
"v_mfma_f32_16x16x32_bf16 v[68:71], a[148:151], a[32:35], v[68:71]\n"
"v_mfma_f32_16x16x32_bf16 v[68:71], a[192:195], a[48:51], v[68:71]\n"
"v_mfma_f32_16x16x32_bf16 v[76:79], a[192:195], a[52:55], v[76:79]\n"
"ds_read_b64_tr_b16 a[164:165], v205 offset:8192\n"
"s_waitcnt lgkmcnt(5)\n"
"s_add_u32 s14, s72, 0x6000\n"
"v_mfma_f32_16x16x32_bf16 v[76:79], a[148:151], a[36:39], v[76:79]\n"
"ds_read_b64_tr_b16 a[166:167], v205 offset:8704\n"
"v_mfma_f32_16x16x32_bf16 v[84:87], a[148:151], a[40:43], v[84:87]\n"
"ds_read_b64_tr_b16 a[208:209], v205 offset:12288\n"
"ds_read_b128 v[216:219], v237 offset:9216\n"
"v_mfma_f32_16x16x32_bf16 v[84:87], a[192:195], a[56:59], v[84:87]\n"
"ds_read_b64_tr_b16 a[210:211], v205 offset:12800\n"
"v_mfma_f32_16x16x32_bf16 v[92:95], a[192:195], a[60:63], v[92:95]\n"
"buffer_store_dwordx4 v[212:215], v254, s[28:31], s14 offen sc1\n"
"v_mfma_f32_16x16x32_bf16 v[92:95], a[148:151], a[44:47], v[92:95]\n"
"s_waitcnt lgkmcnt(10)\n"
"v_mfma_f32_16x16x32_bf16 v[96:99], a[152:155], a[32:35], v[96:99]\n"
"v_mfma_f32_16x16x32_bf16 v[96:99], a[196:199], a[48:51], v[96:99]\n"
"v_mfma_f32_16x16x32_bf16 v[104:107], a[196:199], a[52:55], v[104:107]\n"
"ds_read_b64_tr_b16 a[168:169], v206 offset:8192\n"
"v_mfma_f32_16x16x32_bf16 v[104:107], a[152:155], a[36:39], v[104:107]\n"
"ds_read_b64_tr_b16 a[170:171], v206 offset:8704\n"
"v_mfma_f32_16x16x32_bf16 v[112:115], a[152:155], a[40:43], v[112:115]\n"
"ds_read_b64_tr_b16 a[212:213], v206 offset:12288\n"
"v_mfma_f32_16x16x32_bf16 v[112:115], a[196:199], a[56:59], v[112:115]\n"
"ds_read_b64_tr_b16 a[214:215], v206 offset:12800\n"
"v_mfma_f32_16x16x32_bf16 v[120:123], a[196:199], a[60:63], v[120:123]\n"
"s_waitcnt lgkmcnt(5)\n"
"s_add_u32 s14, s72, 0x6000\n"
"v_mfma_f32_16x16x32_bf16 v[120:123], a[152:155], a[44:47], v[120:123]\n"
"buffer_store_dwordx4 v[216:219], v254, s[28:31], s14 offen offset:1024 sc1\n"
"v_mfma_f32_16x16x32_bf16 v[100:103], a[156:159], a[32:35], v[100:103]\n"
"ds_read_b128 v[220:223], v237 offset:10240\n"
"v_mfma_f32_16x16x32_bf16 v[100:103], a[200:203], a[48:51], v[100:103]\n"
"v_mfma_f32_16x16x32_bf16 v[108:111], a[200:203], a[52:55], v[108:111]\n"
"v_mfma_f32_16x16x32_bf16 v[108:111], a[156:159], a[36:39], v[108:111]\n"
"s_waitcnt vmcnt(18) lgkmcnt(0)\n"
"s_barrier\n"
"v_mfma_f32_16x16x32_bf16 v[116:119], a[156:159], a[40:43], v[116:119]\n"
"ds_read_b64_tr_b16 a[216:217], v204 offset:24576\n"
"v_mfma_f32_16x16x32_bf16 v[116:119], a[200:203], a[56:59], v[116:119]\n"
"ds_read_b64_tr_b16 a[218:219], v204 offset:25088\n"
"v_mfma_f32_16x16x32_bf16 v[124:127], a[200:203], a[60:63], v[124:127]\n"
"ds_read_b64_tr_b16 a[228:229], v204 offset:28672\n"
"v_mfma_f32_16x16x32_bf16 v[124:127], a[156:159], a[44:47], v[124:127]\n"
"ds_read_b64_tr_b16 a[230:231], v204 offset:29184\n"
"s_waitcnt lgkmcnt(9)\n"
"v_mfma_f32_16x16x32_bf16 v[128:131], a[160:163], a[32:35], v[128:131]\n"
"v_mfma_f32_16x16x32_bf16 v[128:131], a[204:207], a[48:51], v[128:131]\n"
"v_mfma_f32_16x16x32_bf16 v[132:135], a[204:207], a[52:55], v[132:135]\n"
"s_waitcnt lgkmcnt(4)\n"
"s_add_u32 s14, s72, 0x7000\n"
"v_mfma_f32_16x16x32_bf16 v[132:135], a[160:163], a[36:39], v[132:135]\n"
"buffer_store_dwordx4 v[220:223], v254, s[28:31], s14 offen sc1\n"
"v_mfma_f32_16x16x32_bf16 v[136:139], a[160:163], a[40:43], v[136:139]\n"
"v_mfma_f32_16x16x32_bf16 v[136:139], a[204:207], a[56:59], v[136:139]\n"
"ds_read_b128 v[208:211], v237 offset:11264\n"
"v_mfma_f32_16x16x32_bf16 v[140:143], a[204:207], a[60:63], v[140:143]\n"
"v_mfma_f32_16x16x32_bf16 v[140:143], a[160:163], a[44:47], v[140:143]\n"
"ds_read_b64_tr_b16 a[204:205], v204 offset:20480\n"
"v_mfma_f32_16x16x32_bf16 v[144:147], a[164:167], a[32:35], v[144:147]\n"
"ds_read_b64_tr_b16 a[206:207], v204 offset:20992\n"
"v_mfma_f32_16x16x32_bf16 v[144:147], a[208:211], a[48:51], v[144:147]\n"
"ds_read_b64_tr_b16 a[160:161], v204 offset:16384\n"
"v_mfma_f32_16x16x32_bf16 v[148:151], a[208:211], a[52:55], v[148:151]\n"
"ds_read_b64_tr_b16 a[162:163], v204 offset:16896\n"
"v_mfma_f32_16x16x32_bf16 v[148:151], a[164:167], a[36:39], v[148:151]\n"
"v_mfma_f32_16x16x32_bf16 v[152:155], a[164:167], a[40:43], v[152:155]\n"
"ds_read_b64_tr_b16 a[220:221], v205 offset:24576\n"
"v_mfma_f32_16x16x32_bf16 v[152:155], a[208:211], a[56:59], v[152:155]\n"
"ds_read_b64_tr_b16 a[222:223], v205 offset:25088\n"
"v_mfma_f32_16x16x32_bf16 v[156:159], a[208:211], a[60:63], v[156:159]\n"
"ds_read_b64_tr_b16 a[232:233], v205 offset:28672\n"
"v_mfma_f32_16x16x32_bf16 v[156:159], a[164:167], a[44:47], v[156:159]\n"
"ds_read_b64_tr_b16 a[234:235], v205 offset:29184\n"
"s_waitcnt lgkmcnt(8)\n"
"s_add_u32 s14, s72, 0x7000\n"
"s_waitcnt lgkmcnt(14)\n"
"v_mfma_f32_16x16x32_bf16 v[160:163], a[168:171], a[32:35], v[160:163]\n"
"ds_read_b64_tr_b16 a[208:209], v205 offset:20480\n"
"v_mfma_f32_16x16x32_bf16 v[160:163], a[212:215], a[48:51], v[160:163]\n"
"ds_read_b64_tr_b16 a[210:211], v205 offset:20992\n"
"v_mfma_f32_16x16x32_bf16 v[164:167], a[212:215], a[52:55], v[164:167]\n"
"ds_read_b64_tr_b16 a[164:165], v205 offset:16384\n"
"v_mfma_f32_16x16x32_bf16 v[164:167], a[168:171], a[36:39], v[164:167]\n"
"ds_read_b64_tr_b16 a[166:167], v205 offset:16896\n"
"v_mfma_f32_16x16x32_bf16 v[168:171], a[168:171], a[40:43], v[168:171]\n"
"buffer_store_dwordx4 v[208:211], v254, s[28:31], s14 offen offset:1024 sc1\n"
"v_mfma_f32_16x16x32_bf16 v[168:171], a[212:215], a[56:59], v[168:171]\n"
"v_mfma_f32_16x16x32_bf16 v[172:175], a[212:215], a[60:63], v[172:175]\n"
"v_mfma_f32_16x16x32_bf16 v[172:175], a[168:171], a[44:47], v[172:175]\n"
"s_waitcnt vmcnt(12) lgkmcnt(8)\n"
"v_mfma_f32_16x16x32_bf16 v[128:131], a[160:163], a[64:67], v[128:131]\n"
"v_mfma_f32_16x16x32_bf16 v[128:131], a[204:207], a[80:83], v[128:131]\n"
"v_mfma_f32_16x16x32_bf16 v[128:131], a[216:219], a[96:99], v[128:131]\n"
"ds_read_b64_tr_b16 a[168:169], v206 offset:16384\n"
"v_mfma_f32_16x16x32_bf16 v[128:131], a[228:231], a[112:115], v[128:131]\n"
"ds_read_b64_tr_b16 a[170:171], v206 offset:16896\n"
"v_mfma_f32_16x16x32_bf16 v[132:135], a[228:231], a[116:119], v[132:135]\n"
"ds_read_b64_tr_b16 a[212:213], v206 offset:20480\n"
"v_mfma_f32_16x16x32_bf16 v[132:135], a[216:219], a[100:103], v[132:135]\n"
"ds_read_b64_tr_b16 a[214:215], v206 offset:20992\n"
"v_mfma_f32_16x16x32_bf16 v[132:135], a[204:207], a[84:87], v[132:135]\n"
"ds_read_b64_tr_b16 a[224:225], v206 offset:24576\n"
"v_mfma_f32_16x16x32_bf16 v[132:135], a[160:163], a[68:71], v[132:135]\n"
"ds_read_b64_tr_b16 a[226:227], v206 offset:25088\n"
"v_mfma_f32_16x16x32_bf16 v[136:139], a[160:163], a[72:75], v[136:139]\n"
"ds_read_b64_tr_b16 a[236:237], v206 offset:28672\n"
"v_mfma_f32_16x16x32_bf16 v[136:139], a[204:207], a[88:91], v[136:139]\n"
"ds_read_b64_tr_b16 a[238:239], v206 offset:29184\n"
"v_mfma_f32_16x16x32_bf16 v[136:139], a[216:219], a[104:107], v[136:139]\n"
"v_mfma_f32_16x16x32_bf16 v[136:139], a[228:231], a[120:123], v[136:139]\n"
"v_mfma_f32_16x16x32_bf16 v[140:143], a[228:231], a[124:127], v[140:143]\n"
"v_mfma_f32_16x16x32_bf16 v[140:143], a[216:219], a[108:111], v[140:143]\n"
"v_mfma_f32_16x16x32_bf16 v[140:143], a[204:207], a[92:95], v[140:143]\n"
"v_mfma_f32_16x16x32_bf16 v[140:143], a[160:163], a[76:79], v[140:143]\n"
"s_waitcnt lgkmcnt(8)\n"
"v_mfma_f32_16x16x32_bf16 v[144:147], a[164:167], a[64:67], v[144:147]\n"
"v_mfma_f32_16x16x32_bf16 v[144:147], a[208:211], a[80:83], v[144:147]\n"
"v_mfma_f32_16x16x32_bf16 v[144:147], a[220:223], a[96:99], v[144:147]\n"
"ds_read_b64_tr_b16 a[128:129], v196 offset:32768\n"
"v_mfma_f32_16x16x32_bf16 v[144:147], a[232:235], a[112:115], v[144:147]\n"
"ds_read_b64_tr_b16 a[130:131], v196 offset:33792\n"
"v_mfma_f32_16x16x32_bf16 v[148:151], a[232:235], a[116:119], v[148:151]\n"
"ds_read_b64_tr_b16 a[172:173], v196 offset:40960\n"
"v_mfma_f32_16x16x32_bf16 v[148:151], a[220:223], a[100:103], v[148:151]\n"
"ds_read_b64_tr_b16 a[174:175], v196 offset:41984\n"
"v_mfma_f32_16x16x32_bf16 v[148:151], a[208:211], a[84:87], v[148:151]\n"
"v_mfma_f32_16x16x32_bf16 v[148:151], a[164:167], a[68:71], v[148:151]\n"
"v_mfma_f32_16x16x32_bf16 v[152:155], a[164:167], a[72:75], v[152:155]\n"
"v_mfma_f32_16x16x32_bf16 v[152:155], a[208:211], a[88:91], v[152:155]\n"
"v_mfma_f32_16x16x32_bf16 v[152:155], a[220:223], a[104:107], v[152:155]\n"
"ds_read_b64_tr_b16 a[132:133], v197 offset:32768\n"
"v_mfma_f32_16x16x32_bf16 v[152:155], a[232:235], a[120:123], v[152:155]\n"
"ds_read_b64_tr_b16 a[134:135], v197 offset:33792\n"
"v_mfma_f32_16x16x32_bf16 v[156:159], a[232:235], a[124:127], v[156:159]\n"
"ds_read_b64_tr_b16 a[176:177], v197 offset:40960\n"
"v_mfma_f32_16x16x32_bf16 v[156:159], a[220:223], a[108:111], v[156:159]\n"
"ds_read_b64_tr_b16 a[178:179], v197 offset:41984\n"
"v_mfma_f32_16x16x32_bf16 v[156:159], a[208:211], a[92:95], v[156:159]\n"
"v_mfma_f32_16x16x32_bf16 v[156:159], a[164:167], a[76:79], v[156:159]\n"
"s_waitcnt lgkmcnt(8)\n"
"v_mfma_f32_16x16x32_bf16 v[160:163], a[168:171], a[64:67], v[160:163]\n"
"v_mfma_f32_16x16x32_bf16 v[160:163], a[212:215], a[80:83], v[160:163]\n"
"v_mfma_f32_16x16x32_bf16 v[160:163], a[224:227], a[96:99], v[160:163]\n"
"ds_read_b64_tr_b16 a[136:137], v198 offset:32768\n"
"v_mfma_f32_16x16x32_bf16 v[160:163], a[236:239], a[112:115], v[160:163]\n"
"ds_read_b64_tr_b16 a[138:139], v198 offset:33792\n"
"v_mfma_f32_16x16x32_bf16 v[164:167], a[236:239], a[116:119], v[164:167]\n"
"ds_read_b64_tr_b16 a[180:181], v198 offset:40960\n"
"v_mfma_f32_16x16x32_bf16 v[164:167], a[224:227], a[100:103], v[164:167]\n"
"ds_read_b64_tr_b16 a[182:183], v198 offset:41984\n"
"v_mfma_f32_16x16x32_bf16 v[164:167], a[212:215], a[84:87], v[164:167]\n"
"v_mfma_f32_16x16x32_bf16 v[164:167], a[168:171], a[68:71], v[164:167]\n"
"v_mfma_f32_16x16x32_bf16 v[168:171], a[168:171], a[72:75], v[168:171]\n"
"v_mfma_f32_16x16x32_bf16 v[168:171], a[212:215], a[88:91], v[168:171]\n"
"v_mfma_f32_16x16x32_bf16 v[168:171], a[224:227], a[104:107], v[168:171]\n"
"ds_read_b64_tr_b16 a[140:141], v199 offset:32768\n"
"v_mfma_f32_16x16x32_bf16 v[168:171], a[236:239], a[120:123], v[168:171]\n"
"ds_read_b64_tr_b16 a[142:143], v199 offset:33792\n"
"v_mfma_f32_16x16x32_bf16 v[172:175], a[236:239], a[124:127], v[172:175]\n"
"ds_read_b64_tr_b16 a[184:185], v199 offset:40960\n"
"v_mfma_f32_16x16x32_bf16 v[172:175], a[224:227], a[108:111], v[172:175]\n"
"ds_read_b64_tr_b16 a[186:187], v199 offset:41984\n"
"v_mfma_f32_16x16x32_bf16 v[172:175], a[212:215], a[92:95], v[172:175]\n"
"v_mfma_f32_16x16x32_bf16 v[172:175], a[168:171], a[76:79], v[172:175]\n"
"s_waitcnt vmcnt(33) lgkmcnt(8)\n"
"v_mfma_f32_16x16x32_bf16 v[0:3], a[128:131], a[64:67], v[0:3]\n"
"v_mfma_f32_16x16x32_bf16 v[0:3], a[172:175], a[80:83], v[0:3]\n"
"v_mfma_f32_16x16x32_bf16 v[8:11], a[172:175], a[84:87], v[8:11]\n"
"ds_read_b64_tr_b16 a[144:145], v200 offset:32768\n"
"v_mul_f32 v176, s80, v128\n"
"v_mfma_f32_16x16x32_bf16 v[8:11], a[128:131], a[68:71], v[8:11]\n"
"ds_read_b64_tr_b16 a[146:147], v200 offset:33792\n"
"v_mul_f32 v177, s80, v129\n"
"v_mfma_f32_16x16x32_bf16 v[16:19], a[128:131], a[72:75], v[16:19]\n"
"ds_read_b64_tr_b16 a[188:189], v200 offset:40960\n"
"v_mul_f32 v178, s80, v130\n"
"v_mfma_f32_16x16x32_bf16 v[16:19], a[172:175], a[88:91], v[16:19]\n"
"ds_read_b64_tr_b16 a[190:191], v200 offset:41984\n"
"v_mul_f32 v179, s80, v131\n"
"v_mfma_f32_16x16x32_bf16 v[24:27], a[172:175], a[92:95], v[24:27]\n"
"v_exp_f32 v176, v176\n"
"v_mfma_f32_16x16x32_bf16 v[24:27], a[128:131], a[76:79], v[24:27]\n"
"v_exp_f32 v177, v177\n"
"v_mfma_f32_16x16x32_bf16 v[4:7], a[132:135], a[64:67], v[4:7]\n"
"v_exp_f32 v178, v178\n"
"v_mfma_f32_16x16x32_bf16 v[4:7], a[176:179], a[80:83], v[4:7]\n"
"v_exp_f32 v179, v179\n"
"v_mfma_f32_16x16x32_bf16 v[12:15], a[176:179], a[84:87], v[12:15]\n"
"ds_read_b64_tr_b16 a[148:149], v201 offset:32768\n"
"v_add_f32 v176, 1.0, v176\n"
"v_mfma_f32_16x16x32_bf16 v[12:15], a[132:135], a[68:71], v[12:15]\n"
"ds_read_b64_tr_b16 a[150:151], v201 offset:33792\n"
"v_add_f32 v177, 1.0, v177\n"
"v_mfma_f32_16x16x32_bf16 v[20:23], a[132:135], a[72:75], v[20:23]\n"
"ds_read_b64_tr_b16 a[192:193], v201 offset:40960\n"
"v_add_f32 v178, 1.0, v178\n"
"v_mfma_f32_16x16x32_bf16 v[20:23], a[176:179], a[88:91], v[20:23]\n"
"ds_read_b64_tr_b16 a[194:195], v201 offset:41984\n"
"v_add_f32 v179, 1.0, v179\n"
"v_mfma_f32_16x16x32_bf16 v[28:31], a[176:179], a[92:95], v[28:31]\n"
"v_fma_f32 v180, v176, v176, 1.0\n"
"v_fma_f32 v181, v177, v177, 1.0\n"
"v_mfma_f32_16x16x32_bf16 v[28:31], a[132:135], a[76:79], v[28:31]\n"
"v_fma_f32 v182, v178, v178, 1.0\n"
"s_waitcnt lgkmcnt(8)\n"
"v_mfma_f32_16x16x32_bf16 v[32:35], a[136:139], a[64:67], v[32:35]\n"
"v_fma_f32 v183, v179, v179, 1.0\n"
"v_mfma_f32_16x16x32_bf16 v[32:35], a[180:183], a[80:83], v[32:35]\n"
"v_rcp_f32 v180, v180\n"
"v_mfma_f32_16x16x32_bf16 v[40:43], a[180:183], a[84:87], v[40:43]\n"
"ds_read_b64_tr_b16 a[152:153], v202 offset:32768\n"
"v_mfma_f32_16x16x32_bf16 v[40:43], a[136:139], a[68:71], v[40:43]\n"
"ds_read_b64_tr_b16 a[154:155], v202 offset:33792\n"
"v_mfma_f32_16x16x32_bf16 v[48:51], a[136:139], a[72:75], v[48:51]\n"
"ds_read_b64_tr_b16 a[196:197], v202 offset:40960\n"
"v_mfma_f32_16x16x32_bf16 v[48:51], a[180:183], a[88:91], v[48:51]\n"
"ds_read_b64_tr_b16 a[198:199], v202 offset:41984\n"
"v_mfma_f32_16x16x32_bf16 v[56:59], a[180:183], a[92:95], v[56:59]\n"
"v_rcp_f32 v181, v181\n"
"v_mfma_f32_16x16x32_bf16 v[56:59], a[136:139], a[76:79], v[56:59]\n"
"v_rcp_f32 v182, v182\n"
"v_mfma_f32_16x16x32_bf16 v[36:39], a[140:143], a[64:67], v[36:39]\n"
"v_rcp_f32 v183, v183\n"
"v_mfma_f32_16x16x32_bf16 v[36:39], a[184:187], a[80:83], v[36:39]\n"
"v_fma_f32 v180, v180, v240, v242\n"
"v_fma_f32 v181, v181, v240, v242\n"
"v_mfma_f32_16x16x32_bf16 v[44:47], a[184:187], a[84:87], v[44:47]\n"
"ds_read_b64_tr_b16 a[156:157], v203 offset:32768\n"
"v_fma_f32 v182, v182, v240, v242\n"
"v_mfma_f32_16x16x32_bf16 v[44:47], a[140:143], a[68:71], v[44:47]\n"
"ds_read_b64_tr_b16 a[158:159], v203 offset:33792\n"
"v_fma_f32 v183, v183, v240, v242\n"
"v_mfma_f32_16x16x32_bf16 v[52:55], a[140:143], a[72:75], v[52:55]\n"
"ds_read_b64_tr_b16 a[200:201], v203 offset:40960\n"
"v_mul_f32 v180, v180, v128\n"
"v_mfma_f32_16x16x32_bf16 v[52:55], a[184:187], a[88:91], v[52:55]\n"
"ds_read_b64_tr_b16 a[202:203], v203 offset:41984\n"
"v_mul_f32 v181, v181, v129\n"
"v_mfma_f32_16x16x32_bf16 v[60:63], a[184:187], a[92:95], v[60:63]\n"
"v_mul_f32 v182, v182, v130\n"
"v_mul_f32 v183, v183, v131\n"
"v_mfma_f32_16x16x32_bf16 v[60:63], a[140:143], a[76:79], v[60:63]\n"
"v_cvt_pk_bf16_f32 v192, v180, v181\n"
"s_waitcnt lgkmcnt(8)\n"
"v_mfma_f32_16x16x32_bf16 v[64:67], a[144:147], a[64:67], v[64:67]\n"
"v_cvt_pk_bf16_f32 v193, v182, v183\n"
"v_mfma_f32_16x16x32_bf16 v[64:67], a[188:191], a[80:83], v[64:67]\n"
"buffer_store_dwordx2 v[192:193], v247, s[36:39], s82 offen sc1\n"
"v_mfma_f32_16x16x32_bf16 v[72:75], a[188:191], a[84:87], v[72:75]\n"
"ds_read_b64_tr_b16 a[128:129], v196 offset:49152\n"
"v_mul_f32 v184, s80, v132\n"
"v_mfma_f32_16x16x32_bf16 v[72:75], a[144:147], a[68:71], v[72:75]\n"
"ds_read_b64_tr_b16 a[130:131], v196 offset:50176\n"
"v_mul_f32 v185, s80, v133\n"
"v_mfma_f32_16x16x32_bf16 v[80:83], a[144:147], a[72:75], v[80:83]\n"
"ds_read_b64_tr_b16 a[172:173], v196 offset:57344\n"
"v_mul_f32 v186, s80, v134\n"
"v_mfma_f32_16x16x32_bf16 v[80:83], a[188:191], a[88:91], v[80:83]\n"
"ds_read_b64_tr_b16 a[174:175], v196 offset:58368\n"
"v_mul_f32 v187, s80, v135\n"
"v_mfma_f32_16x16x32_bf16 v[88:91], a[188:191], a[92:95], v[88:91]\n"
"v_exp_f32 v184, v184\n"
"v_mfma_f32_16x16x32_bf16 v[88:91], a[144:147], a[76:79], v[88:91]\n"
"v_exp_f32 v185, v185\n"
"v_mfma_f32_16x16x32_bf16 v[68:71], a[148:151], a[64:67], v[68:71]\n"
"v_exp_f32 v186, v186\n"
"v_mfma_f32_16x16x32_bf16 v[68:71], a[192:195], a[80:83], v[68:71]\n"
"v_exp_f32 v187, v187\n"
"v_mfma_f32_16x16x32_bf16 v[76:79], a[192:195], a[84:87], v[76:79]\n"
"ds_read_b64_tr_b16 a[132:133], v197 offset:49152\n"
"v_add_f32 v184, 1.0, v184\n"
"v_mfma_f32_16x16x32_bf16 v[76:79], a[148:151], a[68:71], v[76:79]\n"
"ds_read_b64_tr_b16 a[134:135], v197 offset:50176\n"
"v_mfma_f32_16x16x32_bf16 v[84:87], a[148:151], a[72:75], v[84:87]\n"
"ds_read_b64_tr_b16 a[176:177], v197 offset:57344\n"
"s_waitcnt vmcnt(1)\n"
"s_barrier\n"
"s_mov_b64 exec, s[34:35]\n"
"buffer_store_dwordx2 v[250:251], v249, s[92:95], s89 offen sc1\n"
"s_mov_b64 exec, -1\n"
"v_mfma_f32_16x16x32_bf16 v[84:87], a[192:195], a[88:91], v[84:87]\n"
"ds_read_b64_tr_b16 a[178:179], v197 offset:58368\n"
"v_add_f32 v185, 1.0, v185\n"
"v_mfma_f32_16x16x32_bf16 v[92:95], a[192:195], a[92:95], v[92:95]\n"
"v_add_f32 v186, 1.0, v186\n"
"v_add_f32 v187, 1.0, v187\n"
"v_mfma_f32_16x16x32_bf16 v[92:95], a[148:151], a[76:79], v[92:95]\n"
"v_fma_f32 v188, v184, v184, 1.0\n"
"s_waitcnt lgkmcnt(8)\n"
"v_mfma_f32_16x16x32_bf16 v[96:99], a[152:155], a[64:67], v[96:99]\n"
"v_fma_f32 v189, v185, v185, 1.0\n"
"v_fma_f32 v190, v186, v186, 1.0\n"
"v_mfma_f32_16x16x32_bf16 v[96:99], a[196:199], a[80:83], v[96:99]\n"
"v_fma_f32 v191, v187, v187, 1.0\n"
"v_mfma_f32_16x16x32_bf16 v[104:107], a[196:199], a[84:87], v[104:107]\n"
"ds_read_b64_tr_b16 a[136:137], v198 offset:49152\n"
"v_mfma_f32_16x16x32_bf16 v[104:107], a[152:155], a[68:71], v[104:107]\n"
"ds_read_b64_tr_b16 a[138:139], v198 offset:50176\n"
"v_mfma_f32_16x16x32_bf16 v[112:115], a[152:155], a[72:75], v[112:115]\n"
"ds_read_b64_tr_b16 a[180:181], v198 offset:57344\n"
"v_mfma_f32_16x16x32_bf16 v[112:115], a[196:199], a[88:91], v[112:115]\n"
"ds_read_b64_tr_b16 a[182:183], v198 offset:58368\n"
"v_mfma_f32_16x16x32_bf16 v[120:123], a[196:199], a[92:95], v[120:123]\n"
"v_rcp_f32 v188, v188\n"
"v_mfma_f32_16x16x32_bf16 v[120:123], a[152:155], a[76:79], v[120:123]\n"
"v_rcp_f32 v189, v189\n"
"v_mfma_f32_16x16x32_bf16 v[100:103], a[156:159], a[64:67], v[100:103]\n"
"v_rcp_f32 v190, v190\n"
"v_mfma_f32_16x16x32_bf16 v[100:103], a[200:203], a[80:83], v[100:103]\n"
"v_rcp_f32 v191, v191\n"
"v_mfma_f32_16x16x32_bf16 v[108:111], a[200:203], a[84:87], v[108:111]\n"
"ds_read_b64_tr_b16 a[140:141], v199 offset:49152\n"
"v_fma_f32 v188, v188, v240, v242\n"
"v_mfma_f32_16x16x32_bf16 v[108:111], a[156:159], a[68:71], v[108:111]\n"
"ds_read_b64_tr_b16 a[142:143], v199 offset:50176\n"
"v_fma_f32 v189, v189, v240, v242\n"
"v_mfma_f32_16x16x32_bf16 v[116:119], a[156:159], a[72:75], v[116:119]\n"
"ds_read_b64_tr_b16 a[184:185], v199 offset:57344\n"
"v_fma_f32 v190, v190, v240, v242\n"
"v_mfma_f32_16x16x32_bf16 v[116:119], a[200:203], a[88:91], v[116:119]\n"
"ds_read_b64_tr_b16 a[186:187], v199 offset:58368\n"
"v_fma_f32 v191, v191, v240, v242\n"
"v_mfma_f32_16x16x32_bf16 v[124:127], a[200:203], a[92:95], v[124:127]\n"
"v_mul_f32 v188, v188, v132\n"
"v_mul_f32 v189, v189, v133\n"
"v_mfma_f32_16x16x32_bf16 v[124:127], a[156:159], a[76:79], v[124:127]\n"
"v_mul_f32 v190, v190, v134\n"
"s_waitcnt vmcnt(14) lgkmcnt(8)\n"
"v_mfma_f32_16x16x32_bf16 v[0:3], a[128:131], a[96:99], v[0:3]\n"
"v_mul_f32 v191, v191, v135\n"
"v_cvt_pk_bf16_f32 v194, v188, v189\n"
"v_mfma_f32_16x16x32_bf16 v[0:3], a[172:175], a[112:115], v[0:3]\n"
"v_cvt_pk_bf16_f32 v195, v190, v191\n"
"v_mfma_f32_16x16x32_bf16 v[8:11], a[172:175], a[116:119], v[8:11]\n"
"ds_read_b64_tr_b16 a[144:145], v200 offset:49152\n"
"v_mfma_f32_16x16x32_bf16 v[8:11], a[128:131], a[100:103], v[8:11]\n"
"ds_read_b64_tr_b16 a[146:147], v200 offset:50176\n"
"v_mfma_f32_16x16x32_bf16 v[16:19], a[128:131], a[104:107], v[16:19]\n"
"ds_read_b64_tr_b16 a[188:189], v200 offset:57344\n"
"v_mfma_f32_16x16x32_bf16 v[16:19], a[172:175], a[120:123], v[16:19]\n"
"ds_read_b64_tr_b16 a[190:191], v200 offset:58368\n"
"v_mfma_f32_16x16x32_bf16 v[24:27], a[172:175], a[124:127], v[24:27]\n"
"buffer_store_dwordx2 v[194:195], v247, s[36:39], s82 offen offset:512 sc1\n"
"v_mfma_f32_16x16x32_bf16 v[24:27], a[128:131], a[108:111], v[24:27]\n"
"v_mul_f32 v176, s80, v136\n"
"v_mul_f32 v177, s80, v137\n"
"v_mfma_f32_16x16x32_bf16 v[4:7], a[132:135], a[96:99], v[4:7]\n"
"v_mul_f32 v178, s80, v138\n"
"v_mul_f32 v179, s80, v139\n"
"v_mfma_f32_16x16x32_bf16 v[4:7], a[176:179], a[112:115], v[4:7]\n"
"v_exp_f32 v176, v176\n"
"v_mfma_f32_16x16x32_bf16 v[12:15], a[176:179], a[116:119], v[12:15]\n"
"ds_read_b64_tr_b16 a[148:149], v201 offset:49152\n"
"v_mfma_f32_16x16x32_bf16 v[12:15], a[132:135], a[100:103], v[12:15]\n"
"ds_read_b64_tr_b16 a[150:151], v201 offset:50176\n"
"v_mfma_f32_16x16x32_bf16 v[20:23], a[132:135], a[104:107], v[20:23]\n"
"ds_read_b64_tr_b16 a[192:193], v201 offset:57344\n"
"v_mfma_f32_16x16x32_bf16 v[20:23], a[176:179], a[120:123], v[20:23]\n"
"ds_read_b64_tr_b16 a[194:195], v201 offset:58368\n"
"v_mfma_f32_16x16x32_bf16 v[28:31], a[176:179], a[124:127], v[28:31]\n"
"v_exp_f32 v177, v177\n"
"v_mfma_f32_16x16x32_bf16 v[28:31], a[132:135], a[108:111], v[28:31]\n"
"s_waitcnt lgkmcnt(8)\n"
"v_mfma_f32_16x16x32_bf16 v[32:35], a[136:139], a[96:99], v[32:35]\n"
"v_exp_f32 v178, v178\n"
"v_mfma_f32_16x16x32_bf16 v[32:35], a[180:183], a[112:115], v[32:35]\n"
"v_exp_f32 v179, v179\n"
"v_mfma_f32_16x16x32_bf16 v[40:43], a[180:183], a[116:119], v[40:43]\n"
"ds_read_b64_tr_b16 a[152:153], v202 offset:49152\n"
"v_add_f32 v176, 1.0, v176\n"
"v_mfma_f32_16x16x32_bf16 v[40:43], a[136:139], a[100:103], v[40:43]\n"
"ds_read_b64_tr_b16 a[154:155], v202 offset:50176\n"
"v_add_f32 v177, 1.0, v177\n"
"v_mfma_f32_16x16x32_bf16 v[48:51], a[136:139], a[104:107], v[48:51]\n"
"ds_read_b64_tr_b16 a[196:197], v202 offset:57344\n"
"v_add_f32 v178, 1.0, v178\n"
"v_mfma_f32_16x16x32_bf16 v[48:51], a[180:183], a[120:123], v[48:51]\n"
"ds_read_b64_tr_b16 a[198:199], v202 offset:58368\n"
"v_add_f32 v179, 1.0, v179\n"
"v_mfma_f32_16x16x32_bf16 v[56:59], a[180:183], a[124:127], v[56:59]\n"
"v_fma_f32 v180, v176, v176, 1.0\n"
"v_fma_f32 v181, v177, v177, 1.0\n"
"v_mfma_f32_16x16x32_bf16 v[56:59], a[136:139], a[108:111], v[56:59]\n"
"v_fma_f32 v182, v178, v178, 1.0\n"
"v_fma_f32 v183, v179, v179, 1.0\n"
"v_mfma_f32_16x16x32_bf16 v[36:39], a[140:143], a[96:99], v[36:39]\n"
"v_rcp_f32 v180, v180\n"
"v_mfma_f32_16x16x32_bf16 v[36:39], a[184:187], a[112:115], v[36:39]\n"
"v_rcp_f32 v181, v181\n"
"v_mfma_f32_16x16x32_bf16 v[44:47], a[184:187], a[116:119], v[44:47]\n"
"ds_read_b64_tr_b16 a[156:157], v203 offset:49152\n"
"v_mfma_f32_16x16x32_bf16 v[44:47], a[140:143], a[100:103], v[44:47]\n"
"ds_read_b64_tr_b16 a[158:159], v203 offset:50176\n"
"v_mfma_f32_16x16x32_bf16 v[52:55], a[140:143], a[104:107], v[52:55]\n"
"ds_read_b64_tr_b16 a[200:201], v203 offset:57344\n"
"v_mfma_f32_16x16x32_bf16 v[52:55], a[184:187], a[120:123], v[52:55]\n"
"ds_read_b64_tr_b16 a[202:203], v203 offset:58368\n"
"v_mfma_f32_16x16x32_bf16 v[60:63], a[184:187], a[124:127], v[60:63]\n"
"v_rcp_f32 v182, v182\n"
"v_mfma_f32_16x16x32_bf16 v[60:63], a[140:143], a[108:111], v[60:63]\n"
"s_waitcnt lgkmcnt(8)\n"
"v_mfma_f32_16x16x32_bf16 v[64:67], a[144:147], a[96:99], v[64:67]\n"
"v_rcp_f32 v183, v183\n"
"v_mfma_f32_16x16x32_bf16 v[64:67], a[188:191], a[112:115], v[64:67]\n"
"v_fma_f32 v180, v180, v240, v242\n"
"v_fma_f32 v181, v181, v240, v242\n"
"v_mfma_f32_16x16x32_bf16 v[72:75], a[188:191], a[116:119], v[72:75]\n"
"v_fma_f32 v182, v182, v240, v242\n"
"v_fma_f32 v183, v183, v240, v242\n"
"v_mfma_f32_16x16x32_bf16 v[72:75], a[144:147], a[100:103], v[72:75]\n"
"v_mul_f32 v180, v180, v136\n"
"v_mul_f32 v181, v181, v137\n"
"v_mfma_f32_16x16x32_bf16 v[80:83], a[144:147], a[104:107], v[80:83]\n"
"v_mul_f32 v182, v182, v138\n"
"v_mul_f32 v183, v183, v139\n"
"v_mfma_f32_16x16x32_bf16 v[80:83], a[188:191], a[120:123], v[80:83]\n"
"v_cvt_pk_bf16_f32 v192, v180, v181\n"
"v_cvt_pk_bf16_f32 v193, v182, v183\n"
"v_mfma_f32_16x16x32_bf16 v[88:91], a[188:191], a[124:127], v[88:91]\n"
"buffer_store_dwordx2 v[192:193], v247, s[36:39], s82 offen offset:1024 sc1\n"
"v_mfma_f32_16x16x32_bf16 v[88:91], a[144:147], a[108:111], v[88:91]\n"
"v_mul_f32 v184, s80, v140\n"
"v_mul_f32 v185, s80, v141\n"
"v_mfma_f32_16x16x32_bf16 v[68:71], a[148:151], a[96:99], v[68:71]\n"
"v_mul_f32 v186, s80, v142\n"
"v_mul_f32 v187, s80, v143\n"
"v_mfma_f32_16x16x32_bf16 v[68:71], a[192:195], a[112:115], v[68:71]\n"
"v_exp_f32 v184, v184\n"
"v_mfma_f32_16x16x32_bf16 v[76:79], a[192:195], a[116:119], v[76:79]\n"
"v_exp_f32 v185, v185\n"
"v_mfma_f32_16x16x32_bf16 v[76:79], a[148:151], a[100:103], v[76:79]\n"
"v_exp_f32 v186, v186\n"
"v_mfma_f32_16x16x32_bf16 v[84:87], a[148:151], a[104:107], v[84:87]\n"
"v_exp_f32 v187, v187\n"
"v_mfma_f32_16x16x32_bf16 v[84:87], a[192:195], a[120:123], v[84:87]\n"
"v_add_f32 v184, 1.0, v184\n"
"v_add_f32 v185, 1.0, v185\n"
"v_mfma_f32_16x16x32_bf16 v[92:95], a[192:195], a[124:127], v[92:95]\n"
"v_add_f32 v186, 1.0, v186\n"
"v_add_f32 v187, 1.0, v187\n"
"v_mfma_f32_16x16x32_bf16 v[92:95], a[148:151], a[108:111], v[92:95]\n"
"v_fma_f32 v188, v184, v184, 1.0\n"
"s_waitcnt lgkmcnt(0)\n"
"v_mfma_f32_16x16x32_bf16 v[96:99], a[152:155], a[96:99], v[96:99]\n"
"v_fma_f32 v189, v185, v185, 1.0\n"
"v_fma_f32 v190, v186, v186, 1.0\n"
"v_mfma_f32_16x16x32_bf16 v[96:99], a[196:199], a[112:115], v[96:99]\n"
"v_fma_f32 v191, v187, v187, 1.0\n"
"v_mfma_f32_16x16x32_bf16 v[104:107], a[196:199], a[116:119], v[104:107]\n"
"v_rcp_f32 v188, v188\n"
"v_mfma_f32_16x16x32_bf16 v[104:107], a[152:155], a[100:103], v[104:107]\n"
"v_rcp_f32 v189, v189\n"
"v_mfma_f32_16x16x32_bf16 v[112:115], a[152:155], a[104:107], v[112:115]\n"
"buffer_load_dwordx2 v[238:239], v207, s[92:95], s54 offen sc1\n"
"v_mfma_f32_16x16x32_bf16 v[112:115], a[196:199], a[120:123], v[112:115]\n"
"v_rcp_f32 v190, v190\n"
"v_mfma_f32_16x16x32_bf16 v[120:123], a[196:199], a[124:127], v[120:123]\n"
"v_rcp_f32 v191, v191\n"
"v_mfma_f32_16x16x32_bf16 v[120:123], a[152:155], a[108:111], v[120:123]\n"
"v_fma_f32 v188, v188, v240, v242\n"
"v_fma_f32 v189, v189, v240, v242\n"
"v_mfma_f32_16x16x32_bf16 v[100:103], a[156:159], a[96:99], v[100:103]\n"
"v_fma_f32 v190, v190, v240, v242\n"
"v_fma_f32 v191, v191, v240, v242\n"
"v_mfma_f32_16x16x32_bf16 v[100:103], a[200:203], a[112:115], v[100:103]\n"
"v_mul_f32 v188, v188, v140\n"
"v_mul_f32 v189, v189, v141\n"
"v_mfma_f32_16x16x32_bf16 v[108:111], a[200:203], a[116:119], v[108:111]\n"
"v_mul_f32 v190, v190, v142\n"
"v_mul_f32 v191, v191, v143\n"
"v_mfma_f32_16x16x32_bf16 v[108:111], a[156:159], a[100:103], v[108:111]\n"
"v_cvt_pk_bf16_f32 v194, v188, v189\n"
"v_cvt_pk_bf16_f32 v195, v190, v191\n"
"v_mfma_f32_16x16x32_bf16 v[116:119], a[156:159], a[104:107], v[116:119]\n"
"buffer_store_dwordx2 v[194:195], v247, s[36:39], s82 offen offset:1536 sc1\n"
"v_mfma_f32_16x16x32_bf16 v[116:119], a[200:203], a[120:123], v[116:119]\n"
"v_mfma_f32_16x16x32_bf16 v[124:127], a[200:203], a[124:127], v[124:127]\n"
"v_mfma_f32_16x16x32_bf16 v[124:127], a[156:159], a[108:111], v[124:127]\n"
"s_lshl_b32 s72, s8, 12\n"
"s_lshl_b32 s73, s10, 19\n"
"s_add_u32 s73, s73, s72\n"
"s_lshl_b32 s65, s11, 1\n"
"s_add_u32 s66, s65, 2\n"
"s_add_u32 s65, s65, 1\n"
"s_mov_b32 s62, 0\n"
"s_mov_b64 s[90:91], 0\n"
"s_or_b32 s12, s9, s8\n"
"s_cmp_lg_u32 s12, 0\n"
"s_cbranch_scc1 .Lnoep_3_%=\n"
"v_lshlrev_b32 v224, 3, v255\n"
"s_mov_b32 s84, 0x800\n"
"buffer_load_dwordx2 v[226:227], v224, s[92:95], s84 offen sc1\n"
"s_mov_b32 s85, 0xa00\n"
"buffer_load_dwordx2 v[228:229], v224, s[92:95], s85 offen sc1\n"
"s_mov_b32 s86, 0xc00\n"
"buffer_load_dwordx2 v[230:231], v224, s[92:95], s86 offen sc1\n"
"s_mov_b32 s87, 0xe00\n"
"buffer_load_dwordx2 v[232:233], v224, s[92:95], s87 offen sc1\n"
"s_waitcnt vmcnt(0)\n"
"v_cmp_eq_u64 vcc, v[226:227], v[250:251]\n"
"s_nop 1\n"
"s_cmp_eq_u64 vcc, exec\n"
"s_cbranch_scc1 .Lpok_4_%=\n"
"s_mov_b32 s15, 0\n"
".Lploop_5_%=:\n"
"s_sleep 4\n"
"buffer_load_dwordx2 v[226:227], v224, s[92:95], s84 offen sc1\n"
"s_waitcnt vmcnt(0)\n"
"v_cmp_eq_u64 vcc, v[226:227], v[250:251]\n"
"s_add_u32 s15, s15, 1\n"
"s_nop 0\n"
"s_cmp_eq_u64 vcc, exec\n"
"s_cbranch_scc1 .Lpok_4_%=\n"
"s_cmp_lt_u32 s15, 0x20000\n"
"s_cbranch_scc1 .Lploop_5_%=\n"
"s_mov_b32 s15, 0x80\n"
"s_mov_b64 exec, 1\n"
"buffer_store_dwordx2 v[250:251], v249, s[92:95], s15 offen sc1\n"
"s_mov_b64 exec, -1\n"
".Lpok_4_%=:\n"
"s_waitcnt vmcnt(0)\n"
"v_cmp_eq_u64 vcc, v[228:229], v[250:251]\n"
"s_nop 1\n"
"s_cmp_eq_u64 vcc, exec\n"
"s_cbranch_scc1 .Lpok_6_%=\n"
"s_mov_b32 s15, 0\n"
".Lploop_7_%=:\n"
"s_sleep 4\n"
"buffer_load_dwordx2 v[228:229], v224, s[92:95], s85 offen sc1\n"
"s_waitcnt vmcnt(0)\n"
"v_cmp_eq_u64 vcc, v[228:229], v[250:251]\n"
"s_add_u32 s15, s15, 1\n"
"s_nop 0\n"
"s_cmp_eq_u64 vcc, exec\n"
"s_cbranch_scc1 .Lpok_6_%=\n"
"s_cmp_lt_u32 s15, 0x20000\n"
"s_cbranch_scc1 .Lploop_7_%=\n"
"s_mov_b32 s15, 0x80\n"
"s_mov_b64 exec, 1\n"
"buffer_store_dwordx2 v[250:251], v249, s[92:95], s15 offen sc1\n"
"s_mov_b64 exec, -1\n"
".Lpok_6_%=:\n"
"s_waitcnt vmcnt(0)\n"
"v_cmp_eq_u64 vcc, v[230:231], v[250:251]\n"
"s_nop 1\n"
"s_cmp_eq_u64 vcc, exec\n"
"s_cbranch_scc1 .Lpok_8_%=\n"
"s_mov_b32 s15, 0\n"
".Lploop_9_%=:\n"
"s_sleep 4\n"
"buffer_load_dwordx2 v[230:231], v224, s[92:95], s86 offen sc1\n"
"s_waitcnt vmcnt(0)\n"
"v_cmp_eq_u64 vcc, v[230:231], v[250:251]\n"
"s_add_u32 s15, s15, 1\n"
"s_nop 0\n"
"s_cmp_eq_u64 vcc, exec\n"
"s_cbranch_scc1 .Lpok_8_%=\n"
"s_cmp_lt_u32 s15, 0x20000\n"
"s_cbranch_scc1 .Lploop_9_%=\n"
"s_mov_b32 s15, 0x80\n"
"s_mov_b64 exec, 1\n"
"buffer_store_dwordx2 v[250:251], v249, s[92:95], s15 offen sc1\n"
"s_mov_b64 exec, -1\n"
".Lpok_8_%=:\n"
"s_waitcnt vmcnt(0)\n"
"v_cmp_eq_u64 vcc, v[232:233], v[250:251]\n"
"s_nop 1\n"
"s_cmp_eq_u64 vcc, exec\n"
"s_cbranch_scc1 .Lpok_10_%=\n"
"s_mov_b32 s15, 0\n"
".Lploop_11_%=:\n"
"s_sleep 4\n"
"buffer_load_dwordx2 v[232:233], v224, s[92:95], s87 offen sc1\n"
"s_waitcnt vmcnt(0)\n"
"v_cmp_eq_u64 vcc, v[232:233], v[250:251]\n"
"s_add_u32 s15, s15, 1\n"
"s_nop 0\n"
"s_cmp_eq_u64 vcc, exec\n"
"s_cbranch_scc1 .Lpok_10_%=\n"
"s_cmp_lt_u32 s15, 0x20000\n"
"s_cbranch_scc1 .Lploop_11_%=\n"
"s_mov_b32 s15, 0x80\n"
"s_mov_b64 exec, 1\n"
"buffer_store_dwordx2 v[250:251], v249, s[92:95], s15 offen sc1\n"
"s_mov_b64 exec, -1\n"
".Lpok_10_%=:\n"
"s_mov_b32 s84, 0\n"
"s_mov_b64 exec, s[34:35]\n"
"buffer_store_dwordx2 v[250:251], v249, s[92:95], s84 offen sc1\n"
"s_mov_b64 exec, -1\n"
".Lnoep_3_%=:\n"
"s_lshl_b32 s14, s8, 15\n"
"s_sub_u32 s15, 0x2c000, s14\n"
"s_mul_i32 s14, s8, 0x1e000\n"
"s_add_u32 s14, s14, 0x4000\n"
"s_cmp_lt_u32 s8, 2\n"
"s_cselect_b32 s14, s14, s15\n"
"v_add_u32 v244, s14, v254\n"
"v_cvt_pk_bf16_f32 v0, v0, v1\n"
"v_cvt_pk_bf16_f32 v1, v2, v3\n"
"v_cvt_pk_bf16_f32 v2, v4, v5\n"
"v_cvt_pk_bf16_f32 v3, v6, v7\n"
"ds_write_b128 v244, v[0:3]\n"
"v_cvt_pk_bf16_f32 v8, v8, v9\n"
"v_cvt_pk_bf16_f32 v9, v10, v11\n"
"v_cvt_pk_bf16_f32 v10, v12, v13\n"
"v_cvt_pk_bf16_f32 v11, v14, v15\n"
"ds_write_b128 v244, v[8:11] offset:1024\n"
"v_cvt_pk_bf16_f32 v16, v16, v17\n"
"v_cvt_pk_bf16_f32 v17, v18, v19\n"
"v_cvt_pk_bf16_f32 v18, v20, v21\n"
"v_cvt_pk_bf16_f32 v19, v22, v23\n"
"ds_write_b128 v244, v[16:19] offset:2048\n"
"v_cvt_pk_bf16_f32 v24, v24, v25\n"
"v_cvt_pk_bf16_f32 v25, v26, v27\n"
"v_cvt_pk_bf16_f32 v26, v28, v29\n"
"v_cvt_pk_bf16_f32 v27, v30, v31\n"
"ds_write_b128 v244, v[24:27] offset:3072\n"
"v_cvt_pk_bf16_f32 v32, v32, v33\n"
"v_cvt_pk_bf16_f32 v33, v34, v35\n"
"v_cvt_pk_bf16_f32 v34, v36, v37\n"
"v_cvt_pk_bf16_f32 v35, v38, v39\n"
"ds_write_b128 v244, v[32:35] offset:4096\n"
"s_waitcnt vmcnt(1)\n"
"v_cmp_eq_u64 vcc, v[238:239], v[250:251]\n"
"s_nop 1\n"
"s_cmp_eq_u64 vcc, exec\n"
"s_cbranch_scc1 .Lpok_12_%=\n"
"s_mov_b32 s15, 0\n"
".Lploop_13_%=:\n"
"s_sleep 4\n"
"buffer_load_dwordx2 v[238:239], v207, s[92:95], s54 offen sc1\n"
"s_waitcnt vmcnt(0)\n"
"v_cmp_eq_u64 vcc, v[238:239], v[250:251]\n"
"s_add_u32 s15, s15, 1\n"
"s_nop 0\n"
"s_cmp_eq_u64 vcc, exec\n"
"s_cbranch_scc1 .Lpok_12_%=\n"
"s_cmp_lt_u32 s15, 0x20000\n"
"s_cbranch_scc1 .Lploop_13_%=\n"
"s_mov_b32 s15, 0x80\n"
"s_mov_b64 exec, 1\n"
"buffer_store_dwordx2 v[250:251], v249, s[92:95], s15 offen sc1\n"
"s_mov_b64 exec, -1\n"
".Lpok_12_%=:\n"
"s_mov_b32 s67, s73\n"
"s_mov_b32 s84, s72\n"
"s_add_u32 s85, s72, 0x10000\n"
"s_mov_b32 m0, s84\n"
"s_nop 0\n"
"buffer_load_dwordx4 v254, s[28:31], s67 offen sc1 lds\n"
"v_cvt_pk_bf16_f32 v40, v40, v41\n"
"v_cvt_pk_bf16_f32 v41, v42, v43\n"
"v_cvt_pk_bf16_f32 v42, v44, v45\n"
"v_cvt_pk_bf16_f32 v43, v46, v47\n"
"ds_write_b128 v244, v[40:43] offset:5120\n"
"s_nop 0\n"
"buffer_load_dwordx4 v254, s[28:31], s67 offen offset:1024 sc1 lds\n"
"v_cvt_pk_bf16_f32 v48, v48, v49\n"
"v_cvt_pk_bf16_f32 v49, v50, v51\n"
"v_cvt_pk_bf16_f32 v50, v52, v53\n"
"v_cvt_pk_bf16_f32 v51, v54, v55\n"
"ds_write_b128 v244, v[48:51] offset:6144\n"
"s_nop 0\n"
"buffer_load_dwordx4 v254, s[28:31], s67 offen offset:2048 sc1 lds\n"
"v_cvt_pk_bf16_f32 v56, v56, v57\n"
"v_cvt_pk_bf16_f32 v57, v58, v59\n"
"v_cvt_pk_bf16_f32 v58, v60, v61\n"
"v_cvt_pk_bf16_f32 v59, v62, v63\n"
"ds_write_b128 v244, v[56:59] offset:7168\n"
"s_nop 0\n"
"buffer_load_dwordx4 v254, s[28:31], s67 offen offset:3072 sc1 lds\n"
"v_cvt_pk_bf16_f32 v64, v64, v65\n"
"v_cvt_pk_bf16_f32 v65, v66, v67\n"
"v_cvt_pk_bf16_f32 v66, v68, v69\n"
"v_cvt_pk_bf16_f32 v67, v70, v71\n"
"ds_write_b128 v244, v[64:67] offset:8192\n"
"s_mov_b32 m0, s85\n"
"s_nop 0\n"
"buffer_load_dwordx4 v254, s[96:99], s67 offen sc1 lds\n"
"v_cvt_pk_bf16_f32 v72, v72, v73\n"
"v_cvt_pk_bf16_f32 v73, v74, v75\n"
"v_cvt_pk_bf16_f32 v74, v76, v77\n"
"v_cvt_pk_bf16_f32 v75, v78, v79\n"
"ds_write_b128 v244, v[72:75] offset:9216\n"
"s_nop 0\n"
"buffer_load_dwordx4 v254, s[96:99], s67 offen offset:1024 sc1 lds\n"
"v_cvt_pk_bf16_f32 v80, v80, v81\n"
"v_cvt_pk_bf16_f32 v81, v82, v83\n"
"v_cvt_pk_bf16_f32 v82, v84, v85\n"
"v_cvt_pk_bf16_f32 v83, v86, v87\n"
"ds_write_b128 v244, v[80:83] offset:10240\n"
"s_nop 0\n"
"buffer_load_dwordx4 v254, s[96:99], s67 offen offset:2048 sc1 lds\n"
"v_cvt_pk_bf16_f32 v88, v88, v89\n"
"v_cvt_pk_bf16_f32 v89, v90, v91\n"
"v_cvt_pk_bf16_f32 v90, v92, v93\n"
"v_cvt_pk_bf16_f32 v91, v94, v95\n"
"ds_write_b128 v244, v[88:91] offset:11264\n"
"s_nop 0\n"
"buffer_load_dwordx4 v254, s[96:99], s67 offen offset:3072 sc1 lds\n"
"v_cvt_pk_bf16_f32 v96, v96, v97\n"
"v_cvt_pk_bf16_f32 v97, v98, v99\n"
"v_cvt_pk_bf16_f32 v98, v100, v101\n"
"v_cvt_pk_bf16_f32 v99, v102, v103\n"
"ds_write_b128 v244, v[96:99] offset:12288\n"
"v_cvt_pk_bf16_f32 v104, v104, v105\n"
"v_cvt_pk_bf16_f32 v105, v106, v107\n"
"v_cvt_pk_bf16_f32 v106, v108, v109\n"
"v_cvt_pk_bf16_f32 v107, v110, v111\n"
"ds_write_b128 v244, v[104:107] offset:13312\n"
"v_cvt_pk_bf16_f32 v112, v112, v113\n"
"v_cvt_pk_bf16_f32 v113, v114, v115\n"
"v_cvt_pk_bf16_f32 v114, v116, v117\n"
"v_cvt_pk_bf16_f32 v115, v118, v119\n"
"ds_write_b128 v244, v[112:115] offset:14336\n"
"v_cvt_pk_bf16_f32 v120, v120, v121\n"
"v_cvt_pk_bf16_f32 v121, v122, v123\n"
"v_cvt_pk_bf16_f32 v122, v124, v125\n"
"v_cvt_pk_bf16_f32 v123, v126, v127\n"
"ds_write_b128 v244, v[120:123] offset:15360\n"
"global_load_dword a255, v248, s[4:5]\n"
"ds_read_b128 a[64:67], v244\n"
"v_pk_mul_f32 v[176:177], v[144:145], s[80:81]\n"
"v_pk_mul_f32 v[178:179], v[146:147], s[80:81]\n"
"v_exp_f32 v176, v176\n"
"ds_read_b128 a[68:71], v244 offset:1024\n"
"v_exp_f32 v177, v177\n"
"v_exp_f32 v178, v178\n"
"v_exp_f32 v179, v179\n"
"ds_read_b128 a[72:75], v244 offset:2048\n"
"v_pk_add_f32 v[176:177], v[176:177], 1.0 op_sel_hi:[1,0]\n"
"v_pk_add_f32 v[178:179], v[178:179], 1.0 op_sel_hi:[1,0]\n"
"v_pk_fma_f32 v[180:181], v[176:177], v[176:177], 1.0 op_sel_hi:[1,1,0]\n"
"ds_read_b128 a[76:79], v244 offset:3072\n"
"v_pk_fma_f32 v[182:183], v[178:179], v[178:179], 1.0 op_sel_hi:[1,1,0]\n"
"v_rcp_f32 v180, v180\n"
"v_rcp_f32 v181, v181\n"
"ds_read_b128 a[80:83], v244 offset:4096\n"
"v_rcp_f32 v182, v182\n"
"v_rcp_f32 v183, v183\n"
"v_pk_fma_f32 v[180:181], v[180:181], v[240:241], v[242:243] op_sel_hi:[1,0,0]\n"
"ds_read_b128 a[84:87], v244 offset:5120\n"
"v_pk_fma_f32 v[182:183], v[182:183], v[240:241], v[242:243] op_sel_hi:[1,0,0]\n"
"v_pk_mul_f32 v[180:181], v[180:181], v[144:145]\n"
"v_pk_mul_f32 v[182:183], v[182:183], v[146:147]\n"
"ds_read_b128 a[88:91], v244 offset:6144\n"
"v_cvt_pk_bf16_f32 v208, v180, v181\n"
"v_cvt_pk_bf16_f32 v209, v182, v183\n"
"buffer_store_dwordx2 v[208:209], v247, s[36:39], s83 offen sc1\n"
"ds_read_b128 a[92:95], v244 offset:7168\n"
"v_pk_mul_f32 v[184:185], v[148:149], s[80:81]\n"
"v_pk_mul_f32 v[186:187], v[150:151], s[80:81]\n"
"v_exp_f32 v184, v184\n"
"ds_read_b128 a[96:99], v244 offset:8192\n"
"v_exp_f32 v185, v185\n"
"v_exp_f32 v186, v186\n"
"v_exp_f32 v187, v187\n"
"ds_read_b128 a[100:103], v244 offset:9216\n"
"v_pk_add_f32 v[184:185], v[184:185], 1.0 op_sel_hi:[1,0]\n"
"v_pk_add_f32 v[186:187], v[186:187], 1.0 op_sel_hi:[1,0]\n"
"v_pk_fma_f32 v[188:189], v[184:185], v[184:185], 1.0 op_sel_hi:[1,1,0]\n"
"ds_read_b128 a[104:107], v244 offset:10240\n"
"v_pk_fma_f32 v[190:191], v[186:187], v[186:187], 1.0 op_sel_hi:[1,1,0]\n"
"v_rcp_f32 v188, v188\n"
"v_rcp_f32 v189, v189\n"
"ds_read_b128 a[108:111], v244 offset:11264\n"
"v_rcp_f32 v190, v190\n"
"v_rcp_f32 v191, v191\n"
"v_pk_fma_f32 v[188:189], v[188:189], v[240:241], v[242:243] op_sel_hi:[1,0,0]\n"
"ds_read_b128 a[112:115], v244 offset:12288\n"
"v_pk_fma_f32 v[190:191], v[190:191], v[240:241], v[242:243] op_sel_hi:[1,0,0]\n"
"v_pk_mul_f32 v[188:189], v[188:189], v[148:149]\n"
"v_pk_mul_f32 v[190:191], v[190:191], v[150:151]\n"
"ds_read_b128 a[116:119], v244 offset:13312\n"
"v_cvt_pk_bf16_f32 v210, v188, v189\n"
"v_cvt_pk_bf16_f32 v211, v190, v191\n"
"buffer_store_dwordx2 v[210:211], v247, s[36:39], s83 offen offset:512 sc1\n"
"ds_read_b128 a[120:123], v244 offset:14336\n"
"v_pk_mul_f32 v[176:177], v[152:153], s[80:81]\n"
"v_pk_mul_f32 v[178:179], v[154:155], s[80:81]\n"
"v_exp_f32 v176, v176\n"
"ds_read_b128 a[124:127], v244 offset:15360\n"
"v_exp_f32 v177, v177\n"
"v_exp_f32 v178, v178\n"
"v_exp_f32 v179, v179\n"
"v_pk_add_f32 v[176:177], v[176:177], 1.0 op_sel_hi:[1,0]\n"
"v_pk_add_f32 v[178:179], v[178:179], 1.0 op_sel_hi:[1,0]\n"
"v_pk_fma_f32 v[180:181], v[176:177], v[176:177], 1.0 op_sel_hi:[1,1,0]\n"
"v_pk_fma_f32 v[182:183], v[178:179], v[178:179], 1.0 op_sel_hi:[1,1,0]\n"
"v_rcp_f32 v180, v180\n"
"v_rcp_f32 v181, v181\n"
"v_rcp_f32 v182, v182\n"
"v_rcp_f32 v183, v183\n"
"v_pk_fma_f32 v[180:181], v[180:181], v[240:241], v[242:243] op_sel_hi:[1,0,0]\n"
"v_pk_fma_f32 v[182:183], v[182:183], v[240:241], v[242:243] op_sel_hi:[1,0,0]\n"
"v_pk_mul_f32 v[180:181], v[180:181], v[152:153]\n"
"v_pk_mul_f32 v[182:183], v[182:183], v[154:155]\n"
"v_cvt_pk_bf16_f32 v212, v180, v181\n"
"v_cvt_pk_bf16_f32 v213, v182, v183\n"
"buffer_store_dwordx2 v[212:213], v247, s[36:39], s83 offen offset:1024 sc1\n"
"v_pk_mul_f32 v[184:185], v[156:157], s[80:81]\n"
"v_pk_mul_f32 v[186:187], v[158:159], s[80:81]\n"
"v_exp_f32 v184, v184\n"
"v_exp_f32 v185, v185\n"
"v_exp_f32 v186, v186\n"
"v_exp_f32 v187, v187\n"
"v_pk_add_f32 v[184:185], v[184:185], 1.0 op_sel_hi:[1,0]\n"
"v_pk_add_f32 v[186:187], v[186:187], 1.0 op_sel_hi:[1,0]\n"
"v_pk_fma_f32 v[188:189], v[184:185], v[184:185], 1.0 op_sel_hi:[1,1,0]\n"
"v_pk_fma_f32 v[190:191], v[186:187], v[186:187], 1.0 op_sel_hi:[1,1,0]\n"
"v_rcp_f32 v188, v188\n"
"v_rcp_f32 v189, v189\n"
"v_rcp_f32 v190, v190\n"
"v_rcp_f32 v191, v191\n"
"v_pk_fma_f32 v[188:189], v[188:189], v[240:241], v[242:243] op_sel_hi:[1,0,0]\n"
"v_pk_fma_f32 v[190:191], v[190:191], v[240:241], v[242:243] op_sel_hi:[1,0,0]\n"
"v_pk_mul_f32 v[188:189], v[188:189], v[156:157]\n"
"v_pk_mul_f32 v[190:191], v[190:191], v[158:159]\n"
"v_cvt_pk_bf16_f32 v214, v188, v189\n"
"v_cvt_pk_bf16_f32 v215, v190, v191\n"
"buffer_store_dwordx2 v[214:215], v247, s[36:39], s83 offen offset:1536 sc1\n"
"v_pk_mul_f32 v[176:177], v[160:161], s[80:81]\n"
"v_pk_mul_f32 v[178:179], v[162:163], s[80:81]\n"
"v_exp_f32 v176, v176\n"
"v_exp_f32 v177, v177\n"
"v_exp_f32 v178, v178\n"
"v_exp_f32 v179, v179\n"
"v_pk_add_f32 v[176:177], v[176:177], 1.0 op_sel_hi:[1,0]\n"
"v_pk_add_f32 v[178:179], v[178:179], 1.0 op_sel_hi:[1,0]\n"
"v_pk_fma_f32 v[180:181], v[176:177], v[176:177], 1.0 op_sel_hi:[1,1,0]\n"
"v_pk_fma_f32 v[182:183], v[178:179], v[178:179], 1.0 op_sel_hi:[1,1,0]\n"
"v_rcp_f32 v180, v180\n"
"v_rcp_f32 v181, v181\n"
"v_rcp_f32 v182, v182\n"
"v_rcp_f32 v183, v183\n"
"v_pk_fma_f32 v[180:181], v[180:181], v[240:241], v[242:243] op_sel_hi:[1,0,0]\n"
"v_pk_fma_f32 v[182:183], v[182:183], v[240:241], v[242:243] op_sel_hi:[1,0,0]\n"
"v_pk_mul_f32 v[180:181], v[180:181], v[160:161]\n"
"v_pk_mul_f32 v[182:183], v[182:183], v[162:163]\n"
"v_cvt_pk_bf16_f32 v216, v180, v181\n"
"v_cvt_pk_bf16_f32 v217, v182, v183\n"
"buffer_store_dwordx2 v[216:217], v247, s[36:39], s55 offen sc1\n"
"v_pk_mul_f32 v[184:185], v[164:165], s[80:81]\n"
"v_pk_mul_f32 v[186:187], v[166:167], s[80:81]\n"
"v_exp_f32 v184, v184\n"
"v_exp_f32 v185, v185\n"
"v_exp_f32 v186, v186\n"
"v_exp_f32 v187, v187\n"
"v_pk_add_f32 v[184:185], v[184:185], 1.0 op_sel_hi:[1,0]\n"
"v_pk_add_f32 v[186:187], v[186:187], 1.0 op_sel_hi:[1,0]\n"
"v_pk_fma_f32 v[188:189], v[184:185], v[184:185], 1.0 op_sel_hi:[1,1,0]\n"
"v_pk_fma_f32 v[190:191], v[186:187], v[186:187], 1.0 op_sel_hi:[1,1,0]\n"
"v_rcp_f32 v188, v188\n"
"v_rcp_f32 v189, v189\n"
"v_rcp_f32 v190, v190\n"
"v_rcp_f32 v191, v191\n"
"v_pk_fma_f32 v[188:189], v[188:189], v[240:241], v[242:243] op_sel_hi:[1,0,0]\n"
"v_pk_fma_f32 v[190:191], v[190:191], v[240:241], v[242:243] op_sel_hi:[1,0,0]\n"
"v_pk_mul_f32 v[188:189], v[188:189], v[164:165]\n"
"v_pk_mul_f32 v[190:191], v[190:191], v[166:167]\n"
"v_cvt_pk_bf16_f32 v218, v188, v189\n"
"v_cvt_pk_bf16_f32 v219, v190, v191\n"
"buffer_store_dwordx2 v[218:219], v247, s[36:39], s55 offen offset:512 sc1\n"
"s_waitcnt vmcnt(11) lgkmcnt(0)\n"
"s_barrier\n"
"s_add_u32 s67, s67, 0x4000\n"
"s_xor_b32 s84, s84, 0x8000\n"
"s_mov_b32 m0, s84\n"
"s_nop 0\n"
"buffer_load_dwordx4 v254, s[28:31], s67 offen sc1 lds\n"
"buffer_load_dwordx4 v254, s[28:31], s67 offen offset:1024 sc1 lds\n"
"buffer_load_dwordx4 v254, s[28:31], s67 offen offset:2048 sc1 lds\n"
"buffer_load_dwordx4 v254, s[28:31], s67 offen offset:3072 sc1 lds\n"
"s_lshl_b32 s12, s9, 2\n"
"s_add_u32 s12, s12, s8\n"
"s_lshl_b32 s13, s12, 12\n"
"v_and_b32 v238, 31, v255\n"
"v_lshl_add_u32 v238, v238, 7, s13\n"
"s_mul_i32 s13, s12, 0x560\n"
"v_and_b32 v239, 15, v255\n"
"v_lshl_add_u32 v239, v239, 7, s13\n"
"v_min_u32 v239, 0x157ffc, v239\n"
"ds_read_b128 a[144:147], v254\n"
"ds_read_b128 a[148:151], v254 offset:1024\n"
"ds_read_b128 a[152:155], v254 offset:2048\n"
"ds_read_b128 a[156:159], v254 offset:3072\n"
"s_waitcnt lgkmcnt(3)\n"
"v_mfma_f32_16x16x32_bf16 v[64:67], a[144:147], a[64:67], 0\n"
"v_mfma_f32_16x16x32_bf16 v[68:71], a[144:147], a[68:71], 0\n"
"v_mfma_f32_16x16x32_bf16 v[72:75], a[144:147], a[72:75], 0\n"
"v_mfma_f32_16x16x32_bf16 v[76:79], a[144:147], a[76:79], 0\n"
"s_waitcnt lgkmcnt(2)\n"
"v_mfma_f32_16x16x32_bf16 v[64:67], a[148:151], a[80:83], v[64:67]\n"
"v_mfma_f32_16x16x32_bf16 v[68:71], a[148:151], a[84:87], v[68:71]\n"
"v_mfma_f32_16x16x32_bf16 v[72:75], a[148:151], a[88:91], v[72:75]\n"
"v_mfma_f32_16x16x32_bf16 v[76:79], a[148:151], a[92:95], v[76:79]\n"
"s_waitcnt lgkmcnt(1)\n"
"v_mfma_f32_16x16x32_bf16 v[64:67], a[152:155], a[96:99], v[64:67]\n"
"v_mfma_f32_16x16x32_bf16 v[68:71], a[152:155], a[100:103], v[68:71]\n"
"v_mfma_f32_16x16x32_bf16 v[72:75], a[152:155], a[104:107], v[72:75]\n"
"v_mfma_f32_16x16x32_bf16 v[76:79], a[152:155], a[108:111], v[76:79]\n"
"s_waitcnt lgkmcnt(0)\n"
"v_mfma_f32_16x16x32_bf16 v[64:67], a[156:159], a[112:115], v[64:67]\n"
"v_mfma_f32_16x16x32_bf16 v[68:71], a[156:159], a[116:119], v[68:71]\n"
"v_mfma_f32_16x16x32_bf16 v[72:75], a[156:159], a[120:123], v[72:75]\n"
"v_mfma_f32_16x16x32_bf16 v[76:79], a[156:159], a[124:127], v[76:79]\n"
"v_pk_mul_f32 v[176:177], v[168:169], s[80:81]\n"
"v_pk_mul_f32 v[178:179], v[170:171], s[80:81]\n"
"v_exp_f32 v176, v176\n"
"v_exp_f32 v177, v177\n"
"v_exp_f32 v178, v178\n"
"v_exp_f32 v179, v179\n"
"v_pk_add_f32 v[176:177], v[176:177], 1.0 op_sel_hi:[1,0]\n"
"v_pk_add_f32 v[178:179], v[178:179], 1.0 op_sel_hi:[1,0]\n"
"v_pk_fma_f32 v[180:181], v[176:177], v[176:177], 1.0 op_sel_hi:[1,1,0]\n"
"v_pk_fma_f32 v[182:183], v[178:179], v[178:179], 1.0 op_sel_hi:[1,1,0]\n"
"v_rcp_f32 v180, v180\n"
"v_rcp_f32 v181, v181\n"
"v_rcp_f32 v182, v182\n"
"v_rcp_f32 v183, v183\n"
"v_pk_fma_f32 v[180:181], v[180:181], v[240:241], v[242:243] op_sel_hi:[1,0,0]\n"
"v_pk_fma_f32 v[182:183], v[182:183], v[240:241], v[242:243] op_sel_hi:[1,0,0]\n"
"v_pk_mul_f32 v[180:181], v[180:181], v[168:169]\n"
"v_pk_mul_f32 v[182:183], v[182:183], v[170:171]\n"
"v_cvt_pk_bf16_f32 v220, v180, v181\n"
"v_cvt_pk_bf16_f32 v221, v182, v183\n"
"buffer_store_dwordx2 v[220:221], v247, s[36:39], s55 offen offset:1024 sc1\n"
"v_pk_mul_f32 v[184:185], v[172:173], s[80:81]\n"
"v_pk_mul_f32 v[186:187], v[174:175], s[80:81]\n"
"v_exp_f32 v184, v184\n"
"v_exp_f32 v185, v185\n"
"v_exp_f32 v186, v186\n"
"v_exp_f32 v187, v187\n"
"v_pk_add_f32 v[184:185], v[184:185], 1.0 op_sel_hi:[1,0]\n"
"v_pk_add_f32 v[186:187], v[186:187], 1.0 op_sel_hi:[1,0]\n"
"v_pk_fma_f32 v[188:189], v[184:185], v[184:185], 1.0 op_sel_hi:[1,1,0]\n"
"v_pk_fma_f32 v[190:191], v[186:187], v[186:187], 1.0 op_sel_hi:[1,1,0]\n"
"v_rcp_f32 v188, v188\n"
"v_rcp_f32 v189, v189\n"
"v_rcp_f32 v190, v190\n"
"v_rcp_f32 v191, v191\n"
"v_pk_fma_f32 v[188:189], v[188:189], v[240:241], v[242:243] op_sel_hi:[1,0,0]\n"
"v_pk_fma_f32 v[190:191], v[190:191], v[240:241], v[242:243] op_sel_hi:[1,0,0]\n"
"v_pk_mul_f32 v[188:189], v[188:189], v[172:173]\n"
"v_pk_mul_f32 v[190:191], v[190:191], v[174:175]\n"
"v_cvt_pk_bf16_f32 v222, v188, v189\n"
"v_cvt_pk_bf16_f32 v223, v190, v191\n"
"buffer_store_dwordx2 v[222:223], v247, s[36:39], s55 offen offset:1536 sc1\n"
"global_load_dword a255, v238, s[50:51]\n"
"global_load_dword a255, v239, s[56:57]\n"
"v_mov_b32 v196, 0xff800000\n"
"v_mov_b32 v197, 0xff800000\n"
"v_mov_b32 v198, 0xff800000\n"
"v_mov_b32 v199, 0xff800000\n"
"v_mov_b32 v200, 0x3f803f80\n"
"v_mov_b32 v201, 0x3f803f80\n"
"v_mov_b32 v202, 0x3f803f80\n"
"v_mov_b32 v203, 0x3f803f80\n"
"v_lshrrev_b32 v244, 4, v255\n"
"v_lshlrev_b32 v244, 2, v244\n"
"v_and_b32 v245, 15, v255\n"
"v_add_u32 v246, 0, v244\n"
"v_cmp_gt_u32 s[42:43], v246, v245\n"
"v_add_u32 v246, 1, v244\n"
"v_cmp_gt_u32 s[44:45], v246, v245\n"
"v_add_u32 v246, 2, v244\n"
"v_cmp_gt_u32 s[46:47], v246, v245\n"
"v_add_u32 v246, 3, v244\n"
"v_cmp_gt_u32 s[48:49], v246, v245\n"
"v_mov_b32 v192, v254\n"
"v_add_u32 v193, 0x18000, v254\n"
"v_mov_b32 v194, v193\n"
"ds_read_b128 a[144:147], v192\n"
"ds_read_b128 a[148:151], v192 offset:1024\n"
"ds_read_b128 a[152:155], v192 offset:2048\n"
"ds_read_b128 a[156:159], v192 offset:3072\n"
"ds_read_b128 a[160:163], v192 offset:4096\n"
"ds_read_b128 a[164:167], v192 offset:5120\n"
"ds_read_b128 a[168:171], v192 offset:6144\n"
"ds_read_b128 a[172:175], v192 offset:7168\n"
"ds_read_b128 a[176:179], v193\n"
"ds_read_b128 a[208:211], v193 offset:8192\n"
"ds_read_b128 a[180:183], v193 offset:1024\n"
"ds_read_b128 a[212:215], v193 offset:9216\n"
"s_and_b32 s12, s8, 1\n"
"s_lshl_b32 s12, s12, 10\n"
"s_add_u32 s12, s12, 0x20000\n"
"s_cmp_lt_u32 s8, 2\n"
"s_cselect_b32 s12, 0x20800, s12\n"
"s_cselect_b32 s13, s8, 0\n"
"s_lshl_b32 s13, s13, 10\n"
"s_add_u32 s12, s12, s13\n"
"v_add_u32 v236, s12, v254\n"
"v_max3_f32 v228, v64, v65, v66\n"
"v_max_f32 v228, v228, v67\n"
"v_max3_f32 v229, v68, v69, v70\n"
"v_max_f32 v229, v229, v71\n"
"v_max3_f32 v230, v72, v73, v74\n"
"v_max_f32 v230, v230, v75\n"
"v_max3_f32 v231, v76, v77, v78\n"
"v_max_f32 v231, v231, v79\n"
"v_mov_b32 v232, v228\n"
"v_mov_b32 v233, v229\n"
"v_mov_b32 v234, v230\n"
"v_mov_b32 v235, v231\n"
"s_nop 0\n"
"v_permlane16_swap_b32 v228, v232\n"
"v_permlane16_swap_b32 v229, v233\n"
"v_permlane16_swap_b32 v230, v234\n"
"v_permlane16_swap_b32 v231, v235\n"
"v_max_f32 v228, v228, v232\n"
"v_max_f32 v229, v229, v233\n"
"v_max_f32 v230, v230, v234\n"
"v_max_f32 v231, v231, v235\n"
"v_mov_b32 v232, v228\n"
"v_mov_b32 v233, v229\n"
"v_mov_b32 v234, v230\n"
"v_mov_b32 v235, v231\n"
"s_nop 0\n"
"v_permlane32_swap_b32 v228, v232\n"
"v_permlane32_swap_b32 v229, v233\n"
"v_permlane32_swap_b32 v230, v234\n"
"v_permlane32_swap_b32 v231, v235\n"
"v_max_f32 v228, v228, v232\n"
"v_max_f32 v229, v229, v233\n"
"v_max_f32 v230, v230, v234\n"
"v_max_f32 v231, v231, v235\n"
"v_sub_f32 v228, 0, v228\n"
"v_sub_f32 v229, 0, v229\n"
"v_sub_f32 v230, 0, v230\n"
"v_sub_f32 v231, 0, v231\n"
"ds_write_b128 v236, v[228:231]\n"
"v_mov_b32 v160, v228\n"
"v_mov_b32 v161, v228\n"
"v_mov_b32 v162, v228\n"
"v_mov_b32 v163, v228\n"
"v_mov_b32 v164, v229\n"
"v_mov_b32 v165, v229\n"
"v_mov_b32 v166, v229\n"
"v_mov_b32 v167, v229\n"
"v_mov_b32 v168, v230\n"
"v_mov_b32 v169, v230\n"
"v_mov_b32 v170, v230\n"
"v_mov_b32 v171, v230\n"
"v_mov_b32 v172, v231\n"
"v_mov_b32 v173, v231\n"
"v_mov_b32 v174, v231\n"
"v_mov_b32 v175, v231\n"
"v_cndmask_b32 v176, v228, v196, s[42:43]\n"
"v_cndmask_b32 v177, v228, v196, s[44:45]\n"
"v_cndmask_b32 v178, v228, v196, s[46:47]\n"
"v_cndmask_b32 v179, v228, v196, s[48:49]\n"
"v_cndmask_b32 v180, v229, v196, s[42:43]\n"
"v_cndmask_b32 v181, v229, v196, s[44:45]\n"
"v_cndmask_b32 v182, v229, v196, s[46:47]\n"
"v_cndmask_b32 v183, v229, v196, s[48:49]\n"
"v_cndmask_b32 v184, v230, v196, s[42:43]\n"
"v_cndmask_b32 v185, v230, v196, s[44:45]\n"
"v_cndmask_b32 v186, v230, v196, s[46:47]\n"
"v_cndmask_b32 v187, v230, v196, s[48:49]\n"
"v_cndmask_b32 v188, v231, v196, s[42:43]\n"
"v_cndmask_b32 v189, v231, v196, s[44:45]\n"
"v_cndmask_b32 v190, v231, v196, s[46:47]\n"
"v_cndmask_b32 v191, v231, v196, s[48:49]\n"
"s_waitcnt lgkmcnt(0)\n"
"s_add_u32 s54, s53, 0x800\n"
"s_cmp_eq_u32 s8, 0\n"
"s_cselect_b32 s12, 1, 0\n"
"s_sub_u32 s71, s65, s12\n"
"s_lshl_b32 s12, s9, 2\n"
"s_add_u32 s12, s12, s8\n"
"s_lshl_b32 s13, s12, 12\n"
"v_and_b32 v242, 31, v255\n"
"v_lshl_add_u32 v242, v242, 7, s13\n"
"s_mul_i32 s13, s12, 0x560\n"
"v_and_b32 v243, 15, v255\n"
"v_lshl_add_u32 v243, v243, 7, s13\n"
"v_min_u32 v243, 0x157ffc, v243\n"
"v_lshlrev_b32 v195, 7, v255\n"
"v_add_u32 v195, s72, v195\n"
"s_cmp_eq_u32 s71, 0\n"
"s_cbranch_scc1 .Lfirstdiag0_15_%=\n"
"s_waitcnt lgkmcnt(8)\n"
"v_mfma_f32_16x16x32_bf16 v[64:67], a[144:147], a[64:67], v[160:163]\n"
"v_mfma_f32_16x16x32_bf16 v[64:67], a[148:151], a[80:83], v[64:67]\n"
"v_mfma_f32_16x16x32_bf16 v[64:67], a[152:155], a[96:99], v[64:67]\n"
"v_mfma_f32_16x16x32_bf16 v[64:67], a[156:159], a[112:115], v[64:67]\n"
"v_mfma_f32_16x16x32_bf16 v[68:71], a[156:159], a[116:119], v[164:167]\n"
"v_mfma_f32_16x16x32_bf16 v[68:71], a[152:155], a[100:103], v[68:71]\n"
"v_mfma_f32_16x16x32_bf16 v[68:71], a[148:151], a[84:87], v[68:71]\n"
"v_mfma_f32_16x16x32_bf16 v[68:71], a[144:147], a[68:71], v[68:71]\n"
"v_mfma_f32_16x16x32_bf16 v[72:75], a[144:147], a[72:75], v[168:171]\n"
"v_mfma_f32_16x16x32_bf16 v[72:75], a[148:151], a[88:91], v[72:75]\n"
"v_mfma_f32_16x16x32_bf16 v[72:75], a[152:155], a[104:107], v[72:75]\n"
"v_mfma_f32_16x16x32_bf16 v[72:75], a[156:159], a[120:123], v[72:75]\n"
"v_mfma_f32_16x16x32_bf16 v[76:79], a[156:159], a[124:127], v[172:175]\n"
"v_mfma_f32_16x16x32_bf16 v[76:79], a[152:155], a[108:111], v[76:79]\n"
"v_mfma_f32_16x16x32_bf16 v[76:79], a[148:151], a[92:95], v[76:79]\n"
"v_mfma_f32_16x16x32_bf16 v[76:79], a[144:147], a[76:79], v[76:79]\n"
"s_waitcnt lgkmcnt(4)\n"
"v_mfma_f32_16x16x32_bf16 v[80:83], a[160:163], a[64:67], v[160:163]\n"
"ds_read_b128 a[144:147], v192 offset:8192\n"
"ds_read_b128 a[148:151], v192 offset:9216\n"
"v_mfma_f32_16x16x32_bf16 v[80:83], a[164:167], a[80:83], v[80:83]\n"
"v_mfma_f32_16x16x32_bf16 v[80:83], a[168:171], a[96:99], v[80:83]\n"
"ds_read_b128 a[152:155], v192 offset:10240\n"
"ds_read_b128 a[156:159], v192 offset:11264\n"
"v_mfma_f32_16x16x32_bf16 v[80:83], a[172:175], a[112:115], v[80:83]\n"
"v_mfma_f32_16x16x32_bf16 v[84:87], a[172:175], a[116:119], v[164:167]\n"
"v_mfma_f32_16x16x32_bf16 v[84:87], a[168:171], a[100:103], v[84:87]\n"
"v_exp_f32 v64, v64\n"
"v_mfma_f32_16x16x32_bf16 v[84:87], a[164:167], a[84:87], v[84:87]\n"
"v_exp_f32 v65, v65\n"
"v_mfma_f32_16x16x32_bf16 v[84:87], a[160:163], a[68:71], v[84:87]\n"
"v_exp_f32 v66, v66\n"
"v_mfma_f32_16x16x32_bf16 v[88:91], a[160:163], a[72:75], v[168:171]\n"
"v_cvt_pk_bf16_f32 v128, v64, v65\n"
"v_mfma_f32_16x16x32_bf16 v[88:91], a[164:167], a[88:91], v[88:91]\n"
"v_exp_f32 v67, v67\n"
"v_mfma_f32_16x16x32_bf16 v[88:91], a[168:171], a[104:107], v[88:91]\n"
"v_exp_f32 v80, v80\n"
"v_mfma_f32_16x16x32_bf16 v[88:91], a[172:175], a[120:123], v[88:91]\n"
"v_cvt_pk_bf16_f32 v129, v66, v67\n"
"v_mfma_f32_16x16x32_bf16 v[92:95], a[172:175], a[124:127], v[172:175]\n"
"v_exp_f32 v81, v81\n"
"v_mfma_f32_16x16x32_bf16 v[92:95], a[168:171], a[108:111], v[92:95]\n"
"v_exp_f32 v82, v82\n"
"v_mfma_f32_16x16x32_bf16 v[92:95], a[164:167], a[92:95], v[92:95]\n"
"v_cvt_pk_bf16_f32 v130, v80, v81\n"
"v_mfma_f32_16x16x32_bf16 v[92:95], a[160:163], a[76:79], v[92:95]\n"
"v_exp_f32 v83, v83\n"
"s_waitcnt lgkmcnt(0)\n"
"v_mfma_f32_16x16x32_bf16 v[96:99], a[144:147], a[64:67], v[160:163]\n"
"ds_read_b128 a[160:163], v192 offset:12288\n"
"ds_read_b128 a[164:167], v192 offset:13312\n"
"v_mfma_f32_16x16x32_bf16 v[96:99], a[148:151], a[80:83], v[96:99]\n"
"v_exp_f32 v68, v68\n"
"v_mfma_f32_16x16x32_bf16 v[96:99], a[152:155], a[96:99], v[96:99]\n"
"ds_read_b128 a[168:171], v192 offset:14336\n"
"ds_read_b128 a[172:175], v192 offset:15360\n"
"v_mfma_f32_16x16x32_bf16 v[96:99], a[156:159], a[112:115], v[96:99]\n"
"v_cvt_pk_bf16_f32 v131, v82, v83\n"
"v_mfma_f32_16x16x32_bf16 v[100:103], a[156:159], a[116:119], v[164:167]\n"
"v_exp_f32 v69, v69\n"
"v_mfma_f32_16x16x32_bf16 v[100:103], a[152:155], a[100:103], v[100:103]\n"
"v_exp_f32 v70, v70\n"
"v_mfma_f32_16x16x32_bf16 v[100:103], a[148:151], a[84:87], v[100:103]\n"
"v_cvt_pk_bf16_f32 v132, v68, v69\n"
"v_mfma_f32_16x16x32_bf16 v[100:103], a[144:147], a[68:71], v[100:103]\n"
"v_exp_f32 v71, v71\n"
"v_mfma_f32_16x16x32_bf16 v[104:107], a[144:147], a[72:75], v[168:171]\n"
"v_exp_f32 v84, v84\n"
"v_mfma_f32_16x16x32_bf16 v[104:107], a[148:151], a[88:91], v[104:107]\n"
"v_cvt_pk_bf16_f32 v133, v70, v71\n"
"v_mfma_f32_16x16x32_bf16 v[104:107], a[152:155], a[104:107], v[104:107]\n"
"v_exp_f32 v85, v85\n"
"v_mfma_f32_16x16x32_bf16 v[104:107], a[156:159], a[120:123], v[104:107]\n"
"v_exp_f32 v86, v86\n"
"v_mfma_f32_16x16x32_bf16 v[108:111], a[156:159], a[124:127], v[172:175]\n"
"v_cvt_pk_bf16_f32 v134, v84, v85\n"
"v_mfma_f32_16x16x32_bf16 v[108:111], a[152:155], a[108:111], v[108:111]\n"
"v_exp_f32 v87, v87\n"
"v_mfma_f32_16x16x32_bf16 v[108:111], a[148:151], a[92:95], v[108:111]\n"
"v_exp_f32 v72, v72\n"
"v_mfma_f32_16x16x32_bf16 v[108:111], a[144:147], a[76:79], v[108:111]\n"
"v_cvt_pk_bf16_f32 v135, v86, v87\n"
"s_waitcnt lgkmcnt(0)\n"
"v_mfma_f32_16x16x32_bf16 v[112:115], a[160:163], a[64:67], v[160:163]\n"
"v_exp_f32 v73, v73\n"
"v_mfma_f32_16x16x32_bf16 v[112:115], a[164:167], a[80:83], v[112:115]\n"
"v_exp_f32 v74, v74\n"
"v_mfma_f32_16x16x32_bf16 v[112:115], a[168:171], a[96:99], v[112:115]\n"
"v_cvt_pk_bf16_f32 v136, v72, v73\n"
"v_mfma_f32_16x16x32_bf16 v[112:115], a[172:175], a[112:115], v[112:115]\n"
"v_exp_f32 v75, v75\n"
"v_mfma_f32_16x16x32_bf16 v[116:119], a[172:175], a[116:119], v[164:167]\n"
"v_exp_f32 v88, v88\n"
"v_mfma_f32_16x16x32_bf16 v[116:119], a[168:171], a[100:103], v[116:119]\n"
"v_cvt_pk_bf16_f32 v137, v74, v75\n"
"v_mfma_f32_16x16x32_bf16 v[116:119], a[164:167], a[84:87], v[116:119]\n"
"v_exp_f32 v89, v89\n"
"v_mfma_f32_16x16x32_bf16 v[116:119], a[160:163], a[68:71], v[116:119]\n"
"v_exp_f32 v90, v90\n"
"v_mfma_f32_16x16x32_bf16 v[120:123], a[160:163], a[72:75], v[168:171]\n"
"v_cvt_pk_bf16_f32 v138, v88, v89\n"
"v_mfma_f32_16x16x32_bf16 v[120:123], a[164:167], a[88:91], v[120:123]\n"
"v_exp_f32 v91, v91\n"
"v_mfma_f32_16x16x32_bf16 v[120:123], a[168:171], a[104:107], v[120:123]\n"
"v_exp_f32 v76, v76\n"
"v_mfma_f32_16x16x32_bf16 v[120:123], a[172:175], a[120:123], v[120:123]\n"
"v_cvt_pk_bf16_f32 v139, v90, v91\n"
"v_mfma_f32_16x16x32_bf16 v[124:127], a[172:175], a[124:127], v[172:175]\n"
"v_exp_f32 v77, v77\n"
"v_mfma_f32_16x16x32_bf16 v[124:127], a[168:171], a[108:111], v[124:127]\n"
"v_exp_f32 v78, v78\n"
"v_mfma_f32_16x16x32_bf16 v[124:127], a[164:167], a[92:95], v[124:127]\n"
"v_exp_f32 v79, v79\n"
"v_mfma_f32_16x16x32_bf16 v[124:127], a[160:163], a[76:79], v[124:127]\n"
"v_exp_f32 v92, v92\n"
"s_nop 1\n"
"v_exp_f32 v93, v93\n"
"v_exp_f32 v94, v94\n"
"v_exp_f32 v95, v95\n"
"v_exp_f32 v96, v96\n"
"v_exp_f32 v97, v97\n"
"v_exp_f32 v98, v98\n"
"v_cvt_pk_bf16_f32 v144, v96, v97\n"
"v_exp_f32 v99, v99\n"
"v_exp_f32 v112, v112\n"
"v_cvt_pk_bf16_f32 v145, v98, v99\n"
"v_exp_f32 v113, v113\n"
"v_exp_f32 v114, v114\n"
"v_cvt_pk_bf16_f32 v146, v112, v113\n"
"v_exp_f32 v115, v115\n"
"v_exp_f32 v100, v100\n"
"v_cvt_pk_bf16_f32 v147, v114, v115\n"
"v_exp_f32 v101, v101\n"
"v_exp_f32 v102, v102\n"
"s_waitcnt vmcnt(4) lgkmcnt(0)\n"
"s_barrier\n"
"v_xor_b32 v192, 0x8000, v192\n"
"v_xor_b32 v193, 0x8000, v193\n"
"v_xor_b32 v194, 0x8000, v194\n"
"ds_read_b128 a[144:147], v192\n"
"v_cvt_pk_bf16_f32 v148, v100, v101\n"
"v_exp_f32 v103, v103\n"
"ds_read_b128 a[148:151], v192 offset:1024\n"
"v_exp_f32 v116, v116\n"
"v_cvt_pk_bf16_f32 v149, v102, v103\n"
"ds_read_b128 a[152:155], v192 offset:2048\n"
"v_exp_f32 v117, v117\n"
"v_exp_f32 v118, v118\n"
"ds_read_b128 a[156:159], v192 offset:3072\n"
"v_cvt_pk_bf16_f32 v150, v116, v117\n"
"v_exp_f32 v119, v119\n"
"ds_read_b128 a[160:163], v192 offset:4096\n"
"v_exp_f32 v104, v104\n"
"v_cvt_pk_bf16_f32 v151, v118, v119\n"
"ds_read_b128 a[164:167], v192 offset:5120\n"
"v_exp_f32 v105, v105\n"
"v_exp_f32 v106, v106\n"
"ds_read_b128 a[168:171], v192 offset:6144\n"
"v_cvt_pk_bf16_f32 v152, v104, v105\n"
"v_exp_f32 v107, v107\n"
"ds_read_b128 a[172:175], v192 offset:7168\n"
"v_exp_f32 v120, v120\n"
"v_cvt_pk_bf16_f32 v153, v106, v107\n"
"ds_read_b128 a[176:179], v193\n"
"v_exp_f32 v121, v121\n"
"v_exp_f32 v122, v122\n"
"ds_read_b128 a[208:211], v193 offset:8192\n"
"v_cvt_pk_bf16_f32 v154, v120, v121\n"
"v_exp_f32 v123, v123\n"
"ds_read_b128 a[180:183], v193 offset:1024\n"
"v_exp_f32 v108, v108\n"
"v_cvt_pk_bf16_f32 v155, v122, v123\n"
"ds_read_b128 a[212:215], v193 offset:9216\n"
"v_exp_f32 v109, v109\n"
"v_exp_f32 v110, v110\n"
"v_exp_f32 v111, v111\n"
"v_exp_f32 v124, v124\n"
"v_exp_f32 v125, v125\n"
"v_exp_f32 v126, v126\n"
"v_exp_f32 v127, v127\n"
"s_nop 0\n"
"s_add_u32 s62, s62, 1\n"
"s_cmp_lt_u32 s71, 2\n"
"s_cbranch_scc1 .Lh_a_39_%=\n"
"s_nop 0\n"
"s_waitcnt lgkmcnt(8)\n"
"v_mfma_f32_16x16x32_bf16 v[64:67], a[144:147], a[64:67], v[160:163]\n"
"v_cvt_pk_bf16_f32 v140, v76, v77\n"
"v_cvt_pk_bf16_f32 v141, v78, v79\n"
"v_mfma_f32_16x16x32_bf16 v[64:67], a[148:151], a[80:83], v[64:67]\n"
"s_xor_b32 s85, s85, 0x8000\n"
"v_mfma_f32_16x16x32_bf16 v[64:67], a[152:155], a[96:99], v[64:67]\n"
"v_cvt_pk_bf16_f32 v142, v92, v93\n"
"v_cvt_pk_bf16_f32 v143, v94, v95\n"
"v_mfma_f32_16x16x32_bf16 v[64:67], a[156:159], a[112:115], v[64:67]\n"
"s_mov_b32 m0, s85\n"
"v_mfma_f32_16x16x32_bf16 v[68:71], a[156:159], a[116:119], v[164:167]\n"
"v_cvt_pk_bf16_f32 v156, v108, v109\n"
"v_cvt_pk_bf16_f32 v157, v110, v111\n"
"v_mfma_f32_16x16x32_bf16 v[68:71], a[152:155], a[100:103], v[68:71]\n"
"buffer_load_dwordx4 v254, s[96:99], s67 offen sc1 lds\n"
"v_mfma_f32_16x16x32_bf16 v[68:71], a[148:151], a[84:87], v[68:71]\n"
"v_cvt_pk_bf16_f32 v158, v124, v125\n"
"v_cvt_pk_bf16_f32 v159, v126, v127\n"
"v_mfma_f32_16x16x32_bf16 v[68:71], a[144:147], a[68:71], v[68:71]\n"
"v_mfma_f32_16x16x32_bf16 v[72:75], a[144:147], a[72:75], v[168:171]\n"
"buffer_load_dwordx4 v254, s[96:99], s67 offen offset:1024 sc1 lds\n"
"v_mfma_f32_16x16x32_bf16 v[72:75], a[148:151], a[88:91], v[72:75]\n"
"ds_read_b128 a[184:187], v193 offset:2048\n"
"ds_read_b128 a[216:219], v193 offset:10240\n"
"v_mfma_f32_16x16x32_bf16 v[72:75], a[152:155], a[104:107], v[72:75]\n"
"v_mfma_f32_16x16x32_bf16 v[72:75], a[156:159], a[120:123], v[72:75]\n"
"ds_read_b128 a[188:191], v193 offset:3072\n"
"v_mfma_f32_16x16x32_bf16 v[76:79], a[156:159], a[124:127], v[172:175]\n"
"ds_read_b128 a[220:223], v193 offset:11264\n"
"v_mfma_f32_16x16x32_bf16 v[76:79], a[152:155], a[108:111], v[76:79]\n"
"ds_read_b128 a[192:195], v194 offset:4096\n"
"v_mfma_f32_16x16x32_bf16 v[76:79], a[148:151], a[92:95], v[76:79]\n"
"ds_read_b128 a[224:227], v194 offset:12288\n"
"s_waitcnt lgkmcnt(10)\n"
"v_mfma_f32_16x16x32_bf16 v[76:79], a[144:147], a[76:79], v[76:79]\n"
"buffer_load_dwordx4 v254, s[96:99], s67 offen offset:2048 sc1 lds\n"
"v_mfma_f32_16x16x32_bf16 v[80:83], a[160:163], a[64:67], v[160:163]\n"
"ds_read_b128 a[144:147], v192 offset:8192\n"
"ds_read_b128 a[148:151], v192 offset:9216\n"
"v_mfma_f32_16x16x32_bf16 v[80:83], a[164:167], a[80:83], v[80:83]\n"
"v_mfma_f32_16x16x32_bf16 v[80:83], a[168:171], a[96:99], v[80:83]\n"
"ds_read_b128 a[152:155], v192 offset:10240\n"
"ds_read_b128 a[156:159], v192 offset:11264\n"
"v_mfma_f32_16x16x32_bf16 v[80:83], a[172:175], a[112:115], v[80:83]\n"
"v_mfma_f32_16x16x32_bf16 v[84:87], a[172:175], a[116:119], v[164:167]\n"
"buffer_load_dwordx4 v254, s[96:99], s67 offen offset:3072 sc1 lds\n"
"v_mfma_f32_16x16x32_bf16 v[84:87], a[168:171], a[100:103], v[84:87]\n"
"v_exp_f32 v64, v64\n"
"v_mfma_f32_16x16x32_bf16 v[84:87], a[164:167], a[84:87], v[84:87]\n"
"s_add_u32 s67, s67, 0x4000\n"
"s_xor_b32 s84, s84, 0x8000\n"
"v_mfma_f32_16x16x32_bf16 v[84:87], a[160:163], a[68:71], v[84:87]\n"
"v_exp_f32 v65, v65\n"
"v_mfma_f32_16x16x32_bf16 v[88:91], a[160:163], a[72:75], v[168:171]\n"
"s_mov_b32 m0, s84\n"
"ds_read_b128 a[196:199], v194 offset:5120\n"
"v_mfma_f32_16x16x32_bf16 v[88:91], a[164:167], a[88:91], v[88:91]\n"
"v_exp_f32 v66, v66\n"
"v_mfma_f32_16x16x32_bf16 v[88:91], a[168:171], a[104:107], v[88:91]\n"
"buffer_load_dwordx4 v254, s[28:31], s67 offen sc1 lds\n"
"v_mfma_f32_16x16x32_bf16 v[88:91], a[172:175], a[120:123], v[88:91]\n"
"v_exp_f32 v67, v67\n"
"v_mfma_f32_16x16x32_bf16 v[92:95], a[172:175], a[124:127], v[172:175]\n"
"ds_read_b128 a[228:231], v194 offset:13312\n"
"ds_read_b128 a[200:203], v194 offset:6144\n"
"v_mfma_f32_16x16x32_bf16 v[92:95], a[168:171], a[108:111], v[92:95]\n"
"v_exp_f32 v80, v80\n"
"v_mfma_f32_16x16x32_bf16 v[92:95], a[164:167], a[92:95], v[92:95]\n"
"ds_read_b128 a[232:235], v194 offset:14336\n"
"s_waitcnt lgkmcnt(4)\n"
"v_mfma_f32_16x16x32_bf16 v[92:95], a[160:163], a[76:79], v[92:95]\n"
"v_exp_f32 v81, v81\n"
"v_mfma_f32_16x16x32_bf16 v[96:99], a[144:147], a[64:67], v[160:163]\n"
"ds_read_b128 a[160:163], v192 offset:12288\n"
"ds_read_b128 a[164:167], v192 offset:13312\n"
"v_mfma_f32_16x16x32_bf16 v[96:99], a[148:151], a[80:83], v[96:99]\n"
"v_exp_f32 v82, v82\n"
"v_mfma_f32_16x16x32_bf16 v[96:99], a[152:155], a[96:99], v[96:99]\n"
"ds_read_b128 a[168:171], v192 offset:14336\n"
"ds_read_b128 a[172:175], v192 offset:15360\n"
"v_mfma_f32_16x16x32_bf16 v[96:99], a[156:159], a[112:115], v[96:99]\n"
"v_exp_f32 v83, v83\n"
"v_mfma_f32_16x16x32_bf16 v[100:103], a[156:159], a[116:119], v[164:167]\n"
"buffer_load_dwordx4 v254, s[28:31], s67 offen offset:1024 sc1 lds\n"
"v_mfma_f32_16x16x32_bf16 v[100:103], a[152:155], a[100:103], v[100:103]\n"
"v_exp_f32 v68, v68\n"
"v_mfma_f32_16x16x32_bf16 v[100:103], a[148:151], a[84:87], v[100:103]\n"
"buffer_load_dwordx4 v254, s[28:31], s67 offen offset:2048 sc1 lds\n"
"v_mfma_f32_16x16x32_bf16 v[100:103], a[144:147], a[68:71], v[100:103]\n"
"v_exp_f32 v69, v69\n"
"v_mfma_f32_16x16x32_bf16 v[104:107], a[144:147], a[72:75], v[168:171]\n"
"ds_read_b128 a[204:207], v194 offset:7168\n"
"ds_read_b128 a[236:239], v194 offset:15360\n"
"v_mfma_f32_16x16x32_bf16 v[104:107], a[148:151], a[88:91], v[104:107]\n"
"v_exp_f32 v70, v70\n"
"v_mfma_f32_16x16x32_bf16 v[104:107], a[152:155], a[104:107], v[104:107]\n"
"v_mfma_f32_16x16x32_bf16 v[104:107], a[156:159], a[120:123], v[104:107]\n"
"v_exp_f32 v71, v71\n"
"v_mfma_f32_16x16x32_bf16 v[108:111], a[156:159], a[124:127], v[172:175]\n"
"buffer_load_dwordx4 v254, s[28:31], s67 offen offset:3072 sc1 lds\n"
"v_mfma_f32_16x16x32_bf16 v[108:111], a[152:155], a[108:111], v[108:111]\n"
"v_exp_f32 v84, v84\n"
"v_mfma_f32_16x16x32_bf16 v[108:111], a[148:151], a[92:95], v[108:111]\n"
"global_load_dword a255, v248, s[4:5]\n"
"s_waitcnt lgkmcnt(2)\n"
"v_mfma_f32_16x16x32_bf16 v[108:111], a[144:147], a[76:79], v[108:111]\n"
"v_exp_f32 v85, v85\n"
"v_mfma_f32_16x16x32_bf16 v[112:115], a[160:163], a[64:67], v[160:163]\n"
"v_mfma_f32_16x16x32_bf16 v[112:115], a[164:167], a[80:83], v[112:115]\n"
"v_exp_f32 v86, v86\n"
"v_mfma_f32_16x16x32_bf16 v[112:115], a[168:171], a[96:99], v[112:115]\n"
"v_mfma_f32_16x16x32_bf16 v[112:115], a[172:175], a[112:115], v[112:115]\n"
"v_exp_f32 v87, v87\n"
"v_mfma_f32_16x16x32_bf16 v[116:119], a[172:175], a[116:119], v[164:167]\n"
"v_mfma_f32_16x16x32_bf16 v[116:119], a[168:171], a[100:103], v[116:119]\n"
"v_exp_f32 v72, v72\n"
"v_mfma_f32_16x16x32_bf16 v[116:119], a[164:167], a[84:87], v[116:119]\n"
"v_mfma_f32_16x16x32_bf16 v[116:119], a[160:163], a[68:71], v[116:119]\n"
"v_exp_f32 v73, v73\n"
"v_mfma_f32_16x16x32_bf16 v[120:123], a[160:163], a[72:75], v[168:171]\n"
"v_mfma_f32_16x16x32_bf16 v[120:123], a[164:167], a[88:91], v[120:123]\n"
"v_exp_f32 v74, v74\n"
"v_mfma_f32_16x16x32_bf16 v[120:123], a[168:171], a[104:107], v[120:123]\n"
"v_mfma_f32_16x16x32_bf16 v[120:123], a[172:175], a[120:123], v[120:123]\n"
"v_exp_f32 v75, v75\n"
"v_mfma_f32_16x16x32_bf16 v[124:127], a[172:175], a[124:127], v[172:175]\n"
"v_mfma_f32_16x16x32_bf16 v[124:127], a[168:171], a[108:111], v[124:127]\n"
"v_exp_f32 v88, v88\n"
"v_mfma_f32_16x16x32_bf16 v[124:127], a[164:167], a[92:95], v[124:127]\n"
"v_mfma_f32_16x16x32_bf16 v[124:127], a[160:163], a[76:79], v[124:127]\n"
"v_exp_f32 v89, v89\n"
"v_mfma_f32_16x16x32_bf16 v[0:3], a[176:179], v[128:131], 0\n"
"v_mfma_f32_16x16x32_bf16 v[0:3], a[208:211], v[144:147], v[0:3]\n"
"v_exp_f32 v90, v90\n"
"v_mfma_f32_16x16x32_bf16 v[16:19], a[212:215], v[144:147], 0\n"
"v_mfma_f32_16x16x32_bf16 v[16:19], a[180:183], v[128:131], v[16:19]\n"
"v_exp_f32 v91, v91\n"
"v_mfma_f32_16x16x32_bf16 v[32:35], a[184:187], v[128:131], 0\n"
"v_mfma_f32_16x16x32_bf16 v[32:35], a[216:219], v[144:147], v[32:35]\n"
"v_exp_f32 v76, v76\n"
"v_mfma_f32_16x16x32_bf16 v[48:51], a[220:223], v[144:147], 0\n"
"s_waitcnt lgkmcnt(0)\n"
"v_mfma_f32_16x16x32_bf16 v[48:51], a[188:191], v[128:131], v[48:51]\n"
"v_exp_f32 v77, v77\n"
"v_mfma_f32_16x16x32_bf16 a[0:3], a[192:195], v[128:131], 0\n"
"v_mfma_f32_16x16x32_bf16 a[0:3], a[224:227], v[144:147], a[0:3]\n"
"v_exp_f32 v78, v78\n"
"v_mfma_f32_16x16x32_bf16 a[16:19], a[228:231], v[144:147], 0\n"
"v_mfma_f32_16x16x32_bf16 a[16:19], a[196:199], v[128:131], a[16:19]\n"
"v_exp_f32 v79, v79\n"
"v_mfma_f32_16x16x32_bf16 a[32:35], a[200:203], v[128:131], 0\n"
"v_exp_f32 v92, v92\n"
"v_mfma_f32_16x16x32_bf16 a[32:35], a[232:235], v[144:147], a[32:35]\n"
"v_exp_f32 v93, v93\n"
"v_mfma_f32_16x16x32_bf16 a[48:51], a[236:239], v[144:147], 0\n"
"v_exp_f32 v94, v94\n"
"v_mfma_f32_16x16x32_bf16 a[48:51], a[204:207], v[128:131], a[48:51]\n"
"v_exp_f32 v95, v95\n"
"v_mfma_f32_16x16x32_bf16 a[128:131], v[200:203], v[128:131], 0\n"
"v_exp_f32 v96, v96\n"
"v_mfma_f32_16x16x32_bf16 a[128:131], v[200:203], v[144:147], a[128:131]\n"
"v_exp_f32 v97, v97\n"
"v_mfma_f32_16x16x32_bf16 v[4:7], a[176:179], v[132:135], 0\n"
"v_cvt_pk_bf16_f32 v128, v64, v65\n"
"v_cvt_pk_bf16_f32 v129, v66, v67\n"
"v_mfma_f32_16x16x32_bf16 v[4:7], a[208:211], v[148:151], v[4:7]\n"
"v_exp_f32 v98, v98\n"
"v_mfma_f32_16x16x32_bf16 v[20:23], a[212:215], v[148:151], 0\n"
"v_cvt_pk_bf16_f32 v130, v80, v81\n"
"v_cvt_pk_bf16_f32 v131, v82, v83\n"
"v_mfma_f32_16x16x32_bf16 v[20:23], a[180:183], v[132:135], v[20:23]\n"
"v_exp_f32 v99, v99\n"
"v_mfma_f32_16x16x32_bf16 v[36:39], a[184:187], v[132:135], 0\n"
"v_exp_f32 v112, v112\n"
"v_mfma_f32_16x16x32_bf16 v[36:39], a[216:219], v[148:151], v[36:39]\n"
"v_exp_f32 v113, v113\n"
"v_mfma_f32_16x16x32_bf16 v[52:55], a[220:223], v[148:151], 0\n"
"v_exp_f32 v114, v114\n"
"v_mfma_f32_16x16x32_bf16 v[52:55], a[188:191], v[132:135], v[52:55]\n"
"v_exp_f32 v115, v115\n"
"v_mfma_f32_16x16x32_bf16 a[4:7], a[192:195], v[132:135], 0\n"
"v_exp_f32 v100, v100\n"
"v_mfma_f32_16x16x32_bf16 a[4:7], a[224:227], v[148:151], a[4:7]\n"
"v_exp_f32 v101, v101\n"
"v_mfma_f32_16x16x32_bf16 a[20:23], a[228:231], v[148:151], 0\n"
"v_cvt_pk_bf16_f32 v144, v96, v97\n"
"v_cvt_pk_bf16_f32 v145, v98, v99\n"
"v_mfma_f32_16x16x32_bf16 a[20:23], a[196:199], v[132:135], a[20:23]\n"
"v_exp_f32 v102, v102\n"
"v_mfma_f32_16x16x32_bf16 a[36:39], a[200:203], v[132:135], 0\n"
"v_cvt_pk_bf16_f32 v146, v112, v113\n"
"v_cvt_pk_bf16_f32 v147, v114, v115\n"
"v_mfma_f32_16x16x32_bf16 a[36:39], a[232:235], v[148:151], a[36:39]\n"
"v_exp_f32 v103, v103\n"
"v_mfma_f32_16x16x32_bf16 a[52:55], a[236:239], v[148:151], 0\n"
"v_exp_f32 v116, v116\n"
"v_mfma_f32_16x16x32_bf16 a[52:55], a[204:207], v[132:135], a[52:55]\n"
"v_exp_f32 v117, v117\n"
"v_mfma_f32_16x16x32_bf16 a[132:135], v[200:203], v[132:135], 0\n"
"v_mfma_f32_16x16x32_bf16 a[132:135], v[200:203], v[148:151], a[132:135]\n"
"v_exp_f32 v118, v118\n"
"v_mfma_f32_16x16x32_bf16 v[8:11], a[176:179], v[136:139], 0\n"
"v_cvt_pk_bf16_f32 v132, v68, v69\n"
"v_cvt_pk_bf16_f32 v133, v70, v71\n"
"v_mfma_f32_16x16x32_bf16 v[8:11], a[208:211], v[152:155], v[8:11]\n"
"v_exp_f32 v119, v119\n"
"v_mfma_f32_16x16x32_bf16 v[24:27], a[212:215], v[152:155], 0\n"
"v_cvt_pk_bf16_f32 v134, v84, v85\n"
"v_cvt_pk_bf16_f32 v135, v86, v87\n"
"v_mfma_f32_16x16x32_bf16 v[24:27], a[180:183], v[136:139], v[24:27]\n"
"v_exp_f32 v104, v104\n"
"v_mfma_f32_16x16x32_bf16 v[40:43], a[184:187], v[136:139], 0\n"
"v_cvt_pk_bf16_f32 v148, v100, v101\n"
"v_cvt_pk_bf16_f32 v149, v102, v103\n"
"v_mfma_f32_16x16x32_bf16 v[40:43], a[216:219], v[152:155], v[40:43]\n"
"v_exp_f32 v105, v105\n"
"v_mfma_f32_16x16x32_bf16 v[56:59], a[220:223], v[152:155], 0\n"
"v_mfma_f32_16x16x32_bf16 v[56:59], a[188:191], v[136:139], v[56:59]\n"
"v_mfma_f32_16x16x32_bf16 a[8:11], a[192:195], v[136:139], 0\n"
"s_waitcnt vmcnt(1) lgkmcnt(0)\n"
"s_barrier\n"
"v_mfma_f32_16x16x32_bf16 a[8:11], a[224:227], v[152:155], a[8:11]\n"
"v_exp_f32 v106, v106\n"
"v_mfma_f32_16x16x32_bf16 a[24:27], a[228:231], v[152:155], 0\n"
"v_xor_b32 v192, 0x8000, v192\n"
"v_xor_b32 v193, 0x8000, v193\n"
"v_mfma_f32_16x16x32_bf16 a[24:27], a[196:199], v[136:139], a[24:27]\n"
"v_exp_f32 v107, v107\n"
"v_mfma_f32_16x16x32_bf16 a[40:43], a[200:203], v[136:139], 0\n"
"ds_read_b128 a[144:147], v192\n"
"ds_read_b128 a[148:151], v192 offset:1024\n"
"v_mfma_f32_16x16x32_bf16 a[40:43], a[232:235], v[152:155], a[40:43]\n"
"v_exp_f32 v120, v120\n"
"v_mfma_f32_16x16x32_bf16 a[56:59], a[236:239], v[152:155], 0\n"
"ds_read_b128 a[152:155], v192 offset:2048\n"
"ds_read_b128 a[156:159], v192 offset:3072\n"
"v_mfma_f32_16x16x32_bf16 a[56:59], a[204:207], v[136:139], a[56:59]\n"
"v_exp_f32 v121, v121\n"
"v_mfma_f32_16x16x32_bf16 a[136:139], v[200:203], v[136:139], 0\n"
"ds_read_b128 a[160:163], v192 offset:4096\n"
"ds_read_b128 a[164:167], v192 offset:5120\n"
"v_mfma_f32_16x16x32_bf16 a[136:139], v[200:203], v[152:155], a[136:139]\n"
"v_exp_f32 v122, v122\n"
"v_mfma_f32_16x16x32_bf16 v[12:15], a[176:179], v[140:143], 0\n"
"ds_read_b128 a[168:171], v192 offset:6144\n"
"ds_read_b128 a[172:175], v192 offset:7168\n"
"v_mfma_f32_16x16x32_bf16 v[12:15], a[208:211], v[156:159], v[12:15]\n"
"v_exp_f32 v123, v123\n"
"v_mfma_f32_16x16x32_bf16 v[28:31], a[212:215], v[156:159], 0\n"
"v_cvt_pk_bf16_f32 v136, v72, v73\n"
"v_cvt_pk_bf16_f32 v137, v74, v75\n"
"v_mfma_f32_16x16x32_bf16 v[28:31], a[180:183], v[140:143], v[28:31]\n"
"v_exp_f32 v108, v108\n"
"v_mfma_f32_16x16x32_bf16 v[44:47], a[184:187], v[140:143], 0\n"
"ds_read_b128 a[176:179], v193\n"
"ds_read_b128 a[208:211], v193 offset:8192\n"
"v_mfma_f32_16x16x32_bf16 v[44:47], a[216:219], v[156:159], v[44:47]\n"
"v_exp_f32 v109, v109\n"
"v_mfma_f32_16x16x32_bf16 v[60:63], a[220:223], v[156:159], 0\n"
"ds_read_b128 a[180:183], v193 offset:1024\n"
"ds_read_b128 a[212:215], v193 offset:9216\n"
"v_mfma_f32_16x16x32_bf16 v[60:63], a[188:191], v[140:143], v[60:63]\n"
"v_exp_f32 v110, v110\n"
"v_mfma_f32_16x16x32_bf16 a[12:15], a[192:195], v[140:143], 0\n"
"v_cvt_pk_bf16_f32 v138, v88, v89\n"
"v_cvt_pk_bf16_f32 v139, v90, v91\n"
"v_mfma_f32_16x16x32_bf16 a[12:15], a[224:227], v[156:159], a[12:15]\n"
"v_exp_f32 v111, v111\n"
"v_mfma_f32_16x16x32_bf16 a[28:31], a[228:231], v[156:159], 0\n"
"v_cvt_pk_bf16_f32 v150, v116, v117\n"
"v_cvt_pk_bf16_f32 v151, v118, v119\n"
"v_mfma_f32_16x16x32_bf16 a[28:31], a[196:199], v[140:143], a[28:31]\n"
"v_exp_f32 v124, v124\n"
"v_mfma_f32_16x16x32_bf16 a[44:47], a[200:203], v[140:143], 0\n"
"v_cvt_pk_bf16_f32 v152, v104, v105\n"
"v_cvt_pk_bf16_f32 v153, v106, v107\n"
"v_mfma_f32_16x16x32_bf16 a[44:47], a[232:235], v[156:159], a[44:47]\n"
"v_exp_f32 v125, v125\n"
"v_mfma_f32_16x16x32_bf16 a[60:63], a[236:239], v[156:159], 0\n"
"v_xor_b32 v194, 0x8000, v194\n"
"v_cvt_pk_bf16_f32 v154, v120, v121\n"
"v_mfma_f32_16x16x32_bf16 a[60:63], a[204:207], v[140:143], a[60:63]\n"
"v_exp_f32 v126, v126\n"
"v_mfma_f32_16x16x32_bf16 a[140:143], v[200:203], v[140:143], 0\n"
"s_add_u32 s62, s62, 1\n"
"s_mov_b64 exec, s[34:35]\n"
"buffer_store_dwordx2 v[250:251], v249, s[92:95], s53 offen sc1\n"
"s_mov_b64 exec, -1\n"
"s_cmp_ge_u32 s62, s71\n"
"v_cvt_pk_bf16_f32 v155, v122, v123\n"
"v_mfma_f32_16x16x32_bf16 a[140:143], v[200:203], v[156:159], a[140:143]\n"
"v_exp_f32 v127, v127\n"
"s_cbranch_scc1 .Lafter1_44_%=\n"
"s_nop 0\n"
".Lfull1_19_%=:\n"
"s_waitcnt lgkmcnt(8)\n"
"v_mfma_f32_16x16x32_bf16 v[64:67], a[144:147], a[64:67], v[160:163]\n"
"v_cvt_pk_bf16_f32 v140, v76, v77\n"
"v_cvt_pk_bf16_f32 v141, v78, v79\n"
"v_mfma_f32_16x16x32_bf16 v[64:67], a[148:151], a[80:83], v[64:67]\n"
"s_xor_b32 s85, s85, 0x8000\n"
"v_mfma_f32_16x16x32_bf16 v[64:67], a[152:155], a[96:99], v[64:67]\n"
"v_cvt_pk_bf16_f32 v142, v92, v93\n"
"v_cvt_pk_bf16_f32 v143, v94, v95\n"
"v_mfma_f32_16x16x32_bf16 v[64:67], a[156:159], a[112:115], v[64:67]\n"
"s_mov_b32 m0, s85\n"
"v_mfma_f32_16x16x32_bf16 v[68:71], a[156:159], a[116:119], v[164:167]\n"
"v_cvt_pk_bf16_f32 v156, v108, v109\n"
"v_cvt_pk_bf16_f32 v157, v110, v111\n"
"v_mfma_f32_16x16x32_bf16 v[68:71], a[152:155], a[100:103], v[68:71]\n"
"buffer_load_dwordx4 v254, s[96:99], s67 offen sc1 lds\n"
"v_mfma_f32_16x16x32_bf16 v[68:71], a[148:151], a[84:87], v[68:71]\n"
"v_cvt_pk_bf16_f32 v158, v124, v125\n"
"v_cvt_pk_bf16_f32 v159, v126, v127\n"
"v_mfma_f32_16x16x32_bf16 v[68:71], a[144:147], a[68:71], v[68:71]\n"
"v_mfma_f32_16x16x32_bf16 v[72:75], a[144:147], a[72:75], v[168:171]\n"
"buffer_load_dwordx4 v254, s[96:99], s67 offen offset:1024 sc1 lds\n"
"v_mfma_f32_16x16x32_bf16 v[72:75], a[148:151], a[88:91], v[72:75]\n"
"ds_read_b128 a[184:187], v193 offset:2048\n"
"ds_read_b128 a[216:219], v193 offset:10240\n"
"v_mfma_f32_16x16x32_bf16 v[72:75], a[152:155], a[104:107], v[72:75]\n"
"v_mfma_f32_16x16x32_bf16 v[72:75], a[156:159], a[120:123], v[72:75]\n"
"ds_read_b128 a[188:191], v193 offset:3072\n"
"v_mfma_f32_16x16x32_bf16 v[76:79], a[156:159], a[124:127], v[172:175]\n"
"ds_read_b128 a[220:223], v193 offset:11264\n"
"v_mfma_f32_16x16x32_bf16 v[76:79], a[152:155], a[108:111], v[76:79]\n"
"ds_read_b128 a[192:195], v194 offset:4096\n"
"v_mfma_f32_16x16x32_bf16 v[76:79], a[148:151], a[92:95], v[76:79]\n"
"ds_read_b128 a[224:227], v194 offset:12288\n"
"s_waitcnt lgkmcnt(10)\n"
"v_mfma_f32_16x16x32_bf16 v[76:79], a[144:147], a[76:79], v[76:79]\n"
"buffer_load_dwordx4 v254, s[96:99], s67 offen offset:2048 sc1 lds\n"
"v_mfma_f32_16x16x32_bf16 v[80:83], a[160:163], a[64:67], v[160:163]\n"
"ds_read_b128 a[144:147], v192 offset:8192\n"
"ds_read_b128 a[148:151], v192 offset:9216\n"
"v_mfma_f32_16x16x32_bf16 v[80:83], a[164:167], a[80:83], v[80:83]\n"
"v_mfma_f32_16x16x32_bf16 v[80:83], a[168:171], a[96:99], v[80:83]\n"
"ds_read_b128 a[152:155], v192 offset:10240\n"
"ds_read_b128 a[156:159], v192 offset:11264\n"
"v_mfma_f32_16x16x32_bf16 v[80:83], a[172:175], a[112:115], v[80:83]\n"
"v_mfma_f32_16x16x32_bf16 v[84:87], a[172:175], a[116:119], v[164:167]\n"
"buffer_load_dwordx4 v254, s[96:99], s67 offen offset:3072 sc1 lds\n"
"v_mfma_f32_16x16x32_bf16 v[84:87], a[168:171], a[100:103], v[84:87]\n"
"v_exp_f32 v64, v64\n"
"v_mfma_f32_16x16x32_bf16 v[84:87], a[164:167], a[84:87], v[84:87]\n"
"s_add_u32 s67, s67, 0x4000\n"
"s_xor_b32 s84, s84, 0x8000\n"
"v_mfma_f32_16x16x32_bf16 v[84:87], a[160:163], a[68:71], v[84:87]\n"
"v_exp_f32 v65, v65\n"
"v_mfma_f32_16x16x32_bf16 v[88:91], a[160:163], a[72:75], v[168:171]\n"
"s_mov_b32 m0, s84\n"
"ds_read_b128 a[196:199], v194 offset:5120\n"
"v_mfma_f32_16x16x32_bf16 v[88:91], a[164:167], a[88:91], v[88:91]\n"
"v_exp_f32 v66, v66\n"
"v_mfma_f32_16x16x32_bf16 v[88:91], a[168:171], a[104:107], v[88:91]\n"
"buffer_load_dwordx4 v254, s[28:31], s67 offen sc1 lds\n"
"v_mfma_f32_16x16x32_bf16 v[88:91], a[172:175], a[120:123], v[88:91]\n"
"v_exp_f32 v67, v67\n"
"v_mfma_f32_16x16x32_bf16 v[92:95], a[172:175], a[124:127], v[172:175]\n"
"ds_read_b128 a[228:231], v194 offset:13312\n"
"ds_read_b128 a[200:203], v194 offset:6144\n"
"v_mfma_f32_16x16x32_bf16 v[92:95], a[168:171], a[108:111], v[92:95]\n"
"v_exp_f32 v80, v80\n"
"v_mfma_f32_16x16x32_bf16 v[92:95], a[164:167], a[92:95], v[92:95]\n"
"ds_read_b128 a[232:235], v194 offset:14336\n"
"s_waitcnt lgkmcnt(4)\n"
"v_mfma_f32_16x16x32_bf16 v[92:95], a[160:163], a[76:79], v[92:95]\n"
"v_exp_f32 v81, v81\n"
"v_mfma_f32_16x16x32_bf16 v[96:99], a[144:147], a[64:67], v[160:163]\n"
"ds_read_b128 a[160:163], v192 offset:12288\n"
"ds_read_b128 a[164:167], v192 offset:13312\n"
"v_mfma_f32_16x16x32_bf16 v[96:99], a[148:151], a[80:83], v[96:99]\n"
"v_exp_f32 v82, v82\n"
"v_mfma_f32_16x16x32_bf16 v[96:99], a[152:155], a[96:99], v[96:99]\n"
"ds_read_b128 a[168:171], v192 offset:14336\n"
"ds_read_b128 a[172:175], v192 offset:15360\n"
"v_mfma_f32_16x16x32_bf16 v[96:99], a[156:159], a[112:115], v[96:99]\n"
"v_exp_f32 v83, v83\n"
"v_mfma_f32_16x16x32_bf16 v[100:103], a[156:159], a[116:119], v[164:167]\n"
"buffer_load_dwordx4 v254, s[28:31], s67 offen offset:1024 sc1 lds\n"
"v_mfma_f32_16x16x32_bf16 v[100:103], a[152:155], a[100:103], v[100:103]\n"
"v_exp_f32 v68, v68\n"
"v_mfma_f32_16x16x32_bf16 v[100:103], a[148:151], a[84:87], v[100:103]\n"
"buffer_load_dwordx4 v254, s[28:31], s67 offen offset:2048 sc1 lds\n"
"v_mfma_f32_16x16x32_bf16 v[100:103], a[144:147], a[68:71], v[100:103]\n"
"v_exp_f32 v69, v69\n"
"v_mfma_f32_16x16x32_bf16 v[104:107], a[144:147], a[72:75], v[168:171]\n"
"ds_read_b128 a[204:207], v194 offset:7168\n"
"ds_read_b128 a[236:239], v194 offset:15360\n"
"v_mfma_f32_16x16x32_bf16 v[104:107], a[148:151], a[88:91], v[104:107]\n"
"v_exp_f32 v70, v70\n"
"v_mfma_f32_16x16x32_bf16 v[104:107], a[152:155], a[104:107], v[104:107]\n"
"v_mfma_f32_16x16x32_bf16 v[104:107], a[156:159], a[120:123], v[104:107]\n"
"v_exp_f32 v71, v71\n"
"v_mfma_f32_16x16x32_bf16 v[108:111], a[156:159], a[124:127], v[172:175]\n"
"buffer_load_dwordx4 v254, s[28:31], s67 offen offset:3072 sc1 lds\n"
"v_mfma_f32_16x16x32_bf16 v[108:111], a[152:155], a[108:111], v[108:111]\n"
"v_exp_f32 v84, v84\n"
"v_mfma_f32_16x16x32_bf16 v[108:111], a[148:151], a[92:95], v[108:111]\n"
"global_load_dword a255, v248, s[4:5]\n"
"s_waitcnt lgkmcnt(2)\n"
"v_mfma_f32_16x16x32_bf16 v[108:111], a[144:147], a[76:79], v[108:111]\n"
"v_exp_f32 v85, v85\n"
"v_mfma_f32_16x16x32_bf16 v[112:115], a[160:163], a[64:67], v[160:163]\n"
"v_mfma_f32_16x16x32_bf16 v[112:115], a[164:167], a[80:83], v[112:115]\n"
"v_exp_f32 v86, v86\n"
"v_mfma_f32_16x16x32_bf16 v[112:115], a[168:171], a[96:99], v[112:115]\n"
"v_mfma_f32_16x16x32_bf16 v[112:115], a[172:175], a[112:115], v[112:115]\n"
"v_exp_f32 v87, v87\n"
"v_mfma_f32_16x16x32_bf16 v[116:119], a[172:175], a[116:119], v[164:167]\n"
"v_mfma_f32_16x16x32_bf16 v[116:119], a[168:171], a[100:103], v[116:119]\n"
"v_exp_f32 v72, v72\n"
"v_mfma_f32_16x16x32_bf16 v[116:119], a[164:167], a[84:87], v[116:119]\n"
"v_mfma_f32_16x16x32_bf16 v[116:119], a[160:163], a[68:71], v[116:119]\n"
"v_exp_f32 v73, v73\n"
"v_mfma_f32_16x16x32_bf16 v[120:123], a[160:163], a[72:75], v[168:171]\n"
"v_mfma_f32_16x16x32_bf16 v[120:123], a[164:167], a[88:91], v[120:123]\n"
"v_exp_f32 v74, v74\n"
"v_mfma_f32_16x16x32_bf16 v[120:123], a[168:171], a[104:107], v[120:123]\n"
"v_mfma_f32_16x16x32_bf16 v[120:123], a[172:175], a[120:123], v[120:123]\n"
"v_exp_f32 v75, v75\n"
"v_mfma_f32_16x16x32_bf16 v[124:127], a[172:175], a[124:127], v[172:175]\n"
"v_mfma_f32_16x16x32_bf16 v[124:127], a[168:171], a[108:111], v[124:127]\n"
"v_exp_f32 v88, v88\n"
"v_mfma_f32_16x16x32_bf16 v[124:127], a[164:167], a[92:95], v[124:127]\n"
"v_mfma_f32_16x16x32_bf16 v[124:127], a[160:163], a[76:79], v[124:127]\n"
"v_exp_f32 v89, v89\n"
"v_mfma_f32_16x16x32_bf16 v[0:3], a[176:179], v[128:131], v[0:3]\n"
"v_mfma_f32_16x16x32_bf16 v[0:3], a[208:211], v[144:147], v[0:3]\n"
"v_exp_f32 v90, v90\n"
"v_mfma_f32_16x16x32_bf16 v[16:19], a[212:215], v[144:147], v[16:19]\n"
"v_mfma_f32_16x16x32_bf16 v[16:19], a[180:183], v[128:131], v[16:19]\n"
"v_exp_f32 v91, v91\n"
"v_mfma_f32_16x16x32_bf16 v[32:35], a[184:187], v[128:131], v[32:35]\n"
"v_mfma_f32_16x16x32_bf16 v[32:35], a[216:219], v[144:147], v[32:35]\n"
"v_exp_f32 v76, v76\n"
"v_mfma_f32_16x16x32_bf16 v[48:51], a[220:223], v[144:147], v[48:51]\n"
"s_waitcnt lgkmcnt(0)\n"
"v_mfma_f32_16x16x32_bf16 v[48:51], a[188:191], v[128:131], v[48:51]\n"
"v_exp_f32 v77, v77\n"
"v_mfma_f32_16x16x32_bf16 a[0:3], a[192:195], v[128:131], a[0:3]\n"
"v_mfma_f32_16x16x32_bf16 a[0:3], a[224:227], v[144:147], a[0:3]\n"
"v_exp_f32 v78, v78\n"
"v_mfma_f32_16x16x32_bf16 a[16:19], a[228:231], v[144:147], a[16:19]\n"
"v_mfma_f32_16x16x32_bf16 a[16:19], a[196:199], v[128:131], a[16:19]\n"
"v_exp_f32 v79, v79\n"
"v_mfma_f32_16x16x32_bf16 a[32:35], a[200:203], v[128:131], a[32:35]\n"
"v_exp_f32 v92, v92\n"
"v_mfma_f32_16x16x32_bf16 a[32:35], a[232:235], v[144:147], a[32:35]\n"
"v_exp_f32 v93, v93\n"
"v_mfma_f32_16x16x32_bf16 a[48:51], a[236:239], v[144:147], a[48:51]\n"
"v_exp_f32 v94, v94\n"
"v_mfma_f32_16x16x32_bf16 a[48:51], a[204:207], v[128:131], a[48:51]\n"
"v_exp_f32 v95, v95\n"
"v_mfma_f32_16x16x32_bf16 a[128:131], v[200:203], v[128:131], a[128:131]\n"
"v_exp_f32 v96, v96\n"
"v_mfma_f32_16x16x32_bf16 a[128:131], v[200:203], v[144:147], a[128:131]\n"
"v_exp_f32 v97, v97\n"
"v_mfma_f32_16x16x32_bf16 v[4:7], a[176:179], v[132:135], v[4:7]\n"
"v_cvt_pk_bf16_f32 v128, v64, v65\n"
"v_cvt_pk_bf16_f32 v129, v66, v67\n"
"v_mfma_f32_16x16x32_bf16 v[4:7], a[208:211], v[148:151], v[4:7]\n"
"v_exp_f32 v98, v98\n"
"v_mfma_f32_16x16x32_bf16 v[20:23], a[212:215], v[148:151], v[20:23]\n"
"v_cvt_pk_bf16_f32 v130, v80, v81\n"
"v_cvt_pk_bf16_f32 v131, v82, v83\n"
"v_mfma_f32_16x16x32_bf16 v[20:23], a[180:183], v[132:135], v[20:23]\n"
"v_exp_f32 v99, v99\n"
"v_mfma_f32_16x16x32_bf16 v[36:39], a[184:187], v[132:135], v[36:39]\n"
"v_exp_f32 v112, v112\n"
"v_mfma_f32_16x16x32_bf16 v[36:39], a[216:219], v[148:151], v[36:39]\n"
"v_exp_f32 v113, v113\n"
"v_mfma_f32_16x16x32_bf16 v[52:55], a[220:223], v[148:151], v[52:55]\n"
"v_exp_f32 v114, v114\n"
"v_mfma_f32_16x16x32_bf16 v[52:55], a[188:191], v[132:135], v[52:55]\n"
"v_exp_f32 v115, v115\n"
"v_mfma_f32_16x16x32_bf16 a[4:7], a[192:195], v[132:135], a[4:7]\n"
"v_exp_f32 v100, v100\n"
"v_mfma_f32_16x16x32_bf16 a[4:7], a[224:227], v[148:151], a[4:7]\n"
"v_exp_f32 v101, v101\n"
"v_mfma_f32_16x16x32_bf16 a[20:23], a[228:231], v[148:151], a[20:23]\n"
"v_cvt_pk_bf16_f32 v144, v96, v97\n"
"v_cvt_pk_bf16_f32 v145, v98, v99\n"
"v_mfma_f32_16x16x32_bf16 a[20:23], a[196:199], v[132:135], a[20:23]\n"
"v_exp_f32 v102, v102\n"
"v_mfma_f32_16x16x32_bf16 a[36:39], a[200:203], v[132:135], a[36:39]\n"
"v_cvt_pk_bf16_f32 v146, v112, v113\n"
"v_cvt_pk_bf16_f32 v147, v114, v115\n"
"v_mfma_f32_16x16x32_bf16 a[36:39], a[232:235], v[148:151], a[36:39]\n"
"v_exp_f32 v103, v103\n"
"v_mfma_f32_16x16x32_bf16 a[52:55], a[236:239], v[148:151], a[52:55]\n"
"v_exp_f32 v116, v116\n"
"v_mfma_f32_16x16x32_bf16 a[52:55], a[204:207], v[132:135], a[52:55]\n"
"v_exp_f32 v117, v117\n"
"v_mfma_f32_16x16x32_bf16 a[132:135], v[200:203], v[132:135], a[132:135]\n"
"v_mfma_f32_16x16x32_bf16 a[132:135], v[200:203], v[148:151], a[132:135]\n"
"v_exp_f32 v118, v118\n"
"v_mfma_f32_16x16x32_bf16 v[8:11], a[176:179], v[136:139], v[8:11]\n"
"v_cvt_pk_bf16_f32 v132, v68, v69\n"
"v_cvt_pk_bf16_f32 v133, v70, v71\n"
"v_mfma_f32_16x16x32_bf16 v[8:11], a[208:211], v[152:155], v[8:11]\n"
"v_exp_f32 v119, v119\n"
"v_mfma_f32_16x16x32_bf16 v[24:27], a[212:215], v[152:155], v[24:27]\n"
"v_cvt_pk_bf16_f32 v134, v84, v85\n"
"v_cvt_pk_bf16_f32 v135, v86, v87\n"
"v_mfma_f32_16x16x32_bf16 v[24:27], a[180:183], v[136:139], v[24:27]\n"
"v_exp_f32 v104, v104\n"
"v_mfma_f32_16x16x32_bf16 v[40:43], a[184:187], v[136:139], v[40:43]\n"
"v_cvt_pk_bf16_f32 v148, v100, v101\n"
"v_cvt_pk_bf16_f32 v149, v102, v103\n"
"v_mfma_f32_16x16x32_bf16 v[40:43], a[216:219], v[152:155], v[40:43]\n"
"v_exp_f32 v105, v105\n"
"v_mfma_f32_16x16x32_bf16 v[56:59], a[220:223], v[152:155], v[56:59]\n"
"v_mfma_f32_16x16x32_bf16 v[56:59], a[188:191], v[136:139], v[56:59]\n"
"v_mfma_f32_16x16x32_bf16 a[8:11], a[192:195], v[136:139], a[8:11]\n"
"s_waitcnt vmcnt(1) lgkmcnt(0)\n"
"s_barrier\n"
"v_mfma_f32_16x16x32_bf16 a[8:11], a[224:227], v[152:155], a[8:11]\n"
"v_exp_f32 v106, v106\n"
"v_mfma_f32_16x16x32_bf16 a[24:27], a[228:231], v[152:155], a[24:27]\n"
"v_xor_b32 v192, 0x8000, v192\n"
"v_xor_b32 v193, 0x8000, v193\n"
"v_mfma_f32_16x16x32_bf16 a[24:27], a[196:199], v[136:139], a[24:27]\n"
"v_exp_f32 v107, v107\n"
"v_mfma_f32_16x16x32_bf16 a[40:43], a[200:203], v[136:139], a[40:43]\n"
"ds_read_b128 a[144:147], v192\n"
"ds_read_b128 a[148:151], v192 offset:1024\n"
"v_mfma_f32_16x16x32_bf16 a[40:43], a[232:235], v[152:155], a[40:43]\n"
"v_exp_f32 v120, v120\n"
"v_mfma_f32_16x16x32_bf16 a[56:59], a[236:239], v[152:155], a[56:59]\n"
"ds_read_b128 a[152:155], v192 offset:2048\n"
"ds_read_b128 a[156:159], v192 offset:3072\n"
"v_mfma_f32_16x16x32_bf16 a[56:59], a[204:207], v[136:139], a[56:59]\n"
"v_exp_f32 v121, v121\n"
"v_mfma_f32_16x16x32_bf16 a[136:139], v[200:203], v[136:139], a[136:139]\n"
"ds_read_b128 a[160:163], v192 offset:4096\n"
"ds_read_b128 a[164:167], v192 offset:5120\n"
"v_mfma_f32_16x16x32_bf16 a[136:139], v[200:203], v[152:155], a[136:139]\n"
"v_exp_f32 v122, v122\n"
"v_mfma_f32_16x16x32_bf16 v[12:15], a[176:179], v[140:143], v[12:15]\n"
"ds_read_b128 a[168:171], v192 offset:6144\n"
"ds_read_b128 a[172:175], v192 offset:7168\n"
"v_mfma_f32_16x16x32_bf16 v[12:15], a[208:211], v[156:159], v[12:15]\n"
"v_exp_f32 v123, v123\n"
"v_mfma_f32_16x16x32_bf16 v[28:31], a[212:215], v[156:159], v[28:31]\n"
"v_cvt_pk_bf16_f32 v136, v72, v73\n"
"v_cvt_pk_bf16_f32 v137, v74, v75\n"
"v_mfma_f32_16x16x32_bf16 v[28:31], a[180:183], v[140:143], v[28:31]\n"
"v_exp_f32 v108, v108\n"
"v_mfma_f32_16x16x32_bf16 v[44:47], a[184:187], v[140:143], v[44:47]\n"
"ds_read_b128 a[176:179], v193\n"
"ds_read_b128 a[208:211], v193 offset:8192\n"
"v_mfma_f32_16x16x32_bf16 v[44:47], a[216:219], v[156:159], v[44:47]\n"
"v_exp_f32 v109, v109\n"
"v_mfma_f32_16x16x32_bf16 v[60:63], a[220:223], v[156:159], v[60:63]\n"
"ds_read_b128 a[180:183], v193 offset:1024\n"
"ds_read_b128 a[212:215], v193 offset:9216\n"
"v_mfma_f32_16x16x32_bf16 v[60:63], a[188:191], v[140:143], v[60:63]\n"
"v_exp_f32 v110, v110\n"
"v_mfma_f32_16x16x32_bf16 a[12:15], a[192:195], v[140:143], a[12:15]\n"
"v_cvt_pk_bf16_f32 v138, v88, v89\n"
"v_cvt_pk_bf16_f32 v139, v90, v91\n"
"v_mfma_f32_16x16x32_bf16 a[12:15], a[224:227], v[156:159], a[12:15]\n"
"v_exp_f32 v111, v111\n"
"v_mfma_f32_16x16x32_bf16 a[28:31], a[228:231], v[156:159], a[28:31]\n"
"v_cvt_pk_bf16_f32 v150, v116, v117\n"
"v_cvt_pk_bf16_f32 v151, v118, v119\n"
"v_mfma_f32_16x16x32_bf16 a[28:31], a[196:199], v[140:143], a[28:31]\n"
"v_exp_f32 v124, v124\n"
"v_mfma_f32_16x16x32_bf16 a[44:47], a[200:203], v[140:143], a[44:47]\n"
"v_cvt_pk_bf16_f32 v152, v104, v105\n"
"v_cvt_pk_bf16_f32 v153, v106, v107\n"
"v_mfma_f32_16x16x32_bf16 a[44:47], a[232:235], v[156:159], a[44:47]\n"
"v_exp_f32 v125, v125\n"
"v_mfma_f32_16x16x32_bf16 a[60:63], a[236:239], v[156:159], a[60:63]\n"
"v_xor_b32 v194, 0x8000, v194\n"
"v_cvt_pk_bf16_f32 v154, v120, v121\n"
"v_mfma_f32_16x16x32_bf16 a[60:63], a[204:207], v[140:143], a[60:63]\n"
"v_exp_f32 v126, v126\n"
"v_mfma_f32_16x16x32_bf16 a[140:143], v[200:203], v[140:143], a[140:143]\n"
"s_add_u32 s62, s62, 1\n"
"s_cmp_lt_u32 s62, s71\n"
"v_cvt_pk_bf16_f32 v155, v122, v123\n"
"v_mfma_f32_16x16x32_bf16 a[140:143], v[200:203], v[156:159], a[140:143]\n"
"v_exp_f32 v127, v127\n"
"s_cbranch_scc1 .Lfull1_19_%=\n"
"s_nop 0\n"
".Lafter1_44_%=:\n"
"s_cmp_eq_u32 s8, 0\n"
"s_cbranch_scc1 .Ldiag1_28_%=\n"
"s_cmp_eq_u32 s8, 1\n"
"s_cbranch_scc1 .Ldiagx1_29_%=\n"
"s_waitcnt lgkmcnt(8)\n"
"v_mfma_f32_16x16x32_bf16 v[64:67], a[144:147], a[64:67], v[160:163]\n"
"v_cvt_pk_bf16_f32 v140, v76, v77\n"
"v_cvt_pk_bf16_f32 v141, v78, v79\n"
"v_mfma_f32_16x16x32_bf16 v[64:67], a[148:151], a[80:83], v[64:67]\n"
"s_xor_b32 s85, s85, 0x8000\n"
"v_mfma_f32_16x16x32_bf16 v[64:67], a[152:155], a[96:99], v[64:67]\n"
"v_cvt_pk_bf16_f32 v142, v92, v93\n"
"v_cvt_pk_bf16_f32 v143, v94, v95\n"
"v_mfma_f32_16x16x32_bf16 v[64:67], a[156:159], a[112:115], v[64:67]\n"
"s_mov_b32 m0, s85\n"
"v_mfma_f32_16x16x32_bf16 v[68:71], a[156:159], a[116:119], v[164:167]\n"
"v_cvt_pk_bf16_f32 v156, v108, v109\n"
"v_cvt_pk_bf16_f32 v157, v110, v111\n"
"v_mfma_f32_16x16x32_bf16 v[68:71], a[152:155], a[100:103], v[68:71]\n"
"buffer_load_dwordx4 v254, s[96:99], s67 offen sc1 lds\n"
"v_mfma_f32_16x16x32_bf16 v[68:71], a[148:151], a[84:87], v[68:71]\n"
"v_cvt_pk_bf16_f32 v158, v124, v125\n"
"v_cvt_pk_bf16_f32 v159, v126, v127\n"
"v_mfma_f32_16x16x32_bf16 v[68:71], a[144:147], a[68:71], v[68:71]\n"
"buffer_load_dwordx4 v254, s[96:99], s67 offen offset:1024 sc1 lds\n"
"v_mfma_f32_16x16x32_bf16 v[72:75], a[144:147], a[72:75], v[168:171]\n"
"ds_read_b128 a[184:187], v193 offset:2048\n"
"ds_read_b128 a[216:219], v193 offset:10240\n"
"v_mfma_f32_16x16x32_bf16 v[72:75], a[148:151], a[88:91], v[72:75]\n"
"v_mfma_f32_16x16x32_bf16 v[72:75], a[152:155], a[104:107], v[72:75]\n"
"ds_read_b128 a[188:191], v193 offset:3072\n"
"v_mfma_f32_16x16x32_bf16 v[72:75], a[156:159], a[120:123], v[72:75]\n"
"buffer_load_dwordx4 v254, s[96:99], s67 offen offset:2048 sc1 lds\n"
"v_mfma_f32_16x16x32_bf16 v[76:79], a[156:159], a[124:127], v[172:175]\n"
"ds_read_b128 a[220:223], v193 offset:11264\n"
"ds_read_b128 a[192:195], v194 offset:4096\n"
"v_mfma_f32_16x16x32_bf16 v[76:79], a[152:155], a[108:111], v[76:79]\n"
"v_mfma_f32_16x16x32_bf16 v[76:79], a[148:151], a[92:95], v[76:79]\n"
"ds_read_b128 a[224:227], v194 offset:12288\n"
"s_waitcnt lgkmcnt(10)\n"
"v_mfma_f32_16x16x32_bf16 v[76:79], a[144:147], a[76:79], v[76:79]\n"
"v_mfma_f32_16x16x32_bf16 v[80:83], a[160:163], a[64:67], v[160:163]\n"
"ds_read_b128 a[144:147], v192 offset:8192\n"
"ds_read_b128 a[148:151], v192 offset:9216\n"
"v_mfma_f32_16x16x32_bf16 v[80:83], a[164:167], a[80:83], v[80:83]\n"
"buffer_load_dwordx4 v254, s[96:99], s67 offen offset:3072 sc1 lds\n"
"v_mfma_f32_16x16x32_bf16 v[80:83], a[168:171], a[96:99], v[80:83]\n"
"ds_read_b128 a[152:155], v192 offset:10240\n"
"ds_read_b128 a[156:159], v192 offset:11264\n"
"v_mfma_f32_16x16x32_bf16 v[80:83], a[172:175], a[112:115], v[80:83]\n"
"s_sub_u32 s89, 32, s66\n"
"v_mfma_f32_16x16x32_bf16 v[84:87], a[172:175], a[116:119], v[164:167]\n"
"s_lshl_b32 s89, s89, 14\n"
"s_add_u32 s67, s73, s89\n"
"v_mfma_f32_16x16x32_bf16 v[84:87], a[168:171], a[100:103], v[84:87]\n"
"v_exp_f32 v64, v64\n"
"v_mfma_f32_16x16x32_bf16 v[84:87], a[164:167], a[84:87], v[84:87]\n"
"s_xor_b32 s84, s84, 0x8000\n"
"s_mov_b32 m0, s84\n"
"v_mfma_f32_16x16x32_bf16 v[84:87], a[160:163], a[68:71], v[84:87]\n"
"v_exp_f32 v65, v65\n"
"v_mfma_f32_16x16x32_bf16 v[88:91], a[160:163], a[72:75], v[168:171]\n"
"buffer_load_dwordx4 v254, s[28:31], s67 offen sc1 lds\n"
"v_mfma_f32_16x16x32_bf16 v[88:91], a[164:167], a[88:91], v[88:91]\n"
"v_exp_f32 v66, v66\n"
"v_mfma_f32_16x16x32_bf16 v[88:91], a[168:171], a[104:107], v[88:91]\n"
"buffer_load_dwordx4 v254, s[28:31], s67 offen offset:1024 sc1 lds\n"
"v_mfma_f32_16x16x32_bf16 v[88:91], a[172:175], a[120:123], v[88:91]\n"
"v_exp_f32 v67, v67\n"
"v_mfma_f32_16x16x32_bf16 v[92:95], a[172:175], a[124:127], v[172:175]\n"
"ds_read_b128 a[196:199], v194 offset:5120\n"
"ds_read_b128 a[228:231], v194 offset:13312\n"
"v_mfma_f32_16x16x32_bf16 v[92:95], a[168:171], a[108:111], v[92:95]\n"
"v_exp_f32 v80, v80\n"
"v_mfma_f32_16x16x32_bf16 v[92:95], a[164:167], a[92:95], v[92:95]\n"
"ds_read_b128 a[200:203], v194 offset:6144\n"
"s_waitcnt lgkmcnt(3)\n"
"v_mfma_f32_16x16x32_bf16 v[92:95], a[160:163], a[76:79], v[92:95]\n"
"v_exp_f32 v81, v81\n"
"v_mfma_f32_16x16x32_bf16 v[96:99], a[144:147], a[64:67], v[160:163]\n"
"ds_read_b128 a[160:163], v192 offset:12288\n"
"ds_read_b128 a[164:167], v192 offset:13312\n"
"v_mfma_f32_16x16x32_bf16 v[96:99], a[148:151], a[80:83], v[96:99]\n"
"v_exp_f32 v82, v82\n"
"v_mfma_f32_16x16x32_bf16 v[96:99], a[152:155], a[96:99], v[96:99]\n"
"ds_read_b128 a[168:171], v192 offset:14336\n"
"ds_read_b128 a[172:175], v192 offset:15360\n"
"v_mfma_f32_16x16x32_bf16 v[96:99], a[156:159], a[112:115], v[96:99]\n"
"v_exp_f32 v83, v83\n"
"v_mfma_f32_16x16x32_bf16 v[100:103], a[156:159], a[116:119], v[164:167]\n"
"buffer_load_dwordx4 v254, s[28:31], s67 offen offset:2048 sc1 lds\n"
"v_mfma_f32_16x16x32_bf16 v[100:103], a[152:155], a[100:103], v[100:103]\n"
"v_exp_f32 v68, v68\n"
"v_mfma_f32_16x16x32_bf16 v[100:103], a[148:151], a[84:87], v[100:103]\n"
"buffer_load_dwordx4 v254, s[28:31], s67 offen offset:3072 sc1 lds\n"
"v_mfma_f32_16x16x32_bf16 v[100:103], a[144:147], a[68:71], v[100:103]\n"
"v_exp_f32 v69, v69\n"
"v_mfma_f32_16x16x32_bf16 v[104:107], a[144:147], a[72:75], v[168:171]\n"
"s_add_u32 s69, s67, 0x4000\n"
"s_add_u32 m0, s84, 0x4000\n"
"v_mfma_f32_16x16x32_bf16 v[104:107], a[148:151], a[88:91], v[104:107]\n"
"v_exp_f32 v70, v70\n"
"v_mfma_f32_16x16x32_bf16 v[104:107], a[152:155], a[104:107], v[104:107]\n"
"buffer_load_dwordx4 v254, s[28:31], s69 offen sc1 lds\n"
"v_mfma_f32_16x16x32_bf16 v[104:107], a[156:159], a[120:123], v[104:107]\n"
"v_exp_f32 v71, v71\n"
"v_mfma_f32_16x16x32_bf16 v[108:111], a[156:159], a[124:127], v[172:175]\n"
"ds_read_b128 a[232:235], v194 offset:14336\n"
"ds_read_b128 a[204:207], v194 offset:7168\n"
"v_mfma_f32_16x16x32_bf16 v[108:111], a[152:155], a[108:111], v[108:111]\n"
"v_exp_f32 v84, v84\n"
"v_mfma_f32_16x16x32_bf16 v[108:111], a[148:151], a[92:95], v[108:111]\n"
"ds_read_b128 a[236:239], v194 offset:15360\n"
"s_waitcnt lgkmcnt(3)\n"
"v_mfma_f32_16x16x32_bf16 v[108:111], a[144:147], a[76:79], v[108:111]\n"
"v_exp_f32 v85, v85\n"
"v_mfma_f32_16x16x32_bf16 v[112:115], a[160:163], a[64:67], v[160:163]\n"
"buffer_load_dwordx4 v254, s[28:31], s69 offen offset:1024 sc1 lds\n"
"v_mfma_f32_16x16x32_bf16 v[112:115], a[164:167], a[80:83], v[112:115]\n"
"v_exp_f32 v86, v86\n"
"v_mfma_f32_16x16x32_bf16 v[112:115], a[168:171], a[96:99], v[112:115]\n"
"v_mfma_f32_16x16x32_bf16 v[112:115], a[172:175], a[112:115], v[112:115]\n"
"v_exp_f32 v87, v87\n"
"v_mfma_f32_16x16x32_bf16 v[116:119], a[172:175], a[116:119], v[164:167]\n"
"buffer_load_dwordx4 v254, s[28:31], s69 offen offset:2048 sc1 lds\n"
"v_mfma_f32_16x16x32_bf16 v[116:119], a[168:171], a[100:103], v[116:119]\n"
"v_exp_f32 v72, v72\n"
"v_mfma_f32_16x16x32_bf16 v[116:119], a[164:167], a[84:87], v[116:119]\n"
"v_mfma_f32_16x16x32_bf16 v[116:119], a[160:163], a[68:71], v[116:119]\n"
"v_exp_f32 v73, v73\n"
"v_mfma_f32_16x16x32_bf16 v[120:123], a[160:163], a[72:75], v[168:171]\n"
"buffer_load_dwordx4 v254, s[28:31], s69 offen offset:3072 sc1 lds\n"
"v_mfma_f32_16x16x32_bf16 v[120:123], a[164:167], a[88:91], v[120:123]\n"
"v_exp_f32 v74, v74\n"
"v_mfma_f32_16x16x32_bf16 v[120:123], a[168:171], a[104:107], v[120:123]\n"
"global_load_dword a255, v248, s[4:5]\n"
"v_mfma_f32_16x16x32_bf16 v[120:123], a[172:175], a[120:123], v[120:123]\n"
"v_exp_f32 v75, v75\n"
"v_mfma_f32_16x16x32_bf16 v[124:127], a[172:175], a[124:127], v[172:175]\n"
"v_mfma_f32_16x16x32_bf16 v[124:127], a[168:171], a[108:111], v[124:127]\n"
"v_exp_f32 v88, v88\n"
"v_mfma_f32_16x16x32_bf16 v[124:127], a[164:167], a[92:95], v[124:127]\n"
"v_mfma_f32_16x16x32_bf16 v[124:127], a[160:163], a[76:79], v[124:127]\n"
"v_exp_f32 v89, v89\n"
"v_mfma_f32_16x16x32_bf16 v[0:3], a[176:179], v[128:131], v[0:3]\n"
"v_mfma_f32_16x16x32_bf16 v[0:3], a[208:211], v[144:147], v[0:3]\n"
"v_exp_f32 v90, v90\n"
"v_mfma_f32_16x16x32_bf16 v[16:19], a[212:215], v[144:147], v[16:19]\n"
"v_mfma_f32_16x16x32_bf16 v[16:19], a[180:183], v[128:131], v[16:19]\n"
"v_exp_f32 v91, v91\n"
"v_mfma_f32_16x16x32_bf16 v[32:35], a[184:187], v[128:131], v[32:35]\n"
"v_mfma_f32_16x16x32_bf16 v[32:35], a[216:219], v[144:147], v[32:35]\n"
"v_exp_f32 v76, v76\n"
"v_mfma_f32_16x16x32_bf16 v[48:51], a[220:223], v[144:147], v[48:51]\n"
"s_waitcnt lgkmcnt(0)\n"
"v_mfma_f32_16x16x32_bf16 v[48:51], a[188:191], v[128:131], v[48:51]\n"
"v_exp_f32 v77, v77\n"
"v_mfma_f32_16x16x32_bf16 a[0:3], a[192:195], v[128:131], a[0:3]\n"
"v_mfma_f32_16x16x32_bf16 a[0:3], a[224:227], v[144:147], a[0:3]\n"
"v_exp_f32 v78, v78\n"
"v_mfma_f32_16x16x32_bf16 a[16:19], a[228:231], v[144:147], a[16:19]\n"
"v_mfma_f32_16x16x32_bf16 a[16:19], a[196:199], v[128:131], a[16:19]\n"
"v_exp_f32 v79, v79\n"
"v_mfma_f32_16x16x32_bf16 a[32:35], a[200:203], v[128:131], a[32:35]\n"
"v_exp_f32 v92, v92\n"
"v_mfma_f32_16x16x32_bf16 a[32:35], a[232:235], v[144:147], a[32:35]\n"
"v_exp_f32 v93, v93\n"
"v_mfma_f32_16x16x32_bf16 a[48:51], a[236:239], v[144:147], a[48:51]\n"
"v_exp_f32 v94, v94\n"
"v_mfma_f32_16x16x32_bf16 a[48:51], a[204:207], v[128:131], a[48:51]\n"
"v_exp_f32 v95, v95\n"
"v_mfma_f32_16x16x32_bf16 a[128:131], v[200:203], v[128:131], a[128:131]\n"
"v_exp_f32 v96, v96\n"
"v_mfma_f32_16x16x32_bf16 a[128:131], v[200:203], v[144:147], a[128:131]\n"
"v_exp_f32 v97, v97\n"
"v_mfma_f32_16x16x32_bf16 v[4:7], a[176:179], v[132:135], v[4:7]\n"
"v_cvt_pk_bf16_f32 v128, v64, v65\n"
"v_cvt_pk_bf16_f32 v129, v66, v67\n"
"v_mfma_f32_16x16x32_bf16 v[4:7], a[208:211], v[148:151], v[4:7]\n"
"v_exp_f32 v98, v98\n"
"v_mfma_f32_16x16x32_bf16 v[20:23], a[212:215], v[148:151], v[20:23]\n"
"v_cvt_pk_bf16_f32 v130, v80, v81\n"
"v_cvt_pk_bf16_f32 v131, v82, v83\n"
"v_mfma_f32_16x16x32_bf16 v[20:23], a[180:183], v[132:135], v[20:23]\n"
"v_exp_f32 v99, v99\n"
"v_mfma_f32_16x16x32_bf16 v[36:39], a[184:187], v[132:135], v[36:39]\n"
"v_exp_f32 v112, v112\n"
"v_mfma_f32_16x16x32_bf16 v[36:39], a[216:219], v[148:151], v[36:39]\n"
"v_exp_f32 v113, v113\n"
"v_mfma_f32_16x16x32_bf16 v[52:55], a[220:223], v[148:151], v[52:55]\n"
"v_exp_f32 v114, v114\n"
"v_mfma_f32_16x16x32_bf16 v[52:55], a[188:191], v[132:135], v[52:55]\n"
"v_exp_f32 v115, v115\n"
"v_mfma_f32_16x16x32_bf16 a[4:7], a[192:195], v[132:135], a[4:7]\n"
"v_exp_f32 v100, v100\n"
"v_mfma_f32_16x16x32_bf16 a[4:7], a[224:227], v[148:151], a[4:7]\n"
"v_exp_f32 v101, v101\n"
"v_mfma_f32_16x16x32_bf16 a[20:23], a[228:231], v[148:151], a[20:23]\n"
"v_cvt_pk_bf16_f32 v144, v96, v97\n"
"v_cvt_pk_bf16_f32 v145, v98, v99\n"
"v_mfma_f32_16x16x32_bf16 a[20:23], a[196:199], v[132:135], a[20:23]\n"
"v_exp_f32 v102, v102\n"
"v_mfma_f32_16x16x32_bf16 a[36:39], a[200:203], v[132:135], a[36:39]\n"
"v_cvt_pk_bf16_f32 v146, v112, v113\n"
"v_cvt_pk_bf16_f32 v147, v114, v115\n"
"v_mfma_f32_16x16x32_bf16 a[36:39], a[232:235], v[148:151], a[36:39]\n"
"v_exp_f32 v103, v103\n"
"v_mfma_f32_16x16x32_bf16 a[52:55], a[236:239], v[148:151], a[52:55]\n"
"v_exp_f32 v116, v116\n"
"v_mfma_f32_16x16x32_bf16 a[52:55], a[204:207], v[132:135], a[52:55]\n"
"v_exp_f32 v117, v117\n"
"v_mfma_f32_16x16x32_bf16 a[132:135], v[200:203], v[132:135], a[132:135]\n"
"v_mfma_f32_16x16x32_bf16 a[132:135], v[200:203], v[148:151], a[132:135]\n"
"v_exp_f32 v118, v118\n"
"v_mfma_f32_16x16x32_bf16 v[8:11], a[176:179], v[136:139], v[8:11]\n"
"v_cvt_pk_bf16_f32 v132, v68, v69\n"
"v_cvt_pk_bf16_f32 v133, v70, v71\n"
"v_mfma_f32_16x16x32_bf16 v[8:11], a[208:211], v[152:155], v[8:11]\n"
"v_exp_f32 v119, v119\n"
"v_mfma_f32_16x16x32_bf16 v[24:27], a[212:215], v[152:155], v[24:27]\n"
"v_cvt_pk_bf16_f32 v134, v84, v85\n"
"v_cvt_pk_bf16_f32 v135, v86, v87\n"
"v_mfma_f32_16x16x32_bf16 v[24:27], a[180:183], v[136:139], v[24:27]\n"
"v_exp_f32 v104, v104\n"
"v_mfma_f32_16x16x32_bf16 v[40:43], a[184:187], v[136:139], v[40:43]\n"
"v_cvt_pk_bf16_f32 v148, v100, v101\n"
"v_cvt_pk_bf16_f32 v149, v102, v103\n"
"v_mfma_f32_16x16x32_bf16 v[40:43], a[216:219], v[152:155], v[40:43]\n"
"v_exp_f32 v105, v105\n"
"v_mfma_f32_16x16x32_bf16 v[56:59], a[220:223], v[152:155], v[56:59]\n"
"v_mfma_f32_16x16x32_bf16 v[56:59], a[188:191], v[136:139], v[56:59]\n"
"v_mfma_f32_16x16x32_bf16 a[8:11], a[192:195], v[136:139], a[8:11]\n"
"s_waitcnt vmcnt(1) lgkmcnt(0)\n"
"s_barrier\n"
"v_mfma_f32_16x16x32_bf16 a[8:11], a[224:227], v[152:155], a[8:11]\n"
"v_exp_f32 v106, v106\n"
"v_mfma_f32_16x16x32_bf16 a[24:27], a[228:231], v[152:155], a[24:27]\n"
"v_xor_b32 v192, 0x8000, v192\n"
"v_xor_b32 v193, 0x8000, v193\n"
"v_mfma_f32_16x16x32_bf16 a[24:27], a[196:199], v[136:139], a[24:27]\n"
"v_exp_f32 v107, v107\n"
"v_mfma_f32_16x16x32_bf16 a[40:43], a[200:203], v[136:139], a[40:43]\n"
"ds_read_b128 a[144:147], v192\n"
"ds_read_b128 a[148:151], v192 offset:1024\n"
"v_mfma_f32_16x16x32_bf16 a[40:43], a[232:235], v[152:155], a[40:43]\n"
"v_exp_f32 v120, v120\n"
"v_mfma_f32_16x16x32_bf16 a[56:59], a[236:239], v[152:155], a[56:59]\n"
"ds_read_b128 a[152:155], v192 offset:2048\n"
"ds_read_b128 a[156:159], v192 offset:3072\n"
"v_mfma_f32_16x16x32_bf16 a[56:59], a[204:207], v[136:139], a[56:59]\n"
"v_exp_f32 v121, v121\n"
"v_mfma_f32_16x16x32_bf16 a[136:139], v[200:203], v[136:139], a[136:139]\n"
"ds_read_b128 a[160:163], v192 offset:4096\n"
"ds_read_b128 a[164:167], v192 offset:5120\n"
"v_mfma_f32_16x16x32_bf16 a[136:139], v[200:203], v[152:155], a[136:139]\n"
"v_exp_f32 v122, v122\n"
"v_mfma_f32_16x16x32_bf16 v[12:15], a[176:179], v[140:143], v[12:15]\n"
"ds_read_b128 a[168:171], v192 offset:6144\n"
"ds_read_b128 a[172:175], v192 offset:7168\n"
"v_mfma_f32_16x16x32_bf16 v[12:15], a[208:211], v[156:159], v[12:15]\n"
"v_exp_f32 v123, v123\n"
"v_mfma_f32_16x16x32_bf16 v[28:31], a[212:215], v[156:159], v[28:31]\n"
"v_cvt_pk_bf16_f32 v136, v72, v73\n"
"v_cvt_pk_bf16_f32 v137, v74, v75\n"
"v_mfma_f32_16x16x32_bf16 v[28:31], a[180:183], v[140:143], v[28:31]\n"
"v_exp_f32 v108, v108\n"
"v_mfma_f32_16x16x32_bf16 v[44:47], a[184:187], v[140:143], v[44:47]\n"
"ds_read_b128 a[176:179], v193\n"
"ds_read_b128 a[208:211], v193 offset:8192\n"
"v_mfma_f32_16x16x32_bf16 v[44:47], a[216:219], v[156:159], v[44:47]\n"
"v_exp_f32 v109, v109\n"
"v_mfma_f32_16x16x32_bf16 v[60:63], a[220:223], v[156:159], v[60:63]\n"
"ds_read_b128 a[180:183], v193 offset:1024\n"
"ds_read_b128 a[212:215], v193 offset:9216\n"
"v_mfma_f32_16x16x32_bf16 v[60:63], a[188:191], v[140:143], v[60:63]\n"
"v_exp_f32 v110, v110\n"
"v_mfma_f32_16x16x32_bf16 a[12:15], a[192:195], v[140:143], a[12:15]\n"
"v_cvt_pk_bf16_f32 v138, v88, v89\n"
"v_cvt_pk_bf16_f32 v139, v90, v91\n"
"v_mfma_f32_16x16x32_bf16 a[12:15], a[224:227], v[156:159], a[12:15]\n"
"v_exp_f32 v111, v111\n"
"v_mfma_f32_16x16x32_bf16 a[28:31], a[228:231], v[156:159], a[28:31]\n"
"v_cvt_pk_bf16_f32 v150, v116, v117\n"
"v_cvt_pk_bf16_f32 v151, v118, v119\n"
"v_mfma_f32_16x16x32_bf16 a[28:31], a[196:199], v[140:143], a[28:31]\n"
"v_exp_f32 v124, v124\n"
"v_mfma_f32_16x16x32_bf16 a[44:47], a[200:203], v[140:143], a[44:47]\n"
"v_cvt_pk_bf16_f32 v152, v104, v105\n"
"v_cvt_pk_bf16_f32 v153, v106, v107\n"
"v_mfma_f32_16x16x32_bf16 a[44:47], a[232:235], v[156:159], a[44:47]\n"
"v_exp_f32 v125, v125\n"
"v_mfma_f32_16x16x32_bf16 a[60:63], a[236:239], v[156:159], a[60:63]\n"
"v_xor_b32 v194, 0x8000, v194\n"
"v_cvt_pk_bf16_f32 v154, v120, v121\n"
"v_mfma_f32_16x16x32_bf16 a[60:63], a[204:207], v[140:143], a[60:63]\n"
"v_exp_f32 v126, v126\n"
"v_mfma_f32_16x16x32_bf16 a[140:143], v[200:203], v[140:143], a[140:143]\n"
"s_add_u32 s62, s62, 1\n"
"s_cmp_eq_u32 s8, 2\n"
"v_cvt_pk_bf16_f32 v155, v122, v123\n"
"v_mfma_f32_16x16x32_bf16 a[140:143], v[200:203], v[156:159], a[140:143]\n"
"v_exp_f32 v127, v127\n"
"s_cbranch_scc1 .Ldiagx2_31_%=\n"
"s_nop 0\n"
".Lfullx2_27_%=:\n"
"s_waitcnt lgkmcnt(8)\n"
"v_mfma_f32_16x16x32_bf16 v[64:67], a[144:147], a[64:67], v[160:163]\n"
"v_cvt_pk_bf16_f32 v140, v76, v77\n"
"v_cvt_pk_bf16_f32 v141, v78, v79\n"
"v_mfma_f32_16x16x32_bf16 v[64:67], a[148:151], a[80:83], v[64:67]\n"
"s_xor_b32 s85, s85, 0x8000\n"
"v_mfma_f32_16x16x32_bf16 v[64:67], a[152:155], a[96:99], v[64:67]\n"
"v_cvt_pk_bf16_f32 v142, v92, v93\n"
"v_cvt_pk_bf16_f32 v143, v94, v95\n"
"v_mfma_f32_16x16x32_bf16 v[64:67], a[156:159], a[112:115], v[64:67]\n"
"s_mov_b32 m0, s85\n"
"v_mfma_f32_16x16x32_bf16 v[68:71], a[156:159], a[116:119], v[164:167]\n"
"v_cvt_pk_bf16_f32 v156, v108, v109\n"
"v_cvt_pk_bf16_f32 v157, v110, v111\n"
"v_mfma_f32_16x16x32_bf16 v[68:71], a[152:155], a[100:103], v[68:71]\n"
"buffer_load_dwordx4 v254, s[96:99], s67 offen sc1 lds\n"
"v_mfma_f32_16x16x32_bf16 v[68:71], a[148:151], a[84:87], v[68:71]\n"
"v_cvt_pk_bf16_f32 v158, v124, v125\n"
"v_cvt_pk_bf16_f32 v159, v126, v127\n"
"v_mfma_f32_16x16x32_bf16 v[68:71], a[144:147], a[68:71], v[68:71]\n"
"buffer_load_dwordx4 v254, s[96:99], s67 offen offset:1024 sc1 lds\n"
"v_mfma_f32_16x16x32_bf16 v[72:75], a[144:147], a[72:75], v[168:171]\n"
"ds_read_b128 a[184:187], v193 offset:2048\n"
"ds_read_b128 a[216:219], v193 offset:10240\n"
"v_mfma_f32_16x16x32_bf16 v[72:75], a[148:151], a[88:91], v[72:75]\n"
"v_mfma_f32_16x16x32_bf16 v[72:75], a[152:155], a[104:107], v[72:75]\n"
"buffer_load_dwordx4 v254, s[96:99], s67 offen offset:2048 sc1 lds\n"
"v_mfma_f32_16x16x32_bf16 v[72:75], a[156:159], a[120:123], v[72:75]\n"
"ds_read_b128 a[188:191], v193 offset:3072\n"
"ds_read_b128 a[220:223], v193 offset:11264\n"
"v_mfma_f32_16x16x32_bf16 v[76:79], a[156:159], a[124:127], v[172:175]\n"
"v_mfma_f32_16x16x32_bf16 v[76:79], a[152:155], a[108:111], v[76:79]\n"
"ds_read_b128 a[192:195], v194 offset:4096\n"
"v_mfma_f32_16x16x32_bf16 v[76:79], a[148:151], a[92:95], v[76:79]\n"
"ds_read_b128 a[224:227], v194 offset:12288\n"
"s_waitcnt lgkmcnt(10)\n"
"v_mfma_f32_16x16x32_bf16 v[76:79], a[144:147], a[76:79], v[76:79]\n"
"buffer_load_dwordx4 v254, s[96:99], s67 offen offset:3072 sc1 lds\n"
"v_mfma_f32_16x16x32_bf16 v[80:83], a[160:163], a[64:67], v[160:163]\n"
"ds_read_b128 a[144:147], v192 offset:8192\n"
"ds_read_b128 a[148:151], v192 offset:9216\n"
"v_mfma_f32_16x16x32_bf16 v[80:83], a[164:167], a[80:83], v[80:83]\n"
"s_add_u32 s70, s67, 0x4000\n"
"v_mfma_f32_16x16x32_bf16 v[80:83], a[168:171], a[96:99], v[80:83]\n"
"ds_read_b128 a[152:155], v192 offset:10240\n"
"ds_read_b128 a[156:159], v192 offset:11264\n"
"v_mfma_f32_16x16x32_bf16 v[80:83], a[172:175], a[112:115], v[80:83]\n"
"s_add_u32 m0, s85, 0x4000\n"
"v_mfma_f32_16x16x32_bf16 v[84:87], a[172:175], a[116:119], v[164:167]\n"
"buffer_load_dwordx4 v254, s[96:99], s70 offen sc1 lds\n"
"v_mfma_f32_16x16x32_bf16 v[84:87], a[168:171], a[100:103], v[84:87]\n"
"v_exp_f32 v64, v64\n"
"v_mfma_f32_16x16x32_bf16 v[84:87], a[164:167], a[84:87], v[84:87]\n"
"buffer_load_dwordx4 v254, s[96:99], s70 offen offset:1024 sc1 lds\n"
"v_mfma_f32_16x16x32_bf16 v[84:87], a[160:163], a[68:71], v[84:87]\n"
"v_exp_f32 v65, v65\n"
"v_mfma_f32_16x16x32_bf16 v[88:91], a[160:163], a[72:75], v[168:171]\n"
"ds_read_b128 a[196:199], v194 offset:5120\n"
"ds_read_b128 a[228:231], v194 offset:13312\n"
"v_mfma_f32_16x16x32_bf16 v[88:91], a[164:167], a[88:91], v[88:91]\n"
"v_exp_f32 v66, v66\n"
"v_mfma_f32_16x16x32_bf16 v[88:91], a[168:171], a[104:107], v[88:91]\n"
"buffer_load_dwordx4 v254, s[96:99], s70 offen offset:2048 sc1 lds\n"
"v_mfma_f32_16x16x32_bf16 v[88:91], a[172:175], a[120:123], v[88:91]\n"
"v_exp_f32 v67, v67\n"
"v_mfma_f32_16x16x32_bf16 v[92:95], a[172:175], a[124:127], v[172:175]\n"
"ds_read_b128 a[200:203], v194 offset:6144\n"
"ds_read_b128 a[232:235], v194 offset:14336\n"
"v_mfma_f32_16x16x32_bf16 v[92:95], a[168:171], a[108:111], v[92:95]\n"
"v_exp_f32 v80, v80\n"
"v_mfma_f32_16x16x32_bf16 v[92:95], a[164:167], a[92:95], v[92:95]\n"
"ds_read_b128 a[204:207], v194 offset:7168\n"
"s_waitcnt lgkmcnt(5)\n"
"v_mfma_f32_16x16x32_bf16 v[92:95], a[160:163], a[76:79], v[92:95]\n"
"v_exp_f32 v81, v81\n"
"v_mfma_f32_16x16x32_bf16 v[96:99], a[144:147], a[64:67], v[160:163]\n"
"ds_read_b128 a[160:163], v192 offset:12288\n"
"ds_read_b128 a[164:167], v192 offset:13312\n"
"v_mfma_f32_16x16x32_bf16 v[96:99], a[148:151], a[80:83], v[96:99]\n"
"v_exp_f32 v82, v82\n"
"v_mfma_f32_16x16x32_bf16 v[96:99], a[152:155], a[96:99], v[96:99]\n"
"ds_read_b128 a[168:171], v192 offset:14336\n"
"ds_read_b128 a[172:175], v192 offset:15360\n"
"v_mfma_f32_16x16x32_bf16 v[96:99], a[156:159], a[112:115], v[96:99]\n"
"v_exp_f32 v83, v83\n"
"v_mfma_f32_16x16x32_bf16 v[100:103], a[156:159], a[116:119], v[164:167]\n"
"buffer_load_dwordx4 v254, s[96:99], s70 offen offset:3072 sc1 lds\n"
"v_mfma_f32_16x16x32_bf16 v[100:103], a[152:155], a[100:103], v[100:103]\n"
"v_exp_f32 v68, v68\n"
"v_mfma_f32_16x16x32_bf16 v[100:103], a[148:151], a[84:87], v[100:103]\n"
"s_lshl_b32 s89, s66, 14\n"
"s_add_u32 s67, s73, s89\n"
"v_mfma_f32_16x16x32_bf16 v[100:103], a[144:147], a[68:71], v[100:103]\n"
"v_exp_f32 v69, v69\n"
"v_mfma_f32_16x16x32_bf16 v[104:107], a[144:147], a[72:75], v[168:171]\n"
"s_xor_b32 s84, s84, 0x8000\n"
"s_mov_b32 m0, s84\n"
"v_mfma_f32_16x16x32_bf16 v[104:107], a[148:151], a[88:91], v[104:107]\n"
"v_exp_f32 v70, v70\n"
"v_mfma_f32_16x16x32_bf16 v[104:107], a[152:155], a[104:107], v[104:107]\n"
"buffer_load_dwordx4 v254, s[28:31], s67 offen sc1 lds\n"
"v_mfma_f32_16x16x32_bf16 v[104:107], a[156:159], a[120:123], v[104:107]\n"
"v_exp_f32 v71, v71\n"
"v_mfma_f32_16x16x32_bf16 v[108:111], a[156:159], a[124:127], v[172:175]\n"
"buffer_load_dwordx4 v254, s[28:31], s67 offen offset:1024 sc1 lds\n"
"v_mfma_f32_16x16x32_bf16 v[108:111], a[152:155], a[108:111], v[108:111]\n"
"v_exp_f32 v84, v84\n"
"v_mfma_f32_16x16x32_bf16 v[108:111], a[148:151], a[92:95], v[108:111]\n"
"ds_read_b128 a[236:239], v194 offset:15360\n"
"s_waitcnt lgkmcnt(1)\n"
"v_mfma_f32_16x16x32_bf16 v[108:111], a[144:147], a[76:79], v[108:111]\n"
"v_exp_f32 v85, v85\n"
"v_mfma_f32_16x16x32_bf16 v[112:115], a[160:163], a[64:67], v[160:163]\n"
"buffer_load_dwordx4 v254, s[28:31], s67 offen offset:2048 sc1 lds\n"
"v_mfma_f32_16x16x32_bf16 v[112:115], a[164:167], a[80:83], v[112:115]\n"
"v_exp_f32 v86, v86\n"
"v_mfma_f32_16x16x32_bf16 v[112:115], a[168:171], a[96:99], v[112:115]\n"
"v_mfma_f32_16x16x32_bf16 v[112:115], a[172:175], a[112:115], v[112:115]\n"
"v_exp_f32 v87, v87\n"
"v_mfma_f32_16x16x32_bf16 v[116:119], a[172:175], a[116:119], v[164:167]\n"
"buffer_load_dwordx4 v254, s[28:31], s67 offen offset:3072 sc1 lds\n"
"v_mfma_f32_16x16x32_bf16 v[116:119], a[168:171], a[100:103], v[116:119]\n"
"v_exp_f32 v72, v72\n"
"v_mfma_f32_16x16x32_bf16 v[116:119], a[164:167], a[84:87], v[116:119]\n"
"s_add_u32 s69, s67, 0x4000\n"
"s_add_u32 m0, s84, 0x4000\n"
"v_mfma_f32_16x16x32_bf16 v[116:119], a[160:163], a[68:71], v[116:119]\n"
"v_exp_f32 v73, v73\n"
"v_mfma_f32_16x16x32_bf16 v[120:123], a[160:163], a[72:75], v[168:171]\n"
"buffer_load_dwordx4 v254, s[28:31], s69 offen sc1 lds\n"
"v_mfma_f32_16x16x32_bf16 v[120:123], a[164:167], a[88:91], v[120:123]\n"
"v_exp_f32 v74, v74\n"
"v_mfma_f32_16x16x32_bf16 v[120:123], a[168:171], a[104:107], v[120:123]\n"
"v_mfma_f32_16x16x32_bf16 v[120:123], a[172:175], a[120:123], v[120:123]\n"
"v_exp_f32 v75, v75\n"
"v_mfma_f32_16x16x32_bf16 v[124:127], a[172:175], a[124:127], v[172:175]\n"
"buffer_load_dwordx4 v254, s[28:31], s69 offen offset:1024 sc1 lds\n"
"v_mfma_f32_16x16x32_bf16 v[124:127], a[168:171], a[108:111], v[124:127]\n"
"v_exp_f32 v88, v88\n"
"v_mfma_f32_16x16x32_bf16 v[124:127], a[164:167], a[92:95], v[124:127]\n"
"v_mfma_f32_16x16x32_bf16 v[124:127], a[160:163], a[76:79], v[124:127]\n"
"v_exp_f32 v89, v89\n"
"v_mfma_f32_16x16x32_bf16 v[0:3], a[176:179], v[128:131], v[0:3]\n"
"buffer_load_dwordx4 v254, s[28:31], s69 offen offset:2048 sc1 lds\n"
"v_mfma_f32_16x16x32_bf16 v[0:3], a[208:211], v[144:147], v[0:3]\n"
"v_exp_f32 v90, v90\n"
"v_mfma_f32_16x16x32_bf16 v[16:19], a[212:215], v[144:147], v[16:19]\n"
"buffer_load_dwordx4 v254, s[28:31], s69 offen offset:3072 sc1 lds\n"
"v_mfma_f32_16x16x32_bf16 v[16:19], a[180:183], v[128:131], v[16:19]\n"
"v_exp_f32 v91, v91\n"
"v_mfma_f32_16x16x32_bf16 v[32:35], a[184:187], v[128:131], v[32:35]\n"
"global_load_dword a255, v248, s[4:5]\n"
"v_mfma_f32_16x16x32_bf16 v[32:35], a[216:219], v[144:147], v[32:35]\n"
"v_exp_f32 v76, v76\n"
"v_mfma_f32_16x16x32_bf16 v[48:51], a[220:223], v[144:147], v[48:51]\n"
"s_waitcnt lgkmcnt(0)\n"
"v_mfma_f32_16x16x32_bf16 v[48:51], a[188:191], v[128:131], v[48:51]\n"
"v_exp_f32 v77, v77\n"
"v_mfma_f32_16x16x32_bf16 a[0:3], a[192:195], v[128:131], a[0:3]\n"
"v_mfma_f32_16x16x32_bf16 a[0:3], a[224:227], v[144:147], a[0:3]\n"
"v_exp_f32 v78, v78\n"
"v_mfma_f32_16x16x32_bf16 a[16:19], a[228:231], v[144:147], a[16:19]\n"
"v_mfma_f32_16x16x32_bf16 a[16:19], a[196:199], v[128:131], a[16:19]\n"
"v_exp_f32 v79, v79\n"
"v_mfma_f32_16x16x32_bf16 a[32:35], a[200:203], v[128:131], a[32:35]\n"
"v_exp_f32 v92, v92\n"
"v_mfma_f32_16x16x32_bf16 a[32:35], a[232:235], v[144:147], a[32:35]\n"
"v_exp_f32 v93, v93\n"
"v_mfma_f32_16x16x32_bf16 a[48:51], a[236:239], v[144:147], a[48:51]\n"
"v_exp_f32 v94, v94\n"
"v_mfma_f32_16x16x32_bf16 a[48:51], a[204:207], v[128:131], a[48:51]\n"
"v_exp_f32 v95, v95\n"
"v_mfma_f32_16x16x32_bf16 a[128:131], v[200:203], v[128:131], a[128:131]\n"
"v_exp_f32 v96, v96\n"
"v_mfma_f32_16x16x32_bf16 a[128:131], v[200:203], v[144:147], a[128:131]\n"
"v_exp_f32 v97, v97\n"
"v_mfma_f32_16x16x32_bf16 v[4:7], a[176:179], v[132:135], v[4:7]\n"
"v_cvt_pk_bf16_f32 v128, v64, v65\n"
"v_cvt_pk_bf16_f32 v129, v66, v67\n"
"v_mfma_f32_16x16x32_bf16 v[4:7], a[208:211], v[148:151], v[4:7]\n"
"v_exp_f32 v98, v98\n"
"v_mfma_f32_16x16x32_bf16 v[20:23], a[212:215], v[148:151], v[20:23]\n"
"v_cvt_pk_bf16_f32 v130, v80, v81\n"
"v_cvt_pk_bf16_f32 v131, v82, v83\n"
"v_mfma_f32_16x16x32_bf16 v[20:23], a[180:183], v[132:135], v[20:23]\n"
"v_exp_f32 v99, v99\n"
"v_mfma_f32_16x16x32_bf16 v[36:39], a[184:187], v[132:135], v[36:39]\n"
"v_exp_f32 v112, v112\n"
"v_mfma_f32_16x16x32_bf16 v[36:39], a[216:219], v[148:151], v[36:39]\n"
"v_exp_f32 v113, v113\n"
"v_mfma_f32_16x16x32_bf16 v[52:55], a[220:223], v[148:151], v[52:55]\n"
"v_exp_f32 v114, v114\n"
"v_mfma_f32_16x16x32_bf16 v[52:55], a[188:191], v[132:135], v[52:55]\n"
"v_exp_f32 v115, v115\n"
"v_mfma_f32_16x16x32_bf16 a[4:7], a[192:195], v[132:135], a[4:7]\n"
"v_exp_f32 v100, v100\n"
"v_mfma_f32_16x16x32_bf16 a[4:7], a[224:227], v[148:151], a[4:7]\n"
"v_exp_f32 v101, v101\n"
"v_mfma_f32_16x16x32_bf16 a[20:23], a[228:231], v[148:151], a[20:23]\n"
"v_cvt_pk_bf16_f32 v144, v96, v97\n"
"v_cvt_pk_bf16_f32 v145, v98, v99\n"
"v_mfma_f32_16x16x32_bf16 a[20:23], a[196:199], v[132:135], a[20:23]\n"
"v_exp_f32 v102, v102\n"
"v_mfma_f32_16x16x32_bf16 a[36:39], a[200:203], v[132:135], a[36:39]\n"
"v_cvt_pk_bf16_f32 v146, v112, v113\n"
"v_cvt_pk_bf16_f32 v147, v114, v115\n"
"v_mfma_f32_16x16x32_bf16 a[36:39], a[232:235], v[148:151], a[36:39]\n"
"v_exp_f32 v103, v103\n"
"v_mfma_f32_16x16x32_bf16 a[52:55], a[236:239], v[148:151], a[52:55]\n"
"v_exp_f32 v116, v116\n"
"v_mfma_f32_16x16x32_bf16 a[52:55], a[204:207], v[132:135], a[52:55]\n"
"v_exp_f32 v117, v117\n"
"v_mfma_f32_16x16x32_bf16 a[132:135], v[200:203], v[132:135], a[132:135]\n"
"v_mfma_f32_16x16x32_bf16 a[132:135], v[200:203], v[148:151], a[132:135]\n"
"v_exp_f32 v118, v118\n"
"v_mfma_f32_16x16x32_bf16 v[8:11], a[176:179], v[136:139], v[8:11]\n"
"v_cvt_pk_bf16_f32 v132, v68, v69\n"
"v_cvt_pk_bf16_f32 v133, v70, v71\n"
"v_mfma_f32_16x16x32_bf16 v[8:11], a[208:211], v[152:155], v[8:11]\n"
"v_exp_f32 v119, v119\n"
"v_mfma_f32_16x16x32_bf16 v[24:27], a[212:215], v[152:155], v[24:27]\n"
"v_cvt_pk_bf16_f32 v134, v84, v85\n"
"v_cvt_pk_bf16_f32 v135, v86, v87\n"
"v_mfma_f32_16x16x32_bf16 v[24:27], a[180:183], v[136:139], v[24:27]\n"
"v_exp_f32 v104, v104\n"
"v_mfma_f32_16x16x32_bf16 v[40:43], a[184:187], v[136:139], v[40:43]\n"
"v_cvt_pk_bf16_f32 v148, v100, v101\n"
"v_cvt_pk_bf16_f32 v149, v102, v103\n"
"v_mfma_f32_16x16x32_bf16 v[40:43], a[216:219], v[152:155], v[40:43]\n"
"v_exp_f32 v105, v105\n"
"v_mfma_f32_16x16x32_bf16 v[56:59], a[220:223], v[152:155], v[56:59]\n"
"v_mfma_f32_16x16x32_bf16 v[56:59], a[188:191], v[136:139], v[56:59]\n"
"v_mfma_f32_16x16x32_bf16 a[8:11], a[192:195], v[136:139], a[8:11]\n"
"s_waitcnt vmcnt(1) lgkmcnt(0)\n"
"s_barrier\n"
"v_mfma_f32_16x16x32_bf16 a[8:11], a[224:227], v[152:155], a[8:11]\n"
"v_exp_f32 v106, v106\n"
"v_mfma_f32_16x16x32_bf16 a[24:27], a[228:231], v[152:155], a[24:27]\n"
"v_xor_b32 v192, 0x8000, v192\n"
"v_xor_b32 v193, 0x8000, v193\n"
"v_mfma_f32_16x16x32_bf16 a[24:27], a[196:199], v[136:139], a[24:27]\n"
"v_exp_f32 v107, v107\n"
"v_mfma_f32_16x16x32_bf16 a[40:43], a[200:203], v[136:139], a[40:43]\n"
"ds_read_b128 a[144:147], v192\n"
"ds_read_b128 a[148:151], v192 offset:1024\n"
"v_mfma_f32_16x16x32_bf16 a[40:43], a[232:235], v[152:155], a[40:43]\n"
"v_exp_f32 v120, v120\n"
"v_mfma_f32_16x16x32_bf16 a[56:59], a[236:239], v[152:155], a[56:59]\n"
"ds_read_b128 a[152:155], v192 offset:2048\n"
"ds_read_b128 a[156:159], v192 offset:3072\n"
"v_mfma_f32_16x16x32_bf16 a[56:59], a[204:207], v[136:139], a[56:59]\n"
"v_exp_f32 v121, v121\n"
"v_mfma_f32_16x16x32_bf16 a[136:139], v[200:203], v[136:139], a[136:139]\n"
"ds_read_b128 a[160:163], v192 offset:4096\n"
"ds_read_b128 a[164:167], v192 offset:5120\n"
"v_mfma_f32_16x16x32_bf16 a[136:139], v[200:203], v[152:155], a[136:139]\n"
"v_exp_f32 v122, v122\n"
"v_mfma_f32_16x16x32_bf16 v[12:15], a[176:179], v[140:143], v[12:15]\n"
"ds_read_b128 a[168:171], v192 offset:6144\n"
"ds_read_b128 a[172:175], v192 offset:7168\n"
"v_mfma_f32_16x16x32_bf16 v[12:15], a[208:211], v[156:159], v[12:15]\n"
"v_exp_f32 v123, v123\n"
"v_mfma_f32_16x16x32_bf16 v[28:31], a[212:215], v[156:159], v[28:31]\n"
"v_cvt_pk_bf16_f32 v136, v72, v73\n"
"v_cvt_pk_bf16_f32 v137, v74, v75\n"
"v_mfma_f32_16x16x32_bf16 v[28:31], a[180:183], v[140:143], v[28:31]\n"
"v_exp_f32 v108, v108\n"
"v_mfma_f32_16x16x32_bf16 v[44:47], a[184:187], v[140:143], v[44:47]\n"
"ds_read_b128 a[176:179], v193\n"
"ds_read_b128 a[208:211], v193 offset:8192\n"
"v_mfma_f32_16x16x32_bf16 v[44:47], a[216:219], v[156:159], v[44:47]\n"
"v_exp_f32 v109, v109\n"
"v_mfma_f32_16x16x32_bf16 v[60:63], a[220:223], v[156:159], v[60:63]\n"
"ds_read_b128 a[180:183], v193 offset:1024\n"
"ds_read_b128 a[212:215], v193 offset:9216\n"
"v_mfma_f32_16x16x32_bf16 v[60:63], a[188:191], v[140:143], v[60:63]\n"
"v_exp_f32 v110, v110\n"
"v_mfma_f32_16x16x32_bf16 a[12:15], a[192:195], v[140:143], a[12:15]\n"
"v_cvt_pk_bf16_f32 v138, v88, v89\n"
"v_cvt_pk_bf16_f32 v139, v90, v91\n"
"v_mfma_f32_16x16x32_bf16 a[12:15], a[224:227], v[156:159], a[12:15]\n"
"v_exp_f32 v111, v111\n"
"v_mfma_f32_16x16x32_bf16 a[28:31], a[228:231], v[156:159], a[28:31]\n"
"v_cvt_pk_bf16_f32 v150, v116, v117\n"
"v_cvt_pk_bf16_f32 v151, v118, v119\n"
"v_mfma_f32_16x16x32_bf16 a[28:31], a[196:199], v[140:143], a[28:31]\n"
"v_exp_f32 v124, v124\n"
"v_mfma_f32_16x16x32_bf16 a[44:47], a[200:203], v[140:143], a[44:47]\n"
"v_cvt_pk_bf16_f32 v152, v104, v105\n"
"v_cvt_pk_bf16_f32 v153, v106, v107\n"
"v_mfma_f32_16x16x32_bf16 a[44:47], a[232:235], v[156:159], a[44:47]\n"
"v_exp_f32 v125, v125\n"
"v_mfma_f32_16x16x32_bf16 a[60:63], a[236:239], v[156:159], a[60:63]\n"
"v_xor_b32 v194, 0x8000, v194\n"
"v_cvt_pk_bf16_f32 v154, v120, v121\n"
"v_mfma_f32_16x16x32_bf16 a[60:63], a[204:207], v[140:143], a[60:63]\n"
"v_exp_f32 v126, v126\n"
"v_mfma_f32_16x16x32_bf16 a[140:143], v[200:203], v[140:143], a[140:143]\n"
"s_add_u32 s62, s62, 1\n"
"v_cvt_pk_bf16_f32 v155, v122, v123\n"
"v_mfma_f32_16x16x32_bf16 a[140:143], v[200:203], v[156:159], a[140:143]\n"
"v_exp_f32 v127, v127\n"
"s_nop 0\n"
".Lph2_45_%=:\n"
"s_cmp_gt_u32 s62, 16\n"
"s_cbranch_scc1 .Ldrain_36_%=\n"
"s_cmp_eq_u32 s62, 16\n"
"s_cbranch_scc1 .Lfullv_23_%=\n"
".Lfull2_21_%=:\n"
"s_waitcnt lgkmcnt(8)\n"
"v_mfma_f32_16x16x32_bf16 v[64:67], a[144:147], a[64:67], v[160:163]\n"
"v_cvt_pk_bf16_f32 v140, v76, v77\n"
"v_cvt_pk_bf16_f32 v141, v78, v79\n"
"v_mfma_f32_16x16x32_bf16 v[64:67], a[148:151], a[80:83], v[64:67]\n"
"s_xor_b32 s85, s85, 0x8000\n"
"v_mfma_f32_16x16x32_bf16 v[64:67], a[152:155], a[96:99], v[64:67]\n"
"v_cvt_pk_bf16_f32 v142, v92, v93\n"
"v_cvt_pk_bf16_f32 v143, v94, v95\n"
"v_mfma_f32_16x16x32_bf16 v[64:67], a[156:159], a[112:115], v[64:67]\n"
"s_mov_b32 m0, s85\n"
"v_mfma_f32_16x16x32_bf16 v[68:71], a[156:159], a[116:119], v[164:167]\n"
"v_cvt_pk_bf16_f32 v156, v108, v109\n"
"v_cvt_pk_bf16_f32 v157, v110, v111\n"
"v_mfma_f32_16x16x32_bf16 v[68:71], a[152:155], a[100:103], v[68:71]\n"
"buffer_load_dwordx4 v254, s[96:99], s67 offen sc1 lds\n"
"v_mfma_f32_16x16x32_bf16 v[68:71], a[148:151], a[84:87], v[68:71]\n"
"v_cvt_pk_bf16_f32 v158, v124, v125\n"
"v_cvt_pk_bf16_f32 v159, v126, v127\n"
"v_mfma_f32_16x16x32_bf16 v[68:71], a[144:147], a[68:71], v[68:71]\n"
"buffer_load_dwordx4 v254, s[96:99], s67 offen offset:1024 sc1 lds\n"
"v_mfma_f32_16x16x32_bf16 v[72:75], a[144:147], a[72:75], v[168:171]\n"
"ds_read_b128 a[184:187], v193 offset:2048\n"
"ds_read_b128 a[216:219], v193 offset:10240\n"
"v_mfma_f32_16x16x32_bf16 v[72:75], a[148:151], a[88:91], v[72:75]\n"
"v_mfma_f32_16x16x32_bf16 v[72:75], a[152:155], a[104:107], v[72:75]\n"
"buffer_load_dwordx4 v254, s[96:99], s67 offen offset:2048 sc1 lds\n"
"v_mfma_f32_16x16x32_bf16 v[72:75], a[156:159], a[120:123], v[72:75]\n"
"ds_read_b128 a[188:191], v193 offset:3072\n"
"ds_read_b128 a[220:223], v193 offset:11264\n"
"v_mfma_f32_16x16x32_bf16 v[76:79], a[156:159], a[124:127], v[172:175]\n"
"v_mfma_f32_16x16x32_bf16 v[76:79], a[152:155], a[108:111], v[76:79]\n"
"ds_read_b128 a[192:195], v194 offset:4096\n"
"v_mfma_f32_16x16x32_bf16 v[76:79], a[148:151], a[92:95], v[76:79]\n"
"ds_read_b128 a[224:227], v194 offset:12288\n"
"s_waitcnt lgkmcnt(10)\n"
"v_mfma_f32_16x16x32_bf16 v[76:79], a[144:147], a[76:79], v[76:79]\n"
"buffer_load_dwordx4 v254, s[96:99], s67 offen offset:3072 sc1 lds\n"
"v_mfma_f32_16x16x32_bf16 v[80:83], a[160:163], a[64:67], v[160:163]\n"
"ds_read_b128 a[144:147], v192 offset:8192\n"
"ds_read_b128 a[148:151], v192 offset:9216\n"
"v_mfma_f32_16x16x32_bf16 v[80:83], a[164:167], a[80:83], v[80:83]\n"
"s_add_u32 s70, s67, 0x4000\n"
"v_mfma_f32_16x16x32_bf16 v[80:83], a[168:171], a[96:99], v[80:83]\n"
"ds_read_b128 a[152:155], v192 offset:10240\n"
"ds_read_b128 a[156:159], v192 offset:11264\n"
"v_mfma_f32_16x16x32_bf16 v[80:83], a[172:175], a[112:115], v[80:83]\n"
"s_add_u32 m0, s85, 0x4000\n"
"v_mfma_f32_16x16x32_bf16 v[84:87], a[172:175], a[116:119], v[164:167]\n"
"buffer_load_dwordx4 v254, s[96:99], s70 offen sc1 lds\n"
"v_mfma_f32_16x16x32_bf16 v[84:87], a[168:171], a[100:103], v[84:87]\n"
"v_exp_f32 v64, v64\n"
"v_mfma_f32_16x16x32_bf16 v[84:87], a[164:167], a[84:87], v[84:87]\n"
"buffer_load_dwordx4 v254, s[96:99], s70 offen offset:1024 sc1 lds\n"
"v_mfma_f32_16x16x32_bf16 v[84:87], a[160:163], a[68:71], v[84:87]\n"
"v_exp_f32 v65, v65\n"
"v_mfma_f32_16x16x32_bf16 v[88:91], a[160:163], a[72:75], v[168:171]\n"
"ds_read_b128 a[196:199], v194 offset:5120\n"
"ds_read_b128 a[228:231], v194 offset:13312\n"
"v_mfma_f32_16x16x32_bf16 v[88:91], a[164:167], a[88:91], v[88:91]\n"
"v_exp_f32 v66, v66\n"
"v_mfma_f32_16x16x32_bf16 v[88:91], a[168:171], a[104:107], v[88:91]\n"
"buffer_load_dwordx4 v254, s[96:99], s70 offen offset:2048 sc1 lds\n"
"v_mfma_f32_16x16x32_bf16 v[88:91], a[172:175], a[120:123], v[88:91]\n"
"v_exp_f32 v67, v67\n"
"v_mfma_f32_16x16x32_bf16 v[92:95], a[172:175], a[124:127], v[172:175]\n"
"ds_read_b128 a[200:203], v194 offset:6144\n"
"ds_read_b128 a[232:235], v194 offset:14336\n"
"v_mfma_f32_16x16x32_bf16 v[92:95], a[168:171], a[108:111], v[92:95]\n"
"v_exp_f32 v80, v80\n"
"v_mfma_f32_16x16x32_bf16 v[92:95], a[164:167], a[92:95], v[92:95]\n"
"ds_read_b128 a[204:207], v194 offset:7168\n"
"s_waitcnt lgkmcnt(5)\n"
"v_mfma_f32_16x16x32_bf16 v[92:95], a[160:163], a[76:79], v[92:95]\n"
"v_exp_f32 v81, v81\n"
"v_mfma_f32_16x16x32_bf16 v[96:99], a[144:147], a[64:67], v[160:163]\n"
"ds_read_b128 a[160:163], v192 offset:12288\n"
"ds_read_b128 a[164:167], v192 offset:13312\n"
"v_mfma_f32_16x16x32_bf16 v[96:99], a[148:151], a[80:83], v[96:99]\n"
"v_exp_f32 v82, v82\n"
"v_mfma_f32_16x16x32_bf16 v[96:99], a[152:155], a[96:99], v[96:99]\n"
"ds_read_b128 a[168:171], v192 offset:14336\n"
"ds_read_b128 a[172:175], v192 offset:15360\n"
"v_mfma_f32_16x16x32_bf16 v[96:99], a[156:159], a[112:115], v[96:99]\n"
"v_exp_f32 v83, v83\n"
"v_mfma_f32_16x16x32_bf16 v[100:103], a[156:159], a[116:119], v[164:167]\n"
"buffer_load_dwordx4 v254, s[96:99], s70 offen offset:3072 sc1 lds\n"
"v_mfma_f32_16x16x32_bf16 v[100:103], a[152:155], a[100:103], v[100:103]\n"
"v_exp_f32 v68, v68\n"
"v_mfma_f32_16x16x32_bf16 v[100:103], a[148:151], a[84:87], v[100:103]\n"
"s_add_u32 s67, s67, 0x8000\n"
"s_xor_b32 s84, s84, 0x8000\n"
"v_mfma_f32_16x16x32_bf16 v[100:103], a[144:147], a[68:71], v[100:103]\n"
"v_exp_f32 v69, v69\n"
"v_mfma_f32_16x16x32_bf16 v[104:107], a[144:147], a[72:75], v[168:171]\n"
"s_mov_b32 m0, s84\n"
"ds_read_b128 a[236:239], v194 offset:15360\n"
"v_mfma_f32_16x16x32_bf16 v[104:107], a[148:151], a[88:91], v[104:107]\n"
"v_exp_f32 v70, v70\n"
"v_mfma_f32_16x16x32_bf16 v[104:107], a[152:155], a[104:107], v[104:107]\n"
"buffer_load_dwordx4 v254, s[28:31], s67 offen sc1 lds\n"
"v_mfma_f32_16x16x32_bf16 v[104:107], a[156:159], a[120:123], v[104:107]\n"
"v_exp_f32 v71, v71\n"
"v_mfma_f32_16x16x32_bf16 v[108:111], a[156:159], a[124:127], v[172:175]\n"
"buffer_load_dwordx4 v254, s[28:31], s67 offen offset:1024 sc1 lds\n"
"v_mfma_f32_16x16x32_bf16 v[108:111], a[152:155], a[108:111], v[108:111]\n"
"v_exp_f32 v84, v84\n"
"v_mfma_f32_16x16x32_bf16 v[108:111], a[148:151], a[92:95], v[108:111]\n"
"s_waitcnt lgkmcnt(1)\n"
"v_mfma_f32_16x16x32_bf16 v[108:111], a[144:147], a[76:79], v[108:111]\n"
"v_exp_f32 v85, v85\n"
"v_mfma_f32_16x16x32_bf16 v[112:115], a[160:163], a[64:67], v[160:163]\n"
"buffer_load_dwordx4 v254, s[28:31], s67 offen offset:2048 sc1 lds\n"
"v_mfma_f32_16x16x32_bf16 v[112:115], a[164:167], a[80:83], v[112:115]\n"
"v_exp_f32 v86, v86\n"
"v_mfma_f32_16x16x32_bf16 v[112:115], a[168:171], a[96:99], v[112:115]\n"
"v_mfma_f32_16x16x32_bf16 v[112:115], a[172:175], a[112:115], v[112:115]\n"
"v_exp_f32 v87, v87\n"
"v_mfma_f32_16x16x32_bf16 v[116:119], a[172:175], a[116:119], v[164:167]\n"
"buffer_load_dwordx4 v254, s[28:31], s67 offen offset:3072 sc1 lds\n"
"v_mfma_f32_16x16x32_bf16 v[116:119], a[168:171], a[100:103], v[116:119]\n"
"v_exp_f32 v72, v72\n"
"v_mfma_f32_16x16x32_bf16 v[116:119], a[164:167], a[84:87], v[116:119]\n"
"s_add_u32 s69, s67, 0x4000\n"
"s_add_u32 m0, s84, 0x4000\n"
"v_mfma_f32_16x16x32_bf16 v[116:119], a[160:163], a[68:71], v[116:119]\n"
"v_exp_f32 v73, v73\n"
"v_mfma_f32_16x16x32_bf16 v[120:123], a[160:163], a[72:75], v[168:171]\n"
"buffer_load_dwordx4 v254, s[28:31], s69 offen sc1 lds\n"
"v_mfma_f32_16x16x32_bf16 v[120:123], a[164:167], a[88:91], v[120:123]\n"
"v_exp_f32 v74, v74\n"
"v_mfma_f32_16x16x32_bf16 v[120:123], a[168:171], a[104:107], v[120:123]\n"
"v_mfma_f32_16x16x32_bf16 v[120:123], a[172:175], a[120:123], v[120:123]\n"
"v_exp_f32 v75, v75\n"
"v_mfma_f32_16x16x32_bf16 v[124:127], a[172:175], a[124:127], v[172:175]\n"
"buffer_load_dwordx4 v254, s[28:31], s69 offen offset:1024 sc1 lds\n"
"v_mfma_f32_16x16x32_bf16 v[124:127], a[168:171], a[108:111], v[124:127]\n"
"v_exp_f32 v88, v88\n"
"v_mfma_f32_16x16x32_bf16 v[124:127], a[164:167], a[92:95], v[124:127]\n"
"v_mfma_f32_16x16x32_bf16 v[124:127], a[160:163], a[76:79], v[124:127]\n"
"v_exp_f32 v89, v89\n"
"v_mfma_f32_16x16x32_bf16 v[0:3], a[176:179], v[128:131], v[0:3]\n"
"buffer_load_dwordx4 v254, s[28:31], s69 offen offset:2048 sc1 lds\n"
"v_mfma_f32_16x16x32_bf16 v[0:3], a[208:211], v[144:147], v[0:3]\n"
"v_exp_f32 v90, v90\n"
"v_mfma_f32_16x16x32_bf16 v[16:19], a[212:215], v[144:147], v[16:19]\n"
"buffer_load_dwordx4 v254, s[28:31], s69 offen offset:3072 sc1 lds\n"
"v_mfma_f32_16x16x32_bf16 v[16:19], a[180:183], v[128:131], v[16:19]\n"
"v_exp_f32 v91, v91\n"
"v_mfma_f32_16x16x32_bf16 v[32:35], a[184:187], v[128:131], v[32:35]\n"
"global_load_dword a255, v248, s[4:5]\n"
"v_mfma_f32_16x16x32_bf16 v[32:35], a[216:219], v[144:147], v[32:35]\n"
"v_exp_f32 v76, v76\n"
"v_mfma_f32_16x16x32_bf16 v[48:51], a[220:223], v[144:147], v[48:51]\n"
"s_waitcnt lgkmcnt(0)\n"
"v_mfma_f32_16x16x32_bf16 v[48:51], a[188:191], v[128:131], v[48:51]\n"
"v_exp_f32 v77, v77\n"
"v_mfma_f32_16x16x32_bf16 a[0:3], a[192:195], v[128:131], a[0:3]\n"
"v_mfma_f32_16x16x32_bf16 a[0:3], a[224:227], v[144:147], a[0:3]\n"
"v_exp_f32 v78, v78\n"
"v_mfma_f32_16x16x32_bf16 a[16:19], a[228:231], v[144:147], a[16:19]\n"
"v_mfma_f32_16x16x32_bf16 a[16:19], a[196:199], v[128:131], a[16:19]\n"
"v_exp_f32 v79, v79\n"
"v_mfma_f32_16x16x32_bf16 a[32:35], a[200:203], v[128:131], a[32:35]\n"
"v_exp_f32 v92, v92\n"
"v_mfma_f32_16x16x32_bf16 a[32:35], a[232:235], v[144:147], a[32:35]\n"
"v_exp_f32 v93, v93\n"
"v_mfma_f32_16x16x32_bf16 a[48:51], a[236:239], v[144:147], a[48:51]\n"
"v_exp_f32 v94, v94\n"
"v_mfma_f32_16x16x32_bf16 a[48:51], a[204:207], v[128:131], a[48:51]\n"
"v_exp_f32 v95, v95\n"
"v_mfma_f32_16x16x32_bf16 a[128:131], v[200:203], v[128:131], a[128:131]\n"
"v_exp_f32 v96, v96\n"
"v_mfma_f32_16x16x32_bf16 a[128:131], v[200:203], v[144:147], a[128:131]\n"
"v_exp_f32 v97, v97\n"
"v_mfma_f32_16x16x32_bf16 v[4:7], a[176:179], v[132:135], v[4:7]\n"
"v_cvt_pk_bf16_f32 v128, v64, v65\n"
"v_cvt_pk_bf16_f32 v129, v66, v67\n"
"v_mfma_f32_16x16x32_bf16 v[4:7], a[208:211], v[148:151], v[4:7]\n"
"v_exp_f32 v98, v98\n"
"v_mfma_f32_16x16x32_bf16 v[20:23], a[212:215], v[148:151], v[20:23]\n"
"v_cvt_pk_bf16_f32 v130, v80, v81\n"
"v_cvt_pk_bf16_f32 v131, v82, v83\n"
"v_mfma_f32_16x16x32_bf16 v[20:23], a[180:183], v[132:135], v[20:23]\n"
"v_exp_f32 v99, v99\n"
"v_mfma_f32_16x16x32_bf16 v[36:39], a[184:187], v[132:135], v[36:39]\n"
"v_exp_f32 v112, v112\n"
"v_mfma_f32_16x16x32_bf16 v[36:39], a[216:219], v[148:151], v[36:39]\n"
"v_exp_f32 v113, v113\n"
"v_mfma_f32_16x16x32_bf16 v[52:55], a[220:223], v[148:151], v[52:55]\n"
"v_exp_f32 v114, v114\n"
"v_mfma_f32_16x16x32_bf16 v[52:55], a[188:191], v[132:135], v[52:55]\n"
"v_exp_f32 v115, v115\n"
"v_mfma_f32_16x16x32_bf16 a[4:7], a[192:195], v[132:135], a[4:7]\n"
"v_exp_f32 v100, v100\n"
"v_mfma_f32_16x16x32_bf16 a[4:7], a[224:227], v[148:151], a[4:7]\n"
"v_exp_f32 v101, v101\n"
"v_mfma_f32_16x16x32_bf16 a[20:23], a[228:231], v[148:151], a[20:23]\n"
"v_cvt_pk_bf16_f32 v144, v96, v97\n"
"v_cvt_pk_bf16_f32 v145, v98, v99\n"
"v_mfma_f32_16x16x32_bf16 a[20:23], a[196:199], v[132:135], a[20:23]\n"
"v_exp_f32 v102, v102\n"
"v_mfma_f32_16x16x32_bf16 a[36:39], a[200:203], v[132:135], a[36:39]\n"
"v_cvt_pk_bf16_f32 v146, v112, v113\n"
"v_cvt_pk_bf16_f32 v147, v114, v115\n"
"v_mfma_f32_16x16x32_bf16 a[36:39], a[232:235], v[148:151], a[36:39]\n"
"v_exp_f32 v103, v103\n"
"v_mfma_f32_16x16x32_bf16 a[52:55], a[236:239], v[148:151], a[52:55]\n"
"v_exp_f32 v116, v116\n"
"v_mfma_f32_16x16x32_bf16 a[52:55], a[204:207], v[132:135], a[52:55]\n"
"v_exp_f32 v117, v117\n"
"v_mfma_f32_16x16x32_bf16 a[132:135], v[200:203], v[132:135], a[132:135]\n"
"v_mfma_f32_16x16x32_bf16 a[132:135], v[200:203], v[148:151], a[132:135]\n"
"v_exp_f32 v118, v118\n"
"v_mfma_f32_16x16x32_bf16 v[8:11], a[176:179], v[136:139], v[8:11]\n"
"v_cvt_pk_bf16_f32 v132, v68, v69\n"
"v_cvt_pk_bf16_f32 v133, v70, v71\n"
"v_mfma_f32_16x16x32_bf16 v[8:11], a[208:211], v[152:155], v[8:11]\n"
"v_exp_f32 v119, v119\n"
"v_mfma_f32_16x16x32_bf16 v[24:27], a[212:215], v[152:155], v[24:27]\n"
"v_cvt_pk_bf16_f32 v134, v84, v85\n"
"v_cvt_pk_bf16_f32 v135, v86, v87\n"
"v_mfma_f32_16x16x32_bf16 v[24:27], a[180:183], v[136:139], v[24:27]\n"
"v_exp_f32 v104, v104\n"
"v_mfma_f32_16x16x32_bf16 v[40:43], a[184:187], v[136:139], v[40:43]\n"
"v_cvt_pk_bf16_f32 v148, v100, v101\n"
"v_cvt_pk_bf16_f32 v149, v102, v103\n"
"v_mfma_f32_16x16x32_bf16 v[40:43], a[216:219], v[152:155], v[40:43]\n"
"v_exp_f32 v105, v105\n"
"v_mfma_f32_16x16x32_bf16 v[56:59], a[220:223], v[152:155], v[56:59]\n"
"v_mfma_f32_16x16x32_bf16 v[56:59], a[188:191], v[136:139], v[56:59]\n"
"v_mfma_f32_16x16x32_bf16 a[8:11], a[192:195], v[136:139], a[8:11]\n"
"s_waitcnt vmcnt(1) lgkmcnt(0)\n"
"s_barrier\n"
"v_mfma_f32_16x16x32_bf16 a[8:11], a[224:227], v[152:155], a[8:11]\n"
"v_exp_f32 v106, v106\n"
"v_mfma_f32_16x16x32_bf16 a[24:27], a[228:231], v[152:155], a[24:27]\n"
"v_xor_b32 v192, 0x8000, v192\n"
"v_xor_b32 v193, 0x8000, v193\n"
"v_mfma_f32_16x16x32_bf16 a[24:27], a[196:199], v[136:139], a[24:27]\n"
"v_exp_f32 v107, v107\n"
"v_mfma_f32_16x16x32_bf16 a[40:43], a[200:203], v[136:139], a[40:43]\n"
"ds_read_b128 a[144:147], v192\n"
"ds_read_b128 a[148:151], v192 offset:1024\n"
"v_mfma_f32_16x16x32_bf16 a[40:43], a[232:235], v[152:155], a[40:43]\n"
"v_exp_f32 v120, v120\n"
"v_mfma_f32_16x16x32_bf16 a[56:59], a[236:239], v[152:155], a[56:59]\n"
"ds_read_b128 a[152:155], v192 offset:2048\n"
"ds_read_b128 a[156:159], v192 offset:3072\n"
"v_mfma_f32_16x16x32_bf16 a[56:59], a[204:207], v[136:139], a[56:59]\n"
"v_exp_f32 v121, v121\n"
"v_mfma_f32_16x16x32_bf16 a[136:139], v[200:203], v[136:139], a[136:139]\n"
"ds_read_b128 a[160:163], v192 offset:4096\n"
"ds_read_b128 a[164:167], v192 offset:5120\n"
"v_mfma_f32_16x16x32_bf16 a[136:139], v[200:203], v[152:155], a[136:139]\n"
"v_exp_f32 v122, v122\n"
"v_mfma_f32_16x16x32_bf16 v[12:15], a[176:179], v[140:143], v[12:15]\n"
"ds_read_b128 a[168:171], v192 offset:6144\n"
"ds_read_b128 a[172:175], v192 offset:7168\n"
"v_mfma_f32_16x16x32_bf16 v[12:15], a[208:211], v[156:159], v[12:15]\n"
"v_exp_f32 v123, v123\n"
"v_mfma_f32_16x16x32_bf16 v[28:31], a[212:215], v[156:159], v[28:31]\n"
"v_cvt_pk_bf16_f32 v136, v72, v73\n"
"v_cvt_pk_bf16_f32 v137, v74, v75\n"
"v_mfma_f32_16x16x32_bf16 v[28:31], a[180:183], v[140:143], v[28:31]\n"
"v_exp_f32 v108, v108\n"
"v_mfma_f32_16x16x32_bf16 v[44:47], a[184:187], v[140:143], v[44:47]\n"
"ds_read_b128 a[176:179], v193\n"
"ds_read_b128 a[208:211], v193 offset:8192\n"
"v_mfma_f32_16x16x32_bf16 v[44:47], a[216:219], v[156:159], v[44:47]\n"
"v_exp_f32 v109, v109\n"
"v_mfma_f32_16x16x32_bf16 v[60:63], a[220:223], v[156:159], v[60:63]\n"
"ds_read_b128 a[180:183], v193 offset:1024\n"
"ds_read_b128 a[212:215], v193 offset:9216\n"
"v_mfma_f32_16x16x32_bf16 v[60:63], a[188:191], v[140:143], v[60:63]\n"
"v_exp_f32 v110, v110\n"
"v_mfma_f32_16x16x32_bf16 a[12:15], a[192:195], v[140:143], a[12:15]\n"
"v_cvt_pk_bf16_f32 v138, v88, v89\n"
"v_cvt_pk_bf16_f32 v139, v90, v91\n"
"v_mfma_f32_16x16x32_bf16 a[12:15], a[224:227], v[156:159], a[12:15]\n"
"v_exp_f32 v111, v111\n"
"v_mfma_f32_16x16x32_bf16 a[28:31], a[228:231], v[156:159], a[28:31]\n"
"v_cvt_pk_bf16_f32 v150, v116, v117\n"
"v_cvt_pk_bf16_f32 v151, v118, v119\n"
"v_mfma_f32_16x16x32_bf16 a[28:31], a[196:199], v[140:143], a[28:31]\n"
"v_exp_f32 v124, v124\n"
"v_mfma_f32_16x16x32_bf16 a[44:47], a[200:203], v[140:143], a[44:47]\n"
"v_cvt_pk_bf16_f32 v152, v104, v105\n"
"v_cvt_pk_bf16_f32 v153, v106, v107\n"
"v_mfma_f32_16x16x32_bf16 a[44:47], a[232:235], v[156:159], a[44:47]\n"
"v_exp_f32 v125, v125\n"
"v_mfma_f32_16x16x32_bf16 a[60:63], a[236:239], v[156:159], a[60:63]\n"
"v_xor_b32 v194, 0x8000, v194\n"
"v_cvt_pk_bf16_f32 v154, v120, v121\n"
"v_mfma_f32_16x16x32_bf16 a[60:63], a[204:207], v[140:143], a[60:63]\n"
"v_exp_f32 v126, v126\n"
"v_mfma_f32_16x16x32_bf16 a[140:143], v[200:203], v[140:143], a[140:143]\n"
"s_add_u32 s62, s62, 1\n"
"s_cmp_lt_u32 s62, 16\n"
"v_cvt_pk_bf16_f32 v155, v122, v123\n"
"v_mfma_f32_16x16x32_bf16 a[140:143], v[200:203], v[156:159], a[140:143]\n"
"v_exp_f32 v127, v127\n"
"s_cbranch_scc1 .Lfull2_21_%=\n"
"s_nop 0\n"
".Lfullv_23_%=:\n"
"s_waitcnt lgkmcnt(8)\n"
"v_mov_b32 v208, 0x21000\n"
"v_mfma_f32_16x16x32_bf16 v[64:67], a[144:147], a[64:67], v[160:163]\n"
"v_cvt_pk_bf16_f32 v140, v76, v77\n"
"v_cvt_pk_bf16_f32 v141, v78, v79\n"
"v_mfma_f32_16x16x32_bf16 v[64:67], a[148:151], a[80:83], v[64:67]\n"
"s_xor_b32 s85, s85, 0x8000\n"
"v_mfma_f32_16x16x32_bf16 v[64:67], a[152:155], a[96:99], v[64:67]\n"
"v_cvt_pk_bf16_f32 v142, v92, v93\n"
"v_cvt_pk_bf16_f32 v143, v94, v95\n"
"v_mfma_f32_16x16x32_bf16 v[64:67], a[156:159], a[112:115], v[64:67]\n"
"s_mov_b32 m0, s85\n"
"v_mfma_f32_16x16x32_bf16 v[68:71], a[156:159], a[116:119], v[164:167]\n"
"v_cvt_pk_bf16_f32 v156, v108, v109\n"
"v_cvt_pk_bf16_f32 v157, v110, v111\n"
"v_mfma_f32_16x16x32_bf16 v[68:71], a[152:155], a[100:103], v[68:71]\n"
"buffer_load_dwordx4 v254, s[96:99], s67 offen sc1 lds\n"
"v_mfma_f32_16x16x32_bf16 v[68:71], a[148:151], a[84:87], v[68:71]\n"
"v_cvt_pk_bf16_f32 v158, v124, v125\n"
"v_cvt_pk_bf16_f32 v159, v126, v127\n"
"v_mfma_f32_16x16x32_bf16 v[68:71], a[144:147], a[68:71], v[68:71]\n"
"v_mfma_f32_16x16x32_bf16 v[72:75], a[144:147], a[72:75], v[168:171]\n"
"buffer_load_dwordx4 v254, s[96:99], s67 offen offset:1024 sc1 lds\n"
"v_mfma_f32_16x16x32_bf16 v[72:75], a[148:151], a[88:91], v[72:75]\n"
"ds_read_b128 a[184:187], v193 offset:2048\n"
"ds_read_b128 a[216:219], v193 offset:10240\n"
"v_mfma_f32_16x16x32_bf16 v[72:75], a[152:155], a[104:107], v[72:75]\n"
"v_mfma_f32_16x16x32_bf16 v[72:75], a[156:159], a[120:123], v[72:75]\n"
"ds_read_b128 a[188:191], v193 offset:3072\n"
"v_mfma_f32_16x16x32_bf16 v[76:79], a[156:159], a[124:127], v[172:175]\n"
"ds_read_b128 a[220:223], v193 offset:11264\n"
"v_mfma_f32_16x16x32_bf16 v[76:79], a[152:155], a[108:111], v[76:79]\n"
"ds_read_b128 a[192:195], v194 offset:4096\n"
"v_mfma_f32_16x16x32_bf16 v[76:79], a[148:151], a[92:95], v[76:79]\n"
"ds_read_b128 a[224:227], v194 offset:12288\n"
"s_waitcnt lgkmcnt(10)\n"
"v_mfma_f32_16x16x32_bf16 v[76:79], a[144:147], a[76:79], v[76:79]\n"
"buffer_load_dwordx4 v254, s[96:99], s67 offen offset:2048 sc1 lds\n"
"v_mfma_f32_16x16x32_bf16 v[80:83], a[160:163], a[64:67], v[160:163]\n"
"ds_read_b128 a[144:147], v192 offset:8192\n"
"ds_read_b128 a[148:151], v192 offset:9216\n"
"v_mfma_f32_16x16x32_bf16 v[80:83], a[164:167], a[80:83], v[80:83]\n"
"v_mfma_f32_16x16x32_bf16 v[80:83], a[168:171], a[96:99], v[80:83]\n"
"ds_read_b128 a[152:155], v192 offset:10240\n"
"ds_read_b128 a[156:159], v192 offset:11264\n"
"v_mfma_f32_16x16x32_bf16 v[80:83], a[172:175], a[112:115], v[80:83]\n"
"v_mfma_f32_16x16x32_bf16 v[84:87], a[172:175], a[116:119], v[164:167]\n"
"buffer_load_dwordx4 v254, s[96:99], s67 offen offset:3072 sc1 lds\n"
"v_mfma_f32_16x16x32_bf16 v[84:87], a[168:171], a[100:103], v[84:87]\n"
"v_exp_f32 v64, v64\n"
"v_mfma_f32_16x16x32_bf16 v[84:87], a[164:167], a[84:87], v[84:87]\n"
"s_add_u32 s70, s67, 0x4000\n"
"s_add_u32 m0, s85, 0x4000\n"
"v_mfma_f32_16x16x32_bf16 v[84:87], a[160:163], a[68:71], v[84:87]\n"
"v_exp_f32 v65, v65\n"
"v_mfma_f32_16x16x32_bf16 v[88:91], a[160:163], a[72:75], v[168:171]\n"
"ds_read_b128 a[196:199], v194 offset:5120\n"
"ds_read_b128 a[228:231], v194 offset:13312\n"
"v_mfma_f32_16x16x32_bf16 v[88:91], a[164:167], a[88:91], v[88:91]\n"
"v_exp_f32 v66, v66\n"
"v_mfma_f32_16x16x32_bf16 v[88:91], a[168:171], a[104:107], v[88:91]\n"
"buffer_load_dwordx4 v254, s[96:99], s70 offen sc1 lds\n"
"v_mfma_f32_16x16x32_bf16 v[88:91], a[172:175], a[120:123], v[88:91]\n"
"v_exp_f32 v67, v67\n"
"v_mfma_f32_16x16x32_bf16 v[92:95], a[172:175], a[124:127], v[172:175]\n"
"ds_read_b128 a[200:203], v194 offset:6144\n"
"ds_read_b128 a[232:235], v194 offset:14336\n"
"v_mfma_f32_16x16x32_bf16 v[92:95], a[168:171], a[108:111], v[92:95]\n"
"v_exp_f32 v80, v80\n"
"v_mfma_f32_16x16x32_bf16 v[92:95], a[164:167], a[92:95], v[92:95]\n"
"ds_read_b128 a[204:207], v194 offset:7168\n"
"s_waitcnt lgkmcnt(5)\n"
"v_mfma_f32_16x16x32_bf16 v[92:95], a[160:163], a[76:79], v[92:95]\n"
"v_exp_f32 v81, v81\n"
"v_mfma_f32_16x16x32_bf16 v[96:99], a[144:147], a[64:67], v[160:163]\n"
"ds_read_b128 a[160:163], v192 offset:12288\n"
"ds_read_b128 a[164:167], v192 offset:13312\n"
"v_mfma_f32_16x16x32_bf16 v[96:99], a[148:151], a[80:83], v[96:99]\n"
"v_exp_f32 v82, v82\n"
"v_mfma_f32_16x16x32_bf16 v[96:99], a[152:155], a[96:99], v[96:99]\n"
"ds_read_b128 a[168:171], v192 offset:14336\n"
"ds_read_b128 a[172:175], v192 offset:15360\n"
"v_mfma_f32_16x16x32_bf16 v[96:99], a[156:159], a[112:115], v[96:99]\n"
"v_exp_f32 v83, v83\n"
"v_mfma_f32_16x16x32_bf16 v[100:103], a[156:159], a[116:119], v[164:167]\n"
"buffer_load_dwordx4 v254, s[96:99], s70 offen offset:1024 sc1 lds\n"
"v_mfma_f32_16x16x32_bf16 v[100:103], a[152:155], a[100:103], v[100:103]\n"
"v_exp_f32 v68, v68\n"
"v_mfma_f32_16x16x32_bf16 v[100:103], a[148:151], a[84:87], v[100:103]\n"
"buffer_load_dwordx4 v254, s[96:99], s70 offen offset:2048 sc1 lds\n"
"v_mfma_f32_16x16x32_bf16 v[100:103], a[144:147], a[68:71], v[100:103]\n"
"v_exp_f32 v69, v69\n"
"v_mfma_f32_16x16x32_bf16 v[104:107], a[144:147], a[72:75], v[168:171]\n"
"ds_read_b128 a[236:239], v194 offset:15360\n"
"v_mfma_f32_16x16x32_bf16 v[104:107], a[148:151], a[88:91], v[104:107]\n"
"v_exp_f32 v70, v70\n"
"v_mfma_f32_16x16x32_bf16 v[104:107], a[152:155], a[104:107], v[104:107]\n"
"v_mfma_f32_16x16x32_bf16 v[104:107], a[156:159], a[120:123], v[104:107]\n"
"v_exp_f32 v71, v71\n"
"v_mfma_f32_16x16x32_bf16 v[108:111], a[156:159], a[124:127], v[172:175]\n"
"buffer_load_dwordx4 v254, s[96:99], s70 offen offset:3072 sc1 lds\n"
"v_mfma_f32_16x16x32_bf16 v[108:111], a[152:155], a[108:111], v[108:111]\n"
"v_exp_f32 v84, v84\n"
"v_mfma_f32_16x16x32_bf16 v[108:111], a[148:151], a[92:95], v[108:111]\n"
"global_load_dword a255, v248, s[4:5]\n"
"s_waitcnt lgkmcnt(1)\n"
"v_mfma_f32_16x16x32_bf16 v[108:111], a[144:147], a[76:79], v[108:111]\n"
"v_exp_f32 v85, v85\n"
"v_mfma_f32_16x16x32_bf16 v[112:115], a[160:163], a[64:67], v[160:163]\n"
"v_mfma_f32_16x16x32_bf16 v[112:115], a[164:167], a[80:83], v[112:115]\n"
"v_exp_f32 v86, v86\n"
"v_mfma_f32_16x16x32_bf16 v[112:115], a[168:171], a[96:99], v[112:115]\n"
"v_mfma_f32_16x16x32_bf16 v[112:115], a[172:175], a[112:115], v[112:115]\n"
"v_exp_f32 v87, v87\n"
"v_mfma_f32_16x16x32_bf16 v[116:119], a[172:175], a[116:119], v[164:167]\n"
"v_mfma_f32_16x16x32_bf16 v[116:119], a[168:171], a[100:103], v[116:119]\n"
"v_exp_f32 v72, v72\n"
"v_mfma_f32_16x16x32_bf16 v[116:119], a[164:167], a[84:87], v[116:119]\n"
"v_mfma_f32_16x16x32_bf16 v[116:119], a[160:163], a[68:71], v[116:119]\n"
"v_exp_f32 v73, v73\n"
"v_mfma_f32_16x16x32_bf16 v[120:123], a[160:163], a[72:75], v[168:171]\n"
"v_mfma_f32_16x16x32_bf16 v[120:123], a[164:167], a[88:91], v[120:123]\n"
"v_exp_f32 v74, v74\n"
"v_mfma_f32_16x16x32_bf16 v[120:123], a[168:171], a[104:107], v[120:123]\n"
"v_mfma_f32_16x16x32_bf16 v[120:123], a[172:175], a[120:123], v[120:123]\n"
"v_exp_f32 v75, v75\n"
"v_mfma_f32_16x16x32_bf16 v[124:127], a[172:175], a[124:127], v[172:175]\n"
"v_mfma_f32_16x16x32_bf16 v[124:127], a[168:171], a[108:111], v[124:127]\n"
"v_exp_f32 v88, v88\n"
"v_mfma_f32_16x16x32_bf16 v[124:127], a[164:167], a[92:95], v[124:127]\n"
"v_mfma_f32_16x16x32_bf16 v[124:127], a[160:163], a[76:79], v[124:127]\n"
"v_exp_f32 v89, v89\n"
"v_mfma_f32_16x16x32_bf16 v[0:3], a[176:179], v[128:131], v[0:3]\n"
"v_mfma_f32_16x16x32_bf16 v[0:3], a[208:211], v[144:147], v[0:3]\n"
"v_exp_f32 v90, v90\n"
"v_mfma_f32_16x16x32_bf16 v[16:19], a[212:215], v[144:147], v[16:19]\n"
"v_mfma_f32_16x16x32_bf16 v[16:19], a[180:183], v[128:131], v[16:19]\n"
"v_exp_f32 v91, v91\n"
"v_mfma_f32_16x16x32_bf16 v[32:35], a[184:187], v[128:131], v[32:35]\n"
"v_mfma_f32_16x16x32_bf16 v[32:35], a[216:219], v[144:147], v[32:35]\n"
"v_exp_f32 v76, v76\n"
"v_mfma_f32_16x16x32_bf16 v[48:51], a[220:223], v[144:147], v[48:51]\n"
"s_waitcnt lgkmcnt(0)\n"
"v_mfma_f32_16x16x32_bf16 v[48:51], a[188:191], v[128:131], v[48:51]\n"
"v_exp_f32 v77, v77\n"
"v_mfma_f32_16x16x32_bf16 a[0:3], a[192:195], v[128:131], a[0:3]\n"
"v_mfma_f32_16x16x32_bf16 a[0:3], a[224:227], v[144:147], a[0:3]\n"
"v_exp_f32 v78, v78\n"
"v_mfma_f32_16x16x32_bf16 a[16:19], a[228:231], v[144:147], a[16:19]\n"
"v_mfma_f32_16x16x32_bf16 a[16:19], a[196:199], v[128:131], a[16:19]\n"
"v_exp_f32 v79, v79\n"
"v_mfma_f32_16x16x32_bf16 a[32:35], a[200:203], v[128:131], a[32:35]\n"
"v_exp_f32 v92, v92\n"
"v_mfma_f32_16x16x32_bf16 a[32:35], a[232:235], v[144:147], a[32:35]\n"
"v_exp_f32 v93, v93\n"
"v_mfma_f32_16x16x32_bf16 a[48:51], a[236:239], v[144:147], a[48:51]\n"
"v_exp_f32 v94, v94\n"
"v_mfma_f32_16x16x32_bf16 a[48:51], a[204:207], v[128:131], a[48:51]\n"
"v_exp_f32 v95, v95\n"
"v_mfma_f32_16x16x32_bf16 a[128:131], v[200:203], v[128:131], a[128:131]\n"
"v_exp_f32 v96, v96\n"
"v_mfma_f32_16x16x32_bf16 a[128:131], v[200:203], v[144:147], a[128:131]\n"
"v_exp_f32 v97, v97\n"
"v_mfma_f32_16x16x32_bf16 v[4:7], a[176:179], v[132:135], v[4:7]\n"
"v_cvt_pk_bf16_f32 v128, v64, v65\n"
"v_cvt_pk_bf16_f32 v129, v66, v67\n"
"v_mfma_f32_16x16x32_bf16 v[4:7], a[208:211], v[148:151], v[4:7]\n"
"v_exp_f32 v98, v98\n"
"v_mfma_f32_16x16x32_bf16 v[20:23], a[212:215], v[148:151], v[20:23]\n"
"v_cvt_pk_bf16_f32 v130, v80, v81\n"
"v_cvt_pk_bf16_f32 v131, v82, v83\n"
"v_mfma_f32_16x16x32_bf16 v[20:23], a[180:183], v[132:135], v[20:23]\n"
"v_exp_f32 v99, v99\n"
"v_mfma_f32_16x16x32_bf16 v[36:39], a[184:187], v[132:135], v[36:39]\n"
"v_exp_f32 v112, v112\n"
"v_mfma_f32_16x16x32_bf16 v[36:39], a[216:219], v[148:151], v[36:39]\n"
"v_exp_f32 v113, v113\n"
"v_mfma_f32_16x16x32_bf16 v[52:55], a[220:223], v[148:151], v[52:55]\n"
"v_exp_f32 v114, v114\n"
"v_mfma_f32_16x16x32_bf16 v[52:55], a[188:191], v[132:135], v[52:55]\n"
"v_exp_f32 v115, v115\n"
"v_mfma_f32_16x16x32_bf16 a[4:7], a[192:195], v[132:135], a[4:7]\n"
"v_exp_f32 v100, v100\n"
"v_mfma_f32_16x16x32_bf16 a[4:7], a[224:227], v[148:151], a[4:7]\n"
"v_exp_f32 v101, v101\n"
"v_mfma_f32_16x16x32_bf16 a[20:23], a[228:231], v[148:151], a[20:23]\n"
"v_cvt_pk_bf16_f32 v144, v96, v97\n"
"v_cvt_pk_bf16_f32 v145, v98, v99\n"
"v_mfma_f32_16x16x32_bf16 a[20:23], a[196:199], v[132:135], a[20:23]\n"
"v_exp_f32 v102, v102\n"
"v_mfma_f32_16x16x32_bf16 a[36:39], a[200:203], v[132:135], a[36:39]\n"
"v_cvt_pk_bf16_f32 v146, v112, v113\n"
"v_cvt_pk_bf16_f32 v147, v114, v115\n"
"v_mfma_f32_16x16x32_bf16 a[36:39], a[232:235], v[148:151], a[36:39]\n"
"v_exp_f32 v103, v103\n"
"ds_read_b128 v[204:207], v208\n"
"s_waitcnt lgkmcnt(0)\n"
"v_or_b32 v204, v204, v206\n"
"s_nop 0\n"
"v_readfirstlane_b32 s12, v204\n"
"s_nop 1\n"
"s_cmp_eq_u32 s12, 0\n"
"s_cselect_b32 s12, s54, 0x40000000\n"
"s_mov_b64 exec, s[34:35]\n"
"buffer_store_dwordx2 v[250:251], v249, s[92:95], s12 offen sc1\n"
"s_mov_b64 exec, -1\n"
"v_mfma_f32_16x16x32_bf16 a[52:55], a[236:239], v[148:151], a[52:55]\n"
"v_exp_f32 v116, v116\n"
"v_mfma_f32_16x16x32_bf16 a[52:55], a[204:207], v[132:135], a[52:55]\n"
"v_exp_f32 v117, v117\n"
"v_mfma_f32_16x16x32_bf16 a[132:135], v[200:203], v[132:135], a[132:135]\n"
"v_mfma_f32_16x16x32_bf16 a[132:135], v[200:203], v[148:151], a[132:135]\n"
"v_exp_f32 v118, v118\n"
"v_mfma_f32_16x16x32_bf16 v[8:11], a[176:179], v[136:139], v[8:11]\n"
"v_cvt_pk_bf16_f32 v132, v68, v69\n"
"v_cvt_pk_bf16_f32 v133, v70, v71\n"
"v_mfma_f32_16x16x32_bf16 v[8:11], a[208:211], v[152:155], v[8:11]\n"
"v_exp_f32 v119, v119\n"
"v_mfma_f32_16x16x32_bf16 v[24:27], a[212:215], v[152:155], v[24:27]\n"
"v_cvt_pk_bf16_f32 v134, v84, v85\n"
"v_cvt_pk_bf16_f32 v135, v86, v87\n"
"v_mfma_f32_16x16x32_bf16 v[24:27], a[180:183], v[136:139], v[24:27]\n"
"v_exp_f32 v104, v104\n"
"v_mfma_f32_16x16x32_bf16 v[40:43], a[184:187], v[136:139], v[40:43]\n"
"v_cvt_pk_bf16_f32 v148, v100, v101\n"
"v_cvt_pk_bf16_f32 v149, v102, v103\n"
"v_mfma_f32_16x16x32_bf16 v[40:43], a[216:219], v[152:155], v[40:43]\n"
"v_exp_f32 v105, v105\n"
"v_mfma_f32_16x16x32_bf16 v[56:59], a[220:223], v[152:155], v[56:59]\n"
"v_mfma_f32_16x16x32_bf16 v[56:59], a[188:191], v[136:139], v[56:59]\n"
"v_mfma_f32_16x16x32_bf16 a[8:11], a[192:195], v[136:139], a[8:11]\n"
"s_waitcnt vmcnt(2) lgkmcnt(0)\n"
"s_barrier\n"
"v_mfma_f32_16x16x32_bf16 a[8:11], a[224:227], v[152:155], a[8:11]\n"
"v_exp_f32 v106, v106\n"
"v_mfma_f32_16x16x32_bf16 a[24:27], a[228:231], v[152:155], a[24:27]\n"
"v_xor_b32 v192, 0x8000, v192\n"
"v_xor_b32 v193, 0x8000, v193\n"
"v_mfma_f32_16x16x32_bf16 a[24:27], a[196:199], v[136:139], a[24:27]\n"
"v_exp_f32 v107, v107\n"
"v_mfma_f32_16x16x32_bf16 a[40:43], a[200:203], v[136:139], a[40:43]\n"
"ds_read_b128 a[144:147], v192\n"
"ds_read_b128 a[148:151], v192 offset:1024\n"
"v_mfma_f32_16x16x32_bf16 a[40:43], a[232:235], v[152:155], a[40:43]\n"
"v_exp_f32 v120, v120\n"
"v_mfma_f32_16x16x32_bf16 a[56:59], a[236:239], v[152:155], a[56:59]\n"
"ds_read_b128 a[152:155], v192 offset:2048\n"
"ds_read_b128 a[156:159], v192 offset:3072\n"
"v_mfma_f32_16x16x32_bf16 a[56:59], a[204:207], v[136:139], a[56:59]\n"
"v_exp_f32 v121, v121\n"
"v_mfma_f32_16x16x32_bf16 a[136:139], v[200:203], v[136:139], a[136:139]\n"
"ds_read_b128 a[160:163], v192 offset:4096\n"
"ds_read_b128 a[164:167], v192 offset:5120\n"
"v_mfma_f32_16x16x32_bf16 a[136:139], v[200:203], v[152:155], a[136:139]\n"
"v_exp_f32 v122, v122\n"
"v_mfma_f32_16x16x32_bf16 v[12:15], a[176:179], v[140:143], v[12:15]\n"
"ds_read_b128 a[168:171], v192 offset:6144\n"
"ds_read_b128 a[172:175], v192 offset:7168\n"
"v_mfma_f32_16x16x32_bf16 v[12:15], a[208:211], v[156:159], v[12:15]\n"
"v_exp_f32 v123, v123\n"
"v_mfma_f32_16x16x32_bf16 v[28:31], a[212:215], v[156:159], v[28:31]\n"
"v_cvt_pk_bf16_f32 v136, v72, v73\n"
"v_cvt_pk_bf16_f32 v137, v74, v75\n"
"v_mfma_f32_16x16x32_bf16 v[28:31], a[180:183], v[140:143], v[28:31]\n"
"v_exp_f32 v108, v108\n"
"v_mfma_f32_16x16x32_bf16 v[44:47], a[184:187], v[140:143], v[44:47]\n"
"ds_read_b128 a[176:179], v193\n"
"ds_read_b128 a[208:211], v193 offset:8192\n"
"v_mfma_f32_16x16x32_bf16 v[44:47], a[216:219], v[156:159], v[44:47]\n"
"v_exp_f32 v109, v109\n"
"v_mfma_f32_16x16x32_bf16 v[60:63], a[220:223], v[156:159], v[60:63]\n"
"ds_read_b128 a[180:183], v193 offset:1024\n"
"ds_read_b128 a[212:215], v193 offset:9216\n"
"v_mfma_f32_16x16x32_bf16 v[60:63], a[188:191], v[140:143], v[60:63]\n"
"v_exp_f32 v110, v110\n"
"v_mfma_f32_16x16x32_bf16 a[12:15], a[192:195], v[140:143], a[12:15]\n"
"v_cvt_pk_bf16_f32 v138, v88, v89\n"
"v_cvt_pk_bf16_f32 v139, v90, v91\n"
"v_mfma_f32_16x16x32_bf16 a[12:15], a[224:227], v[156:159], a[12:15]\n"
"v_exp_f32 v111, v111\n"
"v_mfma_f32_16x16x32_bf16 a[28:31], a[228:231], v[156:159], a[28:31]\n"
"v_cvt_pk_bf16_f32 v150, v116, v117\n"
"v_cvt_pk_bf16_f32 v151, v118, v119\n"
"v_mfma_f32_16x16x32_bf16 a[28:31], a[196:199], v[140:143], a[28:31]\n"
"v_exp_f32 v124, v124\n"
"v_mfma_f32_16x16x32_bf16 a[44:47], a[200:203], v[140:143], a[44:47]\n"
"v_cvt_pk_bf16_f32 v152, v104, v105\n"
"v_cvt_pk_bf16_f32 v153, v106, v107\n"
"v_mfma_f32_16x16x32_bf16 a[44:47], a[232:235], v[156:159], a[44:47]\n"
"v_exp_f32 v125, v125\n"
"v_mfma_f32_16x16x32_bf16 a[60:63], a[236:239], v[156:159], a[60:63]\n"
"v_xor_b32 v194, 0x8000, v194\n"
"v_cvt_pk_bf16_f32 v154, v120, v121\n"
"v_mfma_f32_16x16x32_bf16 a[60:63], a[204:207], v[140:143], a[60:63]\n"
"v_exp_f32 v126, v126\n"
"v_mfma_f32_16x16x32_bf16 a[140:143], v[200:203], v[140:143], a[140:143]\n"
"s_add_u32 s62, s62, 1\n"
"v_cvt_pk_bf16_f32 v155, v122, v123\n"
"v_mfma_f32_16x16x32_bf16 a[140:143], v[200:203], v[156:159], a[140:143]\n"
"v_exp_f32 v127, v127\n"
"s_nop 0\n"
".Ldrain_36_%=:\n"
"s_waitcnt lgkmcnt(2)\n"
"v_mfma_f32_16x16x32_bf16 v[0:3], a[176:179], v[128:131], v[0:3]\n"
"ds_read_b128 a[184:187], v193 offset:2048\n"
"ds_read_b128 a[216:219], v193 offset:10240\n"
"s_lshr_b32 s62, s9, 1\n"
"s_and_b32 s62, s62, 3\n"
"s_and_b32 s63, s9, 1\n"
"s_lshl_b32 s63, s63, 3\n"
"v_mfma_f32_16x16x32_bf16 v[0:3], a[208:211], v[144:147], v[0:3]\n"
"ds_read_b128 a[188:191], v193 offset:3072\n"
"ds_read_b128 a[220:223], v193 offset:11264\n"
"s_lshr_b32 s12, s9, 5\n"
"s_add_u32 s63, s63, s12\n"
"s_lshr_b32 s64, s9, 3\n"
"s_and_b32 s64, s64, 3\n"
"s_waitcnt lgkmcnt(4)\n"
"v_mfma_f32_16x16x32_bf16 v[16:19], a[212:215], v[144:147], v[16:19]\n"
"ds_read_b128 a[192:195], v194 offset:4096\n"
"ds_read_b128 a[224:227], v194 offset:12288\n"
"s_lshr_b32 s12, s63, 1\n"
"s_lshl_b32 s13, s12, 8\n"
"s_add_u32 s71, s13, 0x1800\n"
"s_lshl_b32 s14, s62, 6\n"
"v_mfma_f32_16x16x32_bf16 v[16:19], a[180:183], v[128:131], v[16:19]\n"
"ds_read_b128 a[196:199], v194 offset:5120\n"
"ds_read_b128 a[228:231], v194 offset:13312\n"
"s_add_u32 s13, s13, s14\n"
"s_add_u32 s81, s13, 0x2000\n"
"s_lshl_b32 s13, s13, 2\n"
"s_add_u32 s72, s13, 0x4000\n"
"s_waitcnt lgkmcnt(6)\n"
"v_mfma_f32_16x16x32_bf16 v[32:35], a[184:187], v[128:131], v[32:35]\n"
"ds_read_b128 a[200:203], v194 offset:6144\n"
"ds_read_b128 a[232:235], v194 offset:14336\n"
"v_and_b32 v228, 31, v255\n"
"v_lshlrev_b32 v228, 3, v228\n"
"v_and_b32 v229, 56, v228\n"
"s_mov_b32 s24, s56\n"
"v_mfma_f32_16x16x32_bf16 v[32:35], a[216:219], v[144:147], v[32:35]\n"
"ds_read_b128 a[204:207], v194 offset:7168\n"
"ds_read_b128 a[236:239], v194 offset:15360\n"
"s_and_b32 s25, s57, 0xffff\n"
"s_mov_b32 s26, 0x158000\n"
"s_mov_b32 s27, 0x20000\n"
"v_lshrrev_b32 v208, 4, v255\n"
"s_waitcnt lgkmcnt(8)\n"
"v_mfma_f32_16x16x32_bf16 v[48:51], a[220:223], v[144:147], v[48:51]\n"
"v_cvt_pk_bf16_f32 v140, v76, v77\n"
"v_cvt_pk_bf16_f32 v141, v78, v79\n"
"v_and_b32 v209, 15, v255\n"
"s_lshl_b32 s13, s8, 5\n"
"v_add_u32 v204, s13, v208\n"
"v_lshlrev_b32 v204, 10, v204\n"
"v_mfma_f32_16x16x32_bf16 v[48:51], a[188:191], v[128:131], v[48:51]\n"
"v_cvt_pk_bf16_f32 v142, v92, v93\n"
"v_cvt_pk_bf16_f32 v143, v94, v95\n"
"s_lshl_b32 s13, s64, 8\n"
"v_add_u32 v204, s13, v204\n"
"v_lshlrev_b32 v207, 1, v208\n"
"v_xor_b32 v206, v209, v207\n"
"s_waitcnt lgkmcnt(6)\n"
"v_mfma_f32_16x16x32_bf16 a[0:3], a[192:195], v[128:131], a[0:3]\n"
"v_cvt_pk_bf16_f32 v156, v108, v109\n"
"v_cvt_pk_bf16_f32 v157, v110, v111\n"
"v_lshl_add_u32 v216, v206, 4, v204\n"
"v_xor_b32 v206, 8, v206\n"
"v_lshl_add_u32 v217, v206, 4, v204\n"
"v_add_u32 v217, 0xc00, v217\n"
"v_mfma_f32_16x16x32_bf16 a[0:3], a[224:227], v[144:147], a[0:3]\n"
"v_cvt_pk_bf16_f32 v158, v124, v125\n"
"v_cvt_pk_bf16_f32 v159, v126, v127\n"
"v_add_u32 v218, 0x1800, v216\n"
"v_add_u32 v219, 0x1800, v217\n"
"s_lshl_b32 s67, s8, 13\n"
"s_mul_i32 s65, s62, 0x58000\n"
"s_waitcnt lgkmcnt(4)\n"
"v_mfma_f32_16x16x32_bf16 a[16:19], a[228:231], v[144:147], a[16:19]\n"
"s_lshl_b32 s66, s62, 20\n"
"s_lshl_b32 s13, s63, 2\n"
"s_add_u32 s13, s13, s8\n"
"s_lshl_b32 s14, s12, 4\n"
"v_mfma_f32_16x16x32_bf16 a[16:19], a[196:199], v[128:131], a[16:19]\n"
"s_sub_u32 s14, 0x78, s14\n"
"s_add_u32 s14, s14, s13\n"
"s_lshl_b32 s13, s13, 9\n"
"s_lshl_b32 s14, s14, 9\n"
"s_waitcnt lgkmcnt(2)\n"
"v_mfma_f32_16x16x32_bf16 a[32:35], a[200:203], v[128:131], a[32:35]\n"
"v_add_u32 v212, s13, v247\n"
"v_add_u32 v213, s14, v247\n"
"v_lshlrev_b32 v214, 1, v212\n"
"v_lshlrev_b32 v215, 1, v213\n"
"v_mfma_f32_16x16x32_bf16 a[32:35], a[232:235], v[144:147], a[32:35]\n"
"s_mul_i32 s68, s62, 22\n"
"s_add_u32 s68, s68, 0x100\n"
"s_lshl_b32 s68, s68, 16\n"
"s_lshl_b32 s69, s62, 22\n"
"s_waitcnt lgkmcnt(0)\n"
"v_mfma_f32_16x16x32_bf16 a[48:51], a[236:239], v[144:147], a[48:51]\n"
"s_mov_b32 s70, s69\n"
"s_lshl_b32 s13, s13, 5\n"
"s_lshl_b32 s14, s14, 5\n"
"s_sub_u32 s80, s14, s13\n"
"v_mfma_f32_16x16x32_bf16 a[48:51], a[204:207], v[128:131], a[48:51]\n"
"s_lshl_b32 s14, s64, 8\n"
"s_add_u32 s13, s13, s14\n"
"s_lshl_b32 s14, s62, 6\n"
"s_add_u32 s13, s13, s14\n"
"v_mfma_f32_16x16x32_bf16 a[128:131], v[200:203], v[128:131], a[128:131]\n"
"v_lshlrev_b32 v204, 10, v209\n"
"v_lshl_add_u32 v204, v208, 3, v204\n"
"v_add_u32 v234, s13, v204\n"
"s_lshl_b32 s12, s8, 14\n"
"v_mfma_f32_16x16x32_bf16 a[128:131], v[200:203], v[144:147], a[128:131]\n"
"v_add_u32 v244, s12, v254\n"
"v_mfma_f32_16x16x32_bf16 v[4:7], a[176:179], v[132:135], v[4:7]\n"
"buffer_load_dwordx2 v[230:231], v228, s[92:95], s71 offen sc1\n"
"v_mfma_f32_16x16x32_bf16 v[4:7], a[208:211], v[148:151], v[4:7]\n"
"s_add_u32 s82, s65, 0\n"
"s_add_u32 s83, s82, 0x4000\n"
"s_add_u32 m0, s67, 0x18000\n"
"v_mfma_f32_16x16x32_bf16 v[20:23], a[212:215], v[148:151], v[20:23]\n"
"buffer_load_dwordx4 v216, s[24:27], s82 offen lds\n"
"v_mfma_f32_16x16x32_bf16 v[20:23], a[180:183], v[132:135], v[20:23]\n"
"v_mfma_f32_16x16x32_bf16 v[36:39], a[184:187], v[132:135], v[36:39]\n"
"buffer_load_dwordx4 v217, s[24:27], s82 offen offset:1024 lds\n"
"ds_write_b128 v244, a[0:3]\n"
"v_mfma_f32_16x16x32_bf16 v[36:39], a[216:219], v[148:151], v[36:39]\n"
"v_mfma_f32_16x16x32_bf16 v[52:55], a[220:223], v[148:151], v[52:55]\n"
"v_mfma_f32_16x16x32_bf16 v[52:55], a[188:191], v[132:135], v[52:55]\n"
"buffer_load_dwordx4 v218, s[24:27], s82 offen offset:2048 lds\n"
"v_mfma_f32_16x16x32_bf16 a[4:7], a[192:195], v[132:135], a[4:7]\n"
"ds_write_b128 v244, a[16:19] offset:4096\n"
"v_mfma_f32_16x16x32_bf16 a[4:7], a[224:227], v[148:151], a[4:7]\n"
"buffer_load_dwordx4 v219, s[24:27], s82 offen offset:3072 lds\n"
"v_mfma_f32_16x16x32_bf16 a[20:23], a[228:231], v[148:151], a[20:23]\n"
"v_mfma_f32_16x16x32_bf16 a[20:23], a[196:199], v[132:135], a[20:23]\n"
"s_add_u32 m0, s67, 0x19000\n"
"v_mfma_f32_16x16x32_bf16 a[36:39], a[200:203], v[132:135], a[36:39]\n"
"buffer_load_dwordx4 v216, s[24:27], s83 offen lds\n"
"ds_write_b128 v244, a[32:35] offset:8192\n"
"v_mfma_f32_16x16x32_bf16 a[36:39], a[232:235], v[148:151], a[36:39]\n"
"v_mfma_f32_16x16x32_bf16 a[52:55], a[236:239], v[148:151], a[52:55]\n"
"buffer_load_dwordx4 v217, s[24:27], s83 offen offset:1024 lds\n"
"v_mfma_f32_16x16x32_bf16 a[52:55], a[204:207], v[132:135], a[52:55]\n"
"v_mfma_f32_16x16x32_bf16 a[132:135], v[200:203], v[132:135], a[132:135]\n"
"ds_write_b128 v244, a[48:51] offset:12288\n"
"v_mfma_f32_16x16x32_bf16 a[132:135], v[200:203], v[148:151], a[132:135]\n"
"buffer_load_dwordx4 v218, s[24:27], s83 offen offset:2048 lds\n"
"v_mfma_f32_16x16x32_bf16 v[8:11], a[176:179], v[136:139], v[8:11]\n"
"v_mfma_f32_16x16x32_bf16 v[8:11], a[208:211], v[152:155], v[8:11]\n"
"buffer_load_dwordx4 v219, s[24:27], s83 offen offset:3072 lds\n"
"v_mfma_f32_16x16x32_bf16 v[24:27], a[212:215], v[152:155], v[24:27]\n"
"v_mfma_f32_16x16x32_bf16 v[24:27], a[180:183], v[136:139], v[24:27]\n"
"v_mfma_f32_16x16x32_bf16 v[40:43], a[184:187], v[136:139], v[40:43]\n"
"ds_write_b128 v244, a[4:7] offset:1024\n"
"v_mfma_f32_16x16x32_bf16 v[40:43], a[216:219], v[152:155], v[40:43]\n"
"s_waitcnt vmcnt(8)\n"
"v_cmp_eq_u64 vcc, v[230:231], v[250:251]\n"
"s_nop 1\n"
"s_cmp_eq_u64 vcc, exec\n"
"s_cbranch_scc1 .Lpok_46_%=\n"
"s_mov_b32 s15, 0\n"
".Lploop_47_%=:\n"
"s_sleep 4\n"
"buffer_load_dwordx2 v[230:231], v228, s[92:95], s71 offen sc1\n"
"s_waitcnt vmcnt(0)\n"
"v_cmp_eq_u64 vcc, v[230:231], v[250:251]\n"
"s_add_u32 s15, s15, 1\n"
"s_nop 0\n"
"s_cmp_eq_u64 vcc, exec\n"
"s_cbranch_scc1 .Lpok_46_%=\n"
"s_cmp_lt_u32 s15, 0x20000\n"
"s_cbranch_scc1 .Lploop_47_%=\n"
"s_mov_b32 s15, 0x80\n"
"s_mov_b64 exec, 1\n"
"buffer_store_dwordx2 v[250:251], v249, s[92:95], s15 offen sc1\n"
"s_mov_b64 exec, -1\n"
".Lpok_46_%=:\n"
"v_mfma_f32_16x16x32_bf16 v[56:59], a[220:223], v[152:155], v[56:59]\n"
"v_mfma_f32_16x16x32_bf16 v[56:59], a[188:191], v[136:139], v[56:59]\n"
"buffer_load_dwordx2 a[64:65], v212, s[36:39], s68 offen sc1\n"
"v_mfma_f32_16x16x32_bf16 a[8:11], a[192:195], v[136:139], a[8:11]\n"
"ds_write_b128 v244, a[20:23] offset:5120\n"
"v_mfma_f32_16x16x32_bf16 a[8:11], a[224:227], v[152:155], a[8:11]\n"
"buffer_load_dwordx2 a[68:69], v213, s[36:39], s68 offen sc1\n"
"s_add_u32 s68, s68, 0x10000\n"
"v_mfma_f32_16x16x32_bf16 a[24:27], a[228:231], v[152:155], a[24:27]\n"
"v_mfma_f32_16x16x32_bf16 a[24:27], a[196:199], v[136:139], a[24:27]\n"
"buffer_load_dwordx2 a[66:67], v212, s[36:39], s68 offen sc1\n"
"v_mfma_f32_16x16x32_bf16 a[40:43], a[200:203], v[136:139], a[40:43]\n"
"ds_write_b128 v244, a[36:39] offset:9216\n"
"v_mfma_f32_16x16x32_bf16 a[40:43], a[232:235], v[152:155], a[40:43]\n"
"buffer_load_dwordx2 a[70:71], v213, s[36:39], s68 offen sc1\n"
"s_add_u32 s68, s68, 0x10000\n"
"v_mfma_f32_16x16x32_bf16 a[56:59], a[236:239], v[152:155], a[56:59]\n"
"v_mfma_f32_16x16x32_bf16 a[56:59], a[204:207], v[136:139], a[56:59]\n"
"buffer_load_dwordx2 a[72:73], v212, s[36:39], s68 offen sc1\n"
"v_mfma_f32_16x16x32_bf16 a[136:139], v[200:203], v[136:139], a[136:139]\n"
"ds_write_b128 v244, a[52:55] offset:13312\n"
"v_mfma_f32_16x16x32_bf16 a[136:139], v[200:203], v[152:155], a[136:139]\n"
"buffer_load_dwordx2 a[76:77], v213, s[36:39], s68 offen sc1\n"
"s_add_u32 s68, s68, 0x10000\n"
"v_mfma_f32_16x16x32_bf16 v[12:15], a[176:179], v[140:143], v[12:15]\n"
"v_mfma_f32_16x16x32_bf16 v[12:15], a[208:211], v[156:159], v[12:15]\n"
"buffer_load_dwordx2 a[74:75], v212, s[36:39], s68 offen sc1\n"
"v_mfma_f32_16x16x32_bf16 v[28:31], a[212:215], v[156:159], v[28:31]\n"
"v_mfma_f32_16x16x32_bf16 v[28:31], a[180:183], v[140:143], v[28:31]\n"
"buffer_load_dwordx2 a[78:79], v213, s[36:39], s68 offen sc1\n"
"s_add_u32 s68, s68, 0x10000\n"
"v_mfma_f32_16x16x32_bf16 v[44:47], a[184:187], v[140:143], v[44:47]\n"
"ds_write_b128 v244, a[8:11] offset:2048\n"
"v_mfma_f32_16x16x32_bf16 v[44:47], a[216:219], v[156:159], v[44:47]\n"
"buffer_load_dwordx2 a[80:81], v212, s[36:39], s68 offen sc1\n"
"v_mfma_f32_16x16x32_bf16 v[60:63], a[220:223], v[156:159], v[60:63]\n"
"v_mfma_f32_16x16x32_bf16 v[60:63], a[188:191], v[140:143], v[60:63]\n"
"buffer_load_dwordx2 a[84:85], v213, s[36:39], s68 offen sc1\n"
"s_add_u32 s68, s68, 0x10000\n"
"v_mfma_f32_16x16x32_bf16 a[12:15], a[192:195], v[140:143], a[12:15]\n"
"ds_write_b128 v244, a[24:27] offset:6144\n"
"v_mfma_f32_16x16x32_bf16 a[12:15], a[224:227], v[156:159], a[12:15]\n"
"buffer_load_dwordx2 a[82:83], v212, s[36:39], s68 offen sc1\n"
"v_mfma_f32_16x16x32_bf16 a[28:31], a[228:231], v[156:159], a[28:31]\n"
"v_mfma_f32_16x16x32_bf16 a[28:31], a[196:199], v[140:143], a[28:31]\n"
"buffer_load_dwordx2 a[86:87], v213, s[36:39], s68 offen sc1\n"
"s_add_u32 s68, s68, 0x10000\n"
"v_mfma_f32_16x16x32_bf16 a[44:47], a[200:203], v[140:143], a[44:47]\n"
"ds_write_b128 v244, a[40:43] offset:10240\n"
"v_mfma_f32_16x16x32_bf16 a[44:47], a[232:235], v[156:159], a[44:47]\n"
"buffer_load_dwordx2 a[88:89], v212, s[36:39], s68 offen sc1\n"
"v_mfma_f32_16x16x32_bf16 a[60:63], a[236:239], v[156:159], a[60:63]\n"
"v_mfma_f32_16x16x32_bf16 a[60:63], a[204:207], v[140:143], a[60:63]\n"
"buffer_load_dwordx2 a[92:93], v213, s[36:39], s68 offen sc1\n"
"s_add_u32 s68, s68, 0x10000\n"
"v_mfma_f32_16x16x32_bf16 a[140:143], v[200:203], v[140:143], a[140:143]\n"
"ds_write_b128 v244, a[56:59] offset:14336\n"
"v_mfma_f32_16x16x32_bf16 a[140:143], v[200:203], v[156:159], a[140:143]\n"
"buffer_load_dwordx2 a[90:91], v212, s[36:39], s68 offen sc1\n"
"buffer_load_dwordx2 a[94:95], v213, s[36:39], s68 offen sc1\n"
"s_add_u32 s68, s68, 0x10000\n"
".Lmerge_38_%=:\n"
"s_lshl_b32 s12, s8, 14\n"
"s_sub_u32 s13, 0xc000, s12\n"
"v_add_u32 v235, s13, v254\n"
"s_lshl_b32 s12, s8, 10\n"
"s_add_u32 s13, s12, 0x20000\n"
"v_add_u32 v242, s13, v254\n"
"s_sub_u32 s13, 0x20c00, s12\n"
"v_add_u32 v243, s13, v254\n"
"ds_write_b128 v244, a[12:15] offset:3072\n"
"ds_write_b128 v244, a[28:31] offset:7168\n"
"ds_write_b128 v244, a[44:47] offset:11264\n"
"ds_write_b128 v244, a[60:63] offset:15360\n"
"v_accvgpr_read_b32 v128, a128\n"
"v_accvgpr_read_b32 v129, a132\n"
"v_accvgpr_read_b32 v130, a136\n"
"v_accvgpr_read_b32 v131, a140\n"
"ds_write_b128 v242, v[128:131]\n"
"buffer_load_dwordx2 v[232:233], v229, s[92:95], s81 offen sc1\n"
"s_lshl_b32 s88, s10, 19\n"
"s_lshl_b32 s12, s77, 10\n"
"s_add_u32 s88, s88, s12\n"
"s_lshr_b32 s12, s8, 1\n"
"s_xor_b32 s12, s12, 1\n"
"s_lshl_b32 s12, s12, 18\n"
"s_add_u32 s88, s88, s12\n"
"v_mov_b32 v208, 0x21000\n"
"s_waitcnt vmcnt(0) lgkmcnt(0)\n"
"s_barrier\n"
"ds_read_b128 v[132:135], v243\n"
"ds_read_b128 v[64:67], v235\n"
"ds_read_b128 v[68:71], v235 offset:1024\n"
"ds_read_b128 v[72:75], v235 offset:2048\n"
"ds_read_b128 v[76:79], v235 offset:3072\n"
"ds_read_b128 v[80:83], v235 offset:4096\n"
"ds_read_b128 v[84:87], v235 offset:5120\n"
"ds_read_b128 v[88:91], v235 offset:6144\n"
"ds_read_b128 v[92:95], v235 offset:7168\n"
"ds_read_b128 v[96:99], v235 offset:8192\n"
"ds_read_b128 v[100:103], v235 offset:9216\n"
"ds_read_b128 v[104:107], v235 offset:10240\n"
"ds_read_b128 v[108:111], v235 offset:11264\n"
"ds_read_b128 v[112:115], v235 offset:12288\n"
"ds_read_b128 v[116:119], v235 offset:13312\n"
"s_waitcnt lgkmcnt(14)\n"
"v_add_f32 v237, v128, v132\n"
"v_add_f32 v239, v129, v133\n"
"v_add_f32 v241, v130, v134\n"
"v_add_f32 v243, v131, v135\n"
"ds_read_b128 v[120:123], v235 offset:14336\n"
"ds_read_b128 v[124:127], v235 offset:15360\n"
"v_rcp_f32 v236, v237\n"
"v_rcp_f32 v238, v239\n"
"v_rcp_f32 v240, v241\n"
"v_rcp_f32 v242, v243\n"
"s_waitcnt lgkmcnt(8)\n"
"v_pk_add_f32 v[0:1], v[64:65], v[0:1]\n"
"v_pk_add_f32 v[2:3], v[66:67], v[2:3]\n"
"v_pk_mul_f32 v[0:1], v[0:1], v[236:237] op_sel_hi:[1,0]\n"
"v_pk_mul_f32 v[2:3], v[2:3], v[236:237] op_sel_hi:[1,0]\n"
"v_pk_add_f32 v[16:17], v[80:81], v[16:17]\n"
"v_pk_add_f32 v[18:19], v[82:83], v[18:19]\n"
"v_pk_mul_f32 v[16:17], v[16:17], v[236:237] op_sel_hi:[1,0]\n"
"v_pk_mul_f32 v[18:19], v[18:19], v[236:237] op_sel_hi:[1,0]\n"
"v_cvt_pk_bf16_f32 v64, v0, v1\n"
"v_cvt_pk_bf16_f32 v65, v2, v3\n"
"v_cvt_pk_bf16_f32 v66, v16, v17\n"
"v_cvt_pk_bf16_f32 v67, v18, v19\n"
"buffer_store_dwordx4 v[64:67], v254, s[36:39], s88 offen sc1\n"
"v_pk_add_f32 v[4:5], v[68:69], v[4:5]\n"
"v_pk_add_f32 v[6:7], v[70:71], v[6:7]\n"
"v_pk_mul_f32 v[4:5], v[4:5], v[238:239] op_sel_hi:[1,0]\n"
"v_pk_mul_f32 v[6:7], v[6:7], v[238:239] op_sel_hi:[1,0]\n"
"v_pk_add_f32 v[20:21], v[84:85], v[20:21]\n"
"v_pk_add_f32 v[22:23], v[86:87], v[22:23]\n"
"v_pk_mul_f32 v[20:21], v[20:21], v[238:239] op_sel_hi:[1,0]\n"
"v_pk_mul_f32 v[22:23], v[22:23], v[238:239] op_sel_hi:[1,0]\n"
"v_cvt_pk_bf16_f32 v68, v4, v5\n"
"v_cvt_pk_bf16_f32 v69, v6, v7\n"
"v_cvt_pk_bf16_f32 v70, v20, v21\n"
"v_cvt_pk_bf16_f32 v71, v22, v23\n"
"buffer_store_dwordx4 v[68:71], v254, s[36:39], s88 offen offset:1024 sc1\n"
"v_pk_add_f32 v[8:9], v[72:73], v[8:9]\n"
"v_pk_add_f32 v[10:11], v[74:75], v[10:11]\n"
"v_pk_mul_f32 v[8:9], v[8:9], v[240:241] op_sel_hi:[1,0]\n"
"v_pk_mul_f32 v[10:11], v[10:11], v[240:241] op_sel_hi:[1,0]\n"
"v_pk_add_f32 v[24:25], v[88:89], v[24:25]\n"
"v_pk_add_f32 v[26:27], v[90:91], v[26:27]\n"
"v_pk_mul_f32 v[24:25], v[24:25], v[240:241] op_sel_hi:[1,0]\n"
"v_pk_mul_f32 v[26:27], v[26:27], v[240:241] op_sel_hi:[1,0]\n"
"v_cvt_pk_bf16_f32 v72, v8, v9\n"
"v_cvt_pk_bf16_f32 v73, v10, v11\n"
"v_cvt_pk_bf16_f32 v74, v24, v25\n"
"v_cvt_pk_bf16_f32 v75, v26, v27\n"
"buffer_store_dwordx4 v[72:75], v254, s[36:39], s88 offen offset:2048 sc1\n"
"v_pk_add_f32 v[12:13], v[76:77], v[12:13]\n"
"v_pk_add_f32 v[14:15], v[78:79], v[14:15]\n"
"v_pk_mul_f32 v[12:13], v[12:13], v[242:243] op_sel_hi:[1,0]\n"
"v_pk_mul_f32 v[14:15], v[14:15], v[242:243] op_sel_hi:[1,0]\n"
"v_pk_add_f32 v[28:29], v[92:93], v[28:29]\n"
"v_pk_add_f32 v[30:31], v[94:95], v[30:31]\n"
"v_pk_mul_f32 v[28:29], v[28:29], v[242:243] op_sel_hi:[1,0]\n"
"v_pk_mul_f32 v[30:31], v[30:31], v[242:243] op_sel_hi:[1,0]\n"
"v_cvt_pk_bf16_f32 v76, v12, v13\n"
"v_cvt_pk_bf16_f32 v77, v14, v15\n"
"v_cvt_pk_bf16_f32 v78, v28, v29\n"
"v_cvt_pk_bf16_f32 v79, v30, v31\n"
"buffer_store_dwordx4 v[76:79], v254, s[36:39], s88 offen offset:3072 sc1\n"
"s_add_u32 s88, s88, 0x20000\n"
"s_waitcnt lgkmcnt(0)\n"
"v_pk_add_f32 v[32:33], v[96:97], v[32:33]\n"
"v_pk_add_f32 v[34:35], v[98:99], v[34:35]\n"
"v_pk_mul_f32 v[32:33], v[32:33], v[236:237] op_sel_hi:[1,0]\n"
"v_pk_mul_f32 v[34:35], v[34:35], v[236:237] op_sel_hi:[1,0]\n"
"v_pk_add_f32 v[48:49], v[112:113], v[48:49]\n"
"v_pk_add_f32 v[50:51], v[114:115], v[50:51]\n"
"v_pk_mul_f32 v[48:49], v[48:49], v[236:237] op_sel_hi:[1,0]\n"
"v_pk_mul_f32 v[50:51], v[50:51], v[236:237] op_sel_hi:[1,0]\n"
"v_cvt_pk_bf16_f32 v96, v32, v33\n"
"v_cvt_pk_bf16_f32 v97, v34, v35\n"
"v_cvt_pk_bf16_f32 v98, v48, v49\n"
"v_cvt_pk_bf16_f32 v99, v50, v51\n"
"buffer_store_dwordx4 v[96:99], v254, s[36:39], s88 offen sc1\n"
"v_pk_add_f32 v[36:37], v[100:101], v[36:37]\n"
"v_pk_add_f32 v[38:39], v[102:103], v[38:39]\n"
"v_pk_mul_f32 v[36:37], v[36:37], v[238:239] op_sel_hi:[1,0]\n"
"v_pk_mul_f32 v[38:39], v[38:39], v[238:239] op_sel_hi:[1,0]\n"
"v_pk_add_f32 v[52:53], v[116:117], v[52:53]\n"
"v_pk_add_f32 v[54:55], v[118:119], v[54:55]\n"
"v_pk_mul_f32 v[52:53], v[52:53], v[238:239] op_sel_hi:[1,0]\n"
"v_pk_mul_f32 v[54:55], v[54:55], v[238:239] op_sel_hi:[1,0]\n"
"v_cvt_pk_bf16_f32 v100, v36, v37\n"
"v_cvt_pk_bf16_f32 v101, v38, v39\n"
"v_cvt_pk_bf16_f32 v102, v52, v53\n"
"v_cvt_pk_bf16_f32 v103, v54, v55\n"
"buffer_store_dwordx4 v[100:103], v254, s[36:39], s88 offen offset:1024 sc1\n"
"v_pk_add_f32 v[40:41], v[104:105], v[40:41]\n"
"v_pk_add_f32 v[42:43], v[106:107], v[42:43]\n"
"v_pk_mul_f32 v[40:41], v[40:41], v[240:241] op_sel_hi:[1,0]\n"
"v_pk_mul_f32 v[42:43], v[42:43], v[240:241] op_sel_hi:[1,0]\n"
"v_pk_add_f32 v[56:57], v[120:121], v[56:57]\n"
"v_pk_add_f32 v[58:59], v[122:123], v[58:59]\n"
"v_pk_mul_f32 v[56:57], v[56:57], v[240:241] op_sel_hi:[1,0]\n"
"v_pk_mul_f32 v[58:59], v[58:59], v[240:241] op_sel_hi:[1,0]\n"
"v_cvt_pk_bf16_f32 v104, v40, v41\n"
"v_cvt_pk_bf16_f32 v105, v42, v43\n"
"v_cvt_pk_bf16_f32 v106, v56, v57\n"
"v_cvt_pk_bf16_f32 v107, v58, v59\n"
"buffer_store_dwordx4 v[104:107], v254, s[36:39], s88 offen offset:2048 sc1\n"
"v_pk_add_f32 v[44:45], v[108:109], v[44:45]\n"
"v_pk_add_f32 v[46:47], v[110:111], v[46:47]\n"
"v_pk_mul_f32 v[44:45], v[44:45], v[242:243] op_sel_hi:[1,0]\n"
"v_pk_mul_f32 v[46:47], v[46:47], v[242:243] op_sel_hi:[1,0]\n"
"v_pk_add_f32 v[60:61], v[124:125], v[60:61]\n"
"v_pk_add_f32 v[62:63], v[126:127], v[62:63]\n"
"v_pk_mul_f32 v[60:61], v[60:61], v[242:243] op_sel_hi:[1,0]\n"
"v_pk_mul_f32 v[62:63], v[62:63], v[242:243] op_sel_hi:[1,0]\n"
"v_cvt_pk_bf16_f32 v108, v44, v45\n"
"v_cvt_pk_bf16_f32 v109, v46, v47\n"
"v_cvt_pk_bf16_f32 v110, v60, v61\n"
"v_cvt_pk_bf16_f32 v111, v62, v63\n"
"buffer_store_dwordx4 v[108:111], v254, s[36:39], s88 offen offset:3072 sc1\n"
"s_add_u32 s88, s88, 0x20000\n"
"v_mov_b32 v246, 0x6f800000\n"
"v_mov_b32 v245, 0x17800000\n"
"v_cmp_nle_f32 vcc, v237, v246\n"
"v_cmp_nge_f32 s[14:15], v237, v245\n"
"s_or_b64 s[90:91], s[90:91], vcc\n"
"s_or_b64 s[90:91], s[90:91], s[14:15]\n"
"v_cmp_nle_f32 vcc, v239, v246\n"
"v_cmp_nge_f32 s[14:15], v239, v245\n"
"s_or_b64 s[90:91], s[90:91], vcc\n"
"s_or_b64 s[90:91], s[90:91], s[14:15]\n"
"v_cmp_nle_f32 vcc, v241, v246\n"
"v_cmp_nge_f32 s[14:15], v241, v245\n"
"s_or_b64 s[90:91], s[90:91], vcc\n"
"s_or_b64 s[90:91], s[90:91], s[14:15]\n"
"v_cmp_nle_f32 vcc, v243, v246\n"
"v_cmp_nge_f32 s[14:15], v243, v245\n"
"s_or_b64 s[90:91], s[90:91], vcc\n"
"s_or_b64 s[90:91], s[90:91], s[14:15]\n"
"s_nop 0\n"
"s_or_b32 s12, s90, s91\n"
"s_lshl_b32 s13, s8, 3\n"
"s_add_u32 s13, s13, 0x10000\n"
"v_mov_b32 v128, s12\n"
"v_mov_b32 v129, s12\n"
"v_mov_b32 v130, s13\n"
"ds_write_b64 v130, v[128:129]\n"
"v_mov_b32 v130, 0x10000\n"
"ds_read_b128 v[204:207], v208\n"
"s_waitcnt lgkmcnt(0)\n"
"v_or_b32 v204, v204, v206\n"
"s_nop 0\n"
"v_readfirstlane_b32 s12, v204\n"
"s_nop 1\n"
"s_cmp_eq_u32 s12, 0\n"
"s_cselect_b32 s12, s54, 0x40000000\n"
"s_mov_b64 exec, s[34:35]\n"
"buffer_store_dwordx2 v[250:251], v249, s[92:95], s12 offen sc1\n"
"s_mov_b64 exec, -1\n"
"s_barrier\n"
"ds_read_b128 v[132:135], v130\n"
"ds_read_b128 v[136:139], v130 offset:16\n"
"buffer_load_dwordx2 a[96:97], v212, s[36:39], s68 offen sc1\n"
"buffer_load_dwordx2 a[100:101], v213, s[36:39], s68 offen sc1\n"
"s_add_u32 s68, s68, 0x10000\n"
"buffer_load_dwordx2 a[98:99], v212, s[36:39], s68 offen sc1\n"
"buffer_load_dwordx2 a[102:103], v213, s[36:39], s68 offen sc1\n"
"s_add_u32 s68, s68, 0x10000\n"
"buffer_load_dwordx2 a[104:105], v212, s[36:39], s68 offen sc1\n"
"buffer_load_dwordx2 a[108:109], v213, s[36:39], s68 offen sc1\n"
"s_add_u32 s68, s68, 0x10000\n"
"buffer_load_dwordx2 a[106:107], v212, s[36:39], s68 offen sc1\n"
"buffer_load_dwordx2 a[110:111], v213, s[36:39], s68 offen sc1\n"
"s_add_u32 s68, s68, 0x10000\n"
"s_sub_u32 s20, s50, 0x18000\n"
"s_subb_u32 s21, s51, 0\n"
"s_and_b32 s21, s21, 0xffff\n"
"s_mov_b32 s22, 0x418000\n"
"s_mov_b32 s23, 0x20000\n"
"v_lshrrev_b32 v208, 4, v255\n"
"v_bfe_u32 v209, v255, 2, 2\n"
"v_and_b32 v210, 3, v255\n"
"v_lshl_add_u32 v204, v208, 2, v209\n"
"v_lshlrev_b32 v205, 8, v204\n"
"v_lshl_add_u32 v205, v210, 3, v205\n"
"v_add_u32 v205, 0x18000, v205\n"
"v_and_b32 v206, 7, v204\n"
"s_lshl_b32 s12, s62, 1\n"
"s_xor_b32 s12, s12, 6\n"
"v_xor_b32 v206, s12, v206\n"
"v_xor_b32 v207, 0, v206\n"
"v_lshl_add_u32 v220, v207, 5, v205\n"
"v_xor_b32 v207, 1, v206\n"
"v_lshl_add_u32 v221, v207, 5, v205\n"
"v_xor_b32 v207, 2, v206\n"
"v_lshl_add_u32 v222, v207, 5, v205\n"
"v_xor_b32 v207, 3, v206\n"
"v_lshl_add_u32 v223, v207, 5, v205\n"
"v_xor_b32 v207, 4, v206\n"
"v_lshl_add_u32 v224, v207, 5, v205\n"
"v_xor_b32 v207, 5, v206\n"
"v_lshl_add_u32 v225, v207, 5, v205\n"
"v_xor_b32 v207, 6, v206\n"
"v_lshl_add_u32 v226, v207, 5, v205\n"
"v_xor_b32 v207, 7, v206\n"
"v_lshl_add_u32 v227, v207, 5, v205\n"
"s_lshr_b32 s12, s9, 5\n"
"s_lshl_b32 s12, s12, 4\n"
"s_lshl_b32 s13, s8, 2\n"
"s_add_u32 s12, s12, s13\n"
"v_add_u32 v204, s12, v210\n"
"v_lshlrev_b32 v204, 10, v204\n"
"v_bfe_u32 v205, v255, 2, 1\n"
"v_lshl_add_u32 v204, v205, 7, v204\n"
"s_lshl_b32 s12, s64, 8\n"
"v_add_u32 v200, s12, v204\n"
"v_lshrrev_b32 v204, 3, v255\n"
"v_lshlrev_b32 v204, 19, v204\n"
"v_and_b32 v205, 7, v255\n"
"v_lshl_add_u32 v204, v205, 7, v204\n"
"s_lshl_b32 s12, s64, 17\n"
"v_add_u32 v204, s12, v204\n"
"v_sub_u32 v204, v204, v254\n"
"v_add_u32 v201, v204, v214\n"
"v_add_u32 v202, v204, v215\n"
"s_lshl_b32 s89, s62, 22\n"
"s_add_u32 s28, s60, 0x3c00000\n"
"s_addc_u32 s29, s61, 0\n"
"s_and_b32 s29, s29, 0xffff\n"
"s_mov_b32 s30, 0x800000\n"
"s_mov_b32 s31, 0x20000\n"
"s_lshl_b32 s73, s11, 5\n"
"s_add_u32 s73, s73, s10\n"
"s_lshl_b32 s73, s73, 2\n"
"s_add_u32 s73, s73, s8\n"
"s_lshl_b32 s73, s73, 3\n"
"s_add_u32 s73, s73, 0x4000\n"
"s_waitcnt lgkmcnt(0)\n"
"v_or3_b32 v132, v132, v134, v136\n"
"v_or_b32 v132, v132, v138\n"
"s_nop 0\n"
"v_readfirstlane_b32 s12, v132\n"
"s_nop 1\n"
"s_cmp_lg_u32 s12, 0\n"
"s_cbranch_scc1 .Lslow_48_%=\n"
".Lback_49_%=:\n"
"ds_read_b64_tr_b16 v[64:65], v220\n"
"ds_read_b64_tr_b16 v[66:67], v220 offset:4096\n"
"ds_read_b64_tr_b16 v[68:69], v221\n"
"ds_read_b64_tr_b16 v[70:71], v221 offset:4096\n"
"ds_read_b64_tr_b16 v[72:73], v222\n"
"ds_read_b64_tr_b16 v[74:75], v222 offset:4096\n"
"ds_read_b64_tr_b16 v[76:77], v223\n"
"ds_read_b64_tr_b16 v[78:79], v223 offset:4096\n"
"ds_read_b64_tr_b16 v[80:81], v224\n"
"ds_read_b64_tr_b16 v[82:83], v224 offset:4096\n"
"ds_read_b64_tr_b16 v[84:85], v225\n"
"ds_read_b64_tr_b16 v[86:87], v225 offset:4096\n"
"ds_read_b64_tr_b16 v[88:89], v226\n"
"ds_read_b64_tr_b16 v[90:91], v226 offset:4096\n"
"ds_read_b64_tr_b16 v[92:93], v227\n"
"ds_read_b64_tr_b16 v[94:95], v227 offset:4096\n"
"ds_read_b64_tr_b16 v[96:97], v220 offset:8192\n"
"ds_read_b64_tr_b16 v[98:99], v220 offset:12288\n"
"ds_read_b64_tr_b16 v[100:101], v221 offset:8192\n"
"ds_read_b64_tr_b16 v[102:103], v221 offset:12288\n"
"ds_read_b64_tr_b16 v[104:105], v222 offset:8192\n"
"ds_read_b64_tr_b16 v[106:107], v222 offset:12288\n"
"ds_read_b64_tr_b16 v[108:109], v223 offset:8192\n"
"ds_read_b64_tr_b16 v[110:111], v223 offset:12288\n"
"ds_read_b64_tr_b16 v[112:113], v224 offset:8192\n"
"ds_read_b64_tr_b16 v[114:115], v224 offset:12288\n"
"ds_read_b64_tr_b16 v[116:117], v225 offset:8192\n"
"ds_read_b64_tr_b16 v[118:119], v225 offset:12288\n"
"ds_read_b64_tr_b16 v[120:121], v226 offset:8192\n"
"ds_read_b64_tr_b16 v[122:123], v226 offset:12288\n"
"ds_read_b64_tr_b16 v[124:125], v227 offset:8192\n"
"ds_read_b64_tr_b16 v[126:127], v227 offset:12288\n"
"s_mov_b32 s76, s40\n"
"s_and_b32 s77, s41, 0xffff\n"
"s_mov_b32 s78, 0x200000\n"
"s_mov_b32 s79, 0x20000\n"
"buffer_load_dwordx2 v[232:233], v229, s[92:95], s81 offen sc1\n"
"s_waitcnt vmcnt(27) lgkmcnt(0)\n"
"v_mfma_f32_16x16x32_bf16 v[0:3], v[64:67], a[64:67], 0\n"
"s_add_u32 s82, s65, 0x20000\n"
"s_add_u32 s83, s82, 0x4000\n"
"s_add_u32 m0, s67, 0x20000\n"
"v_add_u32 v236, 0x8000, v220\n"
"v_add_u32 v237, 0x8000, v221\n"
"v_mfma_f32_16x16x32_bf16 v[0:3], v[96:99], a[72:75], v[0:3]\n"
"buffer_load_dwordx4 v216, s[24:27], s82 offen lds\n"
"v_add_u32 v244, 0xfffe8000, v226\n"
"v_add_u32 v196, 0x10000, v244\n"
"v_mfma_f32_16x16x32_bf16 v[4:7], v[96:99], a[76:79], 0\n"
"v_add_u32 v198, 0x20000, v244\n"
"v_add_u32 v245, 0xfffe8000, v227\n"
"v_mfma_f32_16x16x32_bf16 v[4:7], v[64:67], a[68:71], v[4:7]\n"
"v_add_u32 v197, 0x10000, v245\n"
"v_add_u32 v199, 0x20000, v245\n"
"v_mfma_f32_16x16x32_bf16 v[8:11], v[68:71], a[64:67], 0\n"
"buffer_load_dwordx4 v217, s[24:27], s82 offen offset:1024 lds\n"
"v_add_u32 v238, 0x8000, v222\n"
"v_add_u32 v239, 0x8000, v223\n"
"v_mfma_f32_16x16x32_bf16 v[8:11], v[100:103], a[72:75], v[8:11]\n"
"v_add_u32 v240, 0x8000, v224\n"
"v_add_u32 v241, 0x8000, v225\n"
"v_mfma_f32_16x16x32_bf16 v[12:15], v[100:103], a[76:79], 0\n"
"v_add_u32 v242, 0x8000, v226\n"
"v_add_u32 v243, 0x8000, v227\n"
"v_mfma_f32_16x16x32_bf16 v[12:15], v[68:71], a[68:71], v[12:15]\n"
"v_mfma_f32_16x16x32_bf16 v[16:19], v[72:75], a[64:67], 0\n"
"buffer_load_dwordx4 v218, s[24:27], s82 offen offset:2048 lds\n"
"v_mfma_f32_16x16x32_bf16 v[16:19], v[104:107], a[72:75], v[16:19]\n"
"v_mfma_f32_16x16x32_bf16 v[20:23], v[104:107], a[76:79], 0\n"
"ds_read_b64_tr_b16 v[64:65], v220 offset:16384\n"
"v_mfma_f32_16x16x32_bf16 v[20:23], v[72:75], a[68:71], v[20:23]\n"
"ds_read_b64_tr_b16 v[66:67], v220 offset:20480\n"
"ds_read_b64_tr_b16 v[96:97], v220 offset:24576\n"
"buffer_load_dwordx4 v219, s[24:27], s82 offen offset:3072 lds\n"
"v_mfma_f32_16x16x32_bf16 v[24:27], v[76:79], a[64:67], 0\n"
"ds_read_b64_tr_b16 v[98:99], v220 offset:28672\n"
"v_mfma_f32_16x16x32_bf16 v[24:27], v[108:111], a[72:75], v[24:27]\n"
"v_mfma_f32_16x16x32_bf16 v[28:31], v[108:111], a[76:79], 0\n"
"ds_read_b64_tr_b16 v[68:69], v221 offset:16384\n"
"v_mfma_f32_16x16x32_bf16 v[28:31], v[76:79], a[68:71], v[28:31]\n"
"ds_read_b64_tr_b16 v[70:71], v221 offset:20480\n"
"ds_read_b64_tr_b16 v[100:101], v221 offset:24576\n"
"s_add_u32 m0, s67, 0x21000\n"
"s_waitcnt vmcnt(4)\n"
"v_cmp_eq_u64 vcc, v[232:233], v[250:251]\n"
"s_nop 1\n"
"s_cmp_eq_u64 vcc, exec\n"
"s_cbranch_scc1 .Lpok_51_%=\n"
"s_mov_b32 s15, 0\n"
".Lploop_52_%=:\n"
"s_sleep 4\n"
"buffer_load_dwordx2 v[232:233], v229, s[92:95], s81 offen sc1\n"
"s_waitcnt vmcnt(0)\n"
"v_cmp_eq_u64 vcc, v[232:233], v[250:251]\n"
"s_add_u32 s15, s15, 1\n"
"s_nop 0\n"
"s_cmp_eq_u64 vcc, exec\n"
"s_cbranch_scc1 .Lpok_51_%=\n"
"s_cmp_lt_u32 s15, 0x20000\n"
"s_cbranch_scc1 .Lploop_52_%=\n"
"s_mov_b32 s15, 0x80\n"
"s_mov_b64 exec, 1\n"
"buffer_store_dwordx2 v[250:251], v249, s[92:95], s15 offen sc1\n"
"s_mov_b64 exec, -1\n"
".Lpok_51_%=:\n"
"v_mfma_f32_16x16x32_bf16 v[32:35], v[80:83], a[64:67], 0\n"
"ds_read_b64_tr_b16 v[102:103], v221 offset:28672\n"
"buffer_load_dwordx4 v216, s[24:27], s83 offen lds\n"
"v_mfma_f32_16x16x32_bf16 v[32:35], v[112:115], a[72:75], v[32:35]\n"
"v_mfma_f32_16x16x32_bf16 v[36:39], v[112:115], a[76:79], 0\n"
"ds_read_b64_tr_b16 v[72:73], v222 offset:16384\n"
"v_mfma_f32_16x16x32_bf16 v[36:39], v[80:83], a[68:71], v[36:39]\n"
"ds_read_b64_tr_b16 v[74:75], v222 offset:20480\n"
"ds_read_b64_tr_b16 v[104:105], v222 offset:24576\n"
"buffer_load_dwordx4 v217, s[24:27], s83 offen offset:1024 lds\n"
"v_mfma_f32_16x16x32_bf16 v[40:43], v[84:87], a[64:67], 0\n"
"ds_read_b64_tr_b16 v[106:107], v222 offset:28672\n"
"v_mfma_f32_16x16x32_bf16 v[40:43], v[116:119], a[72:75], v[40:43]\n"
"s_waitcnt vmcnt(16)\n"
"s_mov_b64 exec, 1\n"
"buffer_store_dwordx2 v[250:251], v249, s[92:95], s73 offen sc1\n"
"s_mov_b64 exec, -1\n"
"v_mfma_f32_16x16x32_bf16 v[44:47], v[116:119], a[76:79], 0\n"
"ds_read_b64_tr_b16 v[76:77], v223 offset:16384\n"
"v_mfma_f32_16x16x32_bf16 v[44:47], v[84:87], a[68:71], v[44:47]\n"
"ds_read_b64_tr_b16 v[78:79], v223 offset:20480\n"
"ds_read_b64_tr_b16 v[108:109], v223 offset:24576\n"
"buffer_load_dwordx4 v218, s[24:27], s83 offen offset:2048 lds\n"
"v_mfma_f32_16x16x32_bf16 v[48:51], v[88:91], a[64:67], 0\n"
"ds_read_b64_tr_b16 v[110:111], v223 offset:28672\n"
"v_mfma_f32_16x16x32_bf16 v[48:51], v[120:123], a[72:75], v[48:51]\n"
"v_mfma_f32_16x16x32_bf16 v[52:55], v[120:123], a[76:79], 0\n"
"ds_read_b64_tr_b16 v[80:81], v224 offset:16384\n"
"buffer_load_dwordx4 v219, s[24:27], s83 offen offset:3072 lds\n"
"v_mfma_f32_16x16x32_bf16 v[52:55], v[88:91], a[68:71], v[52:55]\n"
"ds_read_b64_tr_b16 v[82:83], v224 offset:20480\n"
"ds_read_b64_tr_b16 v[112:113], v224 offset:24576\n"
"v_mfma_f32_16x16x32_bf16 v[56:59], v[92:95], a[64:67], 0\n"
"ds_read_b64_tr_b16 v[114:115], v224 offset:28672\n"
"v_mfma_f32_16x16x32_bf16 v[56:59], v[124:127], a[72:75], v[56:59]\n"
"v_mfma_f32_16x16x32_bf16 v[60:63], v[124:127], a[76:79], 0\n"
"ds_read_b64_tr_b16 v[84:85], v225 offset:16384\n"
"buffer_load_dwordx2 a[112:113], v212, s[36:39], s68 offen sc1\n"
"v_mfma_f32_16x16x32_bf16 v[60:63], v[92:95], a[68:71], v[60:63]\n"
"ds_read_b64_tr_b16 v[86:87], v225 offset:20480\n"
"ds_read_b64_tr_b16 v[116:117], v225 offset:24576\n"
"s_waitcnt vmcnt(29) lgkmcnt(15)\n"
"v_mfma_f32_16x16x32_bf16 v[0:3], v[64:67], a[80:83], v[0:3]\n"
"ds_read_b64_tr_b16 v[118:119], v225 offset:28672\n"
"buffer_load_dwordx2 a[116:117], v213, s[36:39], s68 offen sc1\n"
"v_mfma_f32_16x16x32_bf16 v[0:3], v[96:99], a[88:91], v[0:3]\n"
"s_add_u32 s68, s68, 0x10000\n"
"v_mfma_f32_16x16x32_bf16 v[4:7], v[96:99], a[92:95], v[4:7]\n"
"ds_read_b64_tr_b16 v[88:89], v226 offset:16384\n"
"buffer_load_dwordx2 a[114:115], v212, s[36:39], s68 offen sc1\n"
"v_mfma_f32_16x16x32_bf16 v[4:7], v[64:67], a[84:87], v[4:7]\n"
"ds_read_b64_tr_b16 v[90:91], v226 offset:20480\n"
"ds_read_b64_tr_b16 v[120:121], v226 offset:24576\n"
"v_mfma_f32_16x16x32_bf16 v[8:11], v[68:71], a[80:83], v[8:11]\n"
"ds_read_b64_tr_b16 v[122:123], v226 offset:28672\n"
"buffer_load_dwordx2 a[118:119], v213, s[36:39], s68 offen sc1\n"
"v_mfma_f32_16x16x32_bf16 v[8:11], v[100:103], a[88:91], v[8:11]\n"
"s_add_u32 s68, s68, 0x10000\n"
"v_mfma_f32_16x16x32_bf16 v[12:15], v[100:103], a[92:95], v[12:15]\n"
"ds_read_b64_tr_b16 v[92:93], v227 offset:16384\n"
"buffer_load_dwordx2 a[120:121], v212, s[36:39], s68 offen sc1\n"
"v_mfma_f32_16x16x32_bf16 v[12:15], v[68:71], a[84:87], v[12:15]\n"
"ds_read_b64_tr_b16 v[94:95], v227 offset:20480\n"
"ds_read_b64_tr_b16 v[124:125], v227 offset:24576\n"
"s_waitcnt lgkmcnt(15)\n"
"v_mfma_f32_16x16x32_bf16 v[16:19], v[72:75], a[80:83], v[16:19]\n"
"ds_read_b64_tr_b16 v[126:127], v227 offset:28672\n"
"buffer_load_dwordx2 a[124:125], v213, s[36:39], s68 offen sc1\n"
"v_mfma_f32_16x16x32_bf16 v[16:19], v[104:107], a[88:91], v[16:19]\n"
"s_waitcnt vmcnt(6)\n"
"s_barrier\n"
"s_add_u32 s68, s68, 0x10000\n"
"v_mfma_f32_16x16x32_bf16 v[20:23], v[104:107], a[92:95], v[20:23]\n"
"ds_read_b64_tr_b16 v[64:65], v236\n"
"buffer_load_dwordx2 a[122:123], v212, s[36:39], s68 offen sc1\n"
"v_add_u32 v220, 0xfffe0000, v236\n"
"v_mfma_f32_16x16x32_bf16 v[20:23], v[72:75], a[84:87], v[20:23]\n"
"ds_read_b64_tr_b16 v[66:67], v236 offset:4096\n"
"ds_read_b64_tr_b16 v[96:97], v236 offset:8192\n"
"v_mfma_f32_16x16x32_bf16 v[24:27], v[76:79], a[80:83], v[24:27]\n"
"ds_read_b64_tr_b16 v[98:99], v236 offset:12288\n"
"buffer_load_dwordx2 a[126:127], v213, s[36:39], s68 offen sc1\n"
"v_add_u32 v221, 0xfffe0000, v237\n"
"v_mfma_f32_16x16x32_bf16 v[24:27], v[108:111], a[88:91], v[24:27]\n"
"s_add_u32 s68, s68, 0x10000\n"
"v_add_u32 v222, 0xfffe0000, v238\n"
"v_add_u32 v223, 0xfffe0000, v239\n"
"v_mfma_f32_16x16x32_bf16 v[28:31], v[108:111], a[92:95], v[28:31]\n"
"ds_read_b64_tr_b16 v[68:69], v237\n"
"s_cmp_lt_u32 s8, 3\n"
"s_cselect_b32 s16, s24, s20\n"
"s_cselect_b32 s17, s25, s21\n"
"s_cselect_b32 s18, s26, s22\n"
"s_mov_b32 s19, 0x20000\n"
"s_add_u32 s82, s65, 0x40000\n"
"s_cmp_lt_u32 s8, 3\n"
"s_cselect_b32 s82, s82, s66\n"
"s_add_u32 s83, s82, 0x4000\n"
"s_add_u32 m0, s67, 0\n"
"v_add_u32 v224, 0xfffe0000, v240\n"
"v_mfma_f32_16x16x32_bf16 v[28:31], v[76:79], a[84:87], v[28:31]\n"
"ds_read_b64_tr_b16 v[70:71], v237 offset:4096\n"
"ds_read_b64_tr_b16 v[100:101], v237 offset:8192\n"
"buffer_load_dwordx4 v216, s[16:19], s82 offen lds\n"
"s_waitcnt lgkmcnt(15)\n"
"v_mfma_f32_16x16x32_bf16 v[32:35], v[80:83], a[80:83], v[32:35]\n"
"ds_read_b64_tr_b16 v[102:103], v237 offset:12288\n"
"v_add_u32 v225, 0xfffe0000, v241\n"
"v_mfma_f32_16x16x32_bf16 v[32:35], v[112:115], a[88:91], v[32:35]\n"
"v_add_u32 v226, 0xfffe0000, v242\n"
"v_add_u32 v227, 0xfffe0000, v243\n"
"v_mfma_f32_16x16x32_bf16 v[36:39], v[112:115], a[92:95], v[36:39]\n"
"ds_read_b64_tr_b16 v[72:73], v238\n"
"buffer_load_dwordx4 v217, s[16:19], s82 offen offset:1024 lds\n"
"v_mfma_f32_16x16x32_bf16 v[36:39], v[80:83], a[84:87], v[36:39]\n"
"ds_read_b64_tr_b16 v[74:75], v238 offset:4096\n"
"ds_read_b64_tr_b16 v[104:105], v238 offset:8192\n"
"v_mfma_f32_16x16x32_bf16 v[40:43], v[84:87], a[80:83], v[40:43]\n"
"ds_read_b64_tr_b16 v[106:107], v238 offset:12288\n"
"v_mfma_f32_16x16x32_bf16 v[40:43], v[116:119], a[88:91], v[40:43]\n"
"v_mfma_f32_16x16x32_bf16 v[44:47], v[116:119], a[92:95], v[44:47]\n"
"ds_read_b64_tr_b16 v[76:77], v239\n"
"buffer_load_dwordx4 v218, s[16:19], s82 offen offset:2048 lds\n"
"v_mfma_f32_16x16x32_bf16 v[44:47], v[84:87], a[84:87], v[44:47]\n"
"ds_read_b64_tr_b16 v[78:79], v239 offset:4096\n"
"ds_read_b64_tr_b16 v[108:109], v239 offset:8192\n"
"s_waitcnt lgkmcnt(15)\n"
"v_mfma_f32_16x16x32_bf16 v[48:51], v[88:91], a[80:83], v[48:51]\n"
"ds_read_b64_tr_b16 v[110:111], v239 offset:12288\n"
"v_mfma_f32_16x16x32_bf16 v[48:51], v[120:123], a[88:91], v[48:51]\n"
"buffer_load_dwordx4 v219, s[16:19], s82 offen offset:3072 lds\n"
"v_mfma_f32_16x16x32_bf16 v[52:55], v[120:123], a[92:95], v[52:55]\n"
"ds_read_b64_tr_b16 v[80:81], v240\n"
"v_mfma_f32_16x16x32_bf16 v[52:55], v[88:91], a[84:87], v[52:55]\n"
"ds_read_b64_tr_b16 v[82:83], v240 offset:4096\n"
"ds_read_b64_tr_b16 v[112:113], v240 offset:8192\n"
"v_mfma_f32_16x16x32_bf16 v[56:59], v[92:95], a[80:83], v[56:59]\n"
"ds_read_b64_tr_b16 v[114:115], v240 offset:12288\n"
"v_mfma_f32_16x16x32_bf16 v[56:59], v[124:127], a[88:91], v[56:59]\n"
"s_add_u32 m0, s67, 0x1000\n"
"v_mfma_f32_16x16x32_bf16 v[60:63], v[124:127], a[92:95], v[60:63]\n"
"ds_read_b64_tr_b16 v[84:85], v241\n"
"buffer_load_dwordx4 v216, s[16:19], s83 offen lds\n"
"v_mfma_f32_16x16x32_bf16 v[60:63], v[92:95], a[84:87], v[60:63]\n"
"ds_read_b64_tr_b16 v[86:87], v241 offset:4096\n"
"ds_read_b64_tr_b16 v[116:117], v241 offset:8192\n"
"s_waitcnt vmcnt(23) lgkmcnt(15)\n"
"v_mfma_f32_16x16x32_bf16 v[0:3], v[64:67], a[96:99], v[0:3]\n"
"ds_read_b64_tr_b16 v[118:119], v241 offset:12288\n"
"v_mfma_f32_16x16x32_bf16 v[0:3], v[96:99], a[104:107], v[0:3]\n"
"buffer_load_dwordx4 v217, s[16:19], s83 offen offset:1024 lds\n"
"v_mfma_f32_16x16x32_bf16 v[4:7], v[96:99], a[108:111], v[4:7]\n"
"ds_read_b64_tr_b16 v[88:89], v242\n"
"v_mfma_f32_16x16x32_bf16 v[4:7], v[64:67], a[100:103], v[4:7]\n"
"ds_read_b64_tr_b16 v[90:91], v242 offset:4096\n"
"ds_read_b64_tr_b16 v[120:121], v242 offset:8192\n"
"v_mfma_f32_16x16x32_bf16 v[8:11], v[68:71], a[96:99], v[8:11]\n"
"ds_read_b64_tr_b16 v[122:123], v242 offset:12288\n"
"v_mfma_f32_16x16x32_bf16 v[8:11], v[100:103], a[104:107], v[8:11]\n"
"buffer_load_dwordx4 v218, s[16:19], s83 offen offset:2048 lds\n"
"v_mfma_f32_16x16x32_bf16 v[12:15], v[100:103], a[108:111], v[12:15]\n"
"ds_read_b64_tr_b16 v[92:93], v243\n"
"v_mfma_f32_16x16x32_bf16 v[12:15], v[68:71], a[100:103], v[12:15]\n"
"ds_read_b64_tr_b16 v[94:95], v243 offset:4096\n"
"ds_read_b64_tr_b16 v[124:125], v243 offset:8192\n"
"s_waitcnt lgkmcnt(15)\n"
"v_mfma_f32_16x16x32_bf16 v[16:19], v[72:75], a[96:99], v[16:19]\n"
"ds_read_b64_tr_b16 v[126:127], v243 offset:12288\n"
"buffer_load_dwordx4 v219, s[16:19], s83 offen offset:3072 lds\n"
"v_mfma_f32_16x16x32_bf16 v[16:19], v[104:107], a[104:107], v[16:19]\n"
"v_mfma_f32_16x16x32_bf16 v[20:23], v[104:107], a[108:111], v[20:23]\n"
"ds_read_b64_tr_b16 v[64:65], v236 offset:16384\n"
"v_mfma_f32_16x16x32_bf16 v[20:23], v[72:75], a[100:103], v[20:23]\n"
"ds_read_b64_tr_b16 v[66:67], v236 offset:20480\n"
"ds_read_b64_tr_b16 v[96:97], v236 offset:24576\n"
"v_mfma_f32_16x16x32_bf16 v[24:27], v[76:79], a[96:99], v[24:27]\n"
"ds_read_b64_tr_b16 v[98:99], v236 offset:28672\n"
"buffer_load_dwordx2 a[144:145], v212, s[36:39], s68 offen sc1\n"
"v_mfma_f32_16x16x32_bf16 v[24:27], v[108:111], a[104:107], v[24:27]\n"
"v_mfma_f32_16x16x32_bf16 v[28:31], v[108:111], a[108:111], v[28:31]\n"
"ds_read_b64_tr_b16 v[68:69], v237 offset:16384\n"
"buffer_load_dwordx2 a[148:149], v213, s[36:39], s68 offen sc1\n"
"v_mfma_f32_16x16x32_bf16 v[28:31], v[76:79], a[100:103], v[28:31]\n"
"ds_read_b64_tr_b16 v[70:71], v237 offset:20480\n"
"ds_read_b64_tr_b16 v[100:101], v237 offset:24576\n"
"s_add_u32 s68, s68, 0x10000\n"
"s_waitcnt lgkmcnt(15)\n"
"v_mfma_f32_16x16x32_bf16 v[32:35], v[80:83], a[96:99], v[32:35]\n"
"ds_read_b64_tr_b16 v[102:103], v237 offset:28672\n"
"buffer_load_dwordx2 a[146:147], v212, s[36:39], s68 offen sc1\n"
"v_mfma_f32_16x16x32_bf16 v[32:35], v[112:115], a[104:107], v[32:35]\n"
"v_mfma_f32_16x16x32_bf16 v[36:39], v[112:115], a[108:111], v[36:39]\n"
"ds_read_b64_tr_b16 v[72:73], v238 offset:16384\n"
"buffer_load_dwordx2 a[150:151], v213, s[36:39], s68 offen sc1\n"
"v_mfma_f32_16x16x32_bf16 v[36:39], v[80:83], a[100:103], v[36:39]\n"
"ds_read_b64_tr_b16 v[74:75], v238 offset:20480\n"
"ds_read_b64_tr_b16 v[104:105], v238 offset:24576\n"
"s_add_u32 s68, s68, 0x10000\n"
"v_mfma_f32_16x16x32_bf16 v[40:43], v[84:87], a[96:99], v[40:43]\n"
"ds_read_b64_tr_b16 v[106:107], v238 offset:28672\n"
"buffer_load_dwordx2 a[152:153], v212, s[36:39], s68 offen sc1\n"
"v_mfma_f32_16x16x32_bf16 v[40:43], v[116:119], a[104:107], v[40:43]\n"
"v_mfma_f32_16x16x32_bf16 v[44:47], v[116:119], a[108:111], v[44:47]\n"
"ds_read_b64_tr_b16 v[76:77], v239 offset:16384\n"
"buffer_load_dwordx2 a[156:157], v213, s[36:39], s68 offen sc1\n"
"v_mfma_f32_16x16x32_bf16 v[44:47], v[84:87], a[100:103], v[44:47]\n"
"ds_read_b64_tr_b16 v[78:79], v239 offset:20480\n"
"ds_read_b64_tr_b16 v[108:109], v239 offset:24576\n"
"s_add_u32 s68, s68, 0x10000\n"
"s_waitcnt lgkmcnt(15)\n"
"v_mfma_f32_16x16x32_bf16 v[48:51], v[88:91], a[96:99], v[48:51]\n"
"ds_read_b64_tr_b16 v[110:111], v239 offset:28672\n"
"buffer_load_dwordx2 a[154:155], v212, s[36:39], s68 offen sc1\n"
"v_mfma_f32_16x16x32_bf16 v[48:51], v[120:123], a[104:107], v[48:51]\n"
"v_mfma_f32_16x16x32_bf16 v[52:55], v[120:123], a[108:111], v[52:55]\n"
"ds_read_b64_tr_b16 v[80:81], v240 offset:16384\n"
"buffer_load_dwordx2 a[158:159], v213, s[36:39], s68 offen sc1\n"
"v_mfma_f32_16x16x32_bf16 v[52:55], v[88:91], a[100:103], v[52:55]\n"
"ds_read_b64_tr_b16 v[82:83], v240 offset:20480\n"
"ds_read_b64_tr_b16 v[112:113], v240 offset:24576\n"
"s_add_u32 s68, s68, 0x10000\n"
"v_mfma_f32_16x16x32_bf16 v[56:59], v[92:95], a[96:99], v[56:59]\n"
"ds_read_b64_tr_b16 v[114:115], v240 offset:28672\n"
"s_add_u32 s82, s66, 0x20000\n"
"s_add_u32 s83, s82, 0x4000\n"
"s_add_u32 m0, s67, 0x8000\n"
"v_mfma_f32_16x16x32_bf16 v[56:59], v[124:127], a[104:107], v[56:59]\n"
"buffer_load_dwordx4 v216, s[20:23], s82 offen lds\n"
"v_mfma_f32_16x16x32_bf16 v[60:63], v[124:127], a[108:111], v[60:63]\n"
"ds_read_b64_tr_b16 v[84:85], v241 offset:16384\n"
"v_mfma_f32_16x16x32_bf16 v[60:63], v[92:95], a[100:103], v[60:63]\n"
"ds_read_b64_tr_b16 v[86:87], v241 offset:20480\n"
"ds_read_b64_tr_b16 v[116:117], v241 offset:24576\n"
"s_waitcnt vmcnt(17) lgkmcnt(15)\n"
"v_mfma_f32_16x16x32_bf16 v[0:3], v[64:67], a[112:115], v[0:3]\n"
"ds_read_b64_tr_b16 v[118:119], v241 offset:28672\n"
"buffer_load_dwordx4 v217, s[20:23], s82 offen offset:1024 lds\n"
"v_mfma_f32_16x16x32_bf16 v[0:3], v[96:99], a[120:123], v[0:3]\n"
"v_mfma_f32_16x16x32_bf16 v[4:7], v[96:99], a[124:127], v[4:7]\n"
"ds_read_b64_tr_b16 v[88:89], v242 offset:16384\n"
"v_mfma_f32_16x16x32_bf16 v[4:7], v[64:67], a[116:119], v[4:7]\n"
"ds_read_b64_tr_b16 v[90:91], v242 offset:20480\n"
"ds_read_b64_tr_b16 v[120:121], v242 offset:24576\n"
"v_mfma_f32_16x16x32_bf16 v[8:11], v[68:71], a[112:115], v[8:11]\n"
"ds_read_b64_tr_b16 v[122:123], v242 offset:28672\n"
"buffer_load_dwordx4 v218, s[20:23], s82 offen offset:2048 lds\n"
"v_mfma_f32_16x16x32_bf16 v[8:11], v[100:103], a[120:123], v[8:11]\n"
"v_mfma_f32_16x16x32_bf16 v[12:15], v[100:103], a[124:127], v[12:15]\n"
"ds_read_b64_tr_b16 v[92:93], v243 offset:16384\n"
"v_mfma_f32_16x16x32_bf16 v[12:15], v[68:71], a[116:119], v[12:15]\n"
"ds_read_b64_tr_b16 v[94:95], v243 offset:20480\n"
"ds_read_b64_tr_b16 v[124:125], v243 offset:24576\n"
"buffer_load_dwordx4 v219, s[20:23], s82 offen offset:3072 lds\n"
"s_waitcnt lgkmcnt(15)\n"
"v_mfma_f32_16x16x32_bf16 v[16:19], v[72:75], a[112:115], v[16:19]\n"
"ds_read_b64_tr_b16 v[126:127], v243 offset:28672\n"
"v_mfma_f32_16x16x32_bf16 v[16:19], v[104:107], a[120:123], v[16:19]\n"
"s_waitcnt vmcnt(12)\n"
"s_barrier\n"
"v_mfma_f32_16x16x32_bf16 v[20:23], v[104:107], a[124:127], v[20:23]\n"
"ds_read_b64_tr_b16 v[64:65], v220\n"
"v_add_u32 v236, 0x8000, v220\n"
"v_mfma_f32_16x16x32_bf16 v[20:23], v[72:75], a[116:119], v[20:23]\n"
"ds_read_b64_tr_b16 v[66:67], v220 offset:4096\n"
"ds_read_b64_tr_b16 v[96:97], v220 offset:8192\n"
"s_add_u32 m0, s67, 0x9000\n"
"v_mfma_f32_16x16x32_bf16 v[24:27], v[76:79], a[112:115], v[24:27]\n"
"ds_read_b64_tr_b16 v[98:99], v220 offset:12288\n"
"buffer_load_dwordx4 v216, s[20:23], s83 offen lds\n"
"v_add_u32 v237, 0x8000, v221\n"
"v_mfma_f32_16x16x32_bf16 v[24:27], v[108:111], a[120:123], v[24:27]\n"
"v_add_u32 v238, 0x8000, v222\n"
"v_add_u32 v239, 0x8000, v223\n"
"v_mfma_f32_16x16x32_bf16 v[28:31], v[108:111], a[124:127], v[28:31]\n"
"ds_read_b64_tr_b16 v[68:69], v221\n"
"v_add_u32 v240, 0x8000, v224\n"
"v_mfma_f32_16x16x32_bf16 v[28:31], v[76:79], a[116:119], v[28:31]\n"
"ds_read_b64_tr_b16 v[70:71], v221 offset:4096\n"
"ds_read_b64_tr_b16 v[100:101], v221 offset:8192\n"
"buffer_load_dwordx4 v217, s[20:23], s83 offen offset:1024 lds\n"
"s_waitcnt lgkmcnt(15)\n"
"v_mfma_f32_16x16x32_bf16 v[32:35], v[80:83], a[112:115], v[32:35]\n"
"ds_read_b64_tr_b16 v[102:103], v221 offset:12288\n"
"v_add_u32 v241, 0x8000, v225\n"
"v_mfma_f32_16x16x32_bf16 v[32:35], v[112:115], a[120:123], v[32:35]\n"
"v_add_u32 v242, 0x8000, v226\n"
"v_add_u32 v243, 0x8000, v227\n"
"v_mfma_f32_16x16x32_bf16 v[36:39], v[112:115], a[124:127], v[36:39]\n"
"ds_read_b64_tr_b16 v[72:73], v222\n"
"v_mfma_f32_16x16x32_bf16 v[36:39], v[80:83], a[116:119], v[36:39]\n"
"ds_read_b64_tr_b16 v[74:75], v222 offset:4096\n"
"ds_read_b64_tr_b16 v[104:105], v222 offset:8192\n"
"buffer_load_dwordx4 v218, s[20:23], s83 offen offset:2048 lds\n"
"v_mfma_f32_16x16x32_bf16 v[40:43], v[84:87], a[112:115], v[40:43]\n"
"ds_read_b64_tr_b16 v[106:107], v222 offset:12288\n"
"v_mfma_f32_16x16x32_bf16 v[40:43], v[116:119], a[120:123], v[40:43]\n"
"v_mfma_f32_16x16x32_bf16 v[44:47], v[116:119], a[124:127], v[44:47]\n"
"ds_read_b64_tr_b16 v[76:77], v223\n"
"buffer_load_dwordx4 v219, s[20:23], s83 offen offset:3072 lds\n"
"v_mfma_f32_16x16x32_bf16 v[44:47], v[84:87], a[116:119], v[44:47]\n"
"ds_read_b64_tr_b16 v[78:79], v223 offset:4096\n"
"ds_read_b64_tr_b16 v[108:109], v223 offset:8192\n"
"s_waitcnt lgkmcnt(15)\n"
"v_mfma_f32_16x16x32_bf16 v[48:51], v[88:91], a[112:115], v[48:51]\n"
"ds_read_b64_tr_b16 v[110:111], v223 offset:12288\n"
"buffer_load_dwordx2 v[230:231], v228, s[92:95], s72 offen sc1\n"
"v_mfma_f32_16x16x32_bf16 v[48:51], v[120:123], a[120:123], v[48:51]\n"
"v_mfma_f32_16x16x32_bf16 v[52:55], v[120:123], a[124:127], v[52:55]\n"
"ds_read_b64_tr_b16 v[80:81], v224\n"
"buffer_load_dwordx2 a[160:161], v212, s[36:39], s68 offen sc1\n"
"v_mfma_f32_16x16x32_bf16 v[52:55], v[88:91], a[116:119], v[52:55]\n"
"ds_read_b64_tr_b16 v[82:83], v224 offset:4096\n"
"ds_read_b64_tr_b16 v[112:113], v224 offset:8192\n"
"v_mfma_f32_16x16x32_bf16 v[56:59], v[92:95], a[112:115], v[56:59]\n"
"ds_read_b64_tr_b16 v[114:115], v224 offset:12288\n"
"buffer_load_dwordx2 a[164:165], v213, s[36:39], s68 offen sc1\n"
"v_mfma_f32_16x16x32_bf16 v[56:59], v[124:127], a[120:123], v[56:59]\n"
"s_add_u32 s68, s68, 0x10000\n"
"v_mfma_f32_16x16x32_bf16 v[60:63], v[124:127], a[124:127], v[60:63]\n"
"ds_read_b64_tr_b16 v[84:85], v225\n"
"buffer_load_dwordx2 a[162:163], v212, s[36:39], s68 offen sc1\n"
"v_mfma_f32_16x16x32_bf16 v[60:63], v[92:95], a[116:119], v[60:63]\n"
"ds_read_b64_tr_b16 v[86:87], v225 offset:4096\n"
"ds_read_b64_tr_b16 v[116:117], v225 offset:8192\n"
"s_waitcnt vmcnt(12) lgkmcnt(15)\n"
"v_mfma_f32_16x16x32_bf16 v[0:3], v[64:67], a[144:147], v[0:3]\n"
"ds_read_b64_tr_b16 v[118:119], v225 offset:12288\n"
"buffer_load_dwordx2 a[166:167], v213, s[36:39], s68 offen sc1\n"
"v_mfma_f32_16x16x32_bf16 v[0:3], v[96:99], a[152:155], v[0:3]\n"
"s_add_u32 s68, s68, 0x10000\n"
"v_mfma_f32_16x16x32_bf16 v[4:7], v[96:99], a[156:159], v[4:7]\n"
"ds_read_b64_tr_b16 v[88:89], v226\n"
"buffer_load_dwordx4 a[168:171], v214, s[36:39], s69 offen sc1\n"
"v_mfma_f32_16x16x32_bf16 v[4:7], v[64:67], a[148:151], v[4:7]\n"
"ds_read_b64_tr_b16 v[90:91], v226 offset:4096\n"
"ds_read_b64_tr_b16 v[120:121], v226 offset:8192\n"
"v_mfma_f32_16x16x32_bf16 v[8:11], v[68:71], a[144:147], v[8:11]\n"
"ds_read_b64_tr_b16 v[122:123], v226 offset:12288\n"
"s_add_u32 s69, s69, 0x20000\n"
"v_mfma_f32_16x16x32_bf16 v[8:11], v[100:103], a[152:155], v[8:11]\n"
"buffer_load_dwordx4 a[172:175], v214, s[36:39], s69 offen sc1\n"
"v_mfma_f32_16x16x32_bf16 v[12:15], v[100:103], a[156:159], v[12:15]\n"
"ds_read_b64_tr_b16 v[92:93], v227\n"
"v_mfma_f32_16x16x32_bf16 v[12:15], v[68:71], a[148:151], v[12:15]\n"
"ds_read_b64_tr_b16 v[94:95], v227 offset:4096\n"
"ds_read_b64_tr_b16 v[124:125], v227 offset:8192\n"
"s_add_u32 s69, s69, 0x20000\n"
"s_waitcnt lgkmcnt(15)\n"
"v_mfma_f32_16x16x32_bf16 v[16:19], v[72:75], a[144:147], v[16:19]\n"
"ds_read_b64_tr_b16 v[126:127], v227 offset:12288\n"
"buffer_load_dwordx4 a[240:243], v214, s[36:39], s69 offen sc1\n"
"v_mfma_f32_16x16x32_bf16 v[16:19], v[104:107], a[152:155], v[16:19]\n"
"v_mfma_f32_16x16x32_bf16 v[20:23], v[104:107], a[156:159], v[20:23]\n"
"ds_read_b64_tr_b16 v[64:65], v220 offset:16384\n"
"s_add_u32 s69, s69, 0x20000\n"
"v_mfma_f32_16x16x32_bf16 v[20:23], v[72:75], a[148:151], v[20:23]\n"
"ds_read_b64_tr_b16 v[66:67], v220 offset:20480\n"
"buffer_load_dwordx4 a[244:247], v214, s[36:39], s69 offen sc1\n"
"v_mfma_f32_16x16x32_bf16 v[24:27], v[76:79], a[144:147], v[24:27]\n"
"ds_read_b64_tr_b16 v[68:69], v221 offset:16384\n"
"v_mfma_f32_16x16x32_bf16 v[24:27], v[108:111], a[152:155], v[24:27]\n"
"ds_read_b64_tr_b16 v[70:71], v221 offset:20480\n"
"s_add_u32 s69, s69, 0x20000\n"
"v_mfma_f32_16x16x32_bf16 v[28:31], v[108:111], a[156:159], v[28:31]\n"
"ds_read_b64_tr_b16 v[72:73], v222 offset:16384\n"
"buffer_load_dwordx4 a[248:251], v214, s[36:39], s69 offen sc1\n"
"v_mfma_f32_16x16x32_bf16 v[28:31], v[76:79], a[148:151], v[28:31]\n"
"ds_read_b64_tr_b16 v[74:75], v222 offset:20480\n"
"s_waitcnt lgkmcnt(14)\n"
"s_waitcnt vmcnt(9)\n"
"v_cmp_eq_u64 vcc, v[230:231], v[250:251]\n"
"s_nop 1\n"
"s_cmp_eq_u64 vcc, exec\n"
"s_cbranch_scc1 .Lpok_53_%=\n"
"s_mov_b32 s15, 0\n"
".Lploop_54_%=:\n"
"s_sleep 4\n"
"buffer_load_dwordx2 v[230:231], v228, s[92:95], s72 offen sc1\n"
"s_waitcnt vmcnt(0)\n"
"v_cmp_eq_u64 vcc, v[230:231], v[250:251]\n"
"s_add_u32 s15, s15, 1\n"
"s_nop 0\n"
"s_cmp_eq_u64 vcc, exec\n"
"s_cbranch_scc1 .Lpok_53_%=\n"
"s_cmp_lt_u32 s15, 0x20000\n"
"s_cbranch_scc1 .Lploop_54_%=\n"
"s_mov_b32 s15, 0x80\n"
"s_mov_b64 exec, 1\n"
"buffer_store_dwordx2 v[250:251], v249, s[92:95], s15 offen sc1\n"
"s_mov_b64 exec, -1\n"
".Lpok_53_%=:\n"
"v_mfma_f32_16x16x32_bf16 v[32:35], v[80:83], a[144:147], v[32:35]\n"
"ds_read_b64_tr_b16 v[76:77], v223 offset:16384\n"
"s_add_u32 s69, s69, 0x20000\n"
"v_mfma_f32_16x16x32_bf16 v[32:35], v[112:115], a[152:155], v[32:35]\n"
"ds_read_b64_tr_b16 v[78:79], v223 offset:20480\n"
"buffer_load_dwordx4 a[40:43], v215, s[36:39], s70 offen sc1\n"
"v_mfma_f32_16x16x32_bf16 v[36:39], v[112:115], a[156:159], v[36:39]\n"
"v_mfma_f32_16x16x32_bf16 v[36:39], v[80:83], a[148:151], v[36:39]\n"
"s_add_u32 s70, s70, 0x20000\n"
"v_mfma_f32_16x16x32_bf16 v[40:43], v[84:87], a[144:147], v[40:43]\n"
"ds_read_b64_tr_b16 v[80:81], v224 offset:16384\n"
"buffer_load_dwordx4 a[44:47], v215, s[36:39], s70 offen sc1\n"
"v_mfma_f32_16x16x32_bf16 v[40:43], v[116:119], a[152:155], v[40:43]\n"
"ds_read_b64_tr_b16 v[82:83], v224 offset:20480\n"
"v_mfma_f32_16x16x32_bf16 v[44:47], v[116:119], a[156:159], v[44:47]\n"
"s_add_u32 s70, s70, 0x20000\n"
"v_mfma_f32_16x16x32_bf16 v[44:47], v[84:87], a[148:151], v[44:47]\n"
"buffer_load_dwordx4 a[48:51], v215, s[36:39], s70 offen sc1\n"
"s_waitcnt lgkmcnt(10)\n"
"v_mfma_f32_16x16x32_bf16 v[48:51], v[88:91], a[144:147], v[48:51]\n"
"ds_read_b64_tr_b16 v[84:85], v225 offset:16384\n"
"v_mfma_f32_16x16x32_bf16 v[48:51], v[120:123], a[152:155], v[48:51]\n"
"ds_read_b64_tr_b16 v[86:87], v225 offset:20480\n"
"s_add_u32 s70, s70, 0x20000\n"
"v_mfma_f32_16x16x32_bf16 v[52:55], v[120:123], a[156:159], v[52:55]\n"
"ds_read_b64_tr_b16 v[96:97], v220 offset:24576\n"
"buffer_load_dwordx4 a[52:55], v215, s[36:39], s70 offen sc1\n"
"v_mfma_f32_16x16x32_bf16 v[52:55], v[88:91], a[148:151], v[52:55]\n"
"ds_read_b64_tr_b16 v[98:99], v220 offset:28672\n"
"v_mfma_f32_16x16x32_bf16 v[56:59], v[92:95], a[144:147], v[56:59]\n"
"ds_read_b64_tr_b16 v[88:89], v226 offset:16384\n"
"s_add_u32 s70, s70, 0x20000\n"
"v_mfma_f32_16x16x32_bf16 v[56:59], v[124:127], a[152:155], v[56:59]\n"
"ds_read_b64_tr_b16 v[90:91], v226 offset:20480\n"
"s_add_u32 s82, s66, 0x40000\n"
"s_add_u32 s83, s82, 0x4000\n"
"s_add_u32 m0, s67, 0x10000\n"
"v_mfma_f32_16x16x32_bf16 v[60:63], v[124:127], a[156:159], v[60:63]\n"
"ds_read_b64_tr_b16 v[100:101], v221 offset:24576\n"
"buffer_load_dwordx4 v216, s[20:23], s82 offen lds\n"
"v_mfma_f32_16x16x32_bf16 v[60:63], v[92:95], a[148:151], v[60:63]\n"
"ds_read_b64_tr_b16 v[102:103], v221 offset:28672\n"
"s_waitcnt vmcnt(10) lgkmcnt(14)\n"
"v_mfma_f32_16x16x32_bf16 v[0:3], v[64:67], a[160:163], v[0:3]\n"
"ds_read_b64_tr_b16 v[92:93], v227 offset:16384\n"
"v_mfma_f32_16x16x32_bf16 v[4:7], v[64:67], a[164:167], v[4:7]\n"
"s_waitcnt vmcnt(15)\n"
"s_barrier\n"
"ds_read_b64_tr_b16 v[94:95], v227 offset:20480\n"
"buffer_load_dwordx4 v217, s[20:23], s82 offen offset:1024 lds\n"
"v_mfma_f32_16x16x32_bf16 v[8:11], v[68:71], a[160:163], v[8:11]\n"
"ds_read_b64_tr_b16 v[104:105], v222 offset:24576\n"
"ds_read_b64_tr_b16 v[64:65], v236\n"
"v_mfma_f32_16x16x32_bf16 v[12:15], v[68:71], a[164:167], v[12:15]\n"
"ds_read_b64_tr_b16 v[106:107], v222 offset:28672\n"
"ds_read_b64_tr_b16 v[66:67], v236 offset:4096\n"
"s_waitcnt lgkmcnt(15)\n"
"v_mfma_f32_16x16x32_bf16 v[16:19], v[72:75], a[160:163], v[16:19]\n"
"ds_read_b64_tr_b16 v[68:69], v237\n"
"v_mfma_f32_16x16x32_bf16 v[20:23], v[72:75], a[164:167], v[20:23]\n"
"ds_read_b64_tr_b16 v[70:71], v237 offset:4096\n"
"buffer_load_dwordx4 v218, s[20:23], s82 offen offset:2048 lds\n"
"v_mfma_f32_16x16x32_bf16 v[24:27], v[76:79], a[160:163], v[24:27]\n"
"ds_read_b64_tr_b16 v[108:109], v223 offset:24576\n"
"ds_read_b64_tr_b16 v[72:73], v238\n"
"v_mfma_f32_16x16x32_bf16 v[28:31], v[76:79], a[164:167], v[28:31]\n"
"ds_read_b64_tr_b16 v[110:111], v223 offset:28672\n"
"ds_read_b64_tr_b16 v[74:75], v238 offset:4096\n"
"s_waitcnt lgkmcnt(15)\n"
"v_mfma_f32_16x16x32_bf16 v[32:35], v[80:83], a[160:163], v[32:35]\n"
"ds_read_b64_tr_b16 v[76:77], v239\n"
"buffer_load_dwordx4 v219, s[20:23], s82 offen offset:3072 lds\n"
"v_mfma_f32_16x16x32_bf16 v[36:39], v[80:83], a[164:167], v[36:39]\n"
"ds_read_b64_tr_b16 v[78:79], v239 offset:4096\n"
"v_mfma_f32_16x16x32_bf16 v[40:43], v[84:87], a[160:163], v[40:43]\n"
"ds_read_b64_tr_b16 v[112:113], v224 offset:24576\n"
"v_mfma_f32_16x16x32_bf16 v[44:47], v[84:87], a[164:167], v[44:47]\n"
"ds_read_b64_tr_b16 v[114:115], v224 offset:28672\n"
"ds_read_b64_tr_b16 v[80:81], v240\n"
"s_waitcnt lgkmcnt(15)\n"
"v_mfma_f32_16x16x32_bf16 v[48:51], v[88:91], a[160:163], v[48:51]\n"
"ds_read_b64_tr_b16 v[82:83], v240 offset:4096\n"
"s_add_u32 m0, s67, 0x11000\n"
"v_mfma_f32_16x16x32_bf16 v[52:55], v[88:91], a[164:167], v[52:55]\n"
"buffer_load_dwordx4 v216, s[20:23], s83 offen lds\n"
"v_mfma_f32_16x16x32_bf16 v[56:59], v[92:95], a[160:163], v[56:59]\n"
"ds_read_b64_tr_b16 v[116:117], v225 offset:24576\n"
"v_mfma_f32_16x16x32_bf16 v[60:63], v[92:95], a[164:167], v[60:63]\n"
"ds_read_b64_tr_b16 v[118:119], v225 offset:28672\n"
"ds_read_b64_tr_b16 v[84:85], v241\n"
"s_waitcnt vmcnt(7) lgkmcnt(13)\n"
"v_mfma_f32_16x16x32_bf16 v[0:3], v[96:99], a[168:171], v[0:3]\n"
"ds_read_b64_tr_b16 v[86:87], v241 offset:4096\n"
"buffer_load_dwordx4 v217, s[20:23], s83 offen offset:1024 lds\n"
"v_mfma_f32_16x16x32_bf16 v[0:3], v[64:67], a[172:175], v[0:3]\n"
"v_mfma_f32_16x16x32_bf16 v[4:7], v[64:67], a[44:47], v[4:7]\n"
"ds_read_b64_tr_b16 v[120:121], v226 offset:24576\n"
"v_mfma_f32_16x16x32_bf16 v[4:7], v[96:99], a[40:43], v[4:7]\n"
"ds_read_b64_tr_b16 v[122:123], v226 offset:28672\n"
"ds_read_b64_tr_b16 v[88:89], v242\n"
"v_mfma_f32_16x16x32_bf16 v[8:11], v[100:103], a[168:171], v[8:11]\n"
"ds_read_b64_tr_b16 v[90:91], v242 offset:4096\n"
"buffer_load_dwordx4 v218, s[20:23], s83 offen offset:2048 lds\n"
"v_mfma_f32_16x16x32_bf16 v[8:11], v[68:71], a[172:175], v[8:11]\n"
"v_mfma_f32_16x16x32_bf16 v[12:15], v[68:71], a[44:47], v[12:15]\n"
"ds_read_b64_tr_b16 v[124:125], v227 offset:24576\n"
"v_mfma_f32_16x16x32_bf16 v[12:15], v[100:103], a[40:43], v[12:15]\n"
"ds_read_b64_tr_b16 v[126:127], v227 offset:28672\n"
"ds_read_b64_tr_b16 v[92:93], v243\n"
"buffer_load_dwordx4 v219, s[20:23], s83 offen offset:3072 lds\n"
"s_waitcnt lgkmcnt(15)\n"
"v_mfma_f32_16x16x32_bf16 v[16:19], v[104:107], a[168:171], v[16:19]\n"
"ds_read_b64_tr_b16 v[94:95], v243 offset:4096\n"
"v_add_u32 v220, 0x8000, v236\n"
"v_mfma_f32_16x16x32_bf16 v[16:19], v[72:75], a[172:175], v[16:19]\n"
"v_add_u32 v221, 0x8000, v237\n"
"v_add_u32 v222, 0x8000, v238\n"
"v_mfma_f32_16x16x32_bf16 v[20:23], v[72:75], a[44:47], v[20:23]\n"
"ds_read_b64_tr_b16 v[96:97], v236 offset:8192\n"
"v_add_u32 v223, 0x8000, v239\n"
"v_mfma_f32_16x16x32_bf16 v[20:23], v[104:107], a[40:43], v[20:23]\n"
"ds_read_b64_tr_b16 v[98:99], v236 offset:12288\n"
"ds_read_b64_tr_b16 v[64:65], v236 offset:16384\n"
"buffer_load_dwordx4 a[56:59], v215, s[36:39], s70 offen sc1\n"
"v_mfma_f32_16x16x32_bf16 v[24:27], v[108:111], a[168:171], v[24:27]\n"
"ds_read_b64_tr_b16 v[66:67], v236 offset:20480\n"
"v_add_u32 v224, 0x8000, v240\n"
"v_mfma_f32_16x16x32_bf16 v[24:27], v[76:79], a[172:175], v[24:27]\n"
"s_add_u32 s70, s70, 0x20000\n"
"v_add_u32 v225, 0x8000, v241\n"
"v_add_u32 v226, 0x8000, v242\n"
"v_mfma_f32_16x16x32_bf16 v[28:31], v[76:79], a[44:47], v[28:31]\n"
"ds_read_b64_tr_b16 v[100:101], v237 offset:8192\n"
"buffer_load_dwordx4 a[60:63], v215, s[36:39], s70 offen sc1\n"
"v_add_u32 v227, 0x8000, v243\n"
"v_mfma_f32_16x16x32_bf16 v[28:31], v[108:111], a[40:43], v[28:31]\n"
"ds_read_b64_tr_b16 v[102:103], v237 offset:12288\n"
"ds_read_b64_tr_b16 v[68:69], v237 offset:16384\n"
"s_waitcnt lgkmcnt(15)\n"
"v_mfma_f32_16x16x32_bf16 v[32:35], v[112:115], a[168:171], v[32:35]\n"
"ds_read_b64_tr_b16 v[70:71], v237 offset:20480\n"
"s_add_u32 s70, s70, 0x20000\n"
"v_mfma_f32_16x16x32_bf16 v[32:35], v[80:83], a[172:175], v[32:35]\n"
"buffer_load_dwordx4 a[252:255], v214, s[36:39], s69 offen sc1\n"
"v_mfma_f32_16x16x32_bf16 v[36:39], v[80:83], a[44:47], v[36:39]\n"
"ds_read_b64_tr_b16 v[104:105], v238 offset:8192\n"
"v_mfma_f32_16x16x32_bf16 v[36:39], v[112:115], a[40:43], v[36:39]\n"
"ds_read_b64_tr_b16 v[106:107], v238 offset:12288\n"
"ds_read_b64_tr_b16 v[72:73], v238 offset:16384\n"
"s_add_u32 s69, s69, 0x20000\n"
"v_mfma_f32_16x16x32_bf16 v[40:43], v[116:119], a[168:171], v[40:43]\n"
"ds_read_b64_tr_b16 v[74:75], v238 offset:20480\n"
"buffer_load_dwordx4 a[128:131], v215, s[36:39], s70 offen sc1\n"
"v_mfma_f32_16x16x32_bf16 v[40:43], v[84:87], a[172:175], v[40:43]\n"
"v_mfma_f32_16x16x32_bf16 v[44:47], v[84:87], a[44:47], v[44:47]\n"
"ds_read_b64_tr_b16 v[108:109], v239 offset:8192\n"
"s_add_u32 s70, s70, 0x20000\n"
"v_mfma_f32_16x16x32_bf16 v[44:47], v[116:119], a[40:43], v[44:47]\n"
"ds_read_b64_tr_b16 v[110:111], v239 offset:12288\n"
"ds_read_b64_tr_b16 v[76:77], v239 offset:16384\n"
"buffer_load_dwordx4 a[132:135], v215, s[36:39], s70 offen sc1\n"
"s_waitcnt lgkmcnt(15)\n"
"v_mfma_f32_16x16x32_bf16 v[48:51], v[120:123], a[168:171], v[48:51]\n"
"ds_read_b64_tr_b16 v[78:79], v239 offset:20480\n"
"v_mfma_f32_16x16x32_bf16 v[48:51], v[88:91], a[172:175], v[48:51]\n"
"s_add_u32 s70, s70, 0x20000\n"
"v_mfma_f32_16x16x32_bf16 v[52:55], v[88:91], a[44:47], v[52:55]\n"
"ds_read_b64_tr_b16 v[112:113], v240 offset:8192\n"
"buffer_load_dwordx4 a[176:179], v214, s[36:39], s69 offen sc1\n"
"v_mfma_f32_16x16x32_bf16 v[52:55], v[120:123], a[40:43], v[52:55]\n"
"ds_read_b64_tr_b16 v[114:115], v240 offset:12288\n"
"ds_read_b64_tr_b16 v[80:81], v240 offset:16384\n"
"v_mfma_f32_16x16x32_bf16 v[56:59], v[124:127], a[168:171], v[56:59]\n"
"ds_read_b64_tr_b16 v[82:83], v240 offset:20480\n"
"s_add_u32 s69, s69, 0x20000\n"
"v_mfma_f32_16x16x32_bf16 v[56:59], v[92:95], a[172:175], v[56:59]\n"
"buffer_load_dwordx4 a[180:183], v214, s[36:39], s69 offen sc1\n"
"v_mfma_f32_16x16x32_bf16 v[60:63], v[92:95], a[44:47], v[60:63]\n"
"ds_read_b64_tr_b16 v[116:117], v241 offset:8192\n"
"v_mfma_f32_16x16x32_bf16 v[60:63], v[124:127], a[40:43], v[60:63]\n"
"ds_read_b64_tr_b16 v[118:119], v241 offset:12288\n"
"ds_read_b64_tr_b16 v[84:85], v241 offset:16384\n"
"s_add_u32 s69, s69, 0x20000\n"
"s_waitcnt vmcnt(15) lgkmcnt(15)\n"
"v_mfma_f32_16x16x32_bf16 v[0:3], v[96:99], a[240:243], v[0:3]\n"
"ds_read_b64_tr_b16 v[86:87], v241 offset:20480\n"
"s_add_u32 s82, s66, 0x60000\n"
"s_add_u32 s83, s82, 0x4000\n"
"s_add_u32 m0, s67, 0x18000\n"
"v_mfma_f32_16x16x32_bf16 v[0:3], v[64:67], a[244:247], v[0:3]\n"
"buffer_load_dwordx4 v216, s[20:23], s82 offen lds\n"
"v_mfma_f32_16x16x32_bf16 v[4:7], v[64:67], a[52:55], v[4:7]\n"
"ds_read_b64_tr_b16 v[120:121], v242 offset:8192\n"
"v_mfma_f32_16x16x32_bf16 v[4:7], v[96:99], a[48:51], v[4:7]\n"
"ds_read_b64_tr_b16 v[122:123], v242 offset:12288\n"
"ds_read_b64_tr_b16 v[88:89], v242 offset:16384\n"
"v_mfma_f32_16x16x32_bf16 v[8:11], v[100:103], a[240:243], v[8:11]\n"
"ds_read_b64_tr_b16 v[90:91], v242 offset:20480\n"
"buffer_load_dwordx4 v217, s[20:23], s82 offen offset:1024 lds\n"
"v_mfma_f32_16x16x32_bf16 v[8:11], v[68:71], a[244:247], v[8:11]\n"
"v_mfma_f32_16x16x32_bf16 v[12:15], v[68:71], a[52:55], v[12:15]\n"
"ds_read_b64_tr_b16 v[124:125], v243 offset:8192\n"
"v_mfma_f32_16x16x32_bf16 v[12:15], v[100:103], a[48:51], v[12:15]\n"
"ds_read_b64_tr_b16 v[126:127], v243 offset:12288\n"
"ds_read_b64_tr_b16 v[92:93], v243 offset:16384\n"
"s_waitcnt lgkmcnt(15)\n"
"v_mfma_f32_16x16x32_bf16 v[16:19], v[104:107], a[240:243], v[16:19]\n"
"ds_read_b64_tr_b16 v[94:95], v243 offset:20480\n"
"buffer_load_dwordx4 v218, s[20:23], s82 offen offset:2048 lds\n"
"v_mfma_f32_16x16x32_bf16 v[16:19], v[72:75], a[244:247], v[16:19]\n"
"v_mfma_f32_16x16x32_bf16 v[20:23], v[72:75], a[52:55], v[20:23]\n"
"s_waitcnt vmcnt(10)\n"
"s_barrier\n"
"ds_read_b64_tr_b16 v[96:97], v236 offset:24576\n"
"v_mfma_f32_16x16x32_bf16 v[20:23], v[104:107], a[48:51], v[20:23]\n"
"ds_read_b64_tr_b16 v[98:99], v236 offset:28672\n"
"ds_read_b64_tr_b16 v[64:65], v220\n"
"buffer_load_dwordx4 v219, s[20:23], s82 offen offset:3072 lds\n"
"v_mfma_f32_16x16x32_bf16 v[24:27], v[108:111], a[240:243], v[24:27]\n"
"ds_read_b64_tr_b16 v[66:67], v220 offset:4096\n"
"v_mfma_f32_16x16x32_bf16 v[24:27], v[76:79], a[244:247], v[24:27]\n"
"v_mfma_f32_16x16x32_bf16 v[28:31], v[76:79], a[52:55], v[28:31]\n"
"ds_read_b64_tr_b16 v[100:101], v237 offset:24576\n"
"v_mfma_f32_16x16x32_bf16 v[28:31], v[108:111], a[48:51], v[28:31]\n"
"ds_read_b64_tr_b16 v[102:103], v237 offset:28672\n"
"ds_read_b64_tr_b16 v[68:69], v221\n"
"s_add_u32 m0, s67, 0x19000\n"
"s_waitcnt lgkmcnt(15)\n"
"v_mfma_f32_16x16x32_bf16 v[32:35], v[112:115], a[240:243], v[32:35]\n"
"ds_read_b64_tr_b16 v[70:71], v221 offset:4096\n"
"buffer_load_dwordx4 v216, s[20:23], s83 offen lds\n"
"v_mfma_f32_16x16x32_bf16 v[32:35], v[80:83], a[244:247], v[32:35]\n"
"v_mfma_f32_16x16x32_bf16 v[36:39], v[80:83], a[52:55], v[36:39]\n"
"ds_read_b64_tr_b16 v[104:105], v238 offset:24576\n"
"v_mfma_f32_16x16x32_bf16 v[36:39], v[112:115], a[48:51], v[36:39]\n"
"ds_read_b64_tr_b16 v[106:107], v238 offset:28672\n"
"ds_read_b64_tr_b16 v[72:73], v222\n"
"buffer_load_dwordx4 v217, s[20:23], s83 offen offset:1024 lds\n"
"v_mfma_f32_16x16x32_bf16 v[40:43], v[116:119], a[240:243], v[40:43]\n"
"ds_read_b64_tr_b16 v[74:75], v222 offset:4096\n"
"v_mfma_f32_16x16x32_bf16 v[40:43], v[84:87], a[244:247], v[40:43]\n"
"v_mfma_f32_16x16x32_bf16 v[44:47], v[84:87], a[52:55], v[44:47]\n"
"ds_read_b64_tr_b16 v[108:109], v239 offset:24576\n"
"v_mfma_f32_16x16x32_bf16 v[44:47], v[116:119], a[48:51], v[44:47]\n"
"ds_read_b64_tr_b16 v[110:111], v239 offset:28672\n"
"ds_read_b64_tr_b16 v[76:77], v223\n"
"buffer_load_dwordx4 v218, s[20:23], s83 offen offset:2048 lds\n"
"s_waitcnt lgkmcnt(15)\n"
"v_mfma_f32_16x16x32_bf16 v[48:51], v[120:123], a[240:243], v[48:51]\n"
"ds_read_b64_tr_b16 v[78:79], v223 offset:4096\n"
"v_mfma_f32_16x16x32_bf16 v[48:51], v[88:91], a[244:247], v[48:51]\n"
"v_mfma_f32_16x16x32_bf16 v[52:55], v[88:91], a[52:55], v[52:55]\n"
"ds_read_b64_tr_b16 v[112:113], v240 offset:24576\n"
"buffer_load_dwordx4 v219, s[20:23], s83 offen offset:3072 lds\n"
"v_mfma_f32_16x16x32_bf16 v[52:55], v[120:123], a[48:51], v[52:55]\n"
"ds_read_b64_tr_b16 v[114:115], v240 offset:28672\n"
"ds_read_b64_tr_b16 v[80:81], v224\n"
"v_mfma_f32_16x16x32_bf16 v[56:59], v[124:127], a[240:243], v[56:59]\n"
"ds_read_b64_tr_b16 v[82:83], v224 offset:4096\n"
"v_mfma_f32_16x16x32_bf16 v[56:59], v[92:95], a[244:247], v[56:59]\n"
"v_mfma_f32_16x16x32_bf16 v[60:63], v[92:95], a[52:55], v[60:63]\n"
"ds_read_b64_tr_b16 v[116:117], v241 offset:24576\n"
"buffer_load_dwordx4 a[136:139], v215, s[36:39], s70 offen sc1\n"
"v_mfma_f32_16x16x32_bf16 v[60:63], v[124:127], a[48:51], v[60:63]\n"
"ds_read_b64_tr_b16 v[118:119], v241 offset:28672\n"
"ds_read_b64_tr_b16 v[84:85], v225\n"
"s_waitcnt vmcnt(13) lgkmcnt(15)\n"
"v_mfma_f32_16x16x32_bf16 v[0:3], v[96:99], a[248:251], v[0:3]\n"
"ds_read_b64_tr_b16 v[86:87], v225 offset:4096\n"
"s_add_u32 s70, s70, 0x20000\n"
"v_mfma_f32_16x16x32_bf16 v[0:3], v[64:67], a[252:255], v[0:3]\n"
"buffer_load_dwordx4 a[140:143], v215, s[36:39], s70 offen sc1\n"
"v_mfma_f32_16x16x32_bf16 v[4:7], v[64:67], a[60:63], v[4:7]\n"
"ds_read_b64_tr_b16 v[120:121], v242 offset:24576\n"
"v_mfma_f32_16x16x32_bf16 v[4:7], v[96:99], a[56:59], v[4:7]\n"
"ds_read_b64_tr_b16 v[122:123], v242 offset:28672\n"
"ds_read_b64_tr_b16 v[88:89], v226\n"
"s_add_u32 s70, s70, 0x20000\n"
"v_mfma_f32_16x16x32_bf16 v[8:11], v[100:103], a[248:251], v[8:11]\n"
"ds_read_b64_tr_b16 v[90:91], v226 offset:4096\n"
"buffer_load_dwordx4 a[184:187], v214, s[36:39], s69 offen sc1\n"
"v_mfma_f32_16x16x32_bf16 v[8:11], v[68:71], a[252:255], v[8:11]\n"
"v_mfma_f32_16x16x32_bf16 v[12:15], v[68:71], a[60:63], v[12:15]\n"
"ds_read_b64_tr_b16 v[124:125], v243 offset:24576\n"
"s_add_u32 s69, s69, 0x20000\n"
"v_mfma_f32_16x16x32_bf16 v[12:15], v[100:103], a[56:59], v[12:15]\n"
"ds_read_b64_tr_b16 v[126:127], v243 offset:28672\n"
"ds_read_b64_tr_b16 v[92:93], v227\n"
"buffer_load_dwordx4 a[188:191], v214, s[36:39], s69 offen sc1\n"
"s_waitcnt lgkmcnt(15)\n"
"v_mfma_f32_16x16x32_bf16 v[16:19], v[104:107], a[248:251], v[16:19]\n"
"ds_read_b64_tr_b16 v[94:95], v227 offset:4096\n"
"v_add_u32 v236, 0x8000, v220\n"
"v_mfma_f32_16x16x32_bf16 v[16:19], v[72:75], a[252:255], v[16:19]\n"
"s_add_u32 s69, s69, 0x20000\n"
"v_add_u32 v237, 0x8000, v221\n"
"v_add_u32 v238, 0x8000, v222\n"
"v_mfma_f32_16x16x32_bf16 v[20:23], v[72:75], a[60:63], v[20:23]\n"
"ds_read_b64_tr_b16 v[96:97], v220 offset:8192\n"
"buffer_load_dwordx4 v[128:131], v215, s[36:39], s70 offen sc1\n"
"v_add_u32 v239, 0x8000, v223\n"
"v_mfma_f32_16x16x32_bf16 v[20:23], v[104:107], a[56:59], v[20:23]\n"
"ds_read_b64_tr_b16 v[98:99], v220 offset:12288\n"
"ds_read_b64_tr_b16 v[64:65], v220 offset:16384\n"
"v_mfma_f32_16x16x32_bf16 v[24:27], v[108:111], a[248:251], v[24:27]\n"
"ds_read_b64_tr_b16 v[66:67], v220 offset:20480\n"
"s_add_u32 s70, s70, 0x20000\n"
"v_add_u32 v240, 0x8000, v224\n"
"v_mfma_f32_16x16x32_bf16 v[24:27], v[76:79], a[252:255], v[24:27]\n"
"buffer_load_dwordx4 v[132:135], v215, s[36:39], s70 offen sc1\n"
"v_add_u32 v241, 0x8000, v225\n"
"v_add_u32 v242, 0x8000, v226\n"
"v_mfma_f32_16x16x32_bf16 v[28:31], v[76:79], a[60:63], v[28:31]\n"
"ds_read_b64_tr_b16 v[100:101], v221 offset:8192\n"
"v_add_u32 v243, 0x8000, v227\n"
"v_mfma_f32_16x16x32_bf16 v[28:31], v[108:111], a[56:59], v[28:31]\n"
"ds_read_b64_tr_b16 v[102:103], v221 offset:12288\n"
"ds_read_b64_tr_b16 v[68:69], v221 offset:16384\n"
"s_add_u32 s70, s70, 0x20000\n"
"s_waitcnt lgkmcnt(15)\n"
"v_mfma_f32_16x16x32_bf16 v[32:35], v[112:115], a[248:251], v[32:35]\n"
"ds_read_b64_tr_b16 v[70:71], v221 offset:20480\n"
"buffer_load_dwordx4 a[192:195], v214, s[36:39], s69 offen sc1\n"
"v_mfma_f32_16x16x32_bf16 v[32:35], v[80:83], a[252:255], v[32:35]\n"
"v_mfma_f32_16x16x32_bf16 v[36:39], v[80:83], a[60:63], v[36:39]\n"
"ds_read_b64_tr_b16 v[104:105], v222 offset:8192\n"
"s_add_u32 s69, s69, 0x20000\n"
"v_mfma_f32_16x16x32_bf16 v[36:39], v[112:115], a[56:59], v[36:39]\n"
"ds_read_b64_tr_b16 v[106:107], v222 offset:12288\n"
"ds_read_b64_tr_b16 v[72:73], v222 offset:16384\n"
"buffer_load_dwordx4 a[196:199], v214, s[36:39], s69 offen sc1\n"
"v_mfma_f32_16x16x32_bf16 v[40:43], v[116:119], a[248:251], v[40:43]\n"
"ds_read_b64_tr_b16 v[74:75], v222 offset:20480\n"
"v_mfma_f32_16x16x32_bf16 v[40:43], v[84:87], a[252:255], v[40:43]\n"
"s_add_u32 s69, s69, 0x20000\n"
"v_mfma_f32_16x16x32_bf16 v[44:47], v[84:87], a[60:63], v[44:47]\n"
"ds_read_b64_tr_b16 v[108:109], v223 offset:8192\n"
"s_add_u32 s82, s66, 0x80000\n"
"s_add_u32 s83, s82, 0x4000\n"
"s_add_u32 m0, s67, 0x20000\n"
"v_mfma_f32_16x16x32_bf16 v[44:47], v[116:119], a[56:59], v[44:47]\n"
"ds_read_b64_tr_b16 v[110:111], v223 offset:12288\n"
"ds_read_b64_tr_b16 v[76:77], v223 offset:16384\n"
"buffer_load_dwordx4 v216, s[20:23], s82 offen lds\n"
"s_waitcnt lgkmcnt(15)\n"
"v_mfma_f32_16x16x32_bf16 v[48:51], v[120:123], a[248:251], v[48:51]\n"
"ds_read_b64_tr_b16 v[78:79], v223 offset:20480\n"
"v_mfma_f32_16x16x32_bf16 v[48:51], v[88:91], a[252:255], v[48:51]\n"
"v_mfma_f32_16x16x32_bf16 v[52:55], v[88:91], a[60:63], v[52:55]\n"
"ds_read_b64_tr_b16 v[112:113], v224 offset:8192\n"
"buffer_load_dwordx4 v217, s[20:23], s82 offen offset:1024 lds\n"
"v_mfma_f32_16x16x32_bf16 v[52:55], v[120:123], a[56:59], v[52:55]\n"
"ds_read_b64_tr_b16 v[114:115], v224 offset:12288\n"
"ds_read_b64_tr_b16 v[80:81], v224 offset:16384\n"
"v_mfma_f32_16x16x32_bf16 v[56:59], v[124:127], a[248:251], v[56:59]\n"
"ds_read_b64_tr_b16 v[82:83], v224 offset:20480\n"
"v_mfma_f32_16x16x32_bf16 v[56:59], v[92:95], a[252:255], v[56:59]\n"
"v_mfma_f32_16x16x32_bf16 v[60:63], v[92:95], a[60:63], v[60:63]\n"
"ds_read_b64_tr_b16 v[116:117], v225 offset:8192\n"
"buffer_load_dwordx4 v218, s[20:23], s82 offen offset:2048 lds\n"
"v_mfma_f32_16x16x32_bf16 v[60:63], v[124:127], a[56:59], v[60:63]\n"
"ds_read_b64_tr_b16 v[118:119], v225 offset:12288\n"
"ds_read_b64_tr_b16 v[84:85], v225 offset:16384\n"
"s_waitcnt vmcnt(19) lgkmcnt(15)\n"
"v_mfma_f32_16x16x32_bf16 v[0:3], v[96:99], a[176:179], v[0:3]\n"
"ds_read_b64_tr_b16 v[86:87], v225 offset:20480\n"
"v_mfma_f32_16x16x32_bf16 v[0:3], v[64:67], a[180:183], v[0:3]\n"
"buffer_load_dwordx4 v219, s[20:23], s82 offen offset:3072 lds\n"
"v_mfma_f32_16x16x32_bf16 v[4:7], v[64:67], a[132:135], v[4:7]\n"
"ds_read_b64_tr_b16 v[120:121], v226 offset:8192\n"
"v_mfma_f32_16x16x32_bf16 v[4:7], v[96:99], a[128:131], v[4:7]\n"
"ds_read_b64_tr_b16 v[122:123], v226 offset:12288\n"
"ds_read_b64_tr_b16 v[88:89], v226 offset:16384\n"
"v_mfma_f32_16x16x32_bf16 v[8:11], v[100:103], a[176:179], v[8:11]\n"
"ds_read_b64_tr_b16 v[90:91], v226 offset:20480\n"
"v_mfma_f32_16x16x32_bf16 v[8:11], v[68:71], a[180:183], v[8:11]\n"
"s_add_u32 m0, s67, 0x21000\n"
"v_mfma_f32_16x16x32_bf16 v[12:15], v[68:71], a[132:135], v[12:15]\n"
"ds_read_b64_tr_b16 v[124:125], v227 offset:8192\n"
"buffer_load_dwordx4 v216, s[20:23], s83 offen lds\n"
"v_mfma_f32_16x16x32_bf16 v[12:15], v[100:103], a[128:131], v[12:15]\n"
"ds_read_b64_tr_b16 v[126:127], v227 offset:12288\n"
"ds_read_b64_tr_b16 v[92:93], v227 offset:16384\n"
"s_waitcnt lgkmcnt(15)\n"
"v_mfma_f32_16x16x32_bf16 v[16:19], v[104:107], a[176:179], v[16:19]\n"
"ds_read_b64_tr_b16 v[94:95], v227 offset:20480\n"
"v_mfma_f32_16x16x32_bf16 v[16:19], v[72:75], a[180:183], v[16:19]\n"
"buffer_load_dwordx4 v217, s[20:23], s83 offen offset:1024 lds\n"
"v_mfma_f32_16x16x32_bf16 v[20:23], v[72:75], a[132:135], v[20:23]\n"
"s_waitcnt vmcnt(14)\n"
"s_barrier\n"
"ds_read_b64_tr_b16 v[96:97], v220 offset:24576\n"
"v_mfma_f32_16x16x32_bf16 v[20:23], v[104:107], a[128:131], v[20:23]\n"
"ds_read_b64_tr_b16 v[98:99], v220 offset:28672\n"
"ds_read_b64_tr_b16 v[64:65], v236\n"
"v_mfma_f32_16x16x32_bf16 v[24:27], v[108:111], a[176:179], v[24:27]\n"
"ds_read_b64_tr_b16 v[66:67], v236 offset:4096\n"
"v_mfma_f32_16x16x32_bf16 v[24:27], v[76:79], a[180:183], v[24:27]\n"
"buffer_load_dwordx4 v218, s[20:23], s83 offen offset:2048 lds\n"
"v_mfma_f32_16x16x32_bf16 v[28:31], v[76:79], a[132:135], v[28:31]\n"
"ds_read_b64_tr_b16 v[100:101], v221 offset:24576\n"
"v_mfma_f32_16x16x32_bf16 v[28:31], v[108:111], a[128:131], v[28:31]\n"
"ds_read_b64_tr_b16 v[102:103], v221 offset:28672\n"
"ds_read_b64_tr_b16 v[68:69], v237\n"
"s_waitcnt lgkmcnt(15)\n"
"v_mfma_f32_16x16x32_bf16 v[32:35], v[112:115], a[176:179], v[32:35]\n"
"ds_read_b64_tr_b16 v[70:71], v237 offset:4096\n"
"buffer_load_dwordx4 v219, s[20:23], s83 offen offset:3072 lds\n"
"v_mfma_f32_16x16x32_bf16 v[32:35], v[80:83], a[180:183], v[32:35]\n"
"v_mfma_f32_16x16x32_bf16 v[36:39], v[80:83], a[132:135], v[36:39]\n"
"ds_read_b64_tr_b16 v[104:105], v222 offset:24576\n"
"v_mfma_f32_16x16x32_bf16 v[36:39], v[112:115], a[128:131], v[36:39]\n"
"ds_read_b64_tr_b16 v[106:107], v222 offset:28672\n"
"ds_read_b64_tr_b16 v[72:73], v238\n"
"v_mfma_f32_16x16x32_bf16 v[40:43], v[116:119], a[176:179], v[40:43]\n"
"ds_read_b64_tr_b16 v[74:75], v238 offset:4096\n"
"buffer_load_dwordx4 v[136:139], v215, s[36:39], s70 offen sc1\n"
"v_mfma_f32_16x16x32_bf16 v[40:43], v[84:87], a[180:183], v[40:43]\n"
"v_mfma_f32_16x16x32_bf16 v[44:47], v[84:87], a[132:135], v[44:47]\n"
"ds_read_b64_tr_b16 v[108:109], v223 offset:24576\n"
"s_add_u32 s70, s70, 0x20000\n"
"v_mfma_f32_16x16x32_bf16 v[44:47], v[116:119], a[128:131], v[44:47]\n"
"ds_read_b64_tr_b16 v[110:111], v223 offset:28672\n"
"ds_read_b64_tr_b16 v[76:77], v239\n"
"buffer_load_dwordx4 v[140:143], v215, s[36:39], s70 offen sc1\n"
"s_waitcnt lgkmcnt(15)\n"
"v_mfma_f32_16x16x32_bf16 v[48:51], v[120:123], a[176:179], v[48:51]\n"
"ds_read_b64_tr_b16 v[78:79], v239 offset:4096\n"
"v_mfma_f32_16x16x32_bf16 v[48:51], v[88:91], a[180:183], v[48:51]\n"
"s_add_u32 s70, s70, 0x20000\n"
"v_mfma_f32_16x16x32_bf16 v[52:55], v[88:91], a[132:135], v[52:55]\n"
"ds_read_b64_tr_b16 v[112:113], v224 offset:24576\n"
"buffer_load_dwordx4 a[200:203], v214, s[36:39], s69 offen sc1\n"
"v_mfma_f32_16x16x32_bf16 v[52:55], v[120:123], a[128:131], v[52:55]\n"
"ds_read_b64_tr_b16 v[114:115], v224 offset:28672\n"
"ds_read_b64_tr_b16 v[80:81], v240\n"
"v_mfma_f32_16x16x32_bf16 v[56:59], v[124:127], a[176:179], v[56:59]\n"
"ds_read_b64_tr_b16 v[82:83], v240 offset:4096\n"
"s_add_u32 s69, s69, 0x20000\n"
"v_mfma_f32_16x16x32_bf16 v[56:59], v[92:95], a[180:183], v[56:59]\n"
"buffer_load_dwordx4 a[204:207], v214, s[36:39], s69 offen sc1\n"
"v_mfma_f32_16x16x32_bf16 v[60:63], v[92:95], a[132:135], v[60:63]\n"
"ds_read_b64_tr_b16 v[116:117], v225 offset:24576\n"
"v_mfma_f32_16x16x32_bf16 v[60:63], v[124:127], a[128:131], v[60:63]\n"
"ds_read_b64_tr_b16 v[118:119], v225 offset:28672\n"
"ds_read_b64_tr_b16 v[84:85], v241\n"
"s_add_u32 s69, s69, 0x20000\n"
"s_waitcnt vmcnt(16) lgkmcnt(15)\n"
"v_mfma_f32_16x16x32_bf16 v[0:3], v[96:99], a[184:187], v[0:3]\n"
"ds_read_b64_tr_b16 v[86:87], v241 offset:4096\n"
"buffer_load_dwordx4 v[144:147], v215, s[36:39], s70 offen sc1\n"
"v_mfma_f32_16x16x32_bf16 v[0:3], v[64:67], a[188:191], v[0:3]\n"
"v_mfma_f32_16x16x32_bf16 v[4:7], v[64:67], a[140:143], v[4:7]\n"
"ds_read_b64_tr_b16 v[120:121], v226 offset:24576\n"
"s_add_u32 s70, s70, 0x20000\n"
"v_mfma_f32_16x16x32_bf16 v[4:7], v[96:99], a[136:139], v[4:7]\n"
"ds_read_b64_tr_b16 v[122:123], v226 offset:28672\n"
"ds_read_b64_tr_b16 v[88:89], v242\n"
"buffer_load_dwordx4 v[148:151], v215, s[36:39], s70 offen sc1\n"
"v_mfma_f32_16x16x32_bf16 v[8:11], v[100:103], a[184:187], v[8:11]\n"
"ds_read_b64_tr_b16 v[90:91], v242 offset:4096\n"
"v_mfma_f32_16x16x32_bf16 v[8:11], v[68:71], a[188:191], v[8:11]\n"
"s_add_u32 s70, s70, 0x20000\n"
"v_mfma_f32_16x16x32_bf16 v[12:15], v[68:71], a[140:143], v[12:15]\n"
"ds_read_b64_tr_b16 v[124:125], v227 offset:24576\n"
"s_add_u32 s82, s66, 0xa0000\n"
"s_add_u32 s83, s82, 0x4000\n"
"s_add_u32 m0, s67, 0\n"
"v_mfma_f32_16x16x32_bf16 v[12:15], v[100:103], a[136:139], v[12:15]\n"
"ds_read_b64_tr_b16 v[126:127], v227 offset:28672\n"
"ds_read_b64_tr_b16 v[92:93], v243\n"
"buffer_load_dwordx4 v216, s[20:23], s82 offen lds\n"
"s_waitcnt lgkmcnt(15)\n"
"v_mfma_f32_16x16x32_bf16 v[16:19], v[104:107], a[184:187], v[16:19]\n"
"ds_read_b64_tr_b16 v[94:95], v243 offset:4096\n"
"v_add_u32 v220, 0x8000, v236\n"
"v_mfma_f32_16x16x32_bf16 v[16:19], v[72:75], a[188:191], v[16:19]\n"
"v_add_u32 v221, 0x8000, v237\n"
"v_add_u32 v222, 0x8000, v238\n"
"v_mfma_f32_16x16x32_bf16 v[20:23], v[72:75], a[140:143], v[20:23]\n"
"ds_read_b64_tr_b16 v[96:97], v236 offset:8192\n"
"buffer_load_dwordx4 v217, s[20:23], s82 offen offset:1024 lds\n"
"v_add_u32 v223, 0x8000, v239\n"
"v_mfma_f32_16x16x32_bf16 v[20:23], v[104:107], a[136:139], v[20:23]\n"
"ds_read_b64_tr_b16 v[98:99], v236 offset:12288\n"
"ds_read_b64_tr_b16 v[64:65], v236 offset:16384\n"
"v_mfma_f32_16x16x32_bf16 v[24:27], v[108:111], a[184:187], v[24:27]\n"
"ds_read_b64_tr_b16 v[66:67], v236 offset:20480\n"
"v_add_u32 v224, 0x8000, v240\n"
"v_mfma_f32_16x16x32_bf16 v[24:27], v[76:79], a[188:191], v[24:27]\n"
"v_add_u32 v225, 0x8000, v241\n"
"v_add_u32 v226, 0x8000, v242\n"
"v_mfma_f32_16x16x32_bf16 v[28:31], v[76:79], a[140:143], v[28:31]\n"
"ds_read_b64_tr_b16 v[100:101], v237 offset:8192\n"
"buffer_load_dwordx4 v218, s[20:23], s82 offen offset:2048 lds\n"
"v_add_u32 v227, 0x8000, v243\n"
"v_mfma_f32_16x16x32_bf16 v[28:31], v[108:111], a[136:139], v[28:31]\n"
"ds_read_b64_tr_b16 v[102:103], v237 offset:12288\n"
"ds_read_b64_tr_b16 v[68:69], v237 offset:16384\n"
"s_waitcnt lgkmcnt(15)\n"
"v_mfma_f32_16x16x32_bf16 v[32:35], v[112:115], a[184:187], v[32:35]\n"
"ds_read_b64_tr_b16 v[70:71], v237 offset:20480\n"
"v_mfma_f32_16x16x32_bf16 v[32:35], v[80:83], a[188:191], v[32:35]\n"
"buffer_load_dwordx4 v219, s[20:23], s82 offen offset:3072 lds\n"
"v_mfma_f32_16x16x32_bf16 v[36:39], v[80:83], a[140:143], v[36:39]\n"
"ds_read_b64_tr_b16 v[104:105], v238 offset:8192\n"
"v_mfma_f32_16x16x32_bf16 v[36:39], v[112:115], a[136:139], v[36:39]\n"
"ds_read_b64_tr_b16 v[106:107], v238 offset:12288\n"
"ds_read_b64_tr_b16 v[72:73], v238 offset:16384\n"
"v_mfma_f32_16x16x32_bf16 v[40:43], v[116:119], a[184:187], v[40:43]\n"
"ds_read_b64_tr_b16 v[74:75], v238 offset:20480\n"
"v_mfma_f32_16x16x32_bf16 v[40:43], v[84:87], a[188:191], v[40:43]\n"
"s_add_u32 m0, s67, 0x1000\n"
"v_mfma_f32_16x16x32_bf16 v[44:47], v[84:87], a[140:143], v[44:47]\n"
"ds_read_b64_tr_b16 v[108:109], v239 offset:8192\n"
"buffer_load_dwordx4 v216, s[20:23], s83 offen lds\n"
"v_mfma_f32_16x16x32_bf16 v[44:47], v[116:119], a[136:139], v[44:47]\n"
"ds_read_b64_tr_b16 v[110:111], v239 offset:12288\n"
"ds_read_b64_tr_b16 v[76:77], v239 offset:16384\n"
"s_waitcnt lgkmcnt(15)\n"
"v_mfma_f32_16x16x32_bf16 v[48:51], v[120:123], a[184:187], v[48:51]\n"
"ds_read_b64_tr_b16 v[78:79], v239 offset:20480\n"
"v_mfma_f32_16x16x32_bf16 v[48:51], v[88:91], a[188:191], v[48:51]\n"
"buffer_load_dwordx4 v217, s[20:23], s83 offen offset:1024 lds\n"
"v_mfma_f32_16x16x32_bf16 v[52:55], v[88:91], a[140:143], v[52:55]\n"
"ds_read_b64_tr_b16 v[112:113], v240 offset:8192\n"
"v_mfma_f32_16x16x32_bf16 v[52:55], v[120:123], a[136:139], v[52:55]\n"
"ds_read_b64_tr_b16 v[114:115], v240 offset:12288\n"
"ds_read_b64_tr_b16 v[80:81], v240 offset:16384\n"
"v_mfma_f32_16x16x32_bf16 v[56:59], v[124:127], a[184:187], v[56:59]\n"
"ds_read_b64_tr_b16 v[82:83], v240 offset:20480\n"
"v_mfma_f32_16x16x32_bf16 v[56:59], v[92:95], a[188:191], v[56:59]\n"
"buffer_load_dwordx4 v218, s[20:23], s83 offen offset:2048 lds\n"
"v_mfma_f32_16x16x32_bf16 v[60:63], v[92:95], a[140:143], v[60:63]\n"
"ds_read_b64_tr_b16 v[116:117], v241 offset:8192\n"
"v_mfma_f32_16x16x32_bf16 v[60:63], v[124:127], a[136:139], v[60:63]\n"
"ds_read_b64_tr_b16 v[118:119], v241 offset:12288\n"
"ds_read_b64_tr_b16 v[84:85], v241 offset:16384\n"
"s_waitcnt vmcnt(21) lgkmcnt(15)\n"
"v_mfma_f32_16x16x32_bf16 v[0:3], v[96:99], a[192:195], v[0:3]\n"
"ds_read_b64_tr_b16 v[86:87], v241 offset:20480\n"
"buffer_load_dwordx4 v219, s[20:23], s83 offen offset:3072 lds\n"
"v_mfma_f32_16x16x32_bf16 v[0:3], v[64:67], a[196:199], v[0:3]\n"
"v_mfma_f32_16x16x32_bf16 v[4:7], v[64:67], v[132:135], v[4:7]\n"
"ds_read_b64_tr_b16 v[120:121], v242 offset:8192\n"
"v_mfma_f32_16x16x32_bf16 v[4:7], v[96:99], v[128:131], v[4:7]\n"
"ds_read_b64_tr_b16 v[122:123], v242 offset:12288\n"
"ds_read_b64_tr_b16 v[88:89], v242 offset:16384\n"
"v_mfma_f32_16x16x32_bf16 v[8:11], v[100:103], a[192:195], v[8:11]\n"
"ds_read_b64_tr_b16 v[90:91], v242 offset:20480\n"
"buffer_load_dwordx4 a[208:211], v214, s[36:39], s69 offen sc1\n"
"v_mfma_f32_16x16x32_bf16 v[8:11], v[68:71], a[196:199], v[8:11]\n"
"v_mfma_f32_16x16x32_bf16 v[12:15], v[68:71], v[132:135], v[12:15]\n"
"ds_read_b64_tr_b16 v[124:125], v243 offset:8192\n"
"s_add_u32 s69, s69, 0x20000\n"
"v_mfma_f32_16x16x32_bf16 v[12:15], v[100:103], v[128:131], v[12:15]\n"
"ds_read_b64_tr_b16 v[126:127], v243 offset:12288\n"
"ds_read_b64_tr_b16 v[92:93], v243 offset:16384\n"
"buffer_load_dwordx4 a[212:215], v214, s[36:39], s69 offen sc1\n"
"s_waitcnt lgkmcnt(15)\n"
"v_mfma_f32_16x16x32_bf16 v[16:19], v[104:107], a[192:195], v[16:19]\n"
"ds_read_b64_tr_b16 v[94:95], v243 offset:20480\n"
"v_mfma_f32_16x16x32_bf16 v[16:19], v[72:75], a[196:199], v[16:19]\n"
"s_add_u32 s69, s69, 0x20000\n"
"v_mfma_f32_16x16x32_bf16 v[20:23], v[72:75], v[132:135], v[20:23]\n"
"s_waitcnt vmcnt(16)\n"
"s_barrier\n"
"ds_read_b64_tr_b16 v[96:97], v236 offset:24576\n"
"buffer_load_dwordx4 v[152:155], v215, s[36:39], s70 offen sc1\n"
"v_mfma_f32_16x16x32_bf16 v[20:23], v[104:107], v[128:131], v[20:23]\n"
"ds_read_b64_tr_b16 v[98:99], v236 offset:28672\n"
"ds_read_b64_tr_b16 v[64:65], v220\n"
"v_mfma_f32_16x16x32_bf16 v[24:27], v[108:111], a[192:195], v[24:27]\n"
"ds_read_b64_tr_b16 v[66:67], v220 offset:4096\n"
"s_add_u32 s70, s70, 0x20000\n"
"v_mfma_f32_16x16x32_bf16 v[24:27], v[76:79], a[196:199], v[24:27]\n"
"buffer_load_dwordx4 v[156:159], v215, s[36:39], s70 offen sc1\n"
"v_mfma_f32_16x16x32_bf16 v[28:31], v[76:79], v[132:135], v[28:31]\n"
"ds_read_b64_tr_b16 v[100:101], v237 offset:24576\n"
"v_mfma_f32_16x16x32_bf16 v[28:31], v[108:111], v[128:131], v[28:31]\n"
"ds_read_b64_tr_b16 v[102:103], v237 offset:28672\n"
"ds_read_b64_tr_b16 v[68:69], v221\n"
"s_add_u32 s70, s70, 0x20000\n"
"s_waitcnt lgkmcnt(15)\n"
"v_mfma_f32_16x16x32_bf16 v[32:35], v[112:115], a[192:195], v[32:35]\n"
"ds_read_b64_tr_b16 v[70:71], v221 offset:4096\n"
"buffer_load_dwordx4 a[216:219], v214, s[36:39], s69 offen sc1\n"
"v_mfma_f32_16x16x32_bf16 v[32:35], v[80:83], a[196:199], v[32:35]\n"
"v_mfma_f32_16x16x32_bf16 v[36:39], v[80:83], v[132:135], v[36:39]\n"
"ds_read_b64_tr_b16 v[104:105], v238 offset:24576\n"
"s_add_u32 s69, s69, 0x20000\n"
"v_mfma_f32_16x16x32_bf16 v[36:39], v[112:115], v[128:131], v[36:39]\n"
"ds_read_b64_tr_b16 v[106:107], v238 offset:28672\n"
"ds_read_b64_tr_b16 v[72:73], v222\n"
"buffer_load_dwordx4 a[220:223], v214, s[36:39], s69 offen sc1\n"
"v_mfma_f32_16x16x32_bf16 v[40:43], v[116:119], a[192:195], v[40:43]\n"
"ds_read_b64_tr_b16 v[74:75], v222 offset:4096\n"
"v_mfma_f32_16x16x32_bf16 v[40:43], v[84:87], a[196:199], v[40:43]\n"
"s_add_u32 s69, s69, 0x20000\n"
"v_mfma_f32_16x16x32_bf16 v[44:47], v[84:87], v[132:135], v[44:47]\n"
"ds_read_b64_tr_b16 v[108:109], v239 offset:24576\n"
"buffer_load_dwordx4 v[160:163], v215, s[36:39], s70 offen sc1\n"
"v_mfma_f32_16x16x32_bf16 v[44:47], v[116:119], v[128:131], v[44:47]\n"
"ds_read_b64_tr_b16 v[110:111], v239 offset:28672\n"
"ds_read_b64_tr_b16 v[76:77], v223\n"
"s_waitcnt lgkmcnt(15)\n"
"v_mfma_f32_16x16x32_bf16 v[48:51], v[120:123], a[192:195], v[48:51]\n"
"ds_read_b64_tr_b16 v[78:79], v223 offset:4096\n"
"s_add_u32 s70, s70, 0x20000\n"
"v_mfma_f32_16x16x32_bf16 v[48:51], v[88:91], a[196:199], v[48:51]\n"
"buffer_load_dwordx4 v[164:167], v215, s[36:39], s70 offen sc1\n"
"v_mfma_f32_16x16x32_bf16 v[52:55], v[88:91], v[132:135], v[52:55]\n"
"ds_read_b64_tr_b16 v[112:113], v240 offset:24576\n"
"v_mfma_f32_16x16x32_bf16 v[52:55], v[120:123], v[128:131], v[52:55]\n"
"ds_read_b64_tr_b16 v[114:115], v240 offset:28672\n"
"ds_read_b64_tr_b16 v[80:81], v224\n"
"s_add_u32 s70, s70, 0x20000\n"
"v_mfma_f32_16x16x32_bf16 v[56:59], v[124:127], a[192:195], v[56:59]\n"
"ds_read_b64_tr_b16 v[82:83], v224 offset:4096\n"
"s_add_u32 s82, s66, 0xc0000\n"
"s_add_u32 s83, s82, 0x4000\n"
"s_add_u32 m0, s67, 0x8000\n"
"v_mfma_f32_16x16x32_bf16 v[56:59], v[92:95], a[196:199], v[56:59]\n"
"buffer_load_dwordx4 v216, s[20:23], s82 offen lds\n"
"v_mfma_f32_16x16x32_bf16 v[60:63], v[92:95], v[132:135], v[60:63]\n"
"ds_read_b64_tr_b16 v[116:117], v241 offset:24576\n"
"v_mfma_f32_16x16x32_bf16 v[60:63], v[124:127], v[128:131], v[60:63]\n"
"ds_read_b64_tr_b16 v[118:119], v241 offset:28672\n"
"ds_read_b64_tr_b16 v[84:85], v225\n"
"s_waitcnt vmcnt(19) lgkmcnt(15)\n"
"v_mfma_f32_16x16x32_bf16 v[0:3], v[96:99], a[200:203], v[0:3]\n"
"ds_read_b64_tr_b16 v[86:87], v225 offset:4096\n"
"buffer_load_dwordx4 v217, s[20:23], s82 offen offset:1024 lds\n"
"v_mfma_f32_16x16x32_bf16 v[0:3], v[64:67], a[204:207], v[0:3]\n"
"v_mfma_f32_16x16x32_bf16 v[4:7], v[64:67], v[140:143], v[4:7]\n"
"ds_read_b64_tr_b16 v[120:121], v242 offset:24576\n"
"v_mfma_f32_16x16x32_bf16 v[4:7], v[96:99], v[136:139], v[4:7]\n"
"ds_read_b64_tr_b16 v[122:123], v242 offset:28672\n"
"ds_read_b64_tr_b16 v[88:89], v226\n"
"v_mfma_f32_16x16x32_bf16 v[8:11], v[100:103], a[200:203], v[8:11]\n"
"ds_read_b64_tr_b16 v[90:91], v226 offset:4096\n"
"buffer_load_dwordx4 v218, s[20:23], s82 offen offset:2048 lds\n"
"v_mfma_f32_16x16x32_bf16 v[8:11], v[68:71], a[204:207], v[8:11]\n"
"v_mfma_f32_16x16x32_bf16 v[12:15], v[68:71], v[140:143], v[12:15]\n"
"ds_read_b64_tr_b16 v[124:125], v243 offset:24576\n"
"v_mfma_f32_16x16x32_bf16 v[12:15], v[100:103], v[136:139], v[12:15]\n"
"ds_read_b64_tr_b16 v[126:127], v243 offset:28672\n"
"ds_read_b64_tr_b16 v[92:93], v227\n"
"buffer_load_dwordx4 v219, s[20:23], s82 offen offset:3072 lds\n"
"s_waitcnt lgkmcnt(15)\n"
"v_mfma_f32_16x16x32_bf16 v[16:19], v[104:107], a[200:203], v[16:19]\n"
"ds_read_b64_tr_b16 v[94:95], v227 offset:4096\n"
"v_add_u32 v236, 0xfffe0000, v220\n"
"v_mfma_f32_16x16x32_bf16 v[16:19], v[72:75], a[204:207], v[16:19]\n"
"v_add_u32 v237, 0xfffe0000, v221\n"
"v_add_u32 v238, 0xfffe0000, v222\n"
"v_mfma_f32_16x16x32_bf16 v[20:23], v[72:75], v[140:143], v[20:23]\n"
"ds_read_b64_tr_b16 v[96:97], v220 offset:8192\n"
"v_add_u32 v239, 0xfffe0000, v223\n"
"v_mfma_f32_16x16x32_bf16 v[20:23], v[104:107], v[136:139], v[20:23]\n"
"ds_read_b64_tr_b16 v[98:99], v220 offset:12288\n"
"ds_read_b64_tr_b16 v[64:65], v220 offset:16384\n"
"s_add_u32 m0, s67, 0x9000\n"
"v_mfma_f32_16x16x32_bf16 v[24:27], v[108:111], a[200:203], v[24:27]\n"
"ds_read_b64_tr_b16 v[66:67], v220 offset:20480\n"
"buffer_load_dwordx4 v216, s[20:23], s83 offen lds\n"
"v_add_u32 v240, 0xfffe0000, v224\n"
"v_mfma_f32_16x16x32_bf16 v[24:27], v[76:79], a[204:207], v[24:27]\n"
"v_add_u32 v241, 0xfffe0000, v225\n"
"v_mfma_f32_16x16x32_bf16 v[28:31], v[76:79], v[140:143], v[28:31]\n"
"ds_read_b64_tr_b16 v[100:101], v221 offset:8192\n"
"v_mfma_f32_16x16x32_bf16 v[28:31], v[108:111], v[136:139], v[28:31]\n"
"ds_read_b64_tr_b16 v[102:103], v221 offset:12288\n"
"ds_read_b64_tr_b16 v[68:69], v221 offset:16384\n"
"buffer_load_dwordx4 v217, s[20:23], s83 offen offset:1024 lds\n"
"s_waitcnt lgkmcnt(15)\n"
"v_mfma_f32_16x16x32_bf16 v[32:35], v[112:115], a[200:203], v[32:35]\n"
"ds_read_b64_tr_b16 v[70:71], v221 offset:20480\n"
"v_mfma_f32_16x16x32_bf16 v[32:35], v[80:83], a[204:207], v[32:35]\n"
"v_mfma_f32_16x16x32_bf16 v[36:39], v[80:83], v[140:143], v[36:39]\n"
"ds_read_b64_tr_b16 v[104:105], v222 offset:8192\n"
"v_mfma_f32_16x16x32_bf16 v[36:39], v[112:115], v[136:139], v[36:39]\n"
"ds_read_b64_tr_b16 v[106:107], v222 offset:12288\n"
"ds_read_b64_tr_b16 v[72:73], v222 offset:16384\n"
"buffer_load_dwordx4 v218, s[20:23], s83 offen offset:2048 lds\n"
"v_mfma_f32_16x16x32_bf16 v[40:43], v[116:119], a[200:203], v[40:43]\n"
"ds_read_b64_tr_b16 v[74:75], v222 offset:20480\n"
"v_mfma_f32_16x16x32_bf16 v[40:43], v[84:87], a[204:207], v[40:43]\n"
"v_mfma_f32_16x16x32_bf16 v[44:47], v[84:87], v[140:143], v[44:47]\n"
"ds_read_b64_tr_b16 v[108:109], v223 offset:8192\n"
"buffer_load_dwordx4 v219, s[20:23], s83 offen offset:3072 lds\n"
"v_mfma_f32_16x16x32_bf16 v[44:47], v[116:119], v[136:139], v[44:47]\n"
"ds_read_b64_tr_b16 v[110:111], v223 offset:12288\n"
"ds_read_b64_tr_b16 v[76:77], v223 offset:16384\n"
"s_waitcnt lgkmcnt(15)\n"
"v_mfma_f32_16x16x32_bf16 v[48:51], v[120:123], a[200:203], v[48:51]\n"
"ds_read_b64_tr_b16 v[78:79], v223 offset:20480\n"
"v_mfma_f32_16x16x32_bf16 v[48:51], v[88:91], a[204:207], v[48:51]\n"
"v_mfma_f32_16x16x32_bf16 v[52:55], v[88:91], v[140:143], v[52:55]\n"
"ds_read_b64_tr_b16 v[112:113], v224 offset:8192\n"
"buffer_load_dwordx4 a[224:227], v214, s[36:39], s69 offen sc1\n"
"v_mfma_f32_16x16x32_bf16 v[52:55], v[120:123], v[136:139], v[52:55]\n"
"ds_read_b64_tr_b16 v[114:115], v224 offset:12288\n"
"ds_read_b64_tr_b16 v[80:81], v224 offset:16384\n"
"v_mfma_f32_16x16x32_bf16 v[56:59], v[124:127], a[200:203], v[56:59]\n"
"ds_read_b64_tr_b16 v[82:83], v224 offset:20480\n"
"s_add_u32 s69, s69, 0x20000\n"
"v_mfma_f32_16x16x32_bf16 v[56:59], v[92:95], a[204:207], v[56:59]\n"
"buffer_load_dwordx4 a[228:231], v214, s[36:39], s69 offen sc1\n"
"v_mfma_f32_16x16x32_bf16 v[60:63], v[92:95], v[140:143], v[60:63]\n"
"ds_read_b64_tr_b16 v[116:117], v225 offset:8192\n"
"v_mfma_f32_16x16x32_bf16 v[60:63], v[124:127], v[136:139], v[60:63]\n"
"ds_read_b64_tr_b16 v[118:119], v225 offset:12288\n"
"ds_read_b64_tr_b16 v[84:85], v225 offset:16384\n"
"s_add_u32 s69, s69, 0x20000\n"
"s_waitcnt vmcnt(16) lgkmcnt(15)\n"
"v_mfma_f32_16x16x32_bf16 v[0:3], v[96:99], a[208:211], v[0:3]\n"
"ds_read_b64_tr_b16 v[86:87], v225 offset:20480\n"
"buffer_load_dwordx4 v[168:171], v215, s[36:39], s70 offen sc1\n"
"v_mfma_f32_16x16x32_bf16 v[0:3], v[64:67], a[212:215], v[0:3]\n"
"v_mfma_f32_16x16x32_bf16 v[4:7], v[64:67], v[148:151], v[4:7]\n"
"s_waitcnt vmcnt(19)\n"
"s_barrier\n"
"s_add_u32 s70, s70, 0x20000\n"
"v_mfma_f32_16x16x32_bf16 v[4:7], v[96:99], v[144:147], v[4:7]\n"
"ds_read_b64_tr_b16 v[64:65], v236\n"
"buffer_load_dwordx4 v[172:175], v215, s[36:39], s70 offen sc1\n"
"v_mfma_f32_16x16x32_bf16 v[8:11], v[100:103], a[208:211], v[8:11]\n"
"ds_read_b64_tr_b16 v[66:67], v236 offset:4096\n"
"ds_read_b64_tr_b16 v[96:97], v220 offset:24576\n"
"v_mfma_f32_16x16x32_bf16 v[8:11], v[68:71], a[212:215], v[8:11]\n"
"ds_read_b64_tr_b16 v[98:99], v220 offset:28672\n"
"s_add_u32 s70, s70, 0x20000\n"
"v_mfma_f32_16x16x32_bf16 v[12:15], v[68:71], v[148:151], v[12:15]\n"
"buffer_load_dwordx4 a[232:235], v214, s[36:39], s69 offen sc1\n"
"v_mfma_f32_16x16x32_bf16 v[12:15], v[100:103], v[144:147], v[12:15]\n"
"ds_read_b64_tr_b16 v[68:69], v237\n"
"s_waitcnt lgkmcnt(13)\n"
"v_mfma_f32_16x16x32_bf16 v[16:19], v[104:107], a[208:211], v[16:19]\n"
"ds_read_b64_tr_b16 v[70:71], v237 offset:4096\n"
"ds_read_b64_tr_b16 v[100:101], v221 offset:24576\n"
"s_add_u32 s69, s69, 0x20000\n"
"v_mfma_f32_16x16x32_bf16 v[16:19], v[72:75], a[212:215], v[16:19]\n"
"ds_read_b64_tr_b16 v[102:103], v221 offset:28672\n"
"buffer_load_dwordx4 a[236:239], v214, s[36:39], s69 offen sc1\n"
"v_mfma_f32_16x16x32_bf16 v[20:23], v[72:75], v[148:151], v[20:23]\n"
"v_mfma_f32_16x16x32_bf16 v[20:23], v[104:107], v[144:147], v[20:23]\n"
"ds_read_b64_tr_b16 v[72:73], v238\n"
"s_add_u32 s69, s69, 0x20000\n"
"v_mfma_f32_16x16x32_bf16 v[24:27], v[108:111], a[208:211], v[24:27]\n"
"ds_read_b64_tr_b16 v[74:75], v238 offset:4096\n"
"ds_read_b64_tr_b16 v[104:105], v222 offset:24576\n"
"buffer_load_dwordx4 v[176:179], v215, s[36:39], s70 offen sc1\n"
"v_mfma_f32_16x16x32_bf16 v[24:27], v[76:79], a[212:215], v[24:27]\n"
"ds_read_b64_tr_b16 v[106:107], v222 offset:28672\n"
"v_mfma_f32_16x16x32_bf16 v[28:31], v[76:79], v[148:151], v[28:31]\n"
"s_add_u32 s70, s70, 0x20000\n"
"v_mfma_f32_16x16x32_bf16 v[28:31], v[108:111], v[144:147], v[28:31]\n"
"ds_read_b64_tr_b16 v[76:77], v239\n"
"buffer_load_dwordx4 v[180:183], v215, s[36:39], s70 offen sc1\n"
"s_waitcnt lgkmcnt(13)\n"
"v_mfma_f32_16x16x32_bf16 v[32:35], v[112:115], a[208:211], v[32:35]\n"
"ds_read_b64_tr_b16 v[78:79], v239 offset:4096\n"
"ds_read_b64_tr_b16 v[108:109], v223 offset:24576\n"
"v_mfma_f32_16x16x32_bf16 v[32:35], v[80:83], a[212:215], v[32:35]\n"
"ds_read_b64_tr_b16 v[110:111], v223 offset:28672\n"
"s_add_u32 s70, s70, 0x20000\n"
"v_mfma_f32_16x16x32_bf16 v[36:39], v[80:83], v[148:151], v[36:39]\n"
"s_add_u32 s82, s66, 0xe0000\n"
"s_add_u32 s83, s82, 0x4000\n"
"s_add_u32 m0, s67, 0x10000\n"
"v_mfma_f32_16x16x32_bf16 v[36:39], v[112:115], v[144:147], v[36:39]\n"
"ds_read_b64_tr_b16 v[80:81], v240\n"
"buffer_load_dwordx4 v216, s[20:23], s82 offen lds\n"
"v_mfma_f32_16x16x32_bf16 v[40:43], v[116:119], a[208:211], v[40:43]\n"
"ds_read_b64_tr_b16 v[82:83], v240 offset:4096\n"
"ds_read_b64_tr_b16 v[112:113], v224 offset:24576\n"
"v_mfma_f32_16x16x32_bf16 v[40:43], v[84:87], a[212:215], v[40:43]\n"
"ds_read_b64_tr_b16 v[114:115], v224 offset:28672\n"
"v_mfma_f32_16x16x32_bf16 v[44:47], v[84:87], v[148:151], v[44:47]\n"
"buffer_load_dwordx4 v217, s[20:23], s82 offen offset:1024 lds\n"
"v_mfma_f32_16x16x32_bf16 v[44:47], v[116:119], v[144:147], v[44:47]\n"
"ds_read_b64_tr_b16 v[84:85], v241\n"
"s_waitcnt vmcnt(20) lgkmcnt(13)\n"
"v_mfma_f32_16x16x32_bf16 v[0:3], v[96:99], a[216:219], v[0:3]\n"
"ds_read_b64_tr_b16 v[86:87], v241 offset:4096\n"
"ds_read_b64_tr_b16 v[116:117], v225 offset:24576\n"
"v_mfma_f32_16x16x32_bf16 v[0:3], v[64:67], a[220:223], v[0:3]\n"
"ds_read_b64_tr_b16 v[118:119], v225 offset:28672\n"
"v_mfma_f32_16x16x32_bf16 v[4:7], v[64:67], v[156:159], v[4:7]\n"
"buffer_load_dwordx4 v218, s[20:23], s82 offen offset:2048 lds\n"
"v_add_u32 v220, 0x8000, v236\n"
"v_add_u32 v221, 0x8000, v237\n"
"v_mfma_f32_16x16x32_bf16 v[4:7], v[96:99], v[152:155], v[4:7]\n"
"ds_read_b64_tr_b16 v[64:65], v236 offset:16384\n"
"v_add_u32 v222, 0x8000, v238\n"
"v_mfma_f32_16x16x32_bf16 v[8:11], v[100:103], a[216:219], v[8:11]\n"
"ds_read_b64_tr_b16 v[66:67], v236 offset:20480\n"
"ds_read_b64_tr_b16 v[96:97], v236 offset:8192\n"
"v_mfma_f32_16x16x32_bf16 v[8:11], v[68:71], a[220:223], v[8:11]\n"
"ds_read_b64_tr_b16 v[98:99], v236 offset:12288\n"
"buffer_load_dwordx4 v219, s[20:23], s82 offen offset:3072 lds\n"
"v_add_u32 v223, 0x8000, v239\n"
"v_mfma_f32_16x16x32_bf16 v[12:15], v[68:71], v[156:159], v[12:15]\n"
"v_add_u32 v224, 0x8000, v240\n"
"v_add_u32 v225, 0x8000, v241\n"
"v_mfma_f32_16x16x32_bf16 v[12:15], v[100:103], v[152:155], v[12:15]\n"
"ds_read_b64_tr_b16 v[68:69], v237 offset:16384\n"
"s_waitcnt lgkmcnt(13)\n"
"v_mfma_f32_16x16x32_bf16 v[16:19], v[104:107], a[216:219], v[16:19]\n"
"ds_read_b64_tr_b16 v[70:71], v237 offset:20480\n"
"ds_read_b64_tr_b16 v[100:101], v237 offset:8192\n"
"v_mfma_f32_16x16x32_bf16 v[16:19], v[72:75], a[220:223], v[16:19]\n"
"ds_read_b64_tr_b16 v[102:103], v237 offset:12288\n"
"s_add_u32 m0, s67, 0x11000\n"
"v_mfma_f32_16x16x32_bf16 v[20:23], v[72:75], v[156:159], v[20:23]\n"
"buffer_load_dwordx4 v216, s[20:23], s83 offen lds\n"
"v_mfma_f32_16x16x32_bf16 v[20:23], v[104:107], v[152:155], v[20:23]\n"
"ds_read_b64_tr_b16 v[72:73], v238 offset:16384\n"
"v_mfma_f32_16x16x32_bf16 v[24:27], v[108:111], a[216:219], v[24:27]\n"
"ds_read_b64_tr_b16 v[74:75], v238 offset:20480\n"
"ds_read_b64_tr_b16 v[104:105], v238 offset:8192\n"
"v_mfma_f32_16x16x32_bf16 v[24:27], v[76:79], a[220:223], v[24:27]\n"
"ds_read_b64_tr_b16 v[106:107], v238 offset:12288\n"
"buffer_load_dwordx4 v217, s[20:23], s83 offen offset:1024 lds\n"
"v_mfma_f32_16x16x32_bf16 v[28:31], v[76:79], v[156:159], v[28:31]\n"
"v_mfma_f32_16x16x32_bf16 v[28:31], v[108:111], v[152:155], v[28:31]\n"
"ds_read_b64_tr_b16 v[76:77], v239 offset:16384\n"
"s_waitcnt lgkmcnt(13)\n"
"v_mfma_f32_16x16x32_bf16 v[32:35], v[112:115], a[216:219], v[32:35]\n"
"ds_read_b64_tr_b16 v[78:79], v239 offset:20480\n"
"ds_read_b64_tr_b16 v[108:109], v239 offset:8192\n"
"v_mfma_f32_16x16x32_bf16 v[32:35], v[80:83], a[220:223], v[32:35]\n"
"ds_read_b64_tr_b16 v[110:111], v239 offset:12288\n"
"buffer_load_dwordx4 v218, s[20:23], s83 offen offset:2048 lds\n"
"v_mfma_f32_16x16x32_bf16 v[36:39], v[80:83], v[156:159], v[36:39]\n"
"v_mfma_f32_16x16x32_bf16 v[36:39], v[112:115], v[152:155], v[36:39]\n"
"ds_read_b64_tr_b16 v[80:81], v240 offset:16384\n"
"v_mfma_f32_16x16x32_bf16 v[40:43], v[116:119], a[216:219], v[40:43]\n"
"ds_read_b64_tr_b16 v[82:83], v240 offset:20480\n"
"ds_read_b64_tr_b16 v[112:113], v240 offset:8192\n"
"buffer_load_dwordx4 v219, s[20:23], s83 offen offset:3072 lds\n"
"v_mfma_f32_16x16x32_bf16 v[40:43], v[84:87], a[220:223], v[40:43]\n"
"ds_read_b64_tr_b16 v[114:115], v240 offset:12288\n"
"v_mfma_f32_16x16x32_bf16 v[44:47], v[84:87], v[156:159], v[44:47]\n"
"v_mfma_f32_16x16x32_bf16 v[44:47], v[116:119], v[152:155], v[44:47]\n"
"ds_read_b64_tr_b16 v[84:85], v241 offset:16384\n"
"s_waitcnt vmcnt(14) lgkmcnt(13)\n"
"v_mfma_f32_16x16x32_bf16 v[0:3], v[96:99], a[224:227], v[0:3]\n"
"ds_read_b64_tr_b16 v[86:87], v241 offset:20480\n"
"ds_read_b64_tr_b16 v[116:117], v241 offset:8192\n"
"buffer_load_dwordx4 a[0:3], v214, s[36:39], s69 offen sc1\n"
"v_mfma_f32_16x16x32_bf16 v[0:3], v[64:67], a[228:231], v[0:3]\n"
"ds_read_b64_tr_b16 v[118:119], v241 offset:12288\n"
"v_mfma_f32_16x16x32_bf16 v[4:7], v[64:67], v[164:167], v[4:7]\n"
"s_waitcnt vmcnt(17)\n"
"s_barrier\n"
"s_add_u32 s69, s69, 0x20000\n"
"v_mfma_f32_16x16x32_bf16 v[4:7], v[96:99], v[160:163], v[4:7]\n"
"ds_read_b64_tr_b16 v[64:65], v220\n"
"buffer_load_dwordx4 a[4:7], v214, s[36:39], s69 offen sc1\n"
"v_mfma_f32_16x16x32_bf16 v[8:11], v[100:103], a[224:227], v[8:11]\n"
"ds_read_b64_tr_b16 v[66:67], v220 offset:4096\n"
"ds_read_b64_tr_b16 v[96:97], v236 offset:24576\n"
"v_mfma_f32_16x16x32_bf16 v[8:11], v[68:71], a[228:231], v[8:11]\n"
"ds_read_b64_tr_b16 v[98:99], v236 offset:28672\n"
"s_add_u32 s69, s69, 0x20000\n"
"v_mfma_f32_16x16x32_bf16 v[12:15], v[68:71], v[164:167], v[12:15]\n"
"buffer_load_dwordx4 v[184:187], v215, s[36:39], s70 offen sc1\n"
"v_mfma_f32_16x16x32_bf16 v[12:15], v[100:103], v[160:163], v[12:15]\n"
"ds_read_b64_tr_b16 v[68:69], v221\n"
"s_waitcnt lgkmcnt(13)\n"
"v_mfma_f32_16x16x32_bf16 v[16:19], v[104:107], a[224:227], v[16:19]\n"
"ds_read_b64_tr_b16 v[70:71], v221 offset:4096\n"
"ds_read_b64_tr_b16 v[100:101], v237 offset:24576\n"
"s_add_u32 s70, s70, 0x20000\n"
"v_mfma_f32_16x16x32_bf16 v[16:19], v[72:75], a[228:231], v[16:19]\n"
"ds_read_b64_tr_b16 v[102:103], v237 offset:28672\n"
"buffer_load_dwordx4 v[188:191], v215, s[36:39], s70 offen sc1\n"
"v_mfma_f32_16x16x32_bf16 v[20:23], v[72:75], v[164:167], v[20:23]\n"
"v_mfma_f32_16x16x32_bf16 v[20:23], v[104:107], v[160:163], v[20:23]\n"
"ds_read_b64_tr_b16 v[72:73], v222\n"
"s_add_u32 s70, s70, 0x20000\n"
"v_mfma_f32_16x16x32_bf16 v[24:27], v[108:111], a[224:227], v[24:27]\n"
"ds_read_b64_tr_b16 v[74:75], v222 offset:4096\n"
"ds_read_b64_tr_b16 v[104:105], v238 offset:24576\n"
"buffer_load_dwordx4 a[8:11], v214, s[36:39], s69 offen sc1\n"
"v_mfma_f32_16x16x32_bf16 v[24:27], v[76:79], a[228:231], v[24:27]\n"
"ds_read_b64_tr_b16 v[106:107], v238 offset:28672\n"
"v_mfma_f32_16x16x32_bf16 v[28:31], v[76:79], v[164:167], v[28:31]\n"
"s_add_u32 s69, s69, 0x20000\n"
"v_mfma_f32_16x16x32_bf16 v[28:31], v[108:111], v[160:163], v[28:31]\n"
"ds_read_b64_tr_b16 v[76:77], v223\n"
"buffer_load_dwordx4 a[12:15], v214, s[36:39], s69 offen sc1\n"
"s_waitcnt lgkmcnt(13)\n"
"v_mfma_f32_16x16x32_bf16 v[32:35], v[112:115], a[224:227], v[32:35]\n"
"ds_read_b64_tr_b16 v[78:79], v223 offset:4096\n"
"ds_read_b64_tr_b16 v[108:109], v239 offset:24576\n"
"v_mfma_f32_16x16x32_bf16 v[32:35], v[80:83], a[228:231], v[32:35]\n"
"ds_read_b64_tr_b16 v[110:111], v239 offset:28672\n"
"s_add_u32 s69, s69, 0x20000\n"
"v_mfma_f32_16x16x32_bf16 v[36:39], v[80:83], v[164:167], v[36:39]\n"
"buffer_load_dwordx4 a[64:67], v215, s[36:39], s70 offen sc1\n"
"v_mfma_f32_16x16x32_bf16 v[36:39], v[112:115], v[160:163], v[36:39]\n"
"ds_read_b64_tr_b16 v[80:81], v224\n"
"v_mfma_f32_16x16x32_bf16 v[40:43], v[116:119], a[224:227], v[40:43]\n"
"ds_read_b64_tr_b16 v[82:83], v224 offset:4096\n"
"ds_read_b64_tr_b16 v[112:113], v240 offset:24576\n"
"s_add_u32 s70, s70, 0x20000\n"
"v_mfma_f32_16x16x32_bf16 v[40:43], v[84:87], a[228:231], v[40:43]\n"
"ds_read_b64_tr_b16 v[114:115], v240 offset:28672\n"
"buffer_load_dwordx4 a[68:71], v215, s[36:39], s70 offen sc1\n"
"v_mfma_f32_16x16x32_bf16 v[44:47], v[84:87], v[164:167], v[44:47]\n"
"v_mfma_f32_16x16x32_bf16 v[44:47], v[116:119], v[160:163], v[44:47]\n"
"ds_read_b64_tr_b16 v[84:85], v225\n"
"s_add_u32 s70, s70, 0x20000\n"
"s_waitcnt vmcnt(18) lgkmcnt(13)\n"
"v_mfma_f32_16x16x32_bf16 v[0:3], v[96:99], a[232:235], v[0:3]\n"
"ds_read_b64_tr_b16 v[86:87], v225 offset:4096\n"
"ds_read_b64_tr_b16 v[116:117], v241 offset:24576\n"
"s_add_u32 s82, s66, 0x100000\n"
"s_add_u32 s83, s82, 0x4000\n"
"s_add_u32 m0, s67, 0x18000\n"
"v_mfma_f32_16x16x32_bf16 v[0:3], v[64:67], a[236:239], v[0:3]\n"
"ds_read_b64_tr_b16 v[118:119], v241 offset:28672\n"
"buffer_load_dwordx4 v216, s[20:23], s82 offen lds\n"
"v_mfma_f32_16x16x32_bf16 v[4:7], v[64:67], v[172:175], v[4:7]\n"
"v_add_u32 v236, 0x8000, v220\n"
"v_add_u32 v237, 0x8000, v221\n"
"v_mfma_f32_16x16x32_bf16 v[4:7], v[96:99], v[168:171], v[4:7]\n"
"ds_read_b64_tr_b16 v[64:65], v220 offset:16384\n"
"v_add_u32 v238, 0x8000, v222\n"
"v_mfma_f32_16x16x32_bf16 v[8:11], v[100:103], a[232:235], v[8:11]\n"
"ds_read_b64_tr_b16 v[66:67], v220 offset:20480\n"
"ds_read_b64_tr_b16 v[96:97], v220 offset:8192\n"
"buffer_load_dwordx4 v217, s[20:23], s82 offen offset:1024 lds\n"
"v_mfma_f32_16x16x32_bf16 v[8:11], v[68:71], a[236:239], v[8:11]\n"
"ds_read_b64_tr_b16 v[98:99], v220 offset:12288\n"
"v_add_u32 v239, 0x8000, v223\n"
"v_mfma_f32_16x16x32_bf16 v[12:15], v[68:71], v[172:175], v[12:15]\n"
"v_add_u32 v240, 0x8000, v224\n"
"v_add_u32 v241, 0x8000, v225\n"
"v_mfma_f32_16x16x32_bf16 v[12:15], v[100:103], v[168:171], v[12:15]\n"
"ds_read_b64_tr_b16 v[68:69], v221 offset:16384\n"
"s_waitcnt lgkmcnt(13)\n"
"v_mfma_f32_16x16x32_bf16 v[16:19], v[104:107], a[232:235], v[16:19]\n"
"ds_read_b64_tr_b16 v[70:71], v221 offset:20480\n"
"ds_read_b64_tr_b16 v[100:101], v221 offset:8192\n"
"buffer_load_dwordx4 v218, s[20:23], s82 offen offset:2048 lds\n"
"v_mfma_f32_16x16x32_bf16 v[16:19], v[72:75], a[236:239], v[16:19]\n"
"ds_read_b64_tr_b16 v[102:103], v221 offset:12288\n"
"v_mfma_f32_16x16x32_bf16 v[20:23], v[72:75], v[172:175], v[20:23]\n"
"v_mfma_f32_16x16x32_bf16 v[20:23], v[104:107], v[168:171], v[20:23]\n"
"ds_read_b64_tr_b16 v[72:73], v222 offset:16384\n"
"buffer_load_dwordx4 v219, s[20:23], s82 offen offset:3072 lds\n"
"v_mfma_f32_16x16x32_bf16 v[24:27], v[108:111], a[232:235], v[24:27]\n"
"ds_read_b64_tr_b16 v[74:75], v222 offset:20480\n"
"ds_read_b64_tr_b16 v[104:105], v222 offset:8192\n"
"v_mfma_f32_16x16x32_bf16 v[24:27], v[76:79], a[236:239], v[24:27]\n"
"ds_read_b64_tr_b16 v[106:107], v222 offset:12288\n"
"v_mfma_f32_16x16x32_bf16 v[28:31], v[76:79], v[172:175], v[28:31]\n"
"v_mfma_f32_16x16x32_bf16 v[28:31], v[108:111], v[168:171], v[28:31]\n"
"ds_read_b64_tr_b16 v[76:77], v223 offset:16384\n"
"s_add_u32 m0, s67, 0x19000\n"
"s_waitcnt lgkmcnt(13)\n"
"v_mfma_f32_16x16x32_bf16 v[32:35], v[112:115], a[232:235], v[32:35]\n"
"ds_read_b64_tr_b16 v[78:79], v223 offset:20480\n"
"ds_read_b64_tr_b16 v[108:109], v223 offset:8192\n"
"buffer_load_dwordx4 v216, s[20:23], s83 offen lds\n"
"v_mfma_f32_16x16x32_bf16 v[32:35], v[80:83], a[236:239], v[32:35]\n"
"ds_read_b64_tr_b16 v[110:111], v223 offset:12288\n"
"v_mfma_f32_16x16x32_bf16 v[36:39], v[80:83], v[172:175], v[36:39]\n"
"v_mfma_f32_16x16x32_bf16 v[36:39], v[112:115], v[168:171], v[36:39]\n"
"ds_read_b64_tr_b16 v[80:81], v224 offset:16384\n"
"buffer_load_dwordx4 v217, s[20:23], s83 offen offset:1024 lds\n"
"v_mfma_f32_16x16x32_bf16 v[40:43], v[116:119], a[232:235], v[40:43]\n"
"ds_read_b64_tr_b16 v[82:83], v224 offset:20480\n"
"ds_read_b64_tr_b16 v[112:113], v224 offset:8192\n"
"v_mfma_f32_16x16x32_bf16 v[40:43], v[84:87], a[236:239], v[40:43]\n"
"ds_read_b64_tr_b16 v[114:115], v224 offset:12288\n"
"v_mfma_f32_16x16x32_bf16 v[44:47], v[84:87], v[172:175], v[44:47]\n"
"v_mfma_f32_16x16x32_bf16 v[44:47], v[116:119], v[168:171], v[44:47]\n"
"ds_read_b64_tr_b16 v[84:85], v225 offset:16384\n"
"buffer_load_dwordx4 v218, s[20:23], s83 offen offset:2048 lds\n"
"s_waitcnt vmcnt(13) lgkmcnt(13)\n"
"v_mfma_f32_16x16x32_bf16 v[0:3], v[96:99], a[0:3], v[0:3]\n"
"ds_read_b64_tr_b16 v[86:87], v225 offset:20480\n"
"ds_read_b64_tr_b16 v[116:117], v225 offset:8192\n"
"v_mfma_f32_16x16x32_bf16 v[0:3], v[64:67], a[4:7], v[0:3]\n"
"ds_read_b64_tr_b16 v[118:119], v225 offset:12288\n"
"s_lshl_b32 s74, s63, 2\n"
"v_mfma_f32_16x16x32_bf16 v[4:7], v[64:67], v[180:183], v[4:7]\n"
"s_waitcnt vmcnt(15)\n"
"s_barrier\n"
"buffer_load_dwordx4 v219, s[20:23], s83 offen offset:3072 lds\n"
"v_mfma_f32_16x16x32_bf16 v[4:7], v[96:99], v[176:179], v[4:7]\n"
"ds_read_b64_tr_b16 v[64:65], v236\n"
"s_add_u32 s74, s74, s64\n"
"v_mfma_f32_16x16x32_bf16 v[8:11], v[100:103], a[0:3], v[8:11]\n"
"ds_read_b64_tr_b16 v[66:67], v236 offset:4096\n"
"ds_read_b64_tr_b16 v[96:97], v220 offset:24576\n"
"v_mfma_f32_16x16x32_bf16 v[8:11], v[68:71], a[4:7], v[8:11]\n"
"ds_read_b64_tr_b16 v[98:99], v220 offset:28672\n"
"s_lshl_b32 s75, s74, 17\n"
"v_mfma_f32_16x16x32_bf16 v[12:15], v[68:71], v[180:183], v[12:15]\n"
"buffer_load_dwordx4 a[16:19], v214, s[36:39], s69 offen sc1\n"
"s_lshl_b32 s12, s8, 11\n"
"s_add_u32 s75, s75, s12\n"
"v_mfma_f32_16x16x32_bf16 v[12:15], v[100:103], v[176:179], v[12:15]\n"
"ds_read_b64_tr_b16 v[68:69], v237\n"
"s_waitcnt lgkmcnt(13)\n"
"v_mfma_f32_16x16x32_bf16 v[16:19], v[104:107], a[0:3], v[16:19]\n"
"ds_read_b64_tr_b16 v[70:71], v237 offset:4096\n"
"ds_read_b64_tr_b16 v[100:101], v221 offset:24576\n"
"s_add_u32 s69, s69, 0x20000\n"
"v_mfma_f32_16x16x32_bf16 v[16:19], v[72:75], a[4:7], v[16:19]\n"
"ds_read_b64_tr_b16 v[102:103], v221 offset:28672\n"
"buffer_load_dwordx4 a[20:23], v214, s[36:39], s69 offen sc1\n"
"s_lshl_b32 s12, s62, 13\n"
"v_mfma_f32_16x16x32_bf16 v[20:23], v[72:75], v[180:183], v[20:23]\n"
"s_add_u32 s14, s75, s12\n"
"s_xor_b32 s12, s62, 3\n"
"v_mfma_f32_16x16x32_bf16 v[20:23], v[104:107], v[176:179], v[20:23]\n"
"ds_read_b64_tr_b16 v[72:73], v238\n"
"s_add_u32 s69, s69, 0x20000\n"
"s_lshl_b32 s12, s12, 15\n"
"v_mfma_f32_16x16x32_bf16 v[24:27], v[108:111], a[0:3], v[24:27]\n"
"ds_read_b64_tr_b16 v[74:75], v238 offset:4096\n"
"ds_read_b64_tr_b16 v[104:105], v222 offset:24576\n"
"buffer_load_dwordx4 a[72:75], v215, s[36:39], s70 offen sc1\n"
"v_mfma_f32_16x16x32_bf16 v[24:27], v[76:79], a[4:7], v[24:27]\n"
"ds_read_b64_tr_b16 v[106:107], v222 offset:28672\n"
"s_add_u32 s84, s14, s12\n"
"v_mfma_f32_16x16x32_bf16 v[28:31], v[76:79], v[180:183], v[28:31]\n"
"s_add_u32 s70, s70, 0x20000\n"
"s_xor_b32 s12, s62, 2\n"
"s_lshl_b32 s12, s12, 15\n"
"v_mfma_f32_16x16x32_bf16 v[28:31], v[108:111], v[176:179], v[28:31]\n"
"ds_read_b64_tr_b16 v[76:77], v239\n"
"buffer_load_dwordx4 a[76:79], v215, s[36:39], s70 offen sc1\n"
"s_waitcnt lgkmcnt(13)\n"
"v_mfma_f32_16x16x32_bf16 v[32:35], v[112:115], a[0:3], v[32:35]\n"
"ds_read_b64_tr_b16 v[78:79], v239 offset:4096\n"
"ds_read_b64_tr_b16 v[108:109], v223 offset:24576\n"
"v_mfma_f32_16x16x32_bf16 v[32:35], v[80:83], a[4:7], v[32:35]\n"
"ds_read_b64_tr_b16 v[110:111], v223 offset:28672\n"
"s_add_u32 s70, s70, 0x20000\n"
"s_add_u32 s85, s14, s12\n"
"v_mfma_f32_16x16x32_bf16 v[36:39], v[80:83], v[180:183], v[36:39]\n"
"buffer_load_dwordx4 a[24:27], v214, s[36:39], s69 offen sc1\n"
"s_xor_b32 s12, s62, 1\n"
"s_lshl_b32 s12, s12, 15\n"
"v_mfma_f32_16x16x32_bf16 v[36:39], v[112:115], v[176:179], v[36:39]\n"
"ds_read_b64_tr_b16 v[80:81], v240\n"
"s_add_u32 s86, s14, s12\n"
"v_mfma_f32_16x16x32_bf16 v[40:43], v[116:119], a[0:3], v[40:43]\n"
"ds_read_b64_tr_b16 v[82:83], v240 offset:4096\n"
"ds_read_b64_tr_b16 v[112:113], v224 offset:24576\n"
"s_add_u32 s69, s69, 0x20000\n"
"v_mfma_f32_16x16x32_bf16 v[40:43], v[84:87], a[4:7], v[40:43]\n"
"ds_read_b64_tr_b16 v[114:115], v224 offset:28672\n"
"buffer_load_dwordx4 a[28:31], v214, s[36:39], s69 offen sc1\n"
"s_lshl_b32 s12, s74, 2\n"
"v_mfma_f32_16x16x32_bf16 v[44:47], v[84:87], v[180:183], v[44:47]\n"
"s_add_u32 s12, s12, s8\n"
"s_lshl_b32 s12, s12, 5\n"
"v_mfma_f32_16x16x32_bf16 v[44:47], v[116:119], v[176:179], v[44:47]\n"
"ds_read_b64_tr_b16 v[84:85], v241\n"
"s_add_u32 s69, s69, 0x20000\n"
"s_waitcnt vmcnt(16) lgkmcnt(13)\n"
"v_mfma_f32_16x16x32_bf16 v[0:3], v[96:99], a[8:11], v[0:3]\n"
"ds_read_b64_tr_b16 v[86:87], v241 offset:4096\n"
"ds_read_b64_tr_b16 v[116:117], v225 offset:24576\n"
"buffer_load_dwordx4 a[80:83], v215, s[36:39], s70 offen sc1\n"
"v_mfma_f32_16x16x32_bf16 v[0:3], v[64:67], a[12:15], v[0:3]\n"
"ds_read_b64_tr_b16 v[118:119], v225 offset:28672\n"
"s_add_u32 s87, s12, 0x6000\n"
"v_mfma_f32_16x16x32_bf16 v[4:7], v[64:67], v[188:191], v[4:7]\n"
"s_add_u32 s70, s70, 0x20000\n"
"v_add_u32 v220, 0x8000, v236\n"
"v_add_u32 v221, 0x8000, v237\n"
"v_mfma_f32_16x16x32_bf16 v[4:7], v[96:99], v[184:187], v[4:7]\n"
"ds_read_b64_tr_b16 v[64:65], v236 offset:16384\n"
"buffer_load_dwordx4 a[84:87], v215, s[36:39], s70 offen sc1\n"
"v_add_u32 v222, 0x8000, v238\n"
"v_mfma_f32_16x16x32_bf16 v[8:11], v[100:103], a[8:11], v[8:11]\n"
"ds_read_b64_tr_b16 v[66:67], v236 offset:20480\n"
"ds_read_b64_tr_b16 v[96:97], v236 offset:8192\n"
"v_mfma_f32_16x16x32_bf16 v[8:11], v[68:71], a[12:15], v[8:11]\n"
"ds_read_b64_tr_b16 v[98:99], v236 offset:12288\n"
"s_add_u32 s70, s70, 0x20000\n"
"v_add_u32 v223, 0x8000, v239\n"
"v_mfma_f32_16x16x32_bf16 v[12:15], v[68:71], v[188:191], v[12:15]\n"
"buffer_load_dwordx4 a[32:35], v214, s[36:39], s69 offen sc1\n"
"v_add_u32 v224, 0x8000, v240\n"
"v_add_u32 v225, 0x8000, v241\n"
"v_mfma_f32_16x16x32_bf16 v[12:15], v[100:103], v[184:187], v[12:15]\n"
"ds_read_b64_tr_b16 v[68:69], v237 offset:16384\n"
"s_waitcnt lgkmcnt(13)\n"
"v_mfma_f32_16x16x32_bf16 v[16:19], v[104:107], a[8:11], v[16:19]\n"
"ds_read_b64_tr_b16 v[70:71], v237 offset:20480\n"
"ds_read_b64_tr_b16 v[100:101], v237 offset:8192\n"
"s_add_u32 s69, s69, 0x20000\n"
"v_mfma_f32_16x16x32_bf16 v[16:19], v[72:75], a[12:15], v[16:19]\n"
"ds_read_b64_tr_b16 v[102:103], v237 offset:12288\n"
"buffer_load_dwordx4 a[36:39], v214, s[36:39], s69 offen sc1\n"
"s_lshl_b32 s13, s62, 3\n"
"v_mfma_f32_16x16x32_bf16 v[20:23], v[72:75], v[188:191], v[20:23]\n"
"s_add_u32 s88, s13, s87\n"
"v_mfma_f32_16x16x32_bf16 v[20:23], v[104:107], v[184:187], v[20:23]\n"
"ds_read_b64_tr_b16 v[72:73], v238 offset:16384\n"
"s_add_u32 s69, s69, 0x20000\n"
"v_mfma_f32_16x16x32_bf16 v[24:27], v[108:111], a[8:11], v[24:27]\n"
"ds_read_b64_tr_b16 v[74:75], v238 offset:20480\n"
"ds_read_b64_tr_b16 v[104:105], v238 offset:8192\n"
"buffer_load_dwordx2 v[204:205], v234, s[76:79], 0 offen\n"
"v_mfma_f32_16x16x32_bf16 v[24:27], v[76:79], a[12:15], v[24:27]\n"
"ds_read_b64_tr_b16 v[106:107], v238 offset:12288\n"
"v_mfma_f32_16x16x32_bf16 v[28:31], v[76:79], v[188:191], v[28:31]\n"
"buffer_load_dwordx2 v[206:207], v234, s[76:79], 0 offen offset:32\n"
"v_mfma_f32_16x16x32_bf16 v[28:31], v[108:111], v[184:187], v[28:31]\n"
"ds_read_b64_tr_b16 v[76:77], v239 offset:16384\n"
"s_waitcnt lgkmcnt(13)\n"
"v_mfma_f32_16x16x32_bf16 v[32:35], v[112:115], a[8:11], v[32:35]\n"
"ds_read_b64_tr_b16 v[78:79], v239 offset:20480\n"
"ds_read_b64_tr_b16 v[108:109], v239 offset:8192\n"
"buffer_load_dwordx2 v[208:209], v234, s[76:79], s80 offen\n"
"v_mfma_f32_16x16x32_bf16 v[32:35], v[80:83], a[12:15], v[32:35]\n"
"ds_read_b64_tr_b16 v[110:111], v239 offset:12288\n"
"v_mfma_f32_16x16x32_bf16 v[36:39], v[80:83], v[188:191], v[36:39]\n"
"buffer_load_dwordx2 v[210:211], v234, s[76:79], s80 offen offset:32\n"
"v_mfma_f32_16x16x32_bf16 v[36:39], v[112:115], v[184:187], v[36:39]\n"
"ds_read_b64_tr_b16 v[80:81], v240 offset:16384\n"
"v_mfma_f32_16x16x32_bf16 v[40:43], v[116:119], a[8:11], v[40:43]\n"
"ds_read_b64_tr_b16 v[82:83], v240 offset:20480\n"
"ds_read_b64_tr_b16 v[112:113], v240 offset:8192\n"
"v_mfma_f32_16x16x32_bf16 v[40:43], v[84:87], a[12:15], v[40:43]\n"
"ds_read_b64_tr_b16 v[114:115], v240 offset:12288\n"
"v_mfma_f32_16x16x32_bf16 v[44:47], v[84:87], v[188:191], v[44:47]\n"
"v_mfma_f32_16x16x32_bf16 v[44:47], v[116:119], v[184:187], v[44:47]\n"
"ds_read_b64_tr_b16 v[84:85], v241 offset:16384\n"
"s_waitcnt vmcnt(12) lgkmcnt(13)\n"
"v_mfma_f32_16x16x32_bf16 v[0:3], v[96:99], a[16:19], v[0:3]\n"
"ds_read_b64_tr_b16 v[86:87], v241 offset:20480\n"
"ds_read_b64_tr_b16 v[116:117], v241 offset:8192\n"
"v_mfma_f32_16x16x32_bf16 v[0:3], v[64:67], a[20:23], v[0:3]\n"
"ds_read_b64_tr_b16 v[118:119], v241 offset:12288\n"
"v_mfma_f32_16x16x32_bf16 v[4:7], v[64:67], a[68:71], v[4:7]\n"
"s_waitcnt vmcnt(14)\n"
"s_barrier\n"
"v_mfma_f32_16x16x32_bf16 v[4:7], v[96:99], a[64:67], v[4:7]\n"
"ds_read_b64_tr_b16 v[64:65], v220\n"
"v_mfma_f32_16x16x32_bf16 v[8:11], v[100:103], a[16:19], v[8:11]\n"
"ds_read_b64_tr_b16 v[66:67], v220 offset:4096\n"
"ds_read_b64_tr_b16 v[96:97], v236 offset:24576\n"
"v_mfma_f32_16x16x32_bf16 v[8:11], v[68:71], a[20:23], v[8:11]\n"
"ds_read_b64_tr_b16 v[98:99], v236 offset:28672\n"
"v_mfma_f32_16x16x32_bf16 v[12:15], v[68:71], a[68:71], v[12:15]\n"
"v_mfma_f32_16x16x32_bf16 v[12:15], v[100:103], a[64:67], v[12:15]\n"
"ds_read_b64_tr_b16 v[68:69], v221\n"
"s_waitcnt lgkmcnt(13)\n"
"v_mfma_f32_16x16x32_bf16 v[16:19], v[104:107], a[16:19], v[16:19]\n"
"ds_read_b64_tr_b16 v[70:71], v221 offset:4096\n"
"ds_read_b64_tr_b16 v[100:101], v237 offset:24576\n"
"v_mfma_f32_16x16x32_bf16 v[16:19], v[72:75], a[20:23], v[16:19]\n"
"ds_read_b64_tr_b16 v[102:103], v237 offset:28672\n"
"v_mfma_f32_16x16x32_bf16 v[20:23], v[72:75], a[68:71], v[20:23]\n"
"v_mfma_f32_16x16x32_bf16 v[20:23], v[104:107], a[64:67], v[20:23]\n"
"ds_read_b64_tr_b16 v[72:73], v222\n"
"v_mfma_f32_16x16x32_bf16 v[24:27], v[108:111], a[16:19], v[24:27]\n"
"ds_read_b64_tr_b16 v[74:75], v222 offset:4096\n"
"ds_read_b64_tr_b16 v[104:105], v238 offset:24576\n"
"v_mfma_f32_16x16x32_bf16 v[24:27], v[76:79], a[20:23], v[24:27]\n"
"ds_read_b64_tr_b16 v[106:107], v238 offset:28672\n"
"v_mfma_f32_16x16x32_bf16 v[28:31], v[76:79], a[68:71], v[28:31]\n"
"v_mfma_f32_16x16x32_bf16 v[28:31], v[108:111], a[64:67], v[28:31]\n"
"ds_read_b64_tr_b16 v[76:77], v223\n"
"s_waitcnt lgkmcnt(13)\n"
"v_mfma_f32_16x16x32_bf16 v[32:35], v[112:115], a[16:19], v[32:35]\n"
"ds_read_b64_tr_b16 v[78:79], v223 offset:4096\n"
"ds_read_b64_tr_b16 v[108:109], v239 offset:24576\n"
"v_mfma_f32_16x16x32_bf16 v[32:35], v[80:83], a[20:23], v[32:35]\n"
"ds_read_b64_tr_b16 v[110:111], v239 offset:28672\n"
"v_mfma_f32_16x16x32_bf16 v[36:39], v[80:83], a[68:71], v[36:39]\n"
"v_mfma_f32_16x16x32_bf16 v[36:39], v[112:115], a[64:67], v[36:39]\n"
"ds_read_b64_tr_b16 v[80:81], v224\n"
"v_mfma_f32_16x16x32_bf16 v[40:43], v[116:119], a[16:19], v[40:43]\n"
"ds_read_b64_tr_b16 v[82:83], v224 offset:4096\n"
"ds_read_b64_tr_b16 v[112:113], v240 offset:24576\n"
"v_mfma_f32_16x16x32_bf16 v[40:43], v[84:87], a[20:23], v[40:43]\n"
"ds_read_b64_tr_b16 v[114:115], v240 offset:28672\n"
"v_mfma_f32_16x16x32_bf16 v[44:47], v[84:87], a[68:71], v[44:47]\n"
"v_mfma_f32_16x16x32_bf16 v[44:47], v[116:119], a[64:67], v[44:47]\n"
"ds_read_b64_tr_b16 v[84:85], v225\n"
"s_waitcnt vmcnt(8) lgkmcnt(13)\n"
"v_mfma_f32_16x16x32_bf16 v[0:3], v[96:99], a[24:27], v[0:3]\n"
"ds_read_b64_tr_b16 v[86:87], v225 offset:4096\n"
"ds_read_b64_tr_b16 v[116:117], v241 offset:24576\n"
"v_mfma_f32_16x16x32_bf16 v[0:3], v[64:67], a[28:31], v[0:3]\n"
"ds_read_b64_tr_b16 v[118:119], v241 offset:28672\n"
"v_mfma_f32_16x16x32_bf16 v[4:7], v[64:67], a[76:79], v[4:7]\n"
"v_mfma_f32_16x16x32_bf16 v[4:7], v[96:99], a[72:75], v[4:7]\n"
"ds_read_b64_tr_b16 v[64:65], v220 offset:16384\n"
"v_mfma_f32_16x16x32_bf16 v[8:11], v[100:103], a[24:27], v[8:11]\n"
"ds_read_b64_tr_b16 v[66:67], v220 offset:20480\n"
"ds_read_b64_tr_b16 v[96:97], v220 offset:8192\n"
"v_mfma_f32_16x16x32_bf16 v[8:11], v[68:71], a[28:31], v[8:11]\n"
"ds_read_b64_tr_b16 v[98:99], v220 offset:12288\n"
"v_mfma_f32_16x16x32_bf16 v[12:15], v[68:71], a[76:79], v[12:15]\n"
"v_mfma_f32_16x16x32_bf16 v[12:15], v[100:103], a[72:75], v[12:15]\n"
"ds_read_b64_tr_b16 v[68:69], v221 offset:16384\n"
"s_waitcnt lgkmcnt(13)\n"
"v_mfma_f32_16x16x32_bf16 v[16:19], v[104:107], a[24:27], v[16:19]\n"
"ds_read_b64_tr_b16 v[70:71], v221 offset:20480\n"
"ds_read_b64_tr_b16 v[100:101], v221 offset:8192\n"
"v_mfma_f32_16x16x32_bf16 v[16:19], v[72:75], a[28:31], v[16:19]\n"
"ds_read_b64_tr_b16 v[102:103], v221 offset:12288\n"
"v_mfma_f32_16x16x32_bf16 v[20:23], v[72:75], a[76:79], v[20:23]\n"
"v_mfma_f32_16x16x32_bf16 v[20:23], v[104:107], a[72:75], v[20:23]\n"
"ds_read_b64_tr_b16 v[72:73], v222 offset:16384\n"
"v_mfma_f32_16x16x32_bf16 v[24:27], v[108:111], a[24:27], v[24:27]\n"
"ds_read_b64_tr_b16 v[74:75], v222 offset:20480\n"
"ds_read_b64_tr_b16 v[104:105], v222 offset:8192\n"
"v_mfma_f32_16x16x32_bf16 v[24:27], v[76:79], a[28:31], v[24:27]\n"
"ds_read_b64_tr_b16 v[106:107], v222 offset:12288\n"
"v_mfma_f32_16x16x32_bf16 v[28:31], v[76:79], a[76:79], v[28:31]\n"
"v_mfma_f32_16x16x32_bf16 v[28:31], v[108:111], a[72:75], v[28:31]\n"
"ds_read_b64_tr_b16 v[76:77], v223 offset:16384\n"
"s_waitcnt lgkmcnt(13)\n"
"v_mfma_f32_16x16x32_bf16 v[32:35], v[112:115], a[24:27], v[32:35]\n"
"ds_read_b64_tr_b16 v[78:79], v223 offset:20480\n"
"ds_read_b64_tr_b16 v[108:109], v223 offset:8192\n"
"v_mfma_f32_16x16x32_bf16 v[32:35], v[80:83], a[28:31], v[32:35]\n"
"ds_read_b64_tr_b16 v[110:111], v223 offset:12288\n"
"v_mfma_f32_16x16x32_bf16 v[36:39], v[80:83], a[76:79], v[36:39]\n"
"v_mfma_f32_16x16x32_bf16 v[36:39], v[112:115], a[72:75], v[36:39]\n"
"ds_read_b64_tr_b16 v[80:81], v224 offset:16384\n"
"v_mfma_f32_16x16x32_bf16 v[40:43], v[116:119], a[24:27], v[40:43]\n"
"ds_read_b64_tr_b16 v[82:83], v224 offset:20480\n"
"ds_read_b64_tr_b16 v[112:113], v224 offset:8192\n"
"v_mfma_f32_16x16x32_bf16 v[40:43], v[84:87], a[28:31], v[40:43]\n"
"ds_read_b64_tr_b16 v[114:115], v224 offset:12288\n"
"v_mfma_f32_16x16x32_bf16 v[44:47], v[84:87], a[76:79], v[44:47]\n"
"v_mfma_f32_16x16x32_bf16 v[44:47], v[116:119], a[72:75], v[44:47]\n"
"ds_read_b64_tr_b16 v[84:85], v225 offset:16384\n"
"s_waitcnt vmcnt(4) lgkmcnt(13)\n"
"v_mfma_f32_16x16x32_bf16 v[0:3], v[96:99], a[32:35], v[0:3]\n"
"ds_read_b64_tr_b16 v[86:87], v225 offset:20480\n"
"ds_read_b64_tr_b16 v[116:117], v225 offset:8192\n"
"v_mfma_f32_16x16x32_bf16 v[0:3], v[64:67], a[36:39], v[0:3]\n"
"ds_read_b64_tr_b16 v[118:119], v225 offset:12288\n"
"v_mfma_f32_16x16x32_bf16 v[4:7], v[64:67], a[84:87], v[4:7]\n"
"v_mfma_f32_16x16x32_bf16 v[4:7], v[96:99], a[80:83], v[4:7]\n"
"ds_read_b64_tr_b16 v[64:65], v198 offset:8192\n"
"v_mfma_f32_16x16x32_bf16 v[8:11], v[100:103], a[32:35], v[8:11]\n"
"ds_read_b64_tr_b16 v[66:67], v198 offset:12288\n"
"v_mfma_f32_16x16x32_bf16 v[8:11], v[68:71], a[36:39], v[8:11]\n"
"ds_read_b64_tr_b16 v[88:89], v244\n"
"v_mfma_f32_16x16x32_bf16 v[12:15], v[68:71], a[84:87], v[12:15]\n"
"ds_read_b64_tr_b16 v[90:91], v244 offset:4096\n"
"v_mfma_f32_16x16x32_bf16 v[12:15], v[100:103], a[80:83], v[12:15]\n"
"s_waitcnt lgkmcnt(12)\n"
"v_mfma_f32_16x16x32_bf16 v[16:19], v[104:107], a[32:35], v[16:19]\n"
"v_mfma_f32_16x16x32_bf16 v[16:19], v[72:75], a[36:39], v[16:19]\n"
"v_mfma_f32_16x16x32_bf16 v[20:23], v[72:75], a[84:87], v[20:23]\n"
"ds_read_b64_tr_b16 v[68:69], v199 offset:8192\n"
"v_mfma_f32_16x16x32_bf16 v[20:23], v[104:107], a[80:83], v[20:23]\n"
"ds_read_b64_tr_b16 v[70:71], v199 offset:12288\n"
"ds_read_b64_tr_b16 v[72:73], v198 offset:16384\n"
"v_mfma_f32_16x16x32_bf16 v[24:27], v[108:111], a[32:35], v[24:27]\n"
"ds_read_b64_tr_b16 v[74:75], v198 offset:20480\n"
"v_mfma_f32_16x16x32_bf16 v[24:27], v[76:79], a[36:39], v[24:27]\n"
"ds_read_b64_tr_b16 v[92:93], v245\n"
"v_mfma_f32_16x16x32_bf16 v[28:31], v[76:79], a[84:87], v[28:31]\n"
"ds_read_b64_tr_b16 v[94:95], v245 offset:4096\n"
"v_mfma_f32_16x16x32_bf16 v[28:31], v[108:111], a[80:83], v[28:31]\n"
"ds_read_b64_tr_b16 v[76:77], v199 offset:16384\n"
"s_waitcnt lgkmcnt(11)\n"
"v_mfma_f32_16x16x32_bf16 v[32:35], v[112:115], a[32:35], v[32:35]\n"
"ds_read_b64_tr_b16 v[78:79], v199 offset:20480\n"
"v_mfma_f32_16x16x32_bf16 v[32:35], v[80:83], a[36:39], v[32:35]\n"
"v_mfma_f32_16x16x32_bf16 v[36:39], v[80:83], a[84:87], v[36:39]\n"
"ds_read_b64_tr_b16 v[96:97], v244 offset:8192\n"
"v_mfma_f32_16x16x32_bf16 v[36:39], v[112:115], a[80:83], v[36:39]\n"
"ds_read_b64_tr_b16 v[98:99], v244 offset:12288\n"
"ds_read_b64_tr_b16 v[80:81], v198 offset:24576\n"
"ds_read_b64_tr_b16 v[104:105], v244 offset:16384\n"
"v_mfma_f32_16x16x32_bf16 v[40:43], v[116:119], a[32:35], v[40:43]\n"
"ds_read_b64_tr_b16 v[82:83], v198 offset:28672\n"
"ds_read_b64_tr_b16 v[106:107], v244 offset:20480\n"
"ds_read_b64_tr_b16 v[112:113], v244 offset:24576\n"
"v_mfma_f32_16x16x32_bf16 v[40:43], v[84:87], a[36:39], v[40:43]\n"
"ds_read_b64_tr_b16 v[114:115], v244 offset:28672\n"
"ds_read_b64_tr_b16 v[120:121], v244 offset:32768\n"
"v_mfma_f32_16x16x32_bf16 v[44:47], v[84:87], a[84:87], v[44:47]\n"
"ds_read_b64_tr_b16 v[122:123], v244 offset:36864\n"
"v_mfma_f32_16x16x32_bf16 v[44:47], v[116:119], a[80:83], v[44:47]\n"
"ds_read_b64_tr_b16 v[84:85], v199 offset:24576\n"
"s_waitcnt lgkmcnt(6)\n"
"v_mfma_f32_16x16x32_bf16 v[48:51], v[64:67], a[208:211], v[48:51]\n"
"ds_read_b64_tr_b16 v[86:87], v199 offset:28672\n"
"v_mfma_f32_16x16x32_bf16 v[48:51], v[72:75], a[212:215], v[48:51]\n"
"v_mfma_f32_16x16x32_bf16 v[48:51], v[80:83], a[216:219], v[48:51]\n"
"ds_read_b64_tr_b16 v[100:101], v245 offset:8192\n"
"v_mfma_f32_16x16x32_bf16 v[48:51], v[88:91], a[220:223], v[48:51]\n"
"ds_read_b64_tr_b16 v[102:103], v245 offset:12288\n"
"ds_read_b64_tr_b16 v[108:109], v245 offset:16384\n"
"v_mfma_f32_16x16x32_bf16 v[52:55], v[88:91], v[156:159], v[52:55]\n"
"ds_read_b64_tr_b16 v[110:111], v245 offset:20480\n"
"ds_read_b64_tr_b16 v[116:117], v245 offset:24576\n"
"v_mfma_f32_16x16x32_bf16 v[52:55], v[80:83], v[152:155], v[52:55]\n"
"ds_read_b64_tr_b16 v[118:119], v245 offset:28672\n"
"ds_read_b64_tr_b16 v[124:125], v245 offset:32768\n"
"v_mfma_f32_16x16x32_bf16 v[52:55], v[72:75], v[148:151], v[52:55]\n"
"ds_read_b64_tr_b16 v[126:127], v245 offset:36864\n"
"v_cvt_pk_bf16_f32 v212, v0, v1\n"
"v_mfma_f32_16x16x32_bf16 v[52:55], v[64:67], v[144:147], v[52:55]\n"
"v_cvt_pk_bf16_f32 v213, v2, v3\n"
"s_waitcnt lgkmcnt(8)\n"
"v_mfma_f32_16x16x32_bf16 v[56:59], v[68:71], a[208:211], v[56:59]\n"
"v_cvt_pk_bf16_f32 v214, v8, v9\n"
"v_cvt_pk_bf16_f32 v215, v10, v11\n"
"v_mfma_f32_16x16x32_bf16 v[56:59], v[76:79], a[212:215], v[56:59]\n"
"buffer_store_dwordx4 v[212:215], v254, s[28:31], s84 offen sc1\n"
"v_mfma_f32_16x16x32_bf16 v[56:59], v[84:87], a[216:219], v[56:59]\n"
"ds_read_b64_tr_b16 v[64:65], v244 offset:40960\n"
"v_cvt_pk_bf16_f32 v216, v4, v5\n"
"v_mfma_f32_16x16x32_bf16 v[56:59], v[92:95], a[220:223], v[56:59]\n"
"ds_read_b64_tr_b16 v[66:67], v244 offset:45056\n"
"ds_read_b64_tr_b16 v[72:73], v244 offset:49152\n"
"v_mfma_f32_16x16x32_bf16 v[60:63], v[92:95], v[156:159], v[60:63]\n"
"ds_read_b64_tr_b16 v[74:75], v244 offset:53248\n"
"ds_read_b64_tr_b16 v[80:81], v244 offset:57344\n"
"v_mfma_f32_16x16x32_bf16 v[60:63], v[84:87], v[152:155], v[60:63]\n"
"ds_read_b64_tr_b16 v[82:83], v244 offset:61440\n"
"ds_read_b64_tr_b16 v[88:89], v196\n"
"v_mfma_f32_16x16x32_bf16 v[60:63], v[76:79], v[148:151], v[60:63]\n"
"ds_read_b64_tr_b16 v[90:91], v196 offset:4096\n"
"v_cvt_pk_bf16_f32 v217, v6, v7\n"
"v_mfma_f32_16x16x32_bf16 v[60:63], v[68:71], v[144:147], v[60:63]\n"
"v_cvt_pk_bf16_f32 v218, v12, v13\n"
"s_waitcnt lgkmcnt(15)\n"
"v_mfma_f32_16x16x32_bf16 v[48:51], v[96:99], a[224:227], v[48:51]\n"
"v_cvt_pk_bf16_f32 v219, v14, v15\n"
"v_mfma_f32_16x16x32_bf16 v[48:51], v[104:107], a[228:231], v[48:51]\n"
"buffer_store_dwordx4 v[216:219], v254, s[28:31], s84 offen offset:1024 sc1\n"
"v_mfma_f32_16x16x32_bf16 v[48:51], v[112:115], a[232:235], v[48:51]\n"
"ds_read_b64_tr_b16 v[68:69], v245 offset:40960\n"
"v_cvt_pk_bf16_f32 v220, v16, v17\n"
"v_mfma_f32_16x16x32_bf16 v[48:51], v[120:123], a[236:239], v[48:51]\n"
"ds_read_b64_tr_b16 v[70:71], v245 offset:45056\n"
"ds_read_b64_tr_b16 v[76:77], v245 offset:49152\n"
"v_mfma_f32_16x16x32_bf16 v[52:55], v[120:123], v[172:175], v[52:55]\n"
"ds_read_b64_tr_b16 v[78:79], v245 offset:53248\n"
"ds_read_b64_tr_b16 v[84:85], v245 offset:57344\n"
"v_mfma_f32_16x16x32_bf16 v[52:55], v[112:115], v[168:171], v[52:55]\n"
"ds_read_b64_tr_b16 v[86:87], v245 offset:61440\n"
"ds_read_b64_tr_b16 v[92:93], v197\n"
"v_mfma_f32_16x16x32_bf16 v[52:55], v[104:107], v[164:167], v[52:55]\n"
"ds_read_b64_tr_b16 v[94:95], v197 offset:4096\n"
"v_cvt_pk_bf16_f32 v221, v18, v19\n"
"v_mfma_f32_16x16x32_bf16 v[52:55], v[96:99], v[160:163], v[52:55]\n"
"v_cvt_pk_bf16_f32 v222, v24, v25\n"
"s_waitcnt lgkmcnt(15)\n"
"v_mfma_f32_16x16x32_bf16 v[56:59], v[100:103], a[224:227], v[56:59]\n"
"v_cvt_pk_bf16_f32 v223, v26, v27\n"
"v_mfma_f32_16x16x32_bf16 v[56:59], v[108:111], a[228:231], v[56:59]\n"
"buffer_store_dwordx4 v[220:223], v254, s[28:31], s85 offen sc1\n"
"v_mfma_f32_16x16x32_bf16 v[56:59], v[116:119], a[232:235], v[56:59]\n"
"ds_read_b64_tr_b16 v[96:97], v196 offset:8192\n"
"v_cvt_pk_bf16_f32 v224, v20, v21\n"
"v_mfma_f32_16x16x32_bf16 v[56:59], v[124:127], a[236:239], v[56:59]\n"
"ds_read_b64_tr_b16 v[98:99], v196 offset:12288\n"
"ds_read_b64_tr_b16 v[104:105], v196 offset:16384\n"
"v_mfma_f32_16x16x32_bf16 v[60:63], v[124:127], v[172:175], v[60:63]\n"
"ds_read_b64_tr_b16 v[106:107], v196 offset:20480\n"
"ds_read_b64_tr_b16 v[112:113], v196 offset:24576\n"
"v_mfma_f32_16x16x32_bf16 v[60:63], v[116:119], v[168:171], v[60:63]\n"
"ds_read_b64_tr_b16 v[114:115], v196 offset:28672\n"
"ds_read_b64_tr_b16 v[120:121], v196 offset:32768\n"
"v_mfma_f32_16x16x32_bf16 v[60:63], v[108:111], v[164:167], v[60:63]\n"
"ds_read_b64_tr_b16 v[122:123], v196 offset:36864\n"
"v_cvt_pk_bf16_f32 v225, v22, v23\n"
"v_mfma_f32_16x16x32_bf16 v[60:63], v[100:103], v[160:163], v[60:63]\n"
"v_cvt_pk_bf16_f32 v226, v28, v29\n"
"s_waitcnt lgkmcnt(15)\n"
"v_mfma_f32_16x16x32_bf16 v[48:51], v[64:67], a[0:3], v[48:51]\n"
"v_cvt_pk_bf16_f32 v227, v30, v31\n"
"v_mfma_f32_16x16x32_bf16 v[48:51], v[72:75], a[4:7], v[48:51]\n"
"buffer_store_dwordx4 v[224:227], v254, s[28:31], s85 offen offset:1024 sc1\n"
"v_mfma_f32_16x16x32_bf16 v[48:51], v[80:83], a[8:11], v[48:51]\n"
"ds_read_b64_tr_b16 v[100:101], v197 offset:8192\n"
"v_cvt_pk_bf16_f32 v236, v32, v33\n"
"v_mfma_f32_16x16x32_bf16 v[48:51], v[88:91], a[12:15], v[48:51]\n"
"ds_read_b64_tr_b16 v[102:103], v197 offset:12288\n"
"ds_read_b64_tr_b16 v[108:109], v197 offset:16384\n"
"v_mfma_f32_16x16x32_bf16 v[52:55], v[88:91], v[188:191], v[52:55]\n"
"ds_read_b64_tr_b16 v[110:111], v197 offset:20480\n"
"ds_read_b64_tr_b16 v[116:117], v197 offset:24576\n"
"v_mfma_f32_16x16x32_bf16 v[52:55], v[80:83], v[184:187], v[52:55]\n"
"ds_read_b64_tr_b16 v[118:119], v197 offset:28672\n"
"ds_read_b64_tr_b16 v[124:125], v197 offset:32768\n"
"v_mfma_f32_16x16x32_bf16 v[52:55], v[72:75], v[180:183], v[52:55]\n"
"ds_read_b64_tr_b16 v[126:127], v197 offset:36864\n"
"v_cvt_pk_bf16_f32 v237, v34, v35\n"
"v_mfma_f32_16x16x32_bf16 v[52:55], v[64:67], v[176:179], v[52:55]\n"
"v_cvt_pk_bf16_f32 v238, v40, v41\n"
"s_waitcnt lgkmcnt(15)\n"
"v_mfma_f32_16x16x32_bf16 v[56:59], v[68:71], a[0:3], v[56:59]\n"
"v_cvt_pk_bf16_f32 v239, v42, v43\n"
"v_mfma_f32_16x16x32_bf16 v[56:59], v[76:79], a[4:7], v[56:59]\n"
"buffer_store_dwordx4 v[236:239], v254, s[28:31], s86 offen sc1\n"
"v_mfma_f32_16x16x32_bf16 v[56:59], v[84:87], a[8:11], v[56:59]\n"
"ds_read_b64_tr_b16 v[64:65], v196 offset:40960\n"
"v_cvt_pk_bf16_f32 v240, v36, v37\n"
"v_mfma_f32_16x16x32_bf16 v[56:59], v[92:95], a[12:15], v[56:59]\n"
"ds_read_b64_tr_b16 v[66:67], v196 offset:45056\n"
"ds_read_b64_tr_b16 v[72:73], v196 offset:49152\n"
"v_mfma_f32_16x16x32_bf16 v[60:63], v[92:95], v[188:191], v[60:63]\n"
"ds_read_b64_tr_b16 v[74:75], v196 offset:53248\n"
"v_cvt_pk_bf16_f32 v241, v38, v39\n"
"v_mfma_f32_16x16x32_bf16 v[60:63], v[84:87], v[184:187], v[60:63]\n"
"v_cvt_pk_bf16_f32 v242, v44, v45\n"
"v_cvt_pk_bf16_f32 v243, v46, v47\n"
"v_mfma_f32_16x16x32_bf16 v[60:63], v[76:79], v[180:183], v[60:63]\n"
"buffer_store_dwordx4 v[240:243], v254, s[28:31], s86 offen offset:1024 sc1\n"
"v_mfma_f32_16x16x32_bf16 v[60:63], v[68:71], v[176:179], v[60:63]\n"
"ds_read_b64_tr_b16 v[76:77], v197 offset:49152\n"
"s_waitcnt lgkmcnt(13)\n"
"v_mfma_f32_16x16x32_bf16 v[48:51], v[96:99], a[16:19], v[48:51]\n"
"ds_read_b64_tr_b16 v[78:79], v197 offset:53248\n"
"ds_read_b64_tr_b16 v[68:69], v197 offset:40960\n"
"v_mfma_f32_16x16x32_bf16 v[48:51], v[104:107], a[20:23], v[48:51]\n"
"ds_read_b64_tr_b16 v[70:71], v197 offset:45056\n"
"v_mfma_f32_16x16x32_bf16 v[48:51], v[112:115], a[24:27], v[48:51]\n"
"v_mfma_f32_16x16x32_bf16 v[48:51], v[120:123], a[28:31], v[48:51]\n"
"v_mfma_f32_16x16x32_bf16 v[52:55], v[120:123], a[76:79], v[52:55]\n"
"v_mfma_f32_16x16x32_bf16 v[52:55], v[112:115], a[72:75], v[52:55]\n"
"v_mfma_f32_16x16x32_bf16 v[52:55], v[104:107], a[68:71], v[52:55]\n"
"v_mfma_f32_16x16x32_bf16 v[52:55], v[96:99], a[64:67], v[52:55]\n"
"s_waitcnt lgkmcnt(8)\n"
"v_mfma_f32_16x16x32_bf16 v[56:59], v[100:103], a[16:19], v[56:59]\n"
"v_mfma_f32_16x16x32_bf16 v[56:59], v[108:111], a[20:23], v[56:59]\n"
"v_mfma_f32_16x16x32_bf16 v[56:59], v[116:119], a[24:27], v[56:59]\n"
"v_mfma_f32_16x16x32_bf16 v[56:59], v[124:127], a[28:31], v[56:59]\n"
"v_mfma_f32_16x16x32_bf16 v[60:63], v[124:127], a[76:79], v[60:63]\n"
"v_mfma_f32_16x16x32_bf16 v[60:63], v[116:119], a[72:75], v[60:63]\n"
"v_mfma_f32_16x16x32_bf16 v[60:63], v[108:111], a[68:71], v[60:63]\n"
"v_mfma_f32_16x16x32_bf16 v[60:63], v[100:103], a[64:67], v[60:63]\n"
"s_waitcnt lgkmcnt(4)\n"
"v_mfma_f32_16x16x32_bf16 v[48:51], v[64:67], a[32:35], v[48:51]\n"
"v_mfma_f32_16x16x32_bf16 v[48:51], v[72:75], a[36:39], v[48:51]\n"
"v_mfma_f32_16x16x32_bf16 v[52:55], v[72:75], a[84:87], v[52:55]\n"
"v_mfma_f32_16x16x32_bf16 v[52:55], v[64:67], a[80:83], v[52:55]\n"
"s_waitcnt lgkmcnt(0)\n"
"v_mfma_f32_16x16x32_bf16 v[56:59], v[68:71], a[32:35], v[56:59]\n"
"v_mfma_f32_16x16x32_bf16 v[56:59], v[76:79], a[36:39], v[56:59]\n"
"v_mfma_f32_16x16x32_bf16 v[60:63], v[76:79], a[84:87], v[60:63]\n"
"v_mfma_f32_16x16x32_bf16 v[60:63], v[68:71], a[80:83], v[60:63]\n"
"v_and_b32 v228, 3, v255\n"
"v_lshlrev_b32 v228, 3, v228\n"
"s_waitcnt vmcnt(0)\n"
"s_mov_b64 exec, 1\n"
"buffer_store_dwordx2 v[250:251], v249, s[92:95], s88 offen sc1\n"
"s_mov_b64 exec, -1\n"
"s_lshl_b32 s12, s62, 15\n"
"s_add_u32 s14, s75, s12\n"
"s_xor_b32 s12, s62, 3\n"
"s_lshl_b32 s12, s12, 13\n"
"s_add_u32 s84, s14, s12\n"
"s_xor_b32 s12, s62, 2\n"
"s_lshl_b32 s12, s12, 13\n"
"s_add_u32 s85, s14, s12\n"
"s_xor_b32 s12, s62, 1\n"
"s_lshl_b32 s12, s12, 13\n"
"s_add_u32 s86, s14, s12\n"
"s_mov_b32 s76, s58\n"
"s_and_b32 s77, s59, 0xffff\n"
"s_mov_b32 s78, 0x200000\n"
"s_mov_b32 s79, 0x20000\n"
"v_lshlrev_b32 v144, 16, v204\n"
"v_and_b32 v145, 0xffff0000, v204\n"
"v_pk_add_f32 v[48:49], v[48:49], v[144:145]\n"
"v_lshlrev_b32 v146, 16, v205\n"
"v_and_b32 v147, 0xffff0000, v205\n"
"v_pk_add_f32 v[50:51], v[50:51], v[146:147]\n"
"v_lshlrev_b32 v144, 16, v206\n"
"v_and_b32 v145, 0xffff0000, v206\n"
"v_pk_add_f32 v[56:57], v[56:57], v[144:145]\n"
"v_lshlrev_b32 v146, 16, v207\n"
"v_and_b32 v147, 0xffff0000, v207\n"
"v_pk_add_f32 v[58:59], v[58:59], v[146:147]\n"
"v_lshlrev_b32 v144, 16, v208\n"
"v_and_b32 v145, 0xffff0000, v208\n"
"v_pk_add_f32 v[52:53], v[52:53], v[144:145]\n"
"v_lshlrev_b32 v146, 16, v209\n"
"v_and_b32 v147, 0xffff0000, v209\n"
"v_pk_add_f32 v[54:55], v[54:55], v[146:147]\n"
"v_lshlrev_b32 v144, 16, v210\n"
"v_and_b32 v145, 0xffff0000, v210\n"
"v_pk_add_f32 v[60:61], v[60:61], v[144:145]\n"
"v_lshlrev_b32 v146, 16, v211\n"
"v_and_b32 v147, 0xffff0000, v211\n"
"v_pk_add_f32 v[62:63], v[62:63], v[146:147]\n"
"s_waitcnt vmcnt(0)\n"
"buffer_load_dwordx2 v[230:231], v228, s[92:95], s87 offen sc1\n"
"s_waitcnt vmcnt(0)\n"
"v_cmp_eq_u64 vcc, v[230:231], v[250:251]\n"
"s_nop 1\n"
"s_cmp_eq_u64 vcc, exec\n"
"s_cbranch_scc1 .Lpok_55_%=\n"
"s_mov_b32 s15, 0\n"
".Lploop_56_%=:\n"
"s_sleep 4\n"
"buffer_load_dwordx2 v[230:231], v228, s[92:95], s87 offen sc1\n"
"s_waitcnt vmcnt(0)\n"
"v_cmp_eq_u64 vcc, v[230:231], v[250:251]\n"
"s_add_u32 s15, s15, 1\n"
"s_nop 0\n"
"s_cmp_eq_u64 vcc, exec\n"
"s_cbranch_scc1 .Lpok_55_%=\n"
"s_cmp_lt_u32 s15, 0x20000\n"
"s_cbranch_scc1 .Lploop_56_%=\n"
"s_mov_b32 s15, 0x80\n"
"s_mov_b64 exec, 1\n"
"buffer_store_dwordx2 v[250:251], v249, s[92:95], s15 offen sc1\n"
"s_mov_b64 exec, -1\n"
".Lpok_55_%=:\n"
"buffer_load_dwordx4 v[64:67], v254, s[28:31], s84 offen sc1\n"
"buffer_load_dwordx4 v[68:71], v254, s[28:31], s84 offen offset:1024 sc1\n"
"buffer_load_dwordx4 v[72:75], v254, s[28:31], s85 offen sc1\n"
"buffer_load_dwordx4 v[76:79], v254, s[28:31], s85 offen offset:1024 sc1\n"
"buffer_load_dwordx4 v[80:83], v254, s[28:31], s86 offen sc1\n"
"buffer_load_dwordx4 v[84:87], v254, s[28:31], s86 offen offset:1024 sc1\n"
"s_waitcnt vmcnt(4)\n"
"v_lshlrev_b32 v144, 16, v64\n"
"v_and_b32 v145, 0xffff0000, v64\n"
"v_pk_add_f32 v[48:49], v[48:49], v[144:145]\n"
"v_lshlrev_b32 v146, 16, v65\n"
"v_and_b32 v147, 0xffff0000, v65\n"
"v_pk_add_f32 v[50:51], v[50:51], v[146:147]\n"
"v_lshlrev_b32 v148, 16, v66\n"
"v_and_b32 v149, 0xffff0000, v66\n"
"v_pk_add_f32 v[56:57], v[56:57], v[148:149]\n"
"v_lshlrev_b32 v150, 16, v67\n"
"v_and_b32 v151, 0xffff0000, v67\n"
"v_pk_add_f32 v[58:59], v[58:59], v[150:151]\n"
"v_lshlrev_b32 v144, 16, v68\n"
"v_and_b32 v145, 0xffff0000, v68\n"
"v_pk_add_f32 v[52:53], v[52:53], v[144:145]\n"
"v_lshlrev_b32 v146, 16, v69\n"
"v_and_b32 v147, 0xffff0000, v69\n"
"v_pk_add_f32 v[54:55], v[54:55], v[146:147]\n"
"v_lshlrev_b32 v148, 16, v70\n"
"v_and_b32 v149, 0xffff0000, v70\n"
"v_pk_add_f32 v[60:61], v[60:61], v[148:149]\n"
"v_lshlrev_b32 v150, 16, v71\n"
"v_and_b32 v151, 0xffff0000, v71\n"
"v_pk_add_f32 v[62:63], v[62:63], v[150:151]\n"
"s_waitcnt vmcnt(2)\n"
"v_lshlrev_b32 v144, 16, v72\n"
"v_and_b32 v145, 0xffff0000, v72\n"
"v_pk_add_f32 v[48:49], v[48:49], v[144:145]\n"
"v_lshlrev_b32 v146, 16, v73\n"
"v_and_b32 v147, 0xffff0000, v73\n"
"v_pk_add_f32 v[50:51], v[50:51], v[146:147]\n"
"v_lshlrev_b32 v148, 16, v74\n"
"v_and_b32 v149, 0xffff0000, v74\n"
"v_pk_add_f32 v[56:57], v[56:57], v[148:149]\n"
"v_lshlrev_b32 v150, 16, v75\n"
"v_and_b32 v151, 0xffff0000, v75\n"
"v_pk_add_f32 v[58:59], v[58:59], v[150:151]\n"
"v_lshlrev_b32 v144, 16, v76\n"
"v_and_b32 v145, 0xffff0000, v76\n"
"v_pk_add_f32 v[52:53], v[52:53], v[144:145]\n"
"v_lshlrev_b32 v146, 16, v77\n"
"v_and_b32 v147, 0xffff0000, v77\n"
"v_pk_add_f32 v[54:55], v[54:55], v[146:147]\n"
"v_lshlrev_b32 v148, 16, v78\n"
"v_and_b32 v149, 0xffff0000, v78\n"
"v_pk_add_f32 v[60:61], v[60:61], v[148:149]\n"
"v_lshlrev_b32 v150, 16, v79\n"
"v_and_b32 v151, 0xffff0000, v79\n"
"v_pk_add_f32 v[62:63], v[62:63], v[150:151]\n"
"s_waitcnt vmcnt(0)\n"
"v_lshlrev_b32 v144, 16, v80\n"
"v_and_b32 v145, 0xffff0000, v80\n"
"v_pk_add_f32 v[48:49], v[48:49], v[144:145]\n"
"v_lshlrev_b32 v146, 16, v81\n"
"v_and_b32 v147, 0xffff0000, v81\n"
"v_pk_add_f32 v[50:51], v[50:51], v[146:147]\n"
"v_lshlrev_b32 v148, 16, v82\n"
"v_and_b32 v149, 0xffff0000, v82\n"
"v_pk_add_f32 v[56:57], v[56:57], v[148:149]\n"
"v_lshlrev_b32 v150, 16, v83\n"
"v_and_b32 v151, 0xffff0000, v83\n"
"v_pk_add_f32 v[58:59], v[58:59], v[150:151]\n"
"v_lshlrev_b32 v144, 16, v84\n"
"v_and_b32 v145, 0xffff0000, v84\n"
"v_pk_add_f32 v[52:53], v[52:53], v[144:145]\n"
"v_lshlrev_b32 v146, 16, v85\n"
"v_and_b32 v147, 0xffff0000, v85\n"
"v_pk_add_f32 v[54:55], v[54:55], v[146:147]\n"
"v_lshlrev_b32 v148, 16, v86\n"
"v_and_b32 v149, 0xffff0000, v86\n"
"v_pk_add_f32 v[60:61], v[60:61], v[148:149]\n"
"v_lshlrev_b32 v150, 16, v87\n"
"v_and_b32 v151, 0xffff0000, v87\n"
"v_pk_add_f32 v[62:63], v[62:63], v[150:151]\n"
"v_cvt_pk_bf16_f32 v152, v48, v49\n"
"v_cvt_pk_bf16_f32 v153, v50, v51\n"
"buffer_store_dwordx2 v[152:153], v234, s[76:79], 0 offen\n"
"v_cvt_pk_bf16_f32 v154, v56, v57\n"
"v_cvt_pk_bf16_f32 v155, v58, v59\n"
"buffer_store_dwordx2 v[154:155], v234, s[76:79], 0 offen offset:32\n"
"v_cvt_pk_bf16_f32 v156, v52, v53\n"
"v_cvt_pk_bf16_f32 v157, v54, v55\n"
"buffer_store_dwordx2 v[156:157], v234, s[76:79], s80 offen\n"
"v_cvt_pk_bf16_f32 v158, v60, v61\n"
"v_cvt_pk_bf16_f32 v159, v62, v63\n"
"buffer_store_dwordx2 v[158:159], v234, s[76:79], s80 offen offset:32\n"
"s_branch .Lend_50_%=\n"
".Lh_a_39_%=:\n"
"s_cmp_eq_u32 s8, 1\n"
"s_cbranch_scc1 .Ldiagx1z_30_%=\n"
"s_waitcnt lgkmcnt(8)\n"
"v_mfma_f32_16x16x32_bf16 v[64:67], a[144:147], a[64:67], v[160:163]\n"
"v_cvt_pk_bf16_f32 v140, v76, v77\n"
"v_cvt_pk_bf16_f32 v141, v78, v79\n"
"v_mfma_f32_16x16x32_bf16 v[64:67], a[148:151], a[80:83], v[64:67]\n"
"s_xor_b32 s85, s85, 0x8000\n"
"v_mfma_f32_16x16x32_bf16 v[64:67], a[152:155], a[96:99], v[64:67]\n"
"v_cvt_pk_bf16_f32 v142, v92, v93\n"
"v_cvt_pk_bf16_f32 v143, v94, v95\n"
"v_mfma_f32_16x16x32_bf16 v[64:67], a[156:159], a[112:115], v[64:67]\n"
"s_mov_b32 m0, s85\n"
"v_mfma_f32_16x16x32_bf16 v[68:71], a[156:159], a[116:119], v[164:167]\n"
"v_cvt_pk_bf16_f32 v156, v108, v109\n"
"v_cvt_pk_bf16_f32 v157, v110, v111\n"
"v_mfma_f32_16x16x32_bf16 v[68:71], a[152:155], a[100:103], v[68:71]\n"
"buffer_load_dwordx4 v254, s[96:99], s67 offen sc1 lds\n"
"v_mfma_f32_16x16x32_bf16 v[68:71], a[148:151], a[84:87], v[68:71]\n"
"v_cvt_pk_bf16_f32 v158, v124, v125\n"
"v_cvt_pk_bf16_f32 v159, v126, v127\n"
"v_mfma_f32_16x16x32_bf16 v[68:71], a[144:147], a[68:71], v[68:71]\n"
"buffer_load_dwordx4 v254, s[96:99], s67 offen offset:1024 sc1 lds\n"
"v_mfma_f32_16x16x32_bf16 v[72:75], a[144:147], a[72:75], v[168:171]\n"
"ds_read_b128 a[184:187], v193 offset:2048\n"
"ds_read_b128 a[216:219], v193 offset:10240\n"
"v_mfma_f32_16x16x32_bf16 v[72:75], a[148:151], a[88:91], v[72:75]\n"
"v_mfma_f32_16x16x32_bf16 v[72:75], a[152:155], a[104:107], v[72:75]\n"
"ds_read_b128 a[188:191], v193 offset:3072\n"
"v_mfma_f32_16x16x32_bf16 v[72:75], a[156:159], a[120:123], v[72:75]\n"
"buffer_load_dwordx4 v254, s[96:99], s67 offen offset:2048 sc1 lds\n"
"v_mfma_f32_16x16x32_bf16 v[76:79], a[156:159], a[124:127], v[172:175]\n"
"ds_read_b128 a[220:223], v193 offset:11264\n"
"ds_read_b128 a[192:195], v194 offset:4096\n"
"v_mfma_f32_16x16x32_bf16 v[76:79], a[152:155], a[108:111], v[76:79]\n"
"v_mfma_f32_16x16x32_bf16 v[76:79], a[148:151], a[92:95], v[76:79]\n"
"ds_read_b128 a[224:227], v194 offset:12288\n"
"s_waitcnt lgkmcnt(10)\n"
"v_mfma_f32_16x16x32_bf16 v[76:79], a[144:147], a[76:79], v[76:79]\n"
"v_mfma_f32_16x16x32_bf16 v[80:83], a[160:163], a[64:67], v[160:163]\n"
"ds_read_b128 a[144:147], v192 offset:8192\n"
"ds_read_b128 a[148:151], v192 offset:9216\n"
"v_mfma_f32_16x16x32_bf16 v[80:83], a[164:167], a[80:83], v[80:83]\n"
"buffer_load_dwordx4 v254, s[96:99], s67 offen offset:3072 sc1 lds\n"
"v_mfma_f32_16x16x32_bf16 v[80:83], a[168:171], a[96:99], v[80:83]\n"
"ds_read_b128 a[152:155], v192 offset:10240\n"
"ds_read_b128 a[156:159], v192 offset:11264\n"
"v_mfma_f32_16x16x32_bf16 v[80:83], a[172:175], a[112:115], v[80:83]\n"
"s_sub_u32 s89, 32, s66\n"
"v_mfma_f32_16x16x32_bf16 v[84:87], a[172:175], a[116:119], v[164:167]\n"
"s_lshl_b32 s89, s89, 14\n"
"s_add_u32 s67, s73, s89\n"
"v_mfma_f32_16x16x32_bf16 v[84:87], a[168:171], a[100:103], v[84:87]\n"
"v_exp_f32 v64, v64\n"
"v_mfma_f32_16x16x32_bf16 v[84:87], a[164:167], a[84:87], v[84:87]\n"
"s_xor_b32 s84, s84, 0x8000\n"
"s_mov_b32 m0, s84\n"
"v_mfma_f32_16x16x32_bf16 v[84:87], a[160:163], a[68:71], v[84:87]\n"
"v_exp_f32 v65, v65\n"
"v_mfma_f32_16x16x32_bf16 v[88:91], a[160:163], a[72:75], v[168:171]\n"
"buffer_load_dwordx4 v254, s[28:31], s67 offen sc1 lds\n"
"v_mfma_f32_16x16x32_bf16 v[88:91], a[164:167], a[88:91], v[88:91]\n"
"v_exp_f32 v66, v66\n"
"v_mfma_f32_16x16x32_bf16 v[88:91], a[168:171], a[104:107], v[88:91]\n"
"buffer_load_dwordx4 v254, s[28:31], s67 offen offset:1024 sc1 lds\n"
"v_mfma_f32_16x16x32_bf16 v[88:91], a[172:175], a[120:123], v[88:91]\n"
"v_exp_f32 v67, v67\n"
"v_mfma_f32_16x16x32_bf16 v[92:95], a[172:175], a[124:127], v[172:175]\n"
"ds_read_b128 a[196:199], v194 offset:5120\n"
"ds_read_b128 a[228:231], v194 offset:13312\n"
"v_mfma_f32_16x16x32_bf16 v[92:95], a[168:171], a[108:111], v[92:95]\n"
"v_exp_f32 v80, v80\n"
"v_mfma_f32_16x16x32_bf16 v[92:95], a[164:167], a[92:95], v[92:95]\n"
"ds_read_b128 a[200:203], v194 offset:6144\n"
"s_waitcnt lgkmcnt(3)\n"
"v_mfma_f32_16x16x32_bf16 v[92:95], a[160:163], a[76:79], v[92:95]\n"
"v_exp_f32 v81, v81\n"
"v_mfma_f32_16x16x32_bf16 v[96:99], a[144:147], a[64:67], v[160:163]\n"
"ds_read_b128 a[160:163], v192 offset:12288\n"
"ds_read_b128 a[164:167], v192 offset:13312\n"
"v_mfma_f32_16x16x32_bf16 v[96:99], a[148:151], a[80:83], v[96:99]\n"
"v_exp_f32 v82, v82\n"
"v_mfma_f32_16x16x32_bf16 v[96:99], a[152:155], a[96:99], v[96:99]\n"
"ds_read_b128 a[168:171], v192 offset:14336\n"
"ds_read_b128 a[172:175], v192 offset:15360\n"
"v_mfma_f32_16x16x32_bf16 v[96:99], a[156:159], a[112:115], v[96:99]\n"
"v_exp_f32 v83, v83\n"
"v_mfma_f32_16x16x32_bf16 v[100:103], a[156:159], a[116:119], v[164:167]\n"
"buffer_load_dwordx4 v254, s[28:31], s67 offen offset:2048 sc1 lds\n"
"v_mfma_f32_16x16x32_bf16 v[100:103], a[152:155], a[100:103], v[100:103]\n"
"v_exp_f32 v68, v68\n"
"v_mfma_f32_16x16x32_bf16 v[100:103], a[148:151], a[84:87], v[100:103]\n"
"buffer_load_dwordx4 v254, s[28:31], s67 offen offset:3072 sc1 lds\n"
"v_mfma_f32_16x16x32_bf16 v[100:103], a[144:147], a[68:71], v[100:103]\n"
"v_exp_f32 v69, v69\n"
"v_mfma_f32_16x16x32_bf16 v[104:107], a[144:147], a[72:75], v[168:171]\n"
"s_add_u32 s69, s67, 0x4000\n"
"s_add_u32 m0, s84, 0x4000\n"
"v_mfma_f32_16x16x32_bf16 v[104:107], a[148:151], a[88:91], v[104:107]\n"
"v_exp_f32 v70, v70\n"
"v_mfma_f32_16x16x32_bf16 v[104:107], a[152:155], a[104:107], v[104:107]\n"
"buffer_load_dwordx4 v254, s[28:31], s69 offen sc1 lds\n"
"v_mfma_f32_16x16x32_bf16 v[104:107], a[156:159], a[120:123], v[104:107]\n"
"v_exp_f32 v71, v71\n"
"v_mfma_f32_16x16x32_bf16 v[108:111], a[156:159], a[124:127], v[172:175]\n"
"ds_read_b128 a[232:235], v194 offset:14336\n"
"ds_read_b128 a[204:207], v194 offset:7168\n"
"v_mfma_f32_16x16x32_bf16 v[108:111], a[152:155], a[108:111], v[108:111]\n"
"v_exp_f32 v84, v84\n"
"v_mfma_f32_16x16x32_bf16 v[108:111], a[148:151], a[92:95], v[108:111]\n"
"ds_read_b128 a[236:239], v194 offset:15360\n"
"s_waitcnt lgkmcnt(3)\n"
"v_mfma_f32_16x16x32_bf16 v[108:111], a[144:147], a[76:79], v[108:111]\n"
"v_exp_f32 v85, v85\n"
"v_mfma_f32_16x16x32_bf16 v[112:115], a[160:163], a[64:67], v[160:163]\n"
"buffer_load_dwordx4 v254, s[28:31], s69 offen offset:1024 sc1 lds\n"
"v_mfma_f32_16x16x32_bf16 v[112:115], a[164:167], a[80:83], v[112:115]\n"
"v_exp_f32 v86, v86\n"
"v_mfma_f32_16x16x32_bf16 v[112:115], a[168:171], a[96:99], v[112:115]\n"
"v_mfma_f32_16x16x32_bf16 v[112:115], a[172:175], a[112:115], v[112:115]\n"
"v_exp_f32 v87, v87\n"
"v_mfma_f32_16x16x32_bf16 v[116:119], a[172:175], a[116:119], v[164:167]\n"
"buffer_load_dwordx4 v254, s[28:31], s69 offen offset:2048 sc1 lds\n"
"v_mfma_f32_16x16x32_bf16 v[116:119], a[168:171], a[100:103], v[116:119]\n"
"v_exp_f32 v72, v72\n"
"v_mfma_f32_16x16x32_bf16 v[116:119], a[164:167], a[84:87], v[116:119]\n"
"v_mfma_f32_16x16x32_bf16 v[116:119], a[160:163], a[68:71], v[116:119]\n"
"v_exp_f32 v73, v73\n"
"v_mfma_f32_16x16x32_bf16 v[120:123], a[160:163], a[72:75], v[168:171]\n"
"buffer_load_dwordx4 v254, s[28:31], s69 offen offset:3072 sc1 lds\n"
"v_mfma_f32_16x16x32_bf16 v[120:123], a[164:167], a[88:91], v[120:123]\n"
"v_exp_f32 v74, v74\n"
"v_mfma_f32_16x16x32_bf16 v[120:123], a[168:171], a[104:107], v[120:123]\n"
"global_load_dword a255, v248, s[4:5]\n"
"v_mfma_f32_16x16x32_bf16 v[120:123], a[172:175], a[120:123], v[120:123]\n"
"v_exp_f32 v75, v75\n"
"v_mfma_f32_16x16x32_bf16 v[124:127], a[172:175], a[124:127], v[172:175]\n"
"v_mfma_f32_16x16x32_bf16 v[124:127], a[168:171], a[108:111], v[124:127]\n"
"v_exp_f32 v88, v88\n"
"v_mfma_f32_16x16x32_bf16 v[124:127], a[164:167], a[92:95], v[124:127]\n"
"v_mfma_f32_16x16x32_bf16 v[124:127], a[160:163], a[76:79], v[124:127]\n"
"v_exp_f32 v89, v89\n"
"v_mfma_f32_16x16x32_bf16 v[0:3], a[176:179], v[128:131], 0\n"
"v_mfma_f32_16x16x32_bf16 v[0:3], a[208:211], v[144:147], v[0:3]\n"
"v_exp_f32 v90, v90\n"
"v_mfma_f32_16x16x32_bf16 v[16:19], a[212:215], v[144:147], 0\n"
"v_mfma_f32_16x16x32_bf16 v[16:19], a[180:183], v[128:131], v[16:19]\n"
"v_exp_f32 v91, v91\n"
"v_mfma_f32_16x16x32_bf16 v[32:35], a[184:187], v[128:131], 0\n"
"v_mfma_f32_16x16x32_bf16 v[32:35], a[216:219], v[144:147], v[32:35]\n"
"v_exp_f32 v76, v76\n"
"v_mfma_f32_16x16x32_bf16 v[48:51], a[220:223], v[144:147], 0\n"
"s_waitcnt lgkmcnt(0)\n"
"v_mfma_f32_16x16x32_bf16 v[48:51], a[188:191], v[128:131], v[48:51]\n"
"v_exp_f32 v77, v77\n"
"v_mfma_f32_16x16x32_bf16 a[0:3], a[192:195], v[128:131], 0\n"
"v_mfma_f32_16x16x32_bf16 a[0:3], a[224:227], v[144:147], a[0:3]\n"
"v_exp_f32 v78, v78\n"
"v_mfma_f32_16x16x32_bf16 a[16:19], a[228:231], v[144:147], 0\n"
"v_mfma_f32_16x16x32_bf16 a[16:19], a[196:199], v[128:131], a[16:19]\n"
"v_exp_f32 v79, v79\n"
"v_mfma_f32_16x16x32_bf16 a[32:35], a[200:203], v[128:131], 0\n"
"v_exp_f32 v92, v92\n"
"v_mfma_f32_16x16x32_bf16 a[32:35], a[232:235], v[144:147], a[32:35]\n"
"v_exp_f32 v93, v93\n"
"v_mfma_f32_16x16x32_bf16 a[48:51], a[236:239], v[144:147], 0\n"
"v_exp_f32 v94, v94\n"
"v_mfma_f32_16x16x32_bf16 a[48:51], a[204:207], v[128:131], a[48:51]\n"
"v_exp_f32 v95, v95\n"
"v_mfma_f32_16x16x32_bf16 a[128:131], v[200:203], v[128:131], 0\n"
"v_exp_f32 v96, v96\n"
"v_mfma_f32_16x16x32_bf16 a[128:131], v[200:203], v[144:147], a[128:131]\n"
"v_exp_f32 v97, v97\n"
"v_mfma_f32_16x16x32_bf16 v[4:7], a[176:179], v[132:135], 0\n"
"v_cvt_pk_bf16_f32 v128, v64, v65\n"
"v_cvt_pk_bf16_f32 v129, v66, v67\n"
"v_mfma_f32_16x16x32_bf16 v[4:7], a[208:211], v[148:151], v[4:7]\n"
"v_exp_f32 v98, v98\n"
"v_mfma_f32_16x16x32_bf16 v[20:23], a[212:215], v[148:151], 0\n"
"v_cvt_pk_bf16_f32 v130, v80, v81\n"
"v_cvt_pk_bf16_f32 v131, v82, v83\n"
"v_mfma_f32_16x16x32_bf16 v[20:23], a[180:183], v[132:135], v[20:23]\n"
"v_exp_f32 v99, v99\n"
"v_mfma_f32_16x16x32_bf16 v[36:39], a[184:187], v[132:135], 0\n"
"v_exp_f32 v112, v112\n"
"v_mfma_f32_16x16x32_bf16 v[36:39], a[216:219], v[148:151], v[36:39]\n"
"v_exp_f32 v113, v113\n"
"v_mfma_f32_16x16x32_bf16 v[52:55], a[220:223], v[148:151], 0\n"
"v_exp_f32 v114, v114\n"
"v_mfma_f32_16x16x32_bf16 v[52:55], a[188:191], v[132:135], v[52:55]\n"
"v_exp_f32 v115, v115\n"
"v_mfma_f32_16x16x32_bf16 a[4:7], a[192:195], v[132:135], 0\n"
"v_exp_f32 v100, v100\n"
"v_mfma_f32_16x16x32_bf16 a[4:7], a[224:227], v[148:151], a[4:7]\n"
"v_exp_f32 v101, v101\n"
"v_mfma_f32_16x16x32_bf16 a[20:23], a[228:231], v[148:151], 0\n"
"v_cvt_pk_bf16_f32 v144, v96, v97\n"
"v_cvt_pk_bf16_f32 v145, v98, v99\n"
"v_mfma_f32_16x16x32_bf16 a[20:23], a[196:199], v[132:135], a[20:23]\n"
"v_exp_f32 v102, v102\n"
"v_mfma_f32_16x16x32_bf16 a[36:39], a[200:203], v[132:135], 0\n"
"v_cvt_pk_bf16_f32 v146, v112, v113\n"
"v_cvt_pk_bf16_f32 v147, v114, v115\n"
"v_mfma_f32_16x16x32_bf16 a[36:39], a[232:235], v[148:151], a[36:39]\n"
"v_exp_f32 v103, v103\n"
"v_mfma_f32_16x16x32_bf16 a[52:55], a[236:239], v[148:151], 0\n"
"v_exp_f32 v116, v116\n"
"v_mfma_f32_16x16x32_bf16 a[52:55], a[204:207], v[132:135], a[52:55]\n"
"v_exp_f32 v117, v117\n"
"v_mfma_f32_16x16x32_bf16 a[132:135], v[200:203], v[132:135], 0\n"
"v_mfma_f32_16x16x32_bf16 a[132:135], v[200:203], v[148:151], a[132:135]\n"
"v_exp_f32 v118, v118\n"
"v_mfma_f32_16x16x32_bf16 v[8:11], a[176:179], v[136:139], 0\n"
"v_cvt_pk_bf16_f32 v132, v68, v69\n"
"v_cvt_pk_bf16_f32 v133, v70, v71\n"
"v_mfma_f32_16x16x32_bf16 v[8:11], a[208:211], v[152:155], v[8:11]\n"
"v_exp_f32 v119, v119\n"
"v_mfma_f32_16x16x32_bf16 v[24:27], a[212:215], v[152:155], 0\n"
"v_cvt_pk_bf16_f32 v134, v84, v85\n"
"v_cvt_pk_bf16_f32 v135, v86, v87\n"
"v_mfma_f32_16x16x32_bf16 v[24:27], a[180:183], v[136:139], v[24:27]\n"
"v_exp_f32 v104, v104\n"
"v_mfma_f32_16x16x32_bf16 v[40:43], a[184:187], v[136:139], 0\n"
"v_cvt_pk_bf16_f32 v148, v100, v101\n"
"v_cvt_pk_bf16_f32 v149, v102, v103\n"
"v_mfma_f32_16x16x32_bf16 v[40:43], a[216:219], v[152:155], v[40:43]\n"
"v_exp_f32 v105, v105\n"
"v_mfma_f32_16x16x32_bf16 v[56:59], a[220:223], v[152:155], 0\n"
"v_mfma_f32_16x16x32_bf16 v[56:59], a[188:191], v[136:139], v[56:59]\n"
"v_mfma_f32_16x16x32_bf16 a[8:11], a[192:195], v[136:139], 0\n"
"s_waitcnt vmcnt(1) lgkmcnt(0)\n"
"s_barrier\n"
"v_mfma_f32_16x16x32_bf16 a[8:11], a[224:227], v[152:155], a[8:11]\n"
"v_exp_f32 v106, v106\n"
"v_mfma_f32_16x16x32_bf16 a[24:27], a[228:231], v[152:155], 0\n"
"v_xor_b32 v192, 0x8000, v192\n"
"v_xor_b32 v193, 0x8000, v193\n"
"v_mfma_f32_16x16x32_bf16 a[24:27], a[196:199], v[136:139], a[24:27]\n"
"v_exp_f32 v107, v107\n"
"v_mfma_f32_16x16x32_bf16 a[40:43], a[200:203], v[136:139], 0\n"
"ds_read_b128 a[144:147], v192\n"
"ds_read_b128 a[148:151], v192 offset:1024\n"
"v_mfma_f32_16x16x32_bf16 a[40:43], a[232:235], v[152:155], a[40:43]\n"
"v_exp_f32 v120, v120\n"
"v_mfma_f32_16x16x32_bf16 a[56:59], a[236:239], v[152:155], 0\n"
"ds_read_b128 a[152:155], v192 offset:2048\n"
"ds_read_b128 a[156:159], v192 offset:3072\n"
"v_mfma_f32_16x16x32_bf16 a[56:59], a[204:207], v[136:139], a[56:59]\n"
"v_exp_f32 v121, v121\n"
"v_mfma_f32_16x16x32_bf16 a[136:139], v[200:203], v[136:139], 0\n"
"ds_read_b128 a[160:163], v192 offset:4096\n"
"ds_read_b128 a[164:167], v192 offset:5120\n"
"v_mfma_f32_16x16x32_bf16 a[136:139], v[200:203], v[152:155], a[136:139]\n"
"v_exp_f32 v122, v122\n"
"v_mfma_f32_16x16x32_bf16 v[12:15], a[176:179], v[140:143], 0\n"
"ds_read_b128 a[168:171], v192 offset:6144\n"
"ds_read_b128 a[172:175], v192 offset:7168\n"
"v_mfma_f32_16x16x32_bf16 v[12:15], a[208:211], v[156:159], v[12:15]\n"
"v_exp_f32 v123, v123\n"
"v_mfma_f32_16x16x32_bf16 v[28:31], a[212:215], v[156:159], 0\n"
"v_cvt_pk_bf16_f32 v136, v72, v73\n"
"v_cvt_pk_bf16_f32 v137, v74, v75\n"
"v_mfma_f32_16x16x32_bf16 v[28:31], a[180:183], v[140:143], v[28:31]\n"
"v_exp_f32 v108, v108\n"
"v_mfma_f32_16x16x32_bf16 v[44:47], a[184:187], v[140:143], 0\n"
"ds_read_b128 a[176:179], v193\n"
"ds_read_b128 a[208:211], v193 offset:8192\n"
"v_mfma_f32_16x16x32_bf16 v[44:47], a[216:219], v[156:159], v[44:47]\n"
"v_exp_f32 v109, v109\n"
"v_mfma_f32_16x16x32_bf16 v[60:63], a[220:223], v[156:159], 0\n"
"ds_read_b128 a[180:183], v193 offset:1024\n"
"ds_read_b128 a[212:215], v193 offset:9216\n"
"v_mfma_f32_16x16x32_bf16 v[60:63], a[188:191], v[140:143], v[60:63]\n"
"v_exp_f32 v110, v110\n"
"v_mfma_f32_16x16x32_bf16 a[12:15], a[192:195], v[140:143], 0\n"
"v_cvt_pk_bf16_f32 v138, v88, v89\n"
"v_cvt_pk_bf16_f32 v139, v90, v91\n"
"v_mfma_f32_16x16x32_bf16 a[12:15], a[224:227], v[156:159], a[12:15]\n"
"v_exp_f32 v111, v111\n"
"v_mfma_f32_16x16x32_bf16 a[28:31], a[228:231], v[156:159], 0\n"
"v_cvt_pk_bf16_f32 v150, v116, v117\n"
"v_cvt_pk_bf16_f32 v151, v118, v119\n"
"v_mfma_f32_16x16x32_bf16 a[28:31], a[196:199], v[140:143], a[28:31]\n"
"v_exp_f32 v124, v124\n"
"v_mfma_f32_16x16x32_bf16 a[44:47], a[200:203], v[140:143], 0\n"
"v_cvt_pk_bf16_f32 v152, v104, v105\n"
"v_cvt_pk_bf16_f32 v153, v106, v107\n"
"v_mfma_f32_16x16x32_bf16 a[44:47], a[232:235], v[156:159], a[44:47]\n"
"v_exp_f32 v125, v125\n"
"v_mfma_f32_16x16x32_bf16 a[60:63], a[236:239], v[156:159], 0\n"
"v_xor_b32 v194, 0x8000, v194\n"
"v_cvt_pk_bf16_f32 v154, v120, v121\n"
"v_mfma_f32_16x16x32_bf16 a[60:63], a[204:207], v[140:143], a[60:63]\n"
"v_exp_f32 v126, v126\n"
"v_mfma_f32_16x16x32_bf16 a[140:143], v[200:203], v[140:143], 0\n"
"s_add_u32 s62, s62, 1\n"
"s_mov_b64 exec, s[34:35]\n"
"buffer_store_dwordx2 v[250:251], v249, s[92:95], s53 offen sc1\n"
"s_mov_b64 exec, -1\n"
"s_cmp_eq_u32 s8, 3\n"
"v_cvt_pk_bf16_f32 v155, v122, v123\n"
"v_mfma_f32_16x16x32_bf16 a[140:143], v[200:203], v[156:159], a[140:143]\n"
"v_exp_f32 v127, v127\n"
"s_cbranch_scc1 .Lfullx2_27_%=\n"
"s_nop 0\n"
".Ldiagx2_31_%=:\n"
"s_waitcnt lgkmcnt(8)\n"
"v_mfma_f32_16x16x32_bf16 v[64:67], a[144:147], a[64:67], v[176:179]\n"
"v_cvt_pk_bf16_f32 v140, v76, v77\n"
"v_cvt_pk_bf16_f32 v141, v78, v79\n"
"v_mfma_f32_16x16x32_bf16 v[64:67], a[148:151], a[80:83], v[64:67]\n"
"s_xor_b32 s85, s85, 0x8000\n"
"v_mfma_f32_16x16x32_bf16 v[64:67], a[152:155], a[96:99], v[64:67]\n"
"v_cvt_pk_bf16_f32 v142, v92, v93\n"
"v_cvt_pk_bf16_f32 v143, v94, v95\n"
"v_mfma_f32_16x16x32_bf16 v[64:67], a[156:159], a[112:115], v[64:67]\n"
"s_mov_b32 m0, s85\n"
"v_mfma_f32_16x16x32_bf16 v[68:71], a[156:159], a[116:119], v[164:167]\n"
"v_cvt_pk_bf16_f32 v156, v108, v109\n"
"v_cvt_pk_bf16_f32 v157, v110, v111\n"
"v_mfma_f32_16x16x32_bf16 v[68:71], a[152:155], a[100:103], v[68:71]\n"
"buffer_load_dwordx4 v254, s[96:99], s67 offen sc1 lds\n"
"v_mfma_f32_16x16x32_bf16 v[68:71], a[148:151], a[84:87], v[68:71]\n"
"v_cvt_pk_bf16_f32 v158, v124, v125\n"
"v_cvt_pk_bf16_f32 v159, v126, v127\n"
"v_mfma_f32_16x16x32_bf16 v[68:71], a[144:147], a[68:71], v[68:71]\n"
"buffer_load_dwordx4 v254, s[96:99], s67 offen offset:1024 sc1 lds\n"
"v_mfma_f32_16x16x32_bf16 v[72:75], a[144:147], a[72:75], v[168:171]\n"
"ds_read_b128 a[184:187], v193 offset:2048\n"
"ds_read_b128 a[216:219], v193 offset:10240\n"
"v_mfma_f32_16x16x32_bf16 v[72:75], a[148:151], a[88:91], v[72:75]\n"
"v_mfma_f32_16x16x32_bf16 v[72:75], a[152:155], a[104:107], v[72:75]\n"
"buffer_load_dwordx4 v254, s[96:99], s67 offen offset:2048 sc1 lds\n"
"v_mfma_f32_16x16x32_bf16 v[72:75], a[156:159], a[120:123], v[72:75]\n"
"ds_read_b128 a[188:191], v193 offset:3072\n"
"ds_read_b128 a[220:223], v193 offset:11264\n"
"v_mfma_f32_16x16x32_bf16 v[76:79], a[156:159], a[124:127], v[172:175]\n"
"v_mfma_f32_16x16x32_bf16 v[76:79], a[152:155], a[108:111], v[76:79]\n"
"ds_read_b128 a[192:195], v194 offset:4096\n"
"v_mfma_f32_16x16x32_bf16 v[76:79], a[148:151], a[92:95], v[76:79]\n"
"ds_read_b128 a[224:227], v194 offset:12288\n"
"s_waitcnt lgkmcnt(10)\n"
"v_mfma_f32_16x16x32_bf16 v[76:79], a[144:147], a[76:79], v[76:79]\n"
"buffer_load_dwordx4 v254, s[96:99], s67 offen offset:3072 sc1 lds\n"
"s_nop 3\n"
"ds_read_b128 a[144:147], v192 offset:8192\n"
"ds_read_b128 a[148:151], v192 offset:9216\n"
"s_nop 3\n"
"s_add_u32 s70, s67, 0x4000\n"
"s_nop 3\n"
"ds_read_b128 a[152:155], v192 offset:10240\n"
"ds_read_b128 a[156:159], v192 offset:11264\n"
"s_nop 3\n"
"s_add_u32 m0, s85, 0x4000\n"
"v_mfma_f32_16x16x32_bf16 v[84:87], a[172:175], a[116:119], v[180:183]\n"
"buffer_load_dwordx4 v254, s[96:99], s70 offen sc1 lds\n"
"v_mfma_f32_16x16x32_bf16 v[84:87], a[168:171], a[100:103], v[84:87]\n"
"v_exp_f32 v64, v64\n"
"v_mfma_f32_16x16x32_bf16 v[84:87], a[164:167], a[84:87], v[84:87]\n"
"buffer_load_dwordx4 v254, s[96:99], s70 offen offset:1024 sc1 lds\n"
"v_mfma_f32_16x16x32_bf16 v[84:87], a[160:163], a[68:71], v[84:87]\n"
"v_exp_f32 v65, v65\n"
"v_mfma_f32_16x16x32_bf16 v[88:91], a[160:163], a[72:75], v[168:171]\n"
"ds_read_b128 a[196:199], v194 offset:5120\n"
"ds_read_b128 a[228:231], v194 offset:13312\n"
"v_mfma_f32_16x16x32_bf16 v[88:91], a[164:167], a[88:91], v[88:91]\n"
"v_exp_f32 v66, v66\n"
"v_mfma_f32_16x16x32_bf16 v[88:91], a[168:171], a[104:107], v[88:91]\n"
"buffer_load_dwordx4 v254, s[96:99], s70 offen offset:2048 sc1 lds\n"
"v_mfma_f32_16x16x32_bf16 v[88:91], a[172:175], a[120:123], v[88:91]\n"
"v_exp_f32 v67, v67\n"
"v_mfma_f32_16x16x32_bf16 v[92:95], a[172:175], a[124:127], v[172:175]\n"
"ds_read_b128 a[200:203], v194 offset:6144\n"
"ds_read_b128 a[232:235], v194 offset:14336\n"
"v_mfma_f32_16x16x32_bf16 v[92:95], a[168:171], a[108:111], v[92:95]\n"
"buffer_load_dwordx4 v254, s[96:99], s70 offen offset:3072 sc1 lds\n"
"v_mfma_f32_16x16x32_bf16 v[92:95], a[164:167], a[92:95], v[92:95]\n"
"s_lshl_b32 s89, s66, 14\n"
"s_waitcnt lgkmcnt(4)\n"
"v_mfma_f32_16x16x32_bf16 v[92:95], a[160:163], a[76:79], v[92:95]\n"
"s_add_u32 s67, s73, s89\n"
"s_nop 3\n"
"ds_read_b128 a[160:163], v192 offset:12288\n"
"ds_read_b128 a[164:167], v192 offset:13312\n"
"s_nop 3\n"
"s_xor_b32 s84, s84, 0x8000\n"
"s_nop 3\n"
"ds_read_b128 a[168:171], v192 offset:14336\n"
"ds_read_b128 a[172:175], v192 offset:15360\n"
"s_nop 3\n"
"s_mov_b32 m0, s84\n"
"s_nop 3\n"
"buffer_load_dwordx4 v254, s[28:31], s67 offen sc1 lds\n"
"s_nop 3\n"
"v_exp_f32 v68, v68\n"
"s_nop 3\n"
"buffer_load_dwordx4 v254, s[28:31], s67 offen offset:1024 sc1 lds\n"
"s_nop 3\n"
"v_exp_f32 v69, v69\n"
"v_mfma_f32_16x16x32_bf16 v[104:107], a[144:147], a[72:75], v[184:187]\n"
"ds_read_b128 a[204:207], v194 offset:7168\n"
"ds_read_b128 a[236:239], v194 offset:15360\n"
"v_mfma_f32_16x16x32_bf16 v[104:107], a[148:151], a[88:91], v[104:107]\n"
"v_exp_f32 v70, v70\n"
"v_mfma_f32_16x16x32_bf16 v[104:107], a[152:155], a[104:107], v[104:107]\n"
"buffer_load_dwordx4 v254, s[28:31], s67 offen offset:2048 sc1 lds\n"
"v_mfma_f32_16x16x32_bf16 v[104:107], a[156:159], a[120:123], v[104:107]\n"
"v_exp_f32 v71, v71\n"
"v_mfma_f32_16x16x32_bf16 v[108:111], a[156:159], a[124:127], v[172:175]\n"
"v_mfma_f32_16x16x32_bf16 v[108:111], a[152:155], a[108:111], v[108:111]\n"
"v_exp_f32 v84, v84\n"
"v_mfma_f32_16x16x32_bf16 v[108:111], a[148:151], a[92:95], v[108:111]\n"
"s_waitcnt lgkmcnt(2)\n"
"v_mfma_f32_16x16x32_bf16 v[108:111], a[144:147], a[76:79], v[108:111]\n"
"v_exp_f32 v85, v85\n"
"s_nop 3\n"
"buffer_load_dwordx4 v254, s[28:31], s67 offen offset:3072 sc1 lds\n"
"s_nop 3\n"
"v_exp_f32 v86, v86\n"
"s_nop 3\n"
"s_add_u32 s69, s67, 0x4000\n"
"s_add_u32 m0, s84, 0x4000\n"
"s_nop 3\n"
"v_exp_f32 v87, v87\n"
"s_nop 3\n"
"buffer_load_dwordx4 v254, s[28:31], s69 offen sc1 lds\n"
"s_nop 3\n"
"v_exp_f32 v72, v72\n"
"s_nop 3\n"
"buffer_load_dwordx4 v254, s[28:31], s69 offen offset:1024 sc1 lds\n"
"s_nop 3\n"
"v_exp_f32 v73, v73\n"
"s_nop 3\n"
"s_nop 3\n"
"v_exp_f32 v74, v74\n"
"s_nop 3\n"
"buffer_load_dwordx4 v254, s[28:31], s69 offen offset:2048 sc1 lds\n"
"s_nop 3\n"
"v_exp_f32 v75, v75\n"
"v_mfma_f32_16x16x32_bf16 v[124:127], a[172:175], a[124:127], v[188:191]\n"
"v_mfma_f32_16x16x32_bf16 v[124:127], a[168:171], a[108:111], v[124:127]\n"
"v_exp_f32 v88, v88\n"
"v_mfma_f32_16x16x32_bf16 v[124:127], a[164:167], a[92:95], v[124:127]\n"
"v_mfma_f32_16x16x32_bf16 v[124:127], a[160:163], a[76:79], v[124:127]\n"
"v_exp_f32 v89, v89\n"
"v_mfma_f32_16x16x32_bf16 v[0:3], a[176:179], v[128:131], v[0:3]\n"
"buffer_load_dwordx4 v254, s[28:31], s69 offen offset:3072 sc1 lds\n"
"v_mfma_f32_16x16x32_bf16 v[0:3], a[208:211], v[144:147], v[0:3]\n"
"v_exp_f32 v90, v90\n"
"v_mfma_f32_16x16x32_bf16 v[16:19], a[212:215], v[144:147], v[16:19]\n"
"global_load_dword a255, v248, s[4:5]\n"
"v_mfma_f32_16x16x32_bf16 v[16:19], a[180:183], v[128:131], v[16:19]\n"
"v_exp_f32 v91, v91\n"
"v_mfma_f32_16x16x32_bf16 v[32:35], a[184:187], v[128:131], v[32:35]\n"
"v_mfma_f32_16x16x32_bf16 v[32:35], a[216:219], v[144:147], v[32:35]\n"
"v_exp_f32 v76, v76\n"
"v_mfma_f32_16x16x32_bf16 v[48:51], a[220:223], v[144:147], v[48:51]\n"
"s_waitcnt lgkmcnt(0)\n"
"v_mfma_f32_16x16x32_bf16 v[48:51], a[188:191], v[128:131], v[48:51]\n"
"v_exp_f32 v77, v77\n"
"v_mfma_f32_16x16x32_bf16 a[0:3], a[192:195], v[128:131], a[0:3]\n"
"v_mfma_f32_16x16x32_bf16 a[0:3], a[224:227], v[144:147], a[0:3]\n"
"v_exp_f32 v78, v78\n"
"v_mfma_f32_16x16x32_bf16 a[16:19], a[228:231], v[144:147], a[16:19]\n"
"v_mfma_f32_16x16x32_bf16 a[16:19], a[196:199], v[128:131], a[16:19]\n"
"v_exp_f32 v79, v79\n"
"v_mfma_f32_16x16x32_bf16 a[32:35], a[200:203], v[128:131], a[32:35]\n"
"v_exp_f32 v92, v92\n"
"v_mfma_f32_16x16x32_bf16 a[32:35], a[232:235], v[144:147], a[32:35]\n"
"v_exp_f32 v93, v93\n"
"v_mfma_f32_16x16x32_bf16 a[48:51], a[236:239], v[144:147], a[48:51]\n"
"v_exp_f32 v94, v94\n"
"v_mfma_f32_16x16x32_bf16 a[48:51], a[204:207], v[128:131], a[48:51]\n"
"v_exp_f32 v95, v95\n"
"v_mfma_f32_16x16x32_bf16 a[128:131], v[200:203], v[128:131], a[128:131]\n"
"v_mfma_f32_16x16x32_bf16 a[128:131], v[200:203], v[144:147], a[128:131]\n"
"v_mfma_f32_16x16x32_bf16 v[4:7], a[176:179], v[132:135], v[4:7]\n"
"v_cvt_pk_bf16_f32 v128, v64, v65\n"
"v_cvt_pk_bf16_f32 v129, v66, v67\n"
"v_mfma_f32_16x16x32_bf16 v[4:7], a[208:211], v[148:151], v[4:7]\n"
"v_mfma_f32_16x16x32_bf16 v[20:23], a[212:215], v[148:151], v[20:23]\n"
"v_mov_b32 v130, 0\n"
"v_mov_b32 v131, 0\n"
"v_mfma_f32_16x16x32_bf16 v[20:23], a[180:183], v[132:135], v[20:23]\n"
"v_mfma_f32_16x16x32_bf16 v[36:39], a[184:187], v[132:135], v[36:39]\n"
"v_mov_b32 v144, 0\n"
"v_mov_b32 v145, 0\n"
"v_mfma_f32_16x16x32_bf16 v[36:39], a[216:219], v[148:151], v[36:39]\n"
"v_mfma_f32_16x16x32_bf16 v[52:55], a[220:223], v[148:151], v[52:55]\n"
"v_mov_b32 v146, 0\n"
"v_mfma_f32_16x16x32_bf16 v[52:55], a[188:191], v[132:135], v[52:55]\n"
"v_mfma_f32_16x16x32_bf16 a[4:7], a[192:195], v[132:135], a[4:7]\n"
"v_mov_b32 v147, 0\n"
"v_mfma_f32_16x16x32_bf16 a[4:7], a[224:227], v[148:151], a[4:7]\n"
"v_mfma_f32_16x16x32_bf16 a[20:23], a[228:231], v[148:151], a[20:23]\n"
"v_mfma_f32_16x16x32_bf16 a[20:23], a[196:199], v[132:135], a[20:23]\n"
"v_mfma_f32_16x16x32_bf16 a[36:39], a[200:203], v[132:135], a[36:39]\n"
"v_mfma_f32_16x16x32_bf16 a[36:39], a[232:235], v[148:151], a[36:39]\n"
"v_mfma_f32_16x16x32_bf16 a[52:55], a[236:239], v[148:151], a[52:55]\n"
"v_mfma_f32_16x16x32_bf16 a[52:55], a[204:207], v[132:135], a[52:55]\n"
"v_mfma_f32_16x16x32_bf16 a[132:135], v[200:203], v[132:135], a[132:135]\n"
"v_mfma_f32_16x16x32_bf16 a[132:135], v[200:203], v[148:151], a[132:135]\n"
"v_mfma_f32_16x16x32_bf16 v[8:11], a[176:179], v[136:139], v[8:11]\n"
"v_cvt_pk_bf16_f32 v132, v68, v69\n"
"v_cvt_pk_bf16_f32 v133, v70, v71\n"
"v_mfma_f32_16x16x32_bf16 v[8:11], a[208:211], v[152:155], v[8:11]\n"
"v_mfma_f32_16x16x32_bf16 v[24:27], a[212:215], v[152:155], v[24:27]\n"
"v_cvt_pk_bf16_f32 v134, v84, v85\n"
"v_cvt_pk_bf16_f32 v135, v86, v87\n"
"v_mfma_f32_16x16x32_bf16 v[24:27], a[180:183], v[136:139], v[24:27]\n"
"v_exp_f32 v104, v104\n"
"v_mfma_f32_16x16x32_bf16 v[40:43], a[184:187], v[136:139], v[40:43]\n"
"v_mov_b32 v148, 0\n"
"v_mov_b32 v149, 0\n"
"v_mfma_f32_16x16x32_bf16 v[40:43], a[216:219], v[152:155], v[40:43]\n"
"v_exp_f32 v105, v105\n"
"v_mfma_f32_16x16x32_bf16 v[56:59], a[220:223], v[152:155], v[56:59]\n"
"v_mfma_f32_16x16x32_bf16 v[56:59], a[188:191], v[136:139], v[56:59]\n"
"v_mfma_f32_16x16x32_bf16 a[8:11], a[192:195], v[136:139], a[8:11]\n"
"s_waitcnt vmcnt(1) lgkmcnt(0)\n"
"s_barrier\n"
"v_mfma_f32_16x16x32_bf16 a[8:11], a[224:227], v[152:155], a[8:11]\n"
"v_exp_f32 v106, v106\n"
"v_mfma_f32_16x16x32_bf16 a[24:27], a[228:231], v[152:155], a[24:27]\n"
"v_xor_b32 v192, 0x8000, v192\n"
"v_xor_b32 v193, 0x8000, v193\n"
"v_mfma_f32_16x16x32_bf16 a[24:27], a[196:199], v[136:139], a[24:27]\n"
"v_exp_f32 v107, v107\n"
"v_mfma_f32_16x16x32_bf16 a[40:43], a[200:203], v[136:139], a[40:43]\n"
"ds_read_b128 a[144:147], v192\n"
"ds_read_b128 a[148:151], v192 offset:1024\n"
"v_mfma_f32_16x16x32_bf16 a[40:43], a[232:235], v[152:155], a[40:43]\n"
"v_mfma_f32_16x16x32_bf16 a[56:59], a[236:239], v[152:155], a[56:59]\n"
"ds_read_b128 a[152:155], v192 offset:2048\n"
"ds_read_b128 a[156:159], v192 offset:3072\n"
"v_mfma_f32_16x16x32_bf16 a[56:59], a[204:207], v[136:139], a[56:59]\n"
"v_mfma_f32_16x16x32_bf16 a[136:139], v[200:203], v[136:139], a[136:139]\n"
"ds_read_b128 a[160:163], v192 offset:4096\n"
"ds_read_b128 a[164:167], v192 offset:5120\n"
"v_mfma_f32_16x16x32_bf16 a[136:139], v[200:203], v[152:155], a[136:139]\n"
"v_mfma_f32_16x16x32_bf16 v[12:15], a[176:179], v[140:143], v[12:15]\n"
"ds_read_b128 a[168:171], v192 offset:6144\n"
"ds_read_b128 a[172:175], v192 offset:7168\n"
"v_mfma_f32_16x16x32_bf16 v[12:15], a[208:211], v[156:159], v[12:15]\n"
"v_mfma_f32_16x16x32_bf16 v[28:31], a[212:215], v[156:159], v[28:31]\n"
"v_cvt_pk_bf16_f32 v136, v72, v73\n"
"v_cvt_pk_bf16_f32 v137, v74, v75\n"
"v_mfma_f32_16x16x32_bf16 v[28:31], a[180:183], v[140:143], v[28:31]\n"
"v_exp_f32 v108, v108\n"
"v_mfma_f32_16x16x32_bf16 v[44:47], a[184:187], v[140:143], v[44:47]\n"
"ds_read_b128 a[176:179], v193\n"
"ds_read_b128 a[208:211], v193 offset:8192\n"
"v_mfma_f32_16x16x32_bf16 v[44:47], a[216:219], v[156:159], v[44:47]\n"
"v_exp_f32 v109, v109\n"
"v_mfma_f32_16x16x32_bf16 v[60:63], a[220:223], v[156:159], v[60:63]\n"
"ds_read_b128 a[180:183], v193 offset:1024\n"
"ds_read_b128 a[212:215], v193 offset:9216\n"
"v_mfma_f32_16x16x32_bf16 v[60:63], a[188:191], v[140:143], v[60:63]\n"
"v_exp_f32 v110, v110\n"
"v_mfma_f32_16x16x32_bf16 a[12:15], a[192:195], v[140:143], a[12:15]\n"
"v_cvt_pk_bf16_f32 v138, v88, v89\n"
"v_cvt_pk_bf16_f32 v139, v90, v91\n"
"v_mfma_f32_16x16x32_bf16 a[12:15], a[224:227], v[156:159], a[12:15]\n"
"v_exp_f32 v111, v111\n"
"v_mfma_f32_16x16x32_bf16 a[28:31], a[228:231], v[156:159], a[28:31]\n"
"v_mov_b32 v150, 0\n"
"v_mov_b32 v151, 0\n"
"v_mfma_f32_16x16x32_bf16 a[28:31], a[196:199], v[140:143], a[28:31]\n"
"v_exp_f32 v124, v124\n"
"v_mfma_f32_16x16x32_bf16 a[44:47], a[200:203], v[140:143], a[44:47]\n"
"v_cvt_pk_bf16_f32 v152, v104, v105\n"
"v_cvt_pk_bf16_f32 v153, v106, v107\n"
"v_mfma_f32_16x16x32_bf16 a[44:47], a[232:235], v[156:159], a[44:47]\n"
"v_exp_f32 v125, v125\n"
"v_mfma_f32_16x16x32_bf16 a[60:63], a[236:239], v[156:159], a[60:63]\n"
"v_xor_b32 v194, 0x8000, v194\n"
"v_mov_b32 v154, 0\n"
"v_mfma_f32_16x16x32_bf16 a[60:63], a[204:207], v[140:143], a[60:63]\n"
"v_exp_f32 v126, v126\n"
"v_mfma_f32_16x16x32_bf16 a[140:143], v[200:203], v[140:143], a[140:143]\n"
"s_add_u32 s62, s62, 1\n"
"v_mov_b32 v155, 0\n"
"v_mfma_f32_16x16x32_bf16 a[140:143], v[200:203], v[156:159], a[140:143]\n"
"v_exp_f32 v127, v127\n"
"s_branch .Lph2_45_%=\n"
".Ldiagx1z_30_%=:\n"
"s_waitcnt lgkmcnt(8)\n"
"v_mfma_f32_16x16x32_bf16 v[64:67], a[144:147], a[64:67], v[176:179]\n"
"v_cvt_pk_bf16_f32 v140, v76, v77\n"
"v_cvt_pk_bf16_f32 v141, v78, v79\n"
"v_mfma_f32_16x16x32_bf16 v[64:67], a[148:151], a[80:83], v[64:67]\n"
"s_xor_b32 s85, s85, 0x8000\n"
"v_mfma_f32_16x16x32_bf16 v[64:67], a[152:155], a[96:99], v[64:67]\n"
"v_cvt_pk_bf16_f32 v142, v92, v93\n"
"v_cvt_pk_bf16_f32 v143, v94, v95\n"
"v_mfma_f32_16x16x32_bf16 v[64:67], a[156:159], a[112:115], v[64:67]\n"
"s_mov_b32 m0, s85\n"
"v_mfma_f32_16x16x32_bf16 v[68:71], a[156:159], a[116:119], v[164:167]\n"
"v_cvt_pk_bf16_f32 v156, v108, v109\n"
"v_cvt_pk_bf16_f32 v157, v110, v111\n"
"v_mfma_f32_16x16x32_bf16 v[68:71], a[152:155], a[100:103], v[68:71]\n"
"buffer_load_dwordx4 v254, s[96:99], s67 offen sc1 lds\n"
"v_mfma_f32_16x16x32_bf16 v[68:71], a[148:151], a[84:87], v[68:71]\n"
"v_cvt_pk_bf16_f32 v158, v124, v125\n"
"v_cvt_pk_bf16_f32 v159, v126, v127\n"
"v_mfma_f32_16x16x32_bf16 v[68:71], a[144:147], a[68:71], v[68:71]\n"
"buffer_load_dwordx4 v254, s[96:99], s67 offen offset:1024 sc1 lds\n"
"v_mfma_f32_16x16x32_bf16 v[72:75], a[144:147], a[72:75], v[168:171]\n"
"ds_read_b128 a[184:187], v193 offset:2048\n"
"ds_read_b128 a[216:219], v193 offset:10240\n"
"v_mfma_f32_16x16x32_bf16 v[72:75], a[148:151], a[88:91], v[72:75]\n"
"v_mfma_f32_16x16x32_bf16 v[72:75], a[152:155], a[104:107], v[72:75]\n"
"ds_read_b128 a[188:191], v193 offset:3072\n"
"v_mfma_f32_16x16x32_bf16 v[72:75], a[156:159], a[120:123], v[72:75]\n"
"buffer_load_dwordx4 v254, s[96:99], s67 offen offset:2048 sc1 lds\n"
"v_mfma_f32_16x16x32_bf16 v[76:79], a[156:159], a[124:127], v[172:175]\n"
"ds_read_b128 a[220:223], v193 offset:11264\n"
"ds_read_b128 a[192:195], v194 offset:4096\n"
"v_mfma_f32_16x16x32_bf16 v[76:79], a[152:155], a[108:111], v[76:79]\n"
"v_mfma_f32_16x16x32_bf16 v[76:79], a[148:151], a[92:95], v[76:79]\n"
"ds_read_b128 a[224:227], v194 offset:12288\n"
"s_waitcnt lgkmcnt(10)\n"
"v_mfma_f32_16x16x32_bf16 v[76:79], a[144:147], a[76:79], v[76:79]\n"
"s_nop 3\n"
"ds_read_b128 a[144:147], v192 offset:8192\n"
"ds_read_b128 a[148:151], v192 offset:9216\n"
"s_nop 3\n"
"buffer_load_dwordx4 v254, s[96:99], s67 offen offset:3072 sc1 lds\n"
"s_nop 3\n"
"ds_read_b128 a[152:155], v192 offset:10240\n"
"ds_read_b128 a[156:159], v192 offset:11264\n"
"s_nop 3\n"
"s_sub_u32 s89, 32, s66\n"
"v_mfma_f32_16x16x32_bf16 v[84:87], a[172:175], a[116:119], v[180:183]\n"
"s_lshl_b32 s89, s89, 14\n"
"s_add_u32 s67, s73, s89\n"
"v_mfma_f32_16x16x32_bf16 v[84:87], a[168:171], a[100:103], v[84:87]\n"
"v_exp_f32 v64, v64\n"
"v_mfma_f32_16x16x32_bf16 v[84:87], a[164:167], a[84:87], v[84:87]\n"
"s_xor_b32 s84, s84, 0x8000\n"
"s_mov_b32 m0, s84\n"
"v_mfma_f32_16x16x32_bf16 v[84:87], a[160:163], a[68:71], v[84:87]\n"
"v_exp_f32 v65, v65\n"
"v_mfma_f32_16x16x32_bf16 v[88:91], a[160:163], a[72:75], v[168:171]\n"
"buffer_load_dwordx4 v254, s[28:31], s67 offen sc1 lds\n"
"v_mfma_f32_16x16x32_bf16 v[88:91], a[164:167], a[88:91], v[88:91]\n"
"v_exp_f32 v66, v66\n"
"v_mfma_f32_16x16x32_bf16 v[88:91], a[168:171], a[104:107], v[88:91]\n"
"buffer_load_dwordx4 v254, s[28:31], s67 offen offset:1024 sc1 lds\n"
"v_mfma_f32_16x16x32_bf16 v[88:91], a[172:175], a[120:123], v[88:91]\n"
"v_exp_f32 v67, v67\n"
"v_mfma_f32_16x16x32_bf16 v[92:95], a[172:175], a[124:127], v[172:175]\n"
"ds_read_b128 a[196:199], v194 offset:5120\n"
"ds_read_b128 a[228:231], v194 offset:13312\n"
"v_mfma_f32_16x16x32_bf16 v[92:95], a[168:171], a[108:111], v[92:95]\n"
"v_mfma_f32_16x16x32_bf16 v[92:95], a[164:167], a[92:95], v[92:95]\n"
"ds_read_b128 a[200:203], v194 offset:6144\n"
"s_waitcnt lgkmcnt(3)\n"
"v_mfma_f32_16x16x32_bf16 v[92:95], a[160:163], a[76:79], v[92:95]\n"
"buffer_load_dwordx4 v254, s[28:31], s67 offen offset:2048 sc1 lds\n"
"s_nop 3\n"
"ds_read_b128 a[160:163], v192 offset:12288\n"
"ds_read_b128 a[164:167], v192 offset:13312\n"
"s_nop 3\n"
"s_nop 3\n"
"ds_read_b128 a[168:171], v192 offset:14336\n"
"ds_read_b128 a[172:175], v192 offset:15360\n"
"s_nop 3\n"
"buffer_load_dwordx4 v254, s[28:31], s67 offen offset:3072 sc1 lds\n"
"s_nop 3\n"
"s_add_u32 s69, s67, 0x4000\n"
"s_add_u32 m0, s84, 0x4000\n"
"s_nop 3\n"
"v_exp_f32 v68, v68\n"
"s_nop 3\n"
"ds_read_b128 a[232:235], v194 offset:14336\n"
"ds_read_b128 a[204:207], v194 offset:7168\n"
"s_nop 3\n"
"v_exp_f32 v69, v69\n"
"v_mfma_f32_16x16x32_bf16 v[104:107], a[144:147], a[72:75], v[184:187]\n"
"buffer_load_dwordx4 v254, s[28:31], s69 offen sc1 lds\n"
"v_mfma_f32_16x16x32_bf16 v[104:107], a[148:151], a[88:91], v[104:107]\n"
"v_exp_f32 v70, v70\n"
"v_mfma_f32_16x16x32_bf16 v[104:107], a[152:155], a[104:107], v[104:107]\n"
"ds_read_b128 a[236:239], v194 offset:15360\n"
"v_mfma_f32_16x16x32_bf16 v[104:107], a[156:159], a[120:123], v[104:107]\n"
"v_exp_f32 v71, v71\n"
"v_mfma_f32_16x16x32_bf16 v[108:111], a[156:159], a[124:127], v[172:175]\n"
"buffer_load_dwordx4 v254, s[28:31], s69 offen offset:1024 sc1 lds\n"
"v_mfma_f32_16x16x32_bf16 v[108:111], a[152:155], a[108:111], v[108:111]\n"
"v_exp_f32 v84, v84\n"
"v_mfma_f32_16x16x32_bf16 v[108:111], a[148:151], a[92:95], v[108:111]\n"
"s_waitcnt lgkmcnt(3)\n"
"v_mfma_f32_16x16x32_bf16 v[108:111], a[144:147], a[76:79], v[108:111]\n"
"v_exp_f32 v85, v85\n"
"s_nop 3\n"
"buffer_load_dwordx4 v254, s[28:31], s69 offen offset:2048 sc1 lds\n"
"s_nop 3\n"
"v_exp_f32 v86, v86\n"
"s_nop 3\n"
"s_nop 3\n"
"v_exp_f32 v87, v87\n"
"s_nop 3\n"
"s_nop 3\n"
"v_exp_f32 v72, v72\n"
"s_nop 3\n"
"buffer_load_dwordx4 v254, s[28:31], s69 offen offset:3072 sc1 lds\n"
"s_nop 3\n"
"v_exp_f32 v73, v73\n"
"s_nop 3\n"
"global_load_dword a255, v248, s[4:5]\n"
"s_nop 3\n"
"v_exp_f32 v74, v74\n"
"s_nop 3\n"
"s_nop 3\n"
"v_exp_f32 v75, v75\n"
"v_mfma_f32_16x16x32_bf16 v[124:127], a[172:175], a[124:127], v[188:191]\n"
"v_mfma_f32_16x16x32_bf16 v[124:127], a[168:171], a[108:111], v[124:127]\n"
"v_exp_f32 v88, v88\n"
"v_mfma_f32_16x16x32_bf16 v[124:127], a[164:167], a[92:95], v[124:127]\n"
"v_mfma_f32_16x16x32_bf16 v[124:127], a[160:163], a[76:79], v[124:127]\n"
"v_exp_f32 v89, v89\n"
"v_mfma_f32_16x16x32_bf16 v[0:3], a[176:179], v[128:131], 0\n"
"v_mfma_f32_16x16x32_bf16 v[0:3], a[208:211], v[144:147], v[0:3]\n"
"v_exp_f32 v90, v90\n"
"v_mfma_f32_16x16x32_bf16 v[16:19], a[212:215], v[144:147], 0\n"
"v_mfma_f32_16x16x32_bf16 v[16:19], a[180:183], v[128:131], v[16:19]\n"
"v_exp_f32 v91, v91\n"
"v_mfma_f32_16x16x32_bf16 v[32:35], a[184:187], v[128:131], 0\n"
"v_mfma_f32_16x16x32_bf16 v[32:35], a[216:219], v[144:147], v[32:35]\n"
"v_exp_f32 v76, v76\n"
"v_mfma_f32_16x16x32_bf16 v[48:51], a[220:223], v[144:147], 0\n"
"s_waitcnt lgkmcnt(0)\n"
"v_mfma_f32_16x16x32_bf16 v[48:51], a[188:191], v[128:131], v[48:51]\n"
"v_exp_f32 v77, v77\n"
"v_mfma_f32_16x16x32_bf16 a[0:3], a[192:195], v[128:131], 0\n"
"v_mfma_f32_16x16x32_bf16 a[0:3], a[224:227], v[144:147], a[0:3]\n"
"v_exp_f32 v78, v78\n"
"v_mfma_f32_16x16x32_bf16 a[16:19], a[228:231], v[144:147], 0\n"
"v_mfma_f32_16x16x32_bf16 a[16:19], a[196:199], v[128:131], a[16:19]\n"
"v_exp_f32 v79, v79\n"
"v_mfma_f32_16x16x32_bf16 a[32:35], a[200:203], v[128:131], 0\n"
"v_exp_f32 v92, v92\n"
"v_mfma_f32_16x16x32_bf16 a[32:35], a[232:235], v[144:147], a[32:35]\n"
"v_exp_f32 v93, v93\n"
"v_mfma_f32_16x16x32_bf16 a[48:51], a[236:239], v[144:147], 0\n"
"v_exp_f32 v94, v94\n"
"v_mfma_f32_16x16x32_bf16 a[48:51], a[204:207], v[128:131], a[48:51]\n"
"v_exp_f32 v95, v95\n"
"v_mfma_f32_16x16x32_bf16 a[128:131], v[200:203], v[128:131], 0\n"
"v_mfma_f32_16x16x32_bf16 a[128:131], v[200:203], v[144:147], a[128:131]\n"
"v_mfma_f32_16x16x32_bf16 v[4:7], a[176:179], v[132:135], 0\n"
"v_cvt_pk_bf16_f32 v128, v64, v65\n"
"v_cvt_pk_bf16_f32 v129, v66, v67\n"
"v_mfma_f32_16x16x32_bf16 v[4:7], a[208:211], v[148:151], v[4:7]\n"
"v_mfma_f32_16x16x32_bf16 v[20:23], a[212:215], v[148:151], 0\n"
"v_mov_b32 v130, 0\n"
"v_mov_b32 v131, 0\n"
"v_mfma_f32_16x16x32_bf16 v[20:23], a[180:183], v[132:135], v[20:23]\n"
"v_mfma_f32_16x16x32_bf16 v[36:39], a[184:187], v[132:135], 0\n"
"v_mov_b32 v144, 0\n"
"v_mov_b32 v145, 0\n"
"v_mfma_f32_16x16x32_bf16 v[36:39], a[216:219], v[148:151], v[36:39]\n"
"v_mfma_f32_16x16x32_bf16 v[52:55], a[220:223], v[148:151], 0\n"
"v_mov_b32 v146, 0\n"
"v_mfma_f32_16x16x32_bf16 v[52:55], a[188:191], v[132:135], v[52:55]\n"
"v_mfma_f32_16x16x32_bf16 a[4:7], a[192:195], v[132:135], 0\n"
"v_mov_b32 v147, 0\n"
"v_mfma_f32_16x16x32_bf16 a[4:7], a[224:227], v[148:151], a[4:7]\n"
"v_mfma_f32_16x16x32_bf16 a[20:23], a[228:231], v[148:151], 0\n"
"v_mfma_f32_16x16x32_bf16 a[20:23], a[196:199], v[132:135], a[20:23]\n"
"v_mfma_f32_16x16x32_bf16 a[36:39], a[200:203], v[132:135], 0\n"
"v_mfma_f32_16x16x32_bf16 a[36:39], a[232:235], v[148:151], a[36:39]\n"
"v_mfma_f32_16x16x32_bf16 a[52:55], a[236:239], v[148:151], 0\n"
"v_mfma_f32_16x16x32_bf16 a[52:55], a[204:207], v[132:135], a[52:55]\n"
"v_mfma_f32_16x16x32_bf16 a[132:135], v[200:203], v[132:135], 0\n"
"v_mfma_f32_16x16x32_bf16 a[132:135], v[200:203], v[148:151], a[132:135]\n"
"v_mfma_f32_16x16x32_bf16 v[8:11], a[176:179], v[136:139], 0\n"
"v_cvt_pk_bf16_f32 v132, v68, v69\n"
"v_cvt_pk_bf16_f32 v133, v70, v71\n"
"v_mfma_f32_16x16x32_bf16 v[8:11], a[208:211], v[152:155], v[8:11]\n"
"v_mfma_f32_16x16x32_bf16 v[24:27], a[212:215], v[152:155], 0\n"
"v_cvt_pk_bf16_f32 v134, v84, v85\n"
"v_cvt_pk_bf16_f32 v135, v86, v87\n"
"v_mfma_f32_16x16x32_bf16 v[24:27], a[180:183], v[136:139], v[24:27]\n"
"v_exp_f32 v104, v104\n"
"v_mfma_f32_16x16x32_bf16 v[40:43], a[184:187], v[136:139], 0\n"
"v_mov_b32 v148, 0\n"
"v_mov_b32 v149, 0\n"
"v_mfma_f32_16x16x32_bf16 v[40:43], a[216:219], v[152:155], v[40:43]\n"
"v_exp_f32 v105, v105\n"
"v_mfma_f32_16x16x32_bf16 v[56:59], a[220:223], v[152:155], 0\n"
"v_mfma_f32_16x16x32_bf16 v[56:59], a[188:191], v[136:139], v[56:59]\n"
"v_mfma_f32_16x16x32_bf16 a[8:11], a[192:195], v[136:139], 0\n"
"s_waitcnt vmcnt(1) lgkmcnt(0)\n"
"s_barrier\n"
"v_mfma_f32_16x16x32_bf16 a[8:11], a[224:227], v[152:155], a[8:11]\n"
"v_exp_f32 v106, v106\n"
"v_mfma_f32_16x16x32_bf16 a[24:27], a[228:231], v[152:155], 0\n"
"v_xor_b32 v192, 0x8000, v192\n"
"v_xor_b32 v193, 0x8000, v193\n"
"v_mfma_f32_16x16x32_bf16 a[24:27], a[196:199], v[136:139], a[24:27]\n"
"v_exp_f32 v107, v107\n"
"v_mfma_f32_16x16x32_bf16 a[40:43], a[200:203], v[136:139], 0\n"
"ds_read_b128 a[144:147], v192\n"
"ds_read_b128 a[148:151], v192 offset:1024\n"
"v_mfma_f32_16x16x32_bf16 a[40:43], a[232:235], v[152:155], a[40:43]\n"
"v_mfma_f32_16x16x32_bf16 a[56:59], a[236:239], v[152:155], 0\n"
"ds_read_b128 a[152:155], v192 offset:2048\n"
"ds_read_b128 a[156:159], v192 offset:3072\n"
"v_mfma_f32_16x16x32_bf16 a[56:59], a[204:207], v[136:139], a[56:59]\n"
"v_mfma_f32_16x16x32_bf16 a[136:139], v[200:203], v[136:139], 0\n"
"ds_read_b128 a[160:163], v192 offset:4096\n"
"ds_read_b128 a[164:167], v192 offset:5120\n"
"v_mfma_f32_16x16x32_bf16 a[136:139], v[200:203], v[152:155], a[136:139]\n"
"v_mfma_f32_16x16x32_bf16 v[12:15], a[176:179], v[140:143], 0\n"
"ds_read_b128 a[168:171], v192 offset:6144\n"
"ds_read_b128 a[172:175], v192 offset:7168\n"
"v_mfma_f32_16x16x32_bf16 v[12:15], a[208:211], v[156:159], v[12:15]\n"
"v_mfma_f32_16x16x32_bf16 v[28:31], a[212:215], v[156:159], 0\n"
"v_cvt_pk_bf16_f32 v136, v72, v73\n"
"v_cvt_pk_bf16_f32 v137, v74, v75\n"
"v_mfma_f32_16x16x32_bf16 v[28:31], a[180:183], v[140:143], v[28:31]\n"
"v_exp_f32 v108, v108\n"
"v_mfma_f32_16x16x32_bf16 v[44:47], a[184:187], v[140:143], 0\n"
"ds_read_b128 a[176:179], v193\n"
"ds_read_b128 a[208:211], v193 offset:8192\n"
"v_mfma_f32_16x16x32_bf16 v[44:47], a[216:219], v[156:159], v[44:47]\n"
"v_exp_f32 v109, v109\n"
"v_mfma_f32_16x16x32_bf16 v[60:63], a[220:223], v[156:159], 0\n"
"ds_read_b128 a[180:183], v193 offset:1024\n"
"ds_read_b128 a[212:215], v193 offset:9216\n"
"v_mfma_f32_16x16x32_bf16 v[60:63], a[188:191], v[140:143], v[60:63]\n"
"v_exp_f32 v110, v110\n"
"v_mfma_f32_16x16x32_bf16 a[12:15], a[192:195], v[140:143], 0\n"
"v_cvt_pk_bf16_f32 v138, v88, v89\n"
"v_cvt_pk_bf16_f32 v139, v90, v91\n"
"v_mfma_f32_16x16x32_bf16 a[12:15], a[224:227], v[156:159], a[12:15]\n"
"v_exp_f32 v111, v111\n"
"v_mfma_f32_16x16x32_bf16 a[28:31], a[228:231], v[156:159], 0\n"
"v_mov_b32 v150, 0\n"
"v_mov_b32 v151, 0\n"
"v_mfma_f32_16x16x32_bf16 a[28:31], a[196:199], v[140:143], a[28:31]\n"
"v_exp_f32 v124, v124\n"
"v_mfma_f32_16x16x32_bf16 a[44:47], a[200:203], v[140:143], 0\n"
"v_cvt_pk_bf16_f32 v152, v104, v105\n"
"v_cvt_pk_bf16_f32 v153, v106, v107\n"
"v_mfma_f32_16x16x32_bf16 a[44:47], a[232:235], v[156:159], a[44:47]\n"
"v_exp_f32 v125, v125\n"
"v_mfma_f32_16x16x32_bf16 a[60:63], a[236:239], v[156:159], 0\n"
"v_xor_b32 v194, 0x8000, v194\n"
"v_mov_b32 v154, 0\n"
"v_mfma_f32_16x16x32_bf16 a[60:63], a[204:207], v[140:143], a[60:63]\n"
"v_exp_f32 v126, v126\n"
"v_mfma_f32_16x16x32_bf16 a[140:143], v[200:203], v[140:143], 0\n"
"s_add_u32 s62, s62, 1\n"
"s_mov_b64 exec, s[34:35]\n"
"buffer_store_dwordx2 v[250:251], v249, s[92:95], s53 offen sc1\n"
"s_mov_b64 exec, -1\n"
"v_mov_b32 v155, 0\n"
"v_mfma_f32_16x16x32_bf16 a[140:143], v[200:203], v[156:159], a[140:143]\n"
"v_exp_f32 v127, v127\n"
"s_branch .Lld1_34_%=\n"
".Ldiagx1_29_%=:\n"
"s_waitcnt lgkmcnt(8)\n"
"v_mfma_f32_16x16x32_bf16 v[64:67], a[144:147], a[64:67], v[176:179]\n"
"v_cvt_pk_bf16_f32 v140, v76, v77\n"
"v_cvt_pk_bf16_f32 v141, v78, v79\n"
"v_mfma_f32_16x16x32_bf16 v[64:67], a[148:151], a[80:83], v[64:67]\n"
"s_xor_b32 s85, s85, 0x8000\n"
"v_mfma_f32_16x16x32_bf16 v[64:67], a[152:155], a[96:99], v[64:67]\n"
"v_cvt_pk_bf16_f32 v142, v92, v93\n"
"v_cvt_pk_bf16_f32 v143, v94, v95\n"
"v_mfma_f32_16x16x32_bf16 v[64:67], a[156:159], a[112:115], v[64:67]\n"
"s_mov_b32 m0, s85\n"
"v_mfma_f32_16x16x32_bf16 v[68:71], a[156:159], a[116:119], v[164:167]\n"
"v_cvt_pk_bf16_f32 v156, v108, v109\n"
"v_cvt_pk_bf16_f32 v157, v110, v111\n"
"v_mfma_f32_16x16x32_bf16 v[68:71], a[152:155], a[100:103], v[68:71]\n"
"buffer_load_dwordx4 v254, s[96:99], s67 offen sc1 lds\n"
"v_mfma_f32_16x16x32_bf16 v[68:71], a[148:151], a[84:87], v[68:71]\n"
"v_cvt_pk_bf16_f32 v158, v124, v125\n"
"v_cvt_pk_bf16_f32 v159, v126, v127\n"
"v_mfma_f32_16x16x32_bf16 v[68:71], a[144:147], a[68:71], v[68:71]\n"
"buffer_load_dwordx4 v254, s[96:99], s67 offen offset:1024 sc1 lds\n"
"v_mfma_f32_16x16x32_bf16 v[72:75], a[144:147], a[72:75], v[168:171]\n"
"ds_read_b128 a[184:187], v193 offset:2048\n"
"ds_read_b128 a[216:219], v193 offset:10240\n"
"v_mfma_f32_16x16x32_bf16 v[72:75], a[148:151], a[88:91], v[72:75]\n"
"v_mfma_f32_16x16x32_bf16 v[72:75], a[152:155], a[104:107], v[72:75]\n"
"ds_read_b128 a[188:191], v193 offset:3072\n"
"v_mfma_f32_16x16x32_bf16 v[72:75], a[156:159], a[120:123], v[72:75]\n"
"buffer_load_dwordx4 v254, s[96:99], s67 offen offset:2048 sc1 lds\n"
"v_mfma_f32_16x16x32_bf16 v[76:79], a[156:159], a[124:127], v[172:175]\n"
"ds_read_b128 a[220:223], v193 offset:11264\n"
"ds_read_b128 a[192:195], v194 offset:4096\n"
"v_mfma_f32_16x16x32_bf16 v[76:79], a[152:155], a[108:111], v[76:79]\n"
"v_mfma_f32_16x16x32_bf16 v[76:79], a[148:151], a[92:95], v[76:79]\n"
"ds_read_b128 a[224:227], v194 offset:12288\n"
"s_waitcnt lgkmcnt(10)\n"
"v_mfma_f32_16x16x32_bf16 v[76:79], a[144:147], a[76:79], v[76:79]\n"
"s_nop 3\n"
"ds_read_b128 a[144:147], v192 offset:8192\n"
"ds_read_b128 a[148:151], v192 offset:9216\n"
"s_nop 3\n"
"buffer_load_dwordx4 v254, s[96:99], s67 offen offset:3072 sc1 lds\n"
"s_nop 3\n"
"ds_read_b128 a[152:155], v192 offset:10240\n"
"ds_read_b128 a[156:159], v192 offset:11264\n"
"s_nop 3\n"
"s_sub_u32 s89, 32, s66\n"
"v_mfma_f32_16x16x32_bf16 v[84:87], a[172:175], a[116:119], v[180:183]\n"
"s_lshl_b32 s89, s89, 14\n"
"s_add_u32 s67, s73, s89\n"
"v_mfma_f32_16x16x32_bf16 v[84:87], a[168:171], a[100:103], v[84:87]\n"
"v_exp_f32 v64, v64\n"
"v_mfma_f32_16x16x32_bf16 v[84:87], a[164:167], a[84:87], v[84:87]\n"
"s_xor_b32 s84, s84, 0x8000\n"
"s_mov_b32 m0, s84\n"
"v_mfma_f32_16x16x32_bf16 v[84:87], a[160:163], a[68:71], v[84:87]\n"
"v_exp_f32 v65, v65\n"
"v_mfma_f32_16x16x32_bf16 v[88:91], a[160:163], a[72:75], v[168:171]\n"
"buffer_load_dwordx4 v254, s[28:31], s67 offen sc1 lds\n"
"v_mfma_f32_16x16x32_bf16 v[88:91], a[164:167], a[88:91], v[88:91]\n"
"v_exp_f32 v66, v66\n"
"v_mfma_f32_16x16x32_bf16 v[88:91], a[168:171], a[104:107], v[88:91]\n"
"buffer_load_dwordx4 v254, s[28:31], s67 offen offset:1024 sc1 lds\n"
"v_mfma_f32_16x16x32_bf16 v[88:91], a[172:175], a[120:123], v[88:91]\n"
"v_exp_f32 v67, v67\n"
"v_mfma_f32_16x16x32_bf16 v[92:95], a[172:175], a[124:127], v[172:175]\n"
"ds_read_b128 a[196:199], v194 offset:5120\n"
"ds_read_b128 a[228:231], v194 offset:13312\n"
"v_mfma_f32_16x16x32_bf16 v[92:95], a[168:171], a[108:111], v[92:95]\n"
"v_mfma_f32_16x16x32_bf16 v[92:95], a[164:167], a[92:95], v[92:95]\n"
"ds_read_b128 a[200:203], v194 offset:6144\n"
"s_waitcnt lgkmcnt(3)\n"
"v_mfma_f32_16x16x32_bf16 v[92:95], a[160:163], a[76:79], v[92:95]\n"
"buffer_load_dwordx4 v254, s[28:31], s67 offen offset:2048 sc1 lds\n"
"s_nop 3\n"
"ds_read_b128 a[160:163], v192 offset:12288\n"
"ds_read_b128 a[164:167], v192 offset:13312\n"
"s_nop 3\n"
"s_nop 3\n"
"ds_read_b128 a[168:171], v192 offset:14336\n"
"ds_read_b128 a[172:175], v192 offset:15360\n"
"s_nop 3\n"
"buffer_load_dwordx4 v254, s[28:31], s67 offen offset:3072 sc1 lds\n"
"s_nop 3\n"
"s_add_u32 s69, s67, 0x4000\n"
"s_add_u32 m0, s84, 0x4000\n"
"s_nop 3\n"
"v_exp_f32 v68, v68\n"
"s_nop 3\n"
"ds_read_b128 a[232:235], v194 offset:14336\n"
"ds_read_b128 a[204:207], v194 offset:7168\n"
"s_nop 3\n"
"v_exp_f32 v69, v69\n"
"v_mfma_f32_16x16x32_bf16 v[104:107], a[144:147], a[72:75], v[184:187]\n"
"buffer_load_dwordx4 v254, s[28:31], s69 offen sc1 lds\n"
"v_mfma_f32_16x16x32_bf16 v[104:107], a[148:151], a[88:91], v[104:107]\n"
"v_exp_f32 v70, v70\n"
"v_mfma_f32_16x16x32_bf16 v[104:107], a[152:155], a[104:107], v[104:107]\n"
"ds_read_b128 a[236:239], v194 offset:15360\n"
"v_mfma_f32_16x16x32_bf16 v[104:107], a[156:159], a[120:123], v[104:107]\n"
"v_exp_f32 v71, v71\n"
"v_mfma_f32_16x16x32_bf16 v[108:111], a[156:159], a[124:127], v[172:175]\n"
"buffer_load_dwordx4 v254, s[28:31], s69 offen offset:1024 sc1 lds\n"
"v_mfma_f32_16x16x32_bf16 v[108:111], a[152:155], a[108:111], v[108:111]\n"
"v_exp_f32 v84, v84\n"
"v_mfma_f32_16x16x32_bf16 v[108:111], a[148:151], a[92:95], v[108:111]\n"
"s_waitcnt lgkmcnt(3)\n"
"v_mfma_f32_16x16x32_bf16 v[108:111], a[144:147], a[76:79], v[108:111]\n"
"v_exp_f32 v85, v85\n"
"s_nop 3\n"
"buffer_load_dwordx4 v254, s[28:31], s69 offen offset:2048 sc1 lds\n"
"s_nop 3\n"
"v_exp_f32 v86, v86\n"
"s_nop 3\n"
"s_nop 3\n"
"v_exp_f32 v87, v87\n"
"s_nop 3\n"
"s_nop 3\n"
"v_exp_f32 v72, v72\n"
"s_nop 3\n"
"buffer_load_dwordx4 v254, s[28:31], s69 offen offset:3072 sc1 lds\n"
"s_nop 3\n"
"v_exp_f32 v73, v73\n"
"s_nop 3\n"
"global_load_dword a255, v248, s[4:5]\n"
"s_nop 3\n"
"v_exp_f32 v74, v74\n"
"s_nop 3\n"
"s_nop 3\n"
"v_exp_f32 v75, v75\n"
"v_mfma_f32_16x16x32_bf16 v[124:127], a[172:175], a[124:127], v[188:191]\n"
"v_mfma_f32_16x16x32_bf16 v[124:127], a[168:171], a[108:111], v[124:127]\n"
"v_exp_f32 v88, v88\n"
"v_mfma_f32_16x16x32_bf16 v[124:127], a[164:167], a[92:95], v[124:127]\n"
"v_mfma_f32_16x16x32_bf16 v[124:127], a[160:163], a[76:79], v[124:127]\n"
"v_exp_f32 v89, v89\n"
"v_mfma_f32_16x16x32_bf16 v[0:3], a[176:179], v[128:131], v[0:3]\n"
"v_mfma_f32_16x16x32_bf16 v[0:3], a[208:211], v[144:147], v[0:3]\n"
"v_exp_f32 v90, v90\n"
"v_mfma_f32_16x16x32_bf16 v[16:19], a[212:215], v[144:147], v[16:19]\n"
"v_mfma_f32_16x16x32_bf16 v[16:19], a[180:183], v[128:131], v[16:19]\n"
"v_exp_f32 v91, v91\n"
"v_mfma_f32_16x16x32_bf16 v[32:35], a[184:187], v[128:131], v[32:35]\n"
"v_mfma_f32_16x16x32_bf16 v[32:35], a[216:219], v[144:147], v[32:35]\n"
"v_exp_f32 v76, v76\n"
"v_mfma_f32_16x16x32_bf16 v[48:51], a[220:223], v[144:147], v[48:51]\n"
"s_waitcnt lgkmcnt(0)\n"
"v_mfma_f32_16x16x32_bf16 v[48:51], a[188:191], v[128:131], v[48:51]\n"
"v_exp_f32 v77, v77\n"
"v_mfma_f32_16x16x32_bf16 a[0:3], a[192:195], v[128:131], a[0:3]\n"
"v_mfma_f32_16x16x32_bf16 a[0:3], a[224:227], v[144:147], a[0:3]\n"
"v_exp_f32 v78, v78\n"
"v_mfma_f32_16x16x32_bf16 a[16:19], a[228:231], v[144:147], a[16:19]\n"
"v_mfma_f32_16x16x32_bf16 a[16:19], a[196:199], v[128:131], a[16:19]\n"
"v_exp_f32 v79, v79\n"
"v_mfma_f32_16x16x32_bf16 a[32:35], a[200:203], v[128:131], a[32:35]\n"
"v_exp_f32 v92, v92\n"
"v_mfma_f32_16x16x32_bf16 a[32:35], a[232:235], v[144:147], a[32:35]\n"
"v_exp_f32 v93, v93\n"
"v_mfma_f32_16x16x32_bf16 a[48:51], a[236:239], v[144:147], a[48:51]\n"
"v_exp_f32 v94, v94\n"
"v_mfma_f32_16x16x32_bf16 a[48:51], a[204:207], v[128:131], a[48:51]\n"
"v_exp_f32 v95, v95\n"
"v_mfma_f32_16x16x32_bf16 a[128:131], v[200:203], v[128:131], a[128:131]\n"
"v_mfma_f32_16x16x32_bf16 a[128:131], v[200:203], v[144:147], a[128:131]\n"
"v_mfma_f32_16x16x32_bf16 v[4:7], a[176:179], v[132:135], v[4:7]\n"
"v_cvt_pk_bf16_f32 v128, v64, v65\n"
"v_cvt_pk_bf16_f32 v129, v66, v67\n"
"v_mfma_f32_16x16x32_bf16 v[4:7], a[208:211], v[148:151], v[4:7]\n"
"v_mfma_f32_16x16x32_bf16 v[20:23], a[212:215], v[148:151], v[20:23]\n"
"v_mov_b32 v130, 0\n"
"v_mov_b32 v131, 0\n"
"v_mfma_f32_16x16x32_bf16 v[20:23], a[180:183], v[132:135], v[20:23]\n"
"v_mfma_f32_16x16x32_bf16 v[36:39], a[184:187], v[132:135], v[36:39]\n"
"v_mov_b32 v144, 0\n"
"v_mov_b32 v145, 0\n"
"v_mfma_f32_16x16x32_bf16 v[36:39], a[216:219], v[148:151], v[36:39]\n"
"v_mfma_f32_16x16x32_bf16 v[52:55], a[220:223], v[148:151], v[52:55]\n"
"v_mov_b32 v146, 0\n"
"v_mfma_f32_16x16x32_bf16 v[52:55], a[188:191], v[132:135], v[52:55]\n"
"v_mfma_f32_16x16x32_bf16 a[4:7], a[192:195], v[132:135], a[4:7]\n"
"v_mov_b32 v147, 0\n"
"v_mfma_f32_16x16x32_bf16 a[4:7], a[224:227], v[148:151], a[4:7]\n"
"v_mfma_f32_16x16x32_bf16 a[20:23], a[228:231], v[148:151], a[20:23]\n"
"v_mfma_f32_16x16x32_bf16 a[20:23], a[196:199], v[132:135], a[20:23]\n"
"v_mfma_f32_16x16x32_bf16 a[36:39], a[200:203], v[132:135], a[36:39]\n"
"v_mfma_f32_16x16x32_bf16 a[36:39], a[232:235], v[148:151], a[36:39]\n"
"v_mfma_f32_16x16x32_bf16 a[52:55], a[236:239], v[148:151], a[52:55]\n"
"v_mfma_f32_16x16x32_bf16 a[52:55], a[204:207], v[132:135], a[52:55]\n"
"v_mfma_f32_16x16x32_bf16 a[132:135], v[200:203], v[132:135], a[132:135]\n"
"v_mfma_f32_16x16x32_bf16 a[132:135], v[200:203], v[148:151], a[132:135]\n"
"v_mfma_f32_16x16x32_bf16 v[8:11], a[176:179], v[136:139], v[8:11]\n"
"v_cvt_pk_bf16_f32 v132, v68, v69\n"
"v_cvt_pk_bf16_f32 v133, v70, v71\n"
"v_mfma_f32_16x16x32_bf16 v[8:11], a[208:211], v[152:155], v[8:11]\n"
"v_mfma_f32_16x16x32_bf16 v[24:27], a[212:215], v[152:155], v[24:27]\n"
"v_cvt_pk_bf16_f32 v134, v84, v85\n"
"v_cvt_pk_bf16_f32 v135, v86, v87\n"
"v_mfma_f32_16x16x32_bf16 v[24:27], a[180:183], v[136:139], v[24:27]\n"
"v_exp_f32 v104, v104\n"
"v_mfma_f32_16x16x32_bf16 v[40:43], a[184:187], v[136:139], v[40:43]\n"
"v_mov_b32 v148, 0\n"
"v_mov_b32 v149, 0\n"
"v_mfma_f32_16x16x32_bf16 v[40:43], a[216:219], v[152:155], v[40:43]\n"
"v_exp_f32 v105, v105\n"
"v_mfma_f32_16x16x32_bf16 v[56:59], a[220:223], v[152:155], v[56:59]\n"
"v_mfma_f32_16x16x32_bf16 v[56:59], a[188:191], v[136:139], v[56:59]\n"
"v_mfma_f32_16x16x32_bf16 a[8:11], a[192:195], v[136:139], a[8:11]\n"
"s_waitcnt vmcnt(1) lgkmcnt(0)\n"
"s_barrier\n"
"v_mfma_f32_16x16x32_bf16 a[8:11], a[224:227], v[152:155], a[8:11]\n"
"v_exp_f32 v106, v106\n"
"v_mfma_f32_16x16x32_bf16 a[24:27], a[228:231], v[152:155], a[24:27]\n"
"v_xor_b32 v192, 0x8000, v192\n"
"v_xor_b32 v193, 0x8000, v193\n"
"v_mfma_f32_16x16x32_bf16 a[24:27], a[196:199], v[136:139], a[24:27]\n"
"v_exp_f32 v107, v107\n"
"v_mfma_f32_16x16x32_bf16 a[40:43], a[200:203], v[136:139], a[40:43]\n"
"ds_read_b128 a[144:147], v192\n"
"ds_read_b128 a[148:151], v192 offset:1024\n"
"v_mfma_f32_16x16x32_bf16 a[40:43], a[232:235], v[152:155], a[40:43]\n"
"v_mfma_f32_16x16x32_bf16 a[56:59], a[236:239], v[152:155], a[56:59]\n"
"ds_read_b128 a[152:155], v192 offset:2048\n"
"ds_read_b128 a[156:159], v192 offset:3072\n"
"v_mfma_f32_16x16x32_bf16 a[56:59], a[204:207], v[136:139], a[56:59]\n"
"v_mfma_f32_16x16x32_bf16 a[136:139], v[200:203], v[136:139], a[136:139]\n"
"ds_read_b128 a[160:163], v192 offset:4096\n"
"ds_read_b128 a[164:167], v192 offset:5120\n"
"v_mfma_f32_16x16x32_bf16 a[136:139], v[200:203], v[152:155], a[136:139]\n"
"v_mfma_f32_16x16x32_bf16 v[12:15], a[176:179], v[140:143], v[12:15]\n"
"ds_read_b128 a[168:171], v192 offset:6144\n"
"ds_read_b128 a[172:175], v192 offset:7168\n"
"v_mfma_f32_16x16x32_bf16 v[12:15], a[208:211], v[156:159], v[12:15]\n"
"v_mfma_f32_16x16x32_bf16 v[28:31], a[212:215], v[156:159], v[28:31]\n"
"v_cvt_pk_bf16_f32 v136, v72, v73\n"
"v_cvt_pk_bf16_f32 v137, v74, v75\n"
"v_mfma_f32_16x16x32_bf16 v[28:31], a[180:183], v[140:143], v[28:31]\n"
"v_exp_f32 v108, v108\n"
"v_mfma_f32_16x16x32_bf16 v[44:47], a[184:187], v[140:143], v[44:47]\n"
"ds_read_b128 a[176:179], v193\n"
"ds_read_b128 a[208:211], v193 offset:8192\n"
"v_mfma_f32_16x16x32_bf16 v[44:47], a[216:219], v[156:159], v[44:47]\n"
"v_exp_f32 v109, v109\n"
"v_mfma_f32_16x16x32_bf16 v[60:63], a[220:223], v[156:159], v[60:63]\n"
"ds_read_b128 a[180:183], v193 offset:1024\n"
"ds_read_b128 a[212:215], v193 offset:9216\n"
"v_mfma_f32_16x16x32_bf16 v[60:63], a[188:191], v[140:143], v[60:63]\n"
"v_exp_f32 v110, v110\n"
"v_mfma_f32_16x16x32_bf16 a[12:15], a[192:195], v[140:143], a[12:15]\n"
"v_cvt_pk_bf16_f32 v138, v88, v89\n"
"v_cvt_pk_bf16_f32 v139, v90, v91\n"
"v_mfma_f32_16x16x32_bf16 a[12:15], a[224:227], v[156:159], a[12:15]\n"
"v_exp_f32 v111, v111\n"
"v_mfma_f32_16x16x32_bf16 a[28:31], a[228:231], v[156:159], a[28:31]\n"
"v_mov_b32 v150, 0\n"
"v_mov_b32 v151, 0\n"
"v_mfma_f32_16x16x32_bf16 a[28:31], a[196:199], v[140:143], a[28:31]\n"
"v_exp_f32 v124, v124\n"
"v_mfma_f32_16x16x32_bf16 a[44:47], a[200:203], v[140:143], a[44:47]\n"
"v_cvt_pk_bf16_f32 v152, v104, v105\n"
"v_cvt_pk_bf16_f32 v153, v106, v107\n"
"v_mfma_f32_16x16x32_bf16 a[44:47], a[232:235], v[156:159], a[44:47]\n"
"v_exp_f32 v125, v125\n"
"v_mfma_f32_16x16x32_bf16 a[60:63], a[236:239], v[156:159], a[60:63]\n"
"v_xor_b32 v194, 0x8000, v194\n"
"v_mov_b32 v154, 0\n"
"v_mfma_f32_16x16x32_bf16 a[60:63], a[204:207], v[140:143], a[60:63]\n"
"v_exp_f32 v126, v126\n"
"v_mfma_f32_16x16x32_bf16 a[140:143], v[200:203], v[140:143], a[140:143]\n"
"s_add_u32 s62, s62, 1\n"
"v_mov_b32 v155, 0\n"
"v_mfma_f32_16x16x32_bf16 a[140:143], v[200:203], v[156:159], a[140:143]\n"
"v_exp_f32 v127, v127\n"
"s_nop 0\n"
".Lld1_34_%=:\n"
"s_waitcnt lgkmcnt(2)\n"
"v_mfma_f32_16x16x32_bf16 v[0:3], a[176:179], v[128:131], v[0:3]\n"
"ds_read_b128 a[184:187], v193 offset:2048\n"
"ds_read_b128 a[216:219], v193 offset:10240\n"
"s_xor_b32 s85, s85, 0x8000\n"
"s_mov_b32 m0, s85\n"
"v_mfma_f32_16x16x32_bf16 v[0:3], a[208:211], v[144:147], v[0:3]\n"
"ds_read_b128 a[188:191], v193 offset:3072\n"
"ds_read_b128 a[220:223], v193 offset:11264\n"
"buffer_load_dwordx4 v254, s[96:99], s67 offen sc1 lds\n"
"s_waitcnt lgkmcnt(4)\n"
"v_mfma_f32_16x16x32_bf16 v[16:19], a[212:215], v[144:147], v[16:19]\n"
"ds_read_b128 a[192:195], v194 offset:4096\n"
"ds_read_b128 a[224:227], v194 offset:12288\n"
"v_mfma_f32_16x16x32_bf16 v[16:19], a[180:183], v[128:131], v[16:19]\n"
"ds_read_b128 a[196:199], v194 offset:5120\n"
"ds_read_b128 a[228:231], v194 offset:13312\n"
"s_waitcnt lgkmcnt(6)\n"
"v_mfma_f32_16x16x32_bf16 v[32:35], a[184:187], v[128:131], v[32:35]\n"
"ds_read_b128 a[200:203], v194 offset:6144\n"
"ds_read_b128 a[232:235], v194 offset:14336\n"
"buffer_load_dwordx4 v254, s[96:99], s67 offen offset:1024 sc1 lds\n"
"v_mfma_f32_16x16x32_bf16 v[32:35], a[216:219], v[144:147], v[32:35]\n"
"ds_read_b128 a[204:207], v194 offset:7168\n"
"ds_read_b128 a[236:239], v194 offset:15360\n"
"s_waitcnt lgkmcnt(8)\n"
"v_mfma_f32_16x16x32_bf16 v[48:51], a[220:223], v[144:147], v[48:51]\n"
"v_cvt_pk_bf16_f32 v140, v76, v77\n"
"v_cvt_pk_bf16_f32 v141, v78, v79\n"
"v_mfma_f32_16x16x32_bf16 v[48:51], a[188:191], v[128:131], v[48:51]\n"
"buffer_load_dwordx4 v254, s[96:99], s67 offen offset:2048 sc1 lds\n"
"s_waitcnt lgkmcnt(6)\n"
"v_mfma_f32_16x16x32_bf16 a[0:3], a[192:195], v[128:131], a[0:3]\n"
"v_cvt_pk_bf16_f32 v142, v92, v93\n"
"v_cvt_pk_bf16_f32 v143, v94, v95\n"
"v_mfma_f32_16x16x32_bf16 a[0:3], a[224:227], v[144:147], a[0:3]\n"
"v_cvt_pk_bf16_f32 v156, v108, v109\n"
"v_cvt_pk_bf16_f32 v157, v110, v111\n"
"s_waitcnt lgkmcnt(4)\n"
"v_mfma_f32_16x16x32_bf16 a[16:19], a[228:231], v[144:147], a[16:19]\n"
"buffer_load_dwordx4 v254, s[96:99], s67 offen offset:3072 sc1 lds\n"
"v_mfma_f32_16x16x32_bf16 a[16:19], a[196:199], v[128:131], a[16:19]\n"
"v_cvt_pk_bf16_f32 v158, v124, v125\n"
"v_cvt_pk_bf16_f32 v159, v126, v127\n"
"s_waitcnt lgkmcnt(2)\n"
"v_mfma_f32_16x16x32_bf16 a[32:35], a[200:203], v[128:131], a[32:35]\n"
"v_mfma_f32_16x16x32_bf16 a[32:35], a[232:235], v[144:147], a[32:35]\n"
"s_add_u32 s70, s67, 0x4000\n"
"s_add_u32 m0, s85, 0x4000\n"
"s_waitcnt lgkmcnt(0)\n"
"v_mfma_f32_16x16x32_bf16 a[48:51], a[236:239], v[144:147], a[48:51]\n"
"buffer_load_dwordx4 v254, s[96:99], s70 offen sc1 lds\n"
"v_mfma_f32_16x16x32_bf16 a[48:51], a[204:207], v[128:131], a[48:51]\n"
"v_mfma_f32_16x16x32_bf16 a[128:131], v[200:203], v[128:131], a[128:131]\n"
"v_mfma_f32_16x16x32_bf16 a[128:131], v[200:203], v[144:147], a[128:131]\n"
"buffer_load_dwordx4 v254, s[96:99], s70 offen offset:1024 sc1 lds\n"
"v_mfma_f32_16x16x32_bf16 v[4:7], a[176:179], v[132:135], v[4:7]\n"
"v_mfma_f32_16x16x32_bf16 v[4:7], a[208:211], v[148:151], v[4:7]\n"
"v_mfma_f32_16x16x32_bf16 v[20:23], a[212:215], v[148:151], v[20:23]\n"
"buffer_load_dwordx4 v254, s[96:99], s70 offen offset:2048 sc1 lds\n"
"v_mfma_f32_16x16x32_bf16 v[20:23], a[180:183], v[132:135], v[20:23]\n"
"v_mfma_f32_16x16x32_bf16 v[36:39], a[184:187], v[132:135], v[36:39]\n"
"v_mfma_f32_16x16x32_bf16 v[36:39], a[216:219], v[148:151], v[36:39]\n"
"buffer_load_dwordx4 v254, s[96:99], s70 offen offset:3072 sc1 lds\n"
"v_mfma_f32_16x16x32_bf16 v[52:55], a[220:223], v[148:151], v[52:55]\n"
"v_mfma_f32_16x16x32_bf16 v[52:55], a[188:191], v[132:135], v[52:55]\n"
"v_mfma_f32_16x16x32_bf16 a[4:7], a[192:195], v[132:135], a[4:7]\n"
"s_lshl_b32 s89, s66, 14\n"
"s_add_u32 s67, s73, s89\n"
"s_xor_b32 s84, s84, 0x8000\n"
"s_mov_b32 m0, s84\n"
"v_mfma_f32_16x16x32_bf16 a[4:7], a[224:227], v[148:151], a[4:7]\n"
"buffer_load_dwordx4 v254, s[28:31], s67 offen sc1 lds\n"
"v_mfma_f32_16x16x32_bf16 a[20:23], a[228:231], v[148:151], a[20:23]\n"
"v_mfma_f32_16x16x32_bf16 a[20:23], a[196:199], v[132:135], a[20:23]\n"
"v_mfma_f32_16x16x32_bf16 a[36:39], a[200:203], v[132:135], a[36:39]\n"
"buffer_load_dwordx4 v254, s[28:31], s67 offen offset:1024 sc1 lds\n"
"v_mfma_f32_16x16x32_bf16 a[36:39], a[232:235], v[148:151], a[36:39]\n"
"v_mfma_f32_16x16x32_bf16 a[52:55], a[236:239], v[148:151], a[52:55]\n"
"v_mfma_f32_16x16x32_bf16 a[52:55], a[204:207], v[132:135], a[52:55]\n"
"buffer_load_dwordx4 v254, s[28:31], s67 offen offset:2048 sc1 lds\n"
"v_mfma_f32_16x16x32_bf16 a[132:135], v[200:203], v[132:135], a[132:135]\n"
"v_mfma_f32_16x16x32_bf16 a[132:135], v[200:203], v[148:151], a[132:135]\n"
"v_mfma_f32_16x16x32_bf16 v[8:11], a[176:179], v[136:139], v[8:11]\n"
"buffer_load_dwordx4 v254, s[28:31], s67 offen offset:3072 sc1 lds\n"
"v_mfma_f32_16x16x32_bf16 v[8:11], a[208:211], v[152:155], v[8:11]\n"
"v_mfma_f32_16x16x32_bf16 v[24:27], a[212:215], v[152:155], v[24:27]\n"
"v_mfma_f32_16x16x32_bf16 v[24:27], a[180:183], v[136:139], v[24:27]\n"
"s_add_u32 s69, s67, 0x4000\n"
"s_add_u32 m0, s84, 0x4000\n"
"v_mfma_f32_16x16x32_bf16 v[40:43], a[184:187], v[136:139], v[40:43]\n"
"buffer_load_dwordx4 v254, s[28:31], s69 offen sc1 lds\n"
"v_mfma_f32_16x16x32_bf16 v[40:43], a[216:219], v[152:155], v[40:43]\n"
"v_mfma_f32_16x16x32_bf16 v[56:59], a[220:223], v[152:155], v[56:59]\n"
"v_mfma_f32_16x16x32_bf16 v[56:59], a[188:191], v[136:139], v[56:59]\n"
"buffer_load_dwordx4 v254, s[28:31], s69 offen offset:1024 sc1 lds\n"
"v_mfma_f32_16x16x32_bf16 a[8:11], a[192:195], v[136:139], a[8:11]\n"
"v_mfma_f32_16x16x32_bf16 a[8:11], a[224:227], v[152:155], a[8:11]\n"
"v_mfma_f32_16x16x32_bf16 a[24:27], a[228:231], v[152:155], a[24:27]\n"
"buffer_load_dwordx4 v254, s[28:31], s69 offen offset:2048 sc1 lds\n"
"v_mfma_f32_16x16x32_bf16 a[24:27], a[196:199], v[136:139], a[24:27]\n"
"v_mfma_f32_16x16x32_bf16 a[40:43], a[200:203], v[136:139], a[40:43]\n"
"v_mfma_f32_16x16x32_bf16 a[40:43], a[232:235], v[152:155], a[40:43]\n"
"buffer_load_dwordx4 v254, s[28:31], s69 offen offset:3072 sc1 lds\n"
"v_mfma_f32_16x16x32_bf16 a[56:59], a[236:239], v[152:155], a[56:59]\n"
"v_mfma_f32_16x16x32_bf16 a[56:59], a[204:207], v[136:139], a[56:59]\n"
"v_mfma_f32_16x16x32_bf16 a[136:139], v[200:203], v[136:139], a[136:139]\n"
"v_mfma_f32_16x16x32_bf16 a[136:139], v[200:203], v[152:155], a[136:139]\n"
"v_mfma_f32_16x16x32_bf16 v[12:15], a[176:179], v[140:143], v[12:15]\n"
"v_mfma_f32_16x16x32_bf16 v[12:15], a[208:211], v[156:159], v[12:15]\n"
"v_mfma_f32_16x16x32_bf16 v[28:31], a[212:215], v[156:159], v[28:31]\n"
"v_mfma_f32_16x16x32_bf16 v[28:31], a[180:183], v[140:143], v[28:31]\n"
"v_mfma_f32_16x16x32_bf16 v[44:47], a[184:187], v[140:143], v[44:47]\n"
"v_mfma_f32_16x16x32_bf16 v[44:47], a[216:219], v[156:159], v[44:47]\n"
"v_mfma_f32_16x16x32_bf16 v[60:63], a[220:223], v[156:159], v[60:63]\n"
"v_mfma_f32_16x16x32_bf16 v[60:63], a[188:191], v[140:143], v[60:63]\n"
"v_mfma_f32_16x16x32_bf16 a[12:15], a[192:195], v[140:143], a[12:15]\n"
"v_mfma_f32_16x16x32_bf16 a[12:15], a[224:227], v[156:159], a[12:15]\n"
"v_mfma_f32_16x16x32_bf16 a[28:31], a[228:231], v[156:159], a[28:31]\n"
"v_mfma_f32_16x16x32_bf16 a[28:31], a[196:199], v[140:143], a[28:31]\n"
"v_mfma_f32_16x16x32_bf16 a[44:47], a[200:203], v[140:143], a[44:47]\n"
"v_mfma_f32_16x16x32_bf16 a[44:47], a[232:235], v[156:159], a[44:47]\n"
"v_mfma_f32_16x16x32_bf16 a[60:63], a[236:239], v[156:159], a[60:63]\n"
"v_mfma_f32_16x16x32_bf16 a[60:63], a[204:207], v[140:143], a[60:63]\n"
"v_mfma_f32_16x16x32_bf16 a[140:143], v[200:203], v[140:143], a[140:143]\n"
"v_mfma_f32_16x16x32_bf16 a[140:143], v[200:203], v[156:159], a[140:143]\n"
"s_branch .Llcommon_35_%=\n"
".Lfirstdiag0_15_%=:\n"
"s_waitcnt lgkmcnt(8)\n"
"v_mfma_f32_16x16x32_bf16 v[64:67], a[144:147], a[64:67], v[176:179]\n"
"v_mfma_f32_16x16x32_bf16 v[64:67], a[148:151], a[80:83], v[64:67]\n"
"v_mfma_f32_16x16x32_bf16 v[64:67], a[152:155], a[96:99], v[64:67]\n"
"v_mfma_f32_16x16x32_bf16 v[64:67], a[156:159], a[112:115], v[64:67]\n"
"v_mfma_f32_16x16x32_bf16 v[68:71], a[156:159], a[116:119], v[164:167]\n"
"v_mfma_f32_16x16x32_bf16 v[68:71], a[152:155], a[100:103], v[68:71]\n"
"v_mfma_f32_16x16x32_bf16 v[68:71], a[148:151], a[84:87], v[68:71]\n"
"v_mfma_f32_16x16x32_bf16 v[68:71], a[144:147], a[68:71], v[68:71]\n"
"v_mfma_f32_16x16x32_bf16 v[72:75], a[144:147], a[72:75], v[168:171]\n"
"v_mfma_f32_16x16x32_bf16 v[72:75], a[148:151], a[88:91], v[72:75]\n"
"v_mfma_f32_16x16x32_bf16 v[72:75], a[152:155], a[104:107], v[72:75]\n"
"v_mfma_f32_16x16x32_bf16 v[72:75], a[156:159], a[120:123], v[72:75]\n"
"v_mfma_f32_16x16x32_bf16 v[76:79], a[156:159], a[124:127], v[172:175]\n"
"v_mfma_f32_16x16x32_bf16 v[76:79], a[152:155], a[108:111], v[76:79]\n"
"v_mfma_f32_16x16x32_bf16 v[76:79], a[148:151], a[92:95], v[76:79]\n"
"v_mfma_f32_16x16x32_bf16 v[76:79], a[144:147], a[76:79], v[76:79]\n"
"s_waitcnt lgkmcnt(4)\n"
"s_nop 0\n"
"ds_read_b128 a[144:147], v192 offset:8192\n"
"ds_read_b128 a[148:151], v192 offset:9216\n"
"s_nop 0\n"
"s_nop 0\n"
"ds_read_b128 a[152:155], v192 offset:10240\n"
"ds_read_b128 a[156:159], v192 offset:11264\n"
"s_nop 0\n"
"v_mfma_f32_16x16x32_bf16 v[84:87], a[172:175], a[116:119], v[180:183]\n"
"v_mfma_f32_16x16x32_bf16 v[84:87], a[168:171], a[100:103], v[84:87]\n"
"v_exp_f32 v64, v64\n"
"v_mfma_f32_16x16x32_bf16 v[84:87], a[164:167], a[84:87], v[84:87]\n"
"v_exp_f32 v65, v65\n"
"v_mfma_f32_16x16x32_bf16 v[84:87], a[160:163], a[68:71], v[84:87]\n"
"v_exp_f32 v66, v66\n"
"v_mfma_f32_16x16x32_bf16 v[88:91], a[160:163], a[72:75], v[168:171]\n"
"v_cvt_pk_bf16_f32 v128, v64, v65\n"
"v_mfma_f32_16x16x32_bf16 v[88:91], a[164:167], a[88:91], v[88:91]\n"
"v_exp_f32 v67, v67\n"
"v_mfma_f32_16x16x32_bf16 v[88:91], a[168:171], a[104:107], v[88:91]\n"
"v_cvt_pk_bf16_f32 v129, v66, v67\n"
"v_mov_b32 v130, 0\n"
"v_mfma_f32_16x16x32_bf16 v[88:91], a[172:175], a[120:123], v[88:91]\n"
"v_exp_f32 v68, v68\n"
"v_mfma_f32_16x16x32_bf16 v[92:95], a[172:175], a[124:127], v[172:175]\n"
"v_mov_b32 v131, 0\n"
"v_mfma_f32_16x16x32_bf16 v[92:95], a[168:171], a[108:111], v[92:95]\n"
"v_exp_f32 v69, v69\n"
"v_mfma_f32_16x16x32_bf16 v[92:95], a[164:167], a[92:95], v[92:95]\n"
"v_exp_f32 v70, v70\n"
"v_mfma_f32_16x16x32_bf16 v[92:95], a[160:163], a[76:79], v[92:95]\n"
"v_cvt_pk_bf16_f32 v132, v68, v69\n"
"s_waitcnt lgkmcnt(0)\n"
"s_nop 0\n"
"ds_read_b128 a[160:163], v192 offset:12288\n"
"ds_read_b128 a[164:167], v192 offset:13312\n"
"s_nop 0\n"
"v_exp_f32 v71, v71\n"
"s_nop 0\n"
"ds_read_b128 a[168:171], v192 offset:14336\n"
"ds_read_b128 a[172:175], v192 offset:15360\n"
"s_nop 0\n"
"v_exp_f32 v84, v84\n"
"s_nop 0\n"
"v_cvt_pk_bf16_f32 v133, v70, v71\n"
"s_nop 0\n"
"v_exp_f32 v85, v85\n"
"s_nop 0\n"
"v_exp_f32 v86, v86\n"
"s_nop 0\n"
"v_cvt_pk_bf16_f32 v134, v84, v85\n"
"v_mfma_f32_16x16x32_bf16 v[104:107], a[144:147], a[72:75], v[184:187]\n"
"v_exp_f32 v87, v87\n"
"v_mfma_f32_16x16x32_bf16 v[104:107], a[148:151], a[88:91], v[104:107]\n"
"v_exp_f32 v72, v72\n"
"v_mfma_f32_16x16x32_bf16 v[104:107], a[152:155], a[104:107], v[104:107]\n"
"v_cvt_pk_bf16_f32 v135, v86, v87\n"
"v_mfma_f32_16x16x32_bf16 v[104:107], a[156:159], a[120:123], v[104:107]\n"
"v_exp_f32 v73, v73\n"
"v_mfma_f32_16x16x32_bf16 v[108:111], a[156:159], a[124:127], v[172:175]\n"
"v_exp_f32 v74, v74\n"
"v_mfma_f32_16x16x32_bf16 v[108:111], a[152:155], a[108:111], v[108:111]\n"
"v_cvt_pk_bf16_f32 v136, v72, v73\n"
"v_mfma_f32_16x16x32_bf16 v[108:111], a[148:151], a[92:95], v[108:111]\n"
"v_exp_f32 v75, v75\n"
"v_mfma_f32_16x16x32_bf16 v[108:111], a[144:147], a[76:79], v[108:111]\n"
"v_exp_f32 v88, v88\n"
"s_waitcnt lgkmcnt(0)\n"
"s_nop 0\n"
"v_cvt_pk_bf16_f32 v137, v74, v75\n"
"s_nop 0\n"
"v_exp_f32 v89, v89\n"
"s_nop 0\n"
"v_exp_f32 v90, v90\n"
"s_nop 0\n"
"v_cvt_pk_bf16_f32 v138, v88, v89\n"
"s_nop 0\n"
"v_exp_f32 v91, v91\n"
"s_nop 0\n"
"v_exp_f32 v76, v76\n"
"s_nop 0\n"
"v_cvt_pk_bf16_f32 v139, v90, v91\n"
"s_nop 0\n"
"v_exp_f32 v77, v77\n"
"s_nop 0\n"
"v_exp_f32 v78, v78\n"
"s_nop 0\n"
"v_exp_f32 v79, v79\n"
"s_nop 0\n"
"v_exp_f32 v92, v92\n"
"s_nop 0\n"
"v_exp_f32 v93, v93\n"
"v_mfma_f32_16x16x32_bf16 v[124:127], a[172:175], a[124:127], v[188:191]\n"
"v_exp_f32 v94, v94\n"
"v_mfma_f32_16x16x32_bf16 v[124:127], a[168:171], a[108:111], v[124:127]\n"
"v_exp_f32 v95, v95\n"
"v_mfma_f32_16x16x32_bf16 v[124:127], a[164:167], a[92:95], v[124:127]\n"
"v_mfma_f32_16x16x32_bf16 v[124:127], a[160:163], a[76:79], v[124:127]\n"
"s_nop 1\n"
"v_mov_b32 v144, 0\n"
"v_mov_b32 v145, 0\n"
"v_mov_b32 v146, 0\n"
"v_mov_b32 v147, 0\n"
"s_waitcnt vmcnt(4) lgkmcnt(0)\n"
"s_barrier\n"
"v_xor_b32 v192, 0x8000, v192\n"
"v_xor_b32 v193, 0x8000, v193\n"
"v_xor_b32 v194, 0x8000, v194\n"
"ds_read_b128 a[144:147], v192\n"
"v_mov_b32 v148, 0\n"
"ds_read_b128 a[148:151], v192 offset:1024\n"
"v_mov_b32 v149, 0\n"
"ds_read_b128 a[152:155], v192 offset:2048\n"
"ds_read_b128 a[156:159], v192 offset:3072\n"
"v_mov_b32 v150, 0\n"
"ds_read_b128 a[160:163], v192 offset:4096\n"
"v_exp_f32 v104, v104\n"
"v_mov_b32 v151, 0\n"
"ds_read_b128 a[164:167], v192 offset:5120\n"
"v_exp_f32 v105, v105\n"
"v_exp_f32 v106, v106\n"
"ds_read_b128 a[168:171], v192 offset:6144\n"
"v_cvt_pk_bf16_f32 v152, v104, v105\n"
"v_exp_f32 v107, v107\n"
"ds_read_b128 a[172:175], v192 offset:7168\n"
"v_cvt_pk_bf16_f32 v153, v106, v107\n"
"ds_read_b128 a[176:179], v193\n"
"ds_read_b128 a[208:211], v193 offset:8192\n"
"v_mov_b32 v154, 0\n"
"ds_read_b128 a[180:183], v193 offset:1024\n"
"v_exp_f32 v108, v108\n"
"v_mov_b32 v155, 0\n"
"ds_read_b128 a[212:215], v193 offset:9216\n"
"v_exp_f32 v109, v109\n"
"v_exp_f32 v110, v110\n"
"v_exp_f32 v111, v111\n"
"v_exp_f32 v124, v124\n"
"v_exp_f32 v125, v125\n"
"v_exp_f32 v126, v126\n"
"v_exp_f32 v127, v127\n"
"s_nop 0\n"
"s_add_u32 s62, s62, 1\n"
"s_nop 0\n"
"s_waitcnt lgkmcnt(2)\n"
"v_mfma_f32_16x16x32_bf16 v[0:3], a[176:179], v[128:131], 0\n"
"ds_read_b128 a[184:187], v193 offset:2048\n"
"ds_read_b128 a[216:219], v193 offset:10240\n"
"s_xor_b32 s85, s85, 0x8000\n"
"s_mov_b32 m0, s85\n"
"v_mfma_f32_16x16x32_bf16 v[0:3], a[208:211], v[144:147], v[0:3]\n"
"ds_read_b128 a[188:191], v193 offset:3072\n"
"ds_read_b128 a[220:223], v193 offset:11264\n"
"buffer_load_dwordx4 v254, s[96:99], s67 offen sc1 lds\n"
"s_waitcnt lgkmcnt(4)\n"
"v_mfma_f32_16x16x32_bf16 v[16:19], a[212:215], v[144:147], 0\n"
"ds_read_b128 a[192:195], v194 offset:4096\n"
"ds_read_b128 a[224:227], v194 offset:12288\n"
"v_mfma_f32_16x16x32_bf16 v[16:19], a[180:183], v[128:131], v[16:19]\n"
"ds_read_b128 a[196:199], v194 offset:5120\n"
"ds_read_b128 a[228:231], v194 offset:13312\n"
"s_waitcnt lgkmcnt(6)\n"
"v_mfma_f32_16x16x32_bf16 v[32:35], a[184:187], v[128:131], 0\n"
"ds_read_b128 a[200:203], v194 offset:6144\n"
"ds_read_b128 a[232:235], v194 offset:14336\n"
"buffer_load_dwordx4 v254, s[96:99], s67 offen offset:1024 sc1 lds\n"
"v_mfma_f32_16x16x32_bf16 v[32:35], a[216:219], v[144:147], v[32:35]\n"
"ds_read_b128 a[204:207], v194 offset:7168\n"
"ds_read_b128 a[236:239], v194 offset:15360\n"
"s_waitcnt lgkmcnt(8)\n"
"v_mfma_f32_16x16x32_bf16 v[48:51], a[220:223], v[144:147], 0\n"
"v_cvt_pk_bf16_f32 v140, v76, v77\n"
"v_cvt_pk_bf16_f32 v141, v78, v79\n"
"v_mfma_f32_16x16x32_bf16 v[48:51], a[188:191], v[128:131], v[48:51]\n"
"buffer_load_dwordx4 v254, s[96:99], s67 offen offset:2048 sc1 lds\n"
"s_waitcnt lgkmcnt(6)\n"
"v_mfma_f32_16x16x32_bf16 a[0:3], a[192:195], v[128:131], 0\n"
"v_cvt_pk_bf16_f32 v142, v92, v93\n"
"v_cvt_pk_bf16_f32 v143, v94, v95\n"
"v_mfma_f32_16x16x32_bf16 a[0:3], a[224:227], v[144:147], a[0:3]\n"
"v_cvt_pk_bf16_f32 v156, v108, v109\n"
"v_cvt_pk_bf16_f32 v157, v110, v111\n"
"s_waitcnt lgkmcnt(4)\n"
"v_mfma_f32_16x16x32_bf16 a[16:19], a[228:231], v[144:147], 0\n"
"buffer_load_dwordx4 v254, s[96:99], s67 offen offset:3072 sc1 lds\n"
"v_mfma_f32_16x16x32_bf16 a[16:19], a[196:199], v[128:131], a[16:19]\n"
"v_cvt_pk_bf16_f32 v158, v124, v125\n"
"v_cvt_pk_bf16_f32 v159, v126, v127\n"
"s_waitcnt lgkmcnt(2)\n"
"v_mfma_f32_16x16x32_bf16 a[32:35], a[200:203], v[128:131], 0\n"
"v_mfma_f32_16x16x32_bf16 a[32:35], a[232:235], v[144:147], a[32:35]\n"
"s_sub_u32 s89, 32, s66\n"
"s_lshl_b32 s89, s89, 14\n"
"s_add_u32 s67, s73, s89\n"
"s_xor_b32 s84, s84, 0x8000\n"
"s_mov_b32 m0, s84\n"
"s_waitcnt lgkmcnt(0)\n"
"v_mfma_f32_16x16x32_bf16 a[48:51], a[236:239], v[144:147], 0\n"
"buffer_load_dwordx4 v254, s[28:31], s67 offen sc1 lds\n"
"v_mfma_f32_16x16x32_bf16 a[48:51], a[204:207], v[128:131], a[48:51]\n"
"v_mfma_f32_16x16x32_bf16 a[128:131], v[200:203], v[128:131], 0\n"
"v_mfma_f32_16x16x32_bf16 a[128:131], v[200:203], v[144:147], a[128:131]\n"
"buffer_load_dwordx4 v254, s[28:31], s67 offen offset:1024 sc1 lds\n"
"v_mfma_f32_16x16x32_bf16 v[4:7], a[176:179], v[132:135], 0\n"
"v_mfma_f32_16x16x32_bf16 v[4:7], a[208:211], v[148:151], v[4:7]\n"
"v_mfma_f32_16x16x32_bf16 v[20:23], a[212:215], v[148:151], 0\n"
"buffer_load_dwordx4 v254, s[28:31], s67 offen offset:2048 sc1 lds\n"
"v_mfma_f32_16x16x32_bf16 v[20:23], a[180:183], v[132:135], v[20:23]\n"
"v_mfma_f32_16x16x32_bf16 v[36:39], a[184:187], v[132:135], 0\n"
"v_mfma_f32_16x16x32_bf16 v[36:39], a[216:219], v[148:151], v[36:39]\n"
"buffer_load_dwordx4 v254, s[28:31], s67 offen offset:3072 sc1 lds\n"
"v_mfma_f32_16x16x32_bf16 v[52:55], a[220:223], v[148:151], 0\n"
"v_mfma_f32_16x16x32_bf16 v[52:55], a[188:191], v[132:135], v[52:55]\n"
"v_mfma_f32_16x16x32_bf16 a[4:7], a[192:195], v[132:135], 0\n"
"s_add_u32 s69, s67, 0x4000\n"
"s_add_u32 m0, s84, 0x4000\n"
"v_mfma_f32_16x16x32_bf16 a[4:7], a[224:227], v[148:151], a[4:7]\n"
"buffer_load_dwordx4 v254, s[28:31], s69 offen sc1 lds\n"
"v_mfma_f32_16x16x32_bf16 a[20:23], a[228:231], v[148:151], 0\n"
"v_mfma_f32_16x16x32_bf16 a[20:23], a[196:199], v[132:135], a[20:23]\n"
"v_mfma_f32_16x16x32_bf16 a[36:39], a[200:203], v[132:135], 0\n"
"buffer_load_dwordx4 v254, s[28:31], s69 offen offset:1024 sc1 lds\n"
"v_mfma_f32_16x16x32_bf16 a[36:39], a[232:235], v[148:151], a[36:39]\n"
"v_mfma_f32_16x16x32_bf16 a[52:55], a[236:239], v[148:151], 0\n"
"v_mfma_f32_16x16x32_bf16 a[52:55], a[204:207], v[132:135], a[52:55]\n"
"buffer_load_dwordx4 v254, s[28:31], s69 offen offset:2048 sc1 lds\n"
"v_mfma_f32_16x16x32_bf16 a[132:135], v[200:203], v[132:135], 0\n"
"v_mfma_f32_16x16x32_bf16 a[132:135], v[200:203], v[148:151], a[132:135]\n"
"v_mfma_f32_16x16x32_bf16 v[8:11], a[176:179], v[136:139], 0\n"
"buffer_load_dwordx4 v254, s[28:31], s69 offen offset:3072 sc1 lds\n"
"v_mfma_f32_16x16x32_bf16 v[8:11], a[208:211], v[152:155], v[8:11]\n"
"v_mfma_f32_16x16x32_bf16 v[24:27], a[212:215], v[152:155], 0\n"
"v_mfma_f32_16x16x32_bf16 v[24:27], a[180:183], v[136:139], v[24:27]\n"
"v_mfma_f32_16x16x32_bf16 v[40:43], a[184:187], v[136:139], 0\n"
"v_mfma_f32_16x16x32_bf16 v[40:43], a[216:219], v[152:155], v[40:43]\n"
"v_mfma_f32_16x16x32_bf16 v[56:59], a[220:223], v[152:155], 0\n"
"v_mfma_f32_16x16x32_bf16 v[56:59], a[188:191], v[136:139], v[56:59]\n"
"v_mfma_f32_16x16x32_bf16 a[8:11], a[192:195], v[136:139], 0\n"
"v_mfma_f32_16x16x32_bf16 a[8:11], a[224:227], v[152:155], a[8:11]\n"
"v_mfma_f32_16x16x32_bf16 a[24:27], a[228:231], v[152:155], 0\n"
"v_mfma_f32_16x16x32_bf16 a[24:27], a[196:199], v[136:139], a[24:27]\n"
"v_mfma_f32_16x16x32_bf16 a[40:43], a[200:203], v[136:139], 0\n"
"v_mfma_f32_16x16x32_bf16 a[40:43], a[232:235], v[152:155], a[40:43]\n"
"v_mfma_f32_16x16x32_bf16 a[56:59], a[236:239], v[152:155], 0\n"
"v_mfma_f32_16x16x32_bf16 a[56:59], a[204:207], v[136:139], a[56:59]\n"
"v_mfma_f32_16x16x32_bf16 a[136:139], v[200:203], v[136:139], 0\n"
"v_mfma_f32_16x16x32_bf16 a[136:139], v[200:203], v[152:155], a[136:139]\n"
"v_mfma_f32_16x16x32_bf16 v[12:15], a[176:179], v[140:143], 0\n"
"v_mfma_f32_16x16x32_bf16 v[12:15], a[208:211], v[156:159], v[12:15]\n"
"v_mfma_f32_16x16x32_bf16 v[28:31], a[212:215], v[156:159], 0\n"
"v_mfma_f32_16x16x32_bf16 v[28:31], a[180:183], v[140:143], v[28:31]\n"
"v_mfma_f32_16x16x32_bf16 v[44:47], a[184:187], v[140:143], 0\n"
"v_mfma_f32_16x16x32_bf16 v[44:47], a[216:219], v[156:159], v[44:47]\n"
"v_mfma_f32_16x16x32_bf16 v[60:63], a[220:223], v[156:159], 0\n"
"v_mfma_f32_16x16x32_bf16 v[60:63], a[188:191], v[140:143], v[60:63]\n"
"v_mfma_f32_16x16x32_bf16 a[12:15], a[192:195], v[140:143], 0\n"
"v_mfma_f32_16x16x32_bf16 a[12:15], a[224:227], v[156:159], a[12:15]\n"
"v_mfma_f32_16x16x32_bf16 a[28:31], a[228:231], v[156:159], 0\n"
"v_mfma_f32_16x16x32_bf16 a[28:31], a[196:199], v[140:143], a[28:31]\n"
"v_mfma_f32_16x16x32_bf16 a[44:47], a[200:203], v[140:143], 0\n"
"v_mfma_f32_16x16x32_bf16 a[44:47], a[232:235], v[156:159], a[44:47]\n"
"v_mfma_f32_16x16x32_bf16 a[60:63], a[236:239], v[156:159], 0\n"
"v_mfma_f32_16x16x32_bf16 a[60:63], a[204:207], v[140:143], a[60:63]\n"
"v_mfma_f32_16x16x32_bf16 a[140:143], v[200:203], v[140:143], 0\n"
"v_mfma_f32_16x16x32_bf16 a[140:143], v[200:203], v[156:159], a[140:143]\n"
"s_branch .Llcommon_35_%=\n"
".Ldiag1_28_%=:\n"
"s_waitcnt lgkmcnt(8)\n"
"v_mfma_f32_16x16x32_bf16 v[64:67], a[144:147], a[64:67], v[176:179]\n"
"v_cvt_pk_bf16_f32 v140, v76, v77\n"
"v_cvt_pk_bf16_f32 v141, v78, v79\n"
"v_mfma_f32_16x16x32_bf16 v[64:67], a[148:151], a[80:83], v[64:67]\n"
"s_xor_b32 s85, s85, 0x8000\n"
"v_mfma_f32_16x16x32_bf16 v[64:67], a[152:155], a[96:99], v[64:67]\n"
"v_cvt_pk_bf16_f32 v142, v92, v93\n"
"v_cvt_pk_bf16_f32 v143, v94, v95\n"
"v_mfma_f32_16x16x32_bf16 v[64:67], a[156:159], a[112:115], v[64:67]\n"
"s_mov_b32 m0, s85\n"
"v_mfma_f32_16x16x32_bf16 v[68:71], a[156:159], a[116:119], v[164:167]\n"
"v_cvt_pk_bf16_f32 v156, v108, v109\n"
"v_cvt_pk_bf16_f32 v157, v110, v111\n"
"v_mfma_f32_16x16x32_bf16 v[68:71], a[152:155], a[100:103], v[68:71]\n"
"buffer_load_dwordx4 v254, s[96:99], s67 offen sc1 lds\n"
"v_mfma_f32_16x16x32_bf16 v[68:71], a[148:151], a[84:87], v[68:71]\n"
"v_cvt_pk_bf16_f32 v158, v124, v125\n"
"v_cvt_pk_bf16_f32 v159, v126, v127\n"
"v_mfma_f32_16x16x32_bf16 v[68:71], a[144:147], a[68:71], v[68:71]\n"
"v_mfma_f32_16x16x32_bf16 v[72:75], a[144:147], a[72:75], v[168:171]\n"
"buffer_load_dwordx4 v254, s[96:99], s67 offen offset:1024 sc1 lds\n"
"v_mfma_f32_16x16x32_bf16 v[72:75], a[148:151], a[88:91], v[72:75]\n"
"ds_read_b128 a[184:187], v193 offset:2048\n"
"ds_read_b128 a[216:219], v193 offset:10240\n"
"v_mfma_f32_16x16x32_bf16 v[72:75], a[152:155], a[104:107], v[72:75]\n"
"v_mfma_f32_16x16x32_bf16 v[72:75], a[156:159], a[120:123], v[72:75]\n"
"ds_read_b128 a[188:191], v193 offset:3072\n"
"v_mfma_f32_16x16x32_bf16 v[76:79], a[156:159], a[124:127], v[172:175]\n"
"ds_read_b128 a[220:223], v193 offset:11264\n"
"v_mfma_f32_16x16x32_bf16 v[76:79], a[152:155], a[108:111], v[76:79]\n"
"ds_read_b128 a[192:195], v194 offset:4096\n"
"v_mfma_f32_16x16x32_bf16 v[76:79], a[148:151], a[92:95], v[76:79]\n"
"ds_read_b128 a[224:227], v194 offset:12288\n"
"s_waitcnt lgkmcnt(10)\n"
"v_mfma_f32_16x16x32_bf16 v[76:79], a[144:147], a[76:79], v[76:79]\n"
"buffer_load_dwordx4 v254, s[96:99], s67 offen offset:2048 sc1 lds\n"
"s_nop 3\n"
"ds_read_b128 a[144:147], v192 offset:8192\n"
"ds_read_b128 a[148:151], v192 offset:9216\n"
"s_nop 3\n"
"s_nop 3\n"
"ds_read_b128 a[152:155], v192 offset:10240\n"
"ds_read_b128 a[156:159], v192 offset:11264\n"
"s_nop 3\n"
"v_mfma_f32_16x16x32_bf16 v[84:87], a[172:175], a[116:119], v[180:183]\n"
"buffer_load_dwordx4 v254, s[96:99], s67 offen offset:3072 sc1 lds\n"
"v_mfma_f32_16x16x32_bf16 v[84:87], a[168:171], a[100:103], v[84:87]\n"
"v_exp_f32 v64, v64\n"
"v_mfma_f32_16x16x32_bf16 v[84:87], a[164:167], a[84:87], v[84:87]\n"
"s_add_u32 s67, s67, 0x4000\n"
"s_xor_b32 s84, s84, 0x8000\n"
"v_mfma_f32_16x16x32_bf16 v[84:87], a[160:163], a[68:71], v[84:87]\n"
"v_exp_f32 v65, v65\n"
"v_mfma_f32_16x16x32_bf16 v[88:91], a[160:163], a[72:75], v[168:171]\n"
"s_mov_b32 m0, s84\n"
"ds_read_b128 a[196:199], v194 offset:5120\n"
"v_mfma_f32_16x16x32_bf16 v[88:91], a[164:167], a[88:91], v[88:91]\n"
"v_exp_f32 v66, v66\n"
"v_mfma_f32_16x16x32_bf16 v[88:91], a[168:171], a[104:107], v[88:91]\n"
"buffer_load_dwordx4 v254, s[28:31], s67 offen sc1 lds\n"
"v_mfma_f32_16x16x32_bf16 v[88:91], a[172:175], a[120:123], v[88:91]\n"
"v_exp_f32 v67, v67\n"
"v_mfma_f32_16x16x32_bf16 v[92:95], a[172:175], a[124:127], v[172:175]\n"
"ds_read_b128 a[228:231], v194 offset:13312\n"
"ds_read_b128 a[200:203], v194 offset:6144\n"
"v_mfma_f32_16x16x32_bf16 v[92:95], a[168:171], a[108:111], v[92:95]\n"
"v_mfma_f32_16x16x32_bf16 v[92:95], a[164:167], a[92:95], v[92:95]\n"
"ds_read_b128 a[232:235], v194 offset:14336\n"
"s_waitcnt lgkmcnt(4)\n"
"v_mfma_f32_16x16x32_bf16 v[92:95], a[160:163], a[76:79], v[92:95]\n"
"s_nop 3\n"
"ds_read_b128 a[160:163], v192 offset:12288\n"
"ds_read_b128 a[164:167], v192 offset:13312\n"
"s_nop 3\n"
"buffer_load_dwordx4 v254, s[28:31], s67 offen offset:1024 sc1 lds\n"
"s_nop 3\n"
"ds_read_b128 a[168:171], v192 offset:14336\n"
"ds_read_b128 a[172:175], v192 offset:15360\n"
"s_nop 3\n"
"s_nop 3\n"
"ds_read_b128 a[204:207], v194 offset:7168\n"
"s_nop 3\n"
"v_exp_f32 v68, v68\n"
"s_nop 3\n"
"buffer_load_dwordx4 v254, s[28:31], s67 offen offset:2048 sc1 lds\n"
"s_nop 3\n"
"v_exp_f32 v69, v69\n"
"v_mfma_f32_16x16x32_bf16 v[104:107], a[144:147], a[72:75], v[184:187]\n"
"ds_read_b128 a[236:239], v194 offset:15360\n"
"v_mfma_f32_16x16x32_bf16 v[104:107], a[148:151], a[88:91], v[104:107]\n"
"v_exp_f32 v70, v70\n"
"v_mfma_f32_16x16x32_bf16 v[104:107], a[152:155], a[104:107], v[104:107]\n"
"v_mfma_f32_16x16x32_bf16 v[104:107], a[156:159], a[120:123], v[104:107]\n"
"v_exp_f32 v71, v71\n"
"v_mfma_f32_16x16x32_bf16 v[108:111], a[156:159], a[124:127], v[172:175]\n"
"buffer_load_dwordx4 v254, s[28:31], s67 offen offset:3072 sc1 lds\n"
"v_mfma_f32_16x16x32_bf16 v[108:111], a[152:155], a[108:111], v[108:111]\n"
"v_exp_f32 v84, v84\n"
"v_mfma_f32_16x16x32_bf16 v[108:111], a[148:151], a[92:95], v[108:111]\n"
"global_load_dword a255, v248, s[4:5]\n"
"s_waitcnt lgkmcnt(2)\n"
"v_mfma_f32_16x16x32_bf16 v[108:111], a[144:147], a[76:79], v[108:111]\n"
"v_exp_f32 v85, v85\n"
"s_nop 3\n"
"s_nop 3\n"
"v_exp_f32 v86, v86\n"
"s_nop 3\n"
"s_nop 3\n"
"v_exp_f32 v87, v87\n"
"s_nop 3\n"
"s_nop 3\n"
"v_exp_f32 v72, v72\n"
"s_nop 3\n"
"s_nop 3\n"
"v_exp_f32 v73, v73\n"
"s_nop 3\n"
"s_nop 3\n"
"v_exp_f32 v74, v74\n"
"s_nop 3\n"
"s_nop 3\n"
"v_exp_f32 v75, v75\n"
"v_mfma_f32_16x16x32_bf16 v[124:127], a[172:175], a[124:127], v[188:191]\n"
"v_mfma_f32_16x16x32_bf16 v[124:127], a[168:171], a[108:111], v[124:127]\n"
"v_exp_f32 v88, v88\n"
"v_mfma_f32_16x16x32_bf16 v[124:127], a[164:167], a[92:95], v[124:127]\n"
"v_mfma_f32_16x16x32_bf16 v[124:127], a[160:163], a[76:79], v[124:127]\n"
"v_exp_f32 v89, v89\n"
"v_mfma_f32_16x16x32_bf16 v[0:3], a[176:179], v[128:131], v[0:3]\n"
"v_mfma_f32_16x16x32_bf16 v[0:3], a[208:211], v[144:147], v[0:3]\n"
"v_exp_f32 v90, v90\n"
"v_mfma_f32_16x16x32_bf16 v[16:19], a[212:215], v[144:147], v[16:19]\n"
"v_mfma_f32_16x16x32_bf16 v[16:19], a[180:183], v[128:131], v[16:19]\n"
"v_exp_f32 v91, v91\n"
"v_mfma_f32_16x16x32_bf16 v[32:35], a[184:187], v[128:131], v[32:35]\n"
"v_mfma_f32_16x16x32_bf16 v[32:35], a[216:219], v[144:147], v[32:35]\n"
"v_exp_f32 v76, v76\n"
"v_mfma_f32_16x16x32_bf16 v[48:51], a[220:223], v[144:147], v[48:51]\n"
"s_waitcnt lgkmcnt(0)\n"
"v_mfma_f32_16x16x32_bf16 v[48:51], a[188:191], v[128:131], v[48:51]\n"
"v_exp_f32 v77, v77\n"
"v_mfma_f32_16x16x32_bf16 a[0:3], a[192:195], v[128:131], a[0:3]\n"
"v_mfma_f32_16x16x32_bf16 a[0:3], a[224:227], v[144:147], a[0:3]\n"
"v_exp_f32 v78, v78\n"
"v_mfma_f32_16x16x32_bf16 a[16:19], a[228:231], v[144:147], a[16:19]\n"
"v_mfma_f32_16x16x32_bf16 a[16:19], a[196:199], v[128:131], a[16:19]\n"
"v_exp_f32 v79, v79\n"
"v_mfma_f32_16x16x32_bf16 a[32:35], a[200:203], v[128:131], a[32:35]\n"
"v_exp_f32 v92, v92\n"
"v_mfma_f32_16x16x32_bf16 a[32:35], a[232:235], v[144:147], a[32:35]\n"
"v_exp_f32 v93, v93\n"
"v_mfma_f32_16x16x32_bf16 a[48:51], a[236:239], v[144:147], a[48:51]\n"
"v_exp_f32 v94, v94\n"
"v_mfma_f32_16x16x32_bf16 a[48:51], a[204:207], v[128:131], a[48:51]\n"
"v_exp_f32 v95, v95\n"
"v_mfma_f32_16x16x32_bf16 a[128:131], v[200:203], v[128:131], a[128:131]\n"
"v_mfma_f32_16x16x32_bf16 a[128:131], v[200:203], v[144:147], a[128:131]\n"
"v_mfma_f32_16x16x32_bf16 v[4:7], a[176:179], v[132:135], v[4:7]\n"
"v_cvt_pk_bf16_f32 v128, v64, v65\n"
"v_cvt_pk_bf16_f32 v129, v66, v67\n"
"v_mfma_f32_16x16x32_bf16 v[4:7], a[208:211], v[148:151], v[4:7]\n"
"v_mfma_f32_16x16x32_bf16 v[20:23], a[212:215], v[148:151], v[20:23]\n"
"v_mov_b32 v130, 0\n"
"v_mov_b32 v131, 0\n"
"v_mfma_f32_16x16x32_bf16 v[20:23], a[180:183], v[132:135], v[20:23]\n"
"v_mfma_f32_16x16x32_bf16 v[36:39], a[184:187], v[132:135], v[36:39]\n"
"v_mov_b32 v144, 0\n"
"v_mov_b32 v145, 0\n"
"v_mfma_f32_16x16x32_bf16 v[36:39], a[216:219], v[148:151], v[36:39]\n"
"v_mfma_f32_16x16x32_bf16 v[52:55], a[220:223], v[148:151], v[52:55]\n"
"v_mov_b32 v146, 0\n"
"v_mfma_f32_16x16x32_bf16 v[52:55], a[188:191], v[132:135], v[52:55]\n"
"v_mfma_f32_16x16x32_bf16 a[4:7], a[192:195], v[132:135], a[4:7]\n"
"v_mov_b32 v147, 0\n"
"v_mfma_f32_16x16x32_bf16 a[4:7], a[224:227], v[148:151], a[4:7]\n"
"v_mfma_f32_16x16x32_bf16 a[20:23], a[228:231], v[148:151], a[20:23]\n"
"v_mfma_f32_16x16x32_bf16 a[20:23], a[196:199], v[132:135], a[20:23]\n"
"v_mfma_f32_16x16x32_bf16 a[36:39], a[200:203], v[132:135], a[36:39]\n"
"v_mfma_f32_16x16x32_bf16 a[36:39], a[232:235], v[148:151], a[36:39]\n"
"v_mfma_f32_16x16x32_bf16 a[52:55], a[236:239], v[148:151], a[52:55]\n"
"v_mfma_f32_16x16x32_bf16 a[52:55], a[204:207], v[132:135], a[52:55]\n"
"v_mfma_f32_16x16x32_bf16 a[132:135], v[200:203], v[132:135], a[132:135]\n"
"v_mfma_f32_16x16x32_bf16 a[132:135], v[200:203], v[148:151], a[132:135]\n"
"v_mfma_f32_16x16x32_bf16 v[8:11], a[176:179], v[136:139], v[8:11]\n"
"v_cvt_pk_bf16_f32 v132, v68, v69\n"
"v_cvt_pk_bf16_f32 v133, v70, v71\n"
"v_mfma_f32_16x16x32_bf16 v[8:11], a[208:211], v[152:155], v[8:11]\n"
"v_mfma_f32_16x16x32_bf16 v[24:27], a[212:215], v[152:155], v[24:27]\n"
"v_cvt_pk_bf16_f32 v134, v84, v85\n"
"v_cvt_pk_bf16_f32 v135, v86, v87\n"
"v_mfma_f32_16x16x32_bf16 v[24:27], a[180:183], v[136:139], v[24:27]\n"
"v_exp_f32 v104, v104\n"
"v_mfma_f32_16x16x32_bf16 v[40:43], a[184:187], v[136:139], v[40:43]\n"
"v_mov_b32 v148, 0\n"
"v_mov_b32 v149, 0\n"
"v_mfma_f32_16x16x32_bf16 v[40:43], a[216:219], v[152:155], v[40:43]\n"
"v_exp_f32 v105, v105\n"
"v_mfma_f32_16x16x32_bf16 v[56:59], a[220:223], v[152:155], v[56:59]\n"
"v_mfma_f32_16x16x32_bf16 v[56:59], a[188:191], v[136:139], v[56:59]\n"
"v_mfma_f32_16x16x32_bf16 a[8:11], a[192:195], v[136:139], a[8:11]\n"
"s_waitcnt vmcnt(1) lgkmcnt(0)\n"
"s_barrier\n"
"v_mfma_f32_16x16x32_bf16 a[8:11], a[224:227], v[152:155], a[8:11]\n"
"v_exp_f32 v106, v106\n"
"v_mfma_f32_16x16x32_bf16 a[24:27], a[228:231], v[152:155], a[24:27]\n"
"v_xor_b32 v192, 0x8000, v192\n"
"v_xor_b32 v193, 0x8000, v193\n"
"v_mfma_f32_16x16x32_bf16 a[24:27], a[196:199], v[136:139], a[24:27]\n"
"v_exp_f32 v107, v107\n"
"v_mfma_f32_16x16x32_bf16 a[40:43], a[200:203], v[136:139], a[40:43]\n"
"ds_read_b128 a[144:147], v192\n"
"ds_read_b128 a[148:151], v192 offset:1024\n"
"v_mfma_f32_16x16x32_bf16 a[40:43], a[232:235], v[152:155], a[40:43]\n"
"v_mfma_f32_16x16x32_bf16 a[56:59], a[236:239], v[152:155], a[56:59]\n"
"ds_read_b128 a[152:155], v192 offset:2048\n"
"ds_read_b128 a[156:159], v192 offset:3072\n"
"v_mfma_f32_16x16x32_bf16 a[56:59], a[204:207], v[136:139], a[56:59]\n"
"v_mfma_f32_16x16x32_bf16 a[136:139], v[200:203], v[136:139], a[136:139]\n"
"ds_read_b128 a[160:163], v192 offset:4096\n"
"ds_read_b128 a[164:167], v192 offset:5120\n"
"v_mfma_f32_16x16x32_bf16 a[136:139], v[200:203], v[152:155], a[136:139]\n"
"v_mfma_f32_16x16x32_bf16 v[12:15], a[176:179], v[140:143], v[12:15]\n"
"ds_read_b128 a[168:171], v192 offset:6144\n"
"ds_read_b128 a[172:175], v192 offset:7168\n"
"v_mfma_f32_16x16x32_bf16 v[12:15], a[208:211], v[156:159], v[12:15]\n"
"v_mfma_f32_16x16x32_bf16 v[28:31], a[212:215], v[156:159], v[28:31]\n"
"v_cvt_pk_bf16_f32 v136, v72, v73\n"
"v_cvt_pk_bf16_f32 v137, v74, v75\n"
"v_mfma_f32_16x16x32_bf16 v[28:31], a[180:183], v[140:143], v[28:31]\n"
"v_exp_f32 v108, v108\n"
"v_mfma_f32_16x16x32_bf16 v[44:47], a[184:187], v[140:143], v[44:47]\n"
"ds_read_b128 a[176:179], v193\n"
"ds_read_b128 a[208:211], v193 offset:8192\n"
"v_mfma_f32_16x16x32_bf16 v[44:47], a[216:219], v[156:159], v[44:47]\n"
"v_exp_f32 v109, v109\n"
"v_mfma_f32_16x16x32_bf16 v[60:63], a[220:223], v[156:159], v[60:63]\n"
"ds_read_b128 a[180:183], v193 offset:1024\n"
"ds_read_b128 a[212:215], v193 offset:9216\n"
"v_mfma_f32_16x16x32_bf16 v[60:63], a[188:191], v[140:143], v[60:63]\n"
"v_exp_f32 v110, v110\n"
"v_mfma_f32_16x16x32_bf16 a[12:15], a[192:195], v[140:143], a[12:15]\n"
"v_cvt_pk_bf16_f32 v138, v88, v89\n"
"v_cvt_pk_bf16_f32 v139, v90, v91\n"
"v_mfma_f32_16x16x32_bf16 a[12:15], a[224:227], v[156:159], a[12:15]\n"
"v_exp_f32 v111, v111\n"
"v_mfma_f32_16x16x32_bf16 a[28:31], a[228:231], v[156:159], a[28:31]\n"
"v_mov_b32 v150, 0\n"
"v_mov_b32 v151, 0\n"
"v_mfma_f32_16x16x32_bf16 a[28:31], a[196:199], v[140:143], a[28:31]\n"
"v_exp_f32 v124, v124\n"
"v_mfma_f32_16x16x32_bf16 a[44:47], a[200:203], v[140:143], a[44:47]\n"
"v_cvt_pk_bf16_f32 v152, v104, v105\n"
"v_cvt_pk_bf16_f32 v153, v106, v107\n"
"v_mfma_f32_16x16x32_bf16 a[44:47], a[232:235], v[156:159], a[44:47]\n"
"v_exp_f32 v125, v125\n"
"v_mfma_f32_16x16x32_bf16 a[60:63], a[236:239], v[156:159], a[60:63]\n"
"v_xor_b32 v194, 0x8000, v194\n"
"v_mov_b32 v154, 0\n"
"v_mfma_f32_16x16x32_bf16 a[60:63], a[204:207], v[140:143], a[60:63]\n"
"v_exp_f32 v126, v126\n"
"v_mfma_f32_16x16x32_bf16 a[140:143], v[200:203], v[140:143], a[140:143]\n"
"s_add_u32 s62, s62, 1\n"
"v_mov_b32 v155, 0\n"
"v_mfma_f32_16x16x32_bf16 a[140:143], v[200:203], v[156:159], a[140:143]\n"
"v_exp_f32 v127, v127\n"
"s_nop 0\n"
"s_waitcnt lgkmcnt(2)\n"
"v_mfma_f32_16x16x32_bf16 v[0:3], a[176:179], v[128:131], v[0:3]\n"
"ds_read_b128 a[184:187], v193 offset:2048\n"
"ds_read_b128 a[216:219], v193 offset:10240\n"
"s_xor_b32 s85, s85, 0x8000\n"
"s_mov_b32 m0, s85\n"
"v_mfma_f32_16x16x32_bf16 v[0:3], a[208:211], v[144:147], v[0:3]\n"
"ds_read_b128 a[188:191], v193 offset:3072\n"
"ds_read_b128 a[220:223], v193 offset:11264\n"
"buffer_load_dwordx4 v254, s[96:99], s67 offen sc1 lds\n"
"s_waitcnt lgkmcnt(4)\n"
"v_mfma_f32_16x16x32_bf16 v[16:19], a[212:215], v[144:147], v[16:19]\n"
"ds_read_b128 a[192:195], v194 offset:4096\n"
"ds_read_b128 a[224:227], v194 offset:12288\n"
"v_mfma_f32_16x16x32_bf16 v[16:19], a[180:183], v[128:131], v[16:19]\n"
"ds_read_b128 a[196:199], v194 offset:5120\n"
"ds_read_b128 a[228:231], v194 offset:13312\n"
"s_waitcnt lgkmcnt(6)\n"
"v_mfma_f32_16x16x32_bf16 v[32:35], a[184:187], v[128:131], v[32:35]\n"
"ds_read_b128 a[200:203], v194 offset:6144\n"
"ds_read_b128 a[232:235], v194 offset:14336\n"
"buffer_load_dwordx4 v254, s[96:99], s67 offen offset:1024 sc1 lds\n"
"v_mfma_f32_16x16x32_bf16 v[32:35], a[216:219], v[144:147], v[32:35]\n"
"ds_read_b128 a[204:207], v194 offset:7168\n"
"ds_read_b128 a[236:239], v194 offset:15360\n"
"s_waitcnt lgkmcnt(8)\n"
"v_mfma_f32_16x16x32_bf16 v[48:51], a[220:223], v[144:147], v[48:51]\n"
"v_cvt_pk_bf16_f32 v140, v76, v77\n"
"v_cvt_pk_bf16_f32 v141, v78, v79\n"
"v_mfma_f32_16x16x32_bf16 v[48:51], a[188:191], v[128:131], v[48:51]\n"
"buffer_load_dwordx4 v254, s[96:99], s67 offen offset:2048 sc1 lds\n"
"s_waitcnt lgkmcnt(6)\n"
"v_mfma_f32_16x16x32_bf16 a[0:3], a[192:195], v[128:131], a[0:3]\n"
"v_cvt_pk_bf16_f32 v142, v92, v93\n"
"v_cvt_pk_bf16_f32 v143, v94, v95\n"
"v_mfma_f32_16x16x32_bf16 a[0:3], a[224:227], v[144:147], a[0:3]\n"
"v_cvt_pk_bf16_f32 v156, v108, v109\n"
"v_cvt_pk_bf16_f32 v157, v110, v111\n"
"s_waitcnt lgkmcnt(4)\n"
"v_mfma_f32_16x16x32_bf16 a[16:19], a[228:231], v[144:147], a[16:19]\n"
"buffer_load_dwordx4 v254, s[96:99], s67 offen offset:3072 sc1 lds\n"
"v_mfma_f32_16x16x32_bf16 a[16:19], a[196:199], v[128:131], a[16:19]\n"
"v_cvt_pk_bf16_f32 v158, v124, v125\n"
"v_cvt_pk_bf16_f32 v159, v126, v127\n"
"s_waitcnt lgkmcnt(2)\n"
"v_mfma_f32_16x16x32_bf16 a[32:35], a[200:203], v[128:131], a[32:35]\n"
"v_mfma_f32_16x16x32_bf16 a[32:35], a[232:235], v[144:147], a[32:35]\n"
"s_sub_u32 s89, 32, s66\n"
"s_lshl_b32 s89, s89, 14\n"
"s_add_u32 s67, s73, s89\n"
"s_xor_b32 s84, s84, 0x8000\n"
"s_mov_b32 m0, s84\n"
"s_waitcnt lgkmcnt(0)\n"
"v_mfma_f32_16x16x32_bf16 a[48:51], a[236:239], v[144:147], a[48:51]\n"
"buffer_load_dwordx4 v254, s[28:31], s67 offen sc1 lds\n"
"v_mfma_f32_16x16x32_bf16 a[48:51], a[204:207], v[128:131], a[48:51]\n"
"v_mfma_f32_16x16x32_bf16 a[128:131], v[200:203], v[128:131], a[128:131]\n"
"v_mfma_f32_16x16x32_bf16 a[128:131], v[200:203], v[144:147], a[128:131]\n"
"buffer_load_dwordx4 v254, s[28:31], s67 offen offset:1024 sc1 lds\n"
"v_mfma_f32_16x16x32_bf16 v[4:7], a[176:179], v[132:135], v[4:7]\n"
"v_mfma_f32_16x16x32_bf16 v[4:7], a[208:211], v[148:151], v[4:7]\n"
"v_mfma_f32_16x16x32_bf16 v[20:23], a[212:215], v[148:151], v[20:23]\n"
"buffer_load_dwordx4 v254, s[28:31], s67 offen offset:2048 sc1 lds\n"
"v_mfma_f32_16x16x32_bf16 v[20:23], a[180:183], v[132:135], v[20:23]\n"
"v_mfma_f32_16x16x32_bf16 v[36:39], a[184:187], v[132:135], v[36:39]\n"
"v_mfma_f32_16x16x32_bf16 v[36:39], a[216:219], v[148:151], v[36:39]\n"
"buffer_load_dwordx4 v254, s[28:31], s67 offen offset:3072 sc1 lds\n"
"v_mfma_f32_16x16x32_bf16 v[52:55], a[220:223], v[148:151], v[52:55]\n"
"v_mfma_f32_16x16x32_bf16 v[52:55], a[188:191], v[132:135], v[52:55]\n"
"v_mfma_f32_16x16x32_bf16 a[4:7], a[192:195], v[132:135], a[4:7]\n"
"s_add_u32 s69, s67, 0x4000\n"
"s_add_u32 m0, s84, 0x4000\n"
"v_mfma_f32_16x16x32_bf16 a[4:7], a[224:227], v[148:151], a[4:7]\n"
"buffer_load_dwordx4 v254, s[28:31], s69 offen sc1 lds\n"
"v_mfma_f32_16x16x32_bf16 a[20:23], a[228:231], v[148:151], a[20:23]\n"
"v_mfma_f32_16x16x32_bf16 a[20:23], a[196:199], v[132:135], a[20:23]\n"
"v_mfma_f32_16x16x32_bf16 a[36:39], a[200:203], v[132:135], a[36:39]\n"
"buffer_load_dwordx4 v254, s[28:31], s69 offen offset:1024 sc1 lds\n"
"v_mfma_f32_16x16x32_bf16 a[36:39], a[232:235], v[148:151], a[36:39]\n"
"v_mfma_f32_16x16x32_bf16 a[52:55], a[236:239], v[148:151], a[52:55]\n"
"v_mfma_f32_16x16x32_bf16 a[52:55], a[204:207], v[132:135], a[52:55]\n"
"buffer_load_dwordx4 v254, s[28:31], s69 offen offset:2048 sc1 lds\n"
"v_mfma_f32_16x16x32_bf16 a[132:135], v[200:203], v[132:135], a[132:135]\n"
"v_mfma_f32_16x16x32_bf16 a[132:135], v[200:203], v[148:151], a[132:135]\n"
"v_mfma_f32_16x16x32_bf16 v[8:11], a[176:179], v[136:139], v[8:11]\n"
"buffer_load_dwordx4 v254, s[28:31], s69 offen offset:3072 sc1 lds\n"
"v_mfma_f32_16x16x32_bf16 v[8:11], a[208:211], v[152:155], v[8:11]\n"
"v_mfma_f32_16x16x32_bf16 v[24:27], a[212:215], v[152:155], v[24:27]\n"
"v_mfma_f32_16x16x32_bf16 v[24:27], a[180:183], v[136:139], v[24:27]\n"
"v_mfma_f32_16x16x32_bf16 v[40:43], a[184:187], v[136:139], v[40:43]\n"
"v_mfma_f32_16x16x32_bf16 v[40:43], a[216:219], v[152:155], v[40:43]\n"
"v_mfma_f32_16x16x32_bf16 v[56:59], a[220:223], v[152:155], v[56:59]\n"
"v_mfma_f32_16x16x32_bf16 v[56:59], a[188:191], v[136:139], v[56:59]\n"
"v_mfma_f32_16x16x32_bf16 a[8:11], a[192:195], v[136:139], a[8:11]\n"
"v_mfma_f32_16x16x32_bf16 a[8:11], a[224:227], v[152:155], a[8:11]\n"
"v_mfma_f32_16x16x32_bf16 a[24:27], a[228:231], v[152:155], a[24:27]\n"
"v_mfma_f32_16x16x32_bf16 a[24:27], a[196:199], v[136:139], a[24:27]\n"
"v_mfma_f32_16x16x32_bf16 a[40:43], a[200:203], v[136:139], a[40:43]\n"
"v_mfma_f32_16x16x32_bf16 a[40:43], a[232:235], v[152:155], a[40:43]\n"
"v_mfma_f32_16x16x32_bf16 a[56:59], a[236:239], v[152:155], a[56:59]\n"
"v_mfma_f32_16x16x32_bf16 a[56:59], a[204:207], v[136:139], a[56:59]\n"
"v_mfma_f32_16x16x32_bf16 a[136:139], v[200:203], v[136:139], a[136:139]\n"
"v_mfma_f32_16x16x32_bf16 a[136:139], v[200:203], v[152:155], a[136:139]\n"
"v_mfma_f32_16x16x32_bf16 v[12:15], a[176:179], v[140:143], v[12:15]\n"
"v_mfma_f32_16x16x32_bf16 v[12:15], a[208:211], v[156:159], v[12:15]\n"
"v_mfma_f32_16x16x32_bf16 v[28:31], a[212:215], v[156:159], v[28:31]\n"
"v_mfma_f32_16x16x32_bf16 v[28:31], a[180:183], v[140:143], v[28:31]\n"
"v_mfma_f32_16x16x32_bf16 v[44:47], a[184:187], v[140:143], v[44:47]\n"
"v_mfma_f32_16x16x32_bf16 v[44:47], a[216:219], v[156:159], v[44:47]\n"
"v_mfma_f32_16x16x32_bf16 v[60:63], a[220:223], v[156:159], v[60:63]\n"
"v_mfma_f32_16x16x32_bf16 v[60:63], a[188:191], v[140:143], v[60:63]\n"
"v_mfma_f32_16x16x32_bf16 a[12:15], a[192:195], v[140:143], a[12:15]\n"
"v_mfma_f32_16x16x32_bf16 a[12:15], a[224:227], v[156:159], a[12:15]\n"
"v_mfma_f32_16x16x32_bf16 a[28:31], a[228:231], v[156:159], a[28:31]\n"
"v_mfma_f32_16x16x32_bf16 a[28:31], a[196:199], v[140:143], a[28:31]\n"
"v_mfma_f32_16x16x32_bf16 a[44:47], a[200:203], v[140:143], a[44:47]\n"
"v_mfma_f32_16x16x32_bf16 a[44:47], a[232:235], v[156:159], a[44:47]\n"
"v_mfma_f32_16x16x32_bf16 a[60:63], a[236:239], v[156:159], a[60:63]\n"
"v_mfma_f32_16x16x32_bf16 a[60:63], a[204:207], v[140:143], a[60:63]\n"
"v_mfma_f32_16x16x32_bf16 a[140:143], v[200:203], v[140:143], a[140:143]\n"
"v_mfma_f32_16x16x32_bf16 a[140:143], v[200:203], v[156:159], a[140:143]\n"
".Llcommon_35_%=:\n"
"s_lshl_b32 s13, s8, 15\n"
"s_add_u32 s13, s13, 0x14000\n"
"v_add_u32 v244, s13, v254\n"
"s_sub_u32 s12, 1, s8\n"
"s_lshl_b32 s13, s12, 10\n"
"s_add_u32 s13, s13, 0x20000\n"
"v_add_u32 v235, s13, v254\n"
"ds_read_b128 v[128:131], v235\n"
"ds_read_b128 a[64:67], v244\n"
"ds_read_b128 a[68:71], v244 offset:1024\n"
"ds_read_b128 a[72:75], v244 offset:2048\n"
"ds_read_b128 a[76:79], v244 offset:3072\n"
"ds_read_b128 a[80:83], v244 offset:4096\n"
"ds_read_b128 a[84:87], v244 offset:5120\n"
"ds_read_b128 a[88:91], v244 offset:6144\n"
"ds_read_b128 a[92:95], v244 offset:7168\n"
"v_accvgpr_read_b32 v237, a128\n"
"v_accvgpr_read_b32 v239, a132\n"
"v_accvgpr_read_b32 v241, a136\n"
"v_accvgpr_read_b32 v243, a140\n"
"s_lshl_b32 s88, s10, 19\n"
"s_lshl_b32 s12, s77, 10\n"
"s_add_u32 s88, s88, s12\n"
"s_add_u32 s74, s88, 0x20000\n"
"s_add_u32 s75, s88, 0x40000\n"
"s_add_u32 s76, s88, 0x60000\n"
"v_mov_b32 v246, 0x6f800000\n"
"v_mov_b32 v245, 0x17800000\n"
"v_cmp_nle_f32 vcc, v237, v246\n"
"v_cmp_nge_f32 s[14:15], v237, v245\n"
"v_rcp_f32 v236, v237\n"
"s_or_b64 s[90:91], s[90:91], vcc\n"
"s_or_b64 s[90:91], s[90:91], s[14:15]\n"
"v_cmp_nle_f32 vcc, v239, v246\n"
"v_cmp_nge_f32 s[14:15], v239, v245\n"
"v_rcp_f32 v238, v239\n"
"s_or_b64 s[90:91], s[90:91], vcc\n"
"s_or_b64 s[90:91], s[90:91], s[14:15]\n"
"v_cmp_nle_f32 vcc, v241, v246\n"
"v_cmp_nge_f32 s[14:15], v241, v245\n"
"v_rcp_f32 v240, v241\n"
"s_or_b64 s[90:91], s[90:91], vcc\n"
"s_or_b64 s[90:91], s[90:91], s[14:15]\n"
"v_cmp_nle_f32 vcc, v243, v246\n"
"v_cmp_nge_f32 s[14:15], v243, v245\n"
"v_rcp_f32 v242, v243\n"
"s_or_b64 s[90:91], s[90:91], vcc\n"
"s_or_b64 s[90:91], s[90:91], s[14:15]\n"
"s_or_b32 s12, s90, s91\n"
"s_lshl_b32 s13, s8, 3\n"
"s_add_u32 s13, s13, 0x21000\n"
"v_mov_b32 v232, s12\n"
"v_mov_b32 v233, s12\n"
"v_mov_b32 v235, s13\n"
"ds_write_b64 v235, v[232:233]\n"
"ds_read_b128 a[96:99], v244 offset:8192\n"
"v_pk_mul_f32 v[0:1], v[0:1], v[236:237] op_sel_hi:[1,0]\n"
"v_pk_mul_f32 v[2:3], v[2:3], v[236:237] op_sel_hi:[1,0]\n"
"v_pk_mul_f32 v[16:17], v[16:17], v[236:237] op_sel_hi:[1,0]\n"
"v_pk_mul_f32 v[18:19], v[18:19], v[236:237] op_sel_hi:[1,0]\n"
"v_cvt_pk_bf16_f32 v220, v0, v1\n"
"v_cvt_pk_bf16_f32 v221, v2, v3\n"
"v_cvt_pk_bf16_f32 v222, v16, v17\n"
"v_cvt_pk_bf16_f32 v223, v18, v19\n"
"buffer_store_dwordx4 v[220:223], v254, s[36:39], s88 offen sc1\n"
"ds_read_b128 a[100:103], v244 offset:9216\n"
"v_pk_mul_f32 v[4:5], v[4:5], v[238:239] op_sel_hi:[1,0]\n"
"v_pk_mul_f32 v[6:7], v[6:7], v[238:239] op_sel_hi:[1,0]\n"
"v_pk_mul_f32 v[20:21], v[20:21], v[238:239] op_sel_hi:[1,0]\n"
"v_pk_mul_f32 v[22:23], v[22:23], v[238:239] op_sel_hi:[1,0]\n"
"v_cvt_pk_bf16_f32 v224, v4, v5\n"
"v_cvt_pk_bf16_f32 v225, v6, v7\n"
"v_cvt_pk_bf16_f32 v226, v20, v21\n"
"v_cvt_pk_bf16_f32 v227, v22, v23\n"
"buffer_store_dwordx4 v[224:227], v254, s[36:39], s88 offen offset:1024 sc1\n"
"ds_read_b128 a[104:107], v244 offset:10240\n"
"v_pk_mul_f32 v[8:9], v[8:9], v[240:241] op_sel_hi:[1,0]\n"
"v_pk_mul_f32 v[10:11], v[10:11], v[240:241] op_sel_hi:[1,0]\n"
"v_pk_mul_f32 v[24:25], v[24:25], v[240:241] op_sel_hi:[1,0]\n"
"v_pk_mul_f32 v[26:27], v[26:27], v[240:241] op_sel_hi:[1,0]\n"
"v_cvt_pk_bf16_f32 v228, v8, v9\n"
"v_cvt_pk_bf16_f32 v229, v10, v11\n"
"v_cvt_pk_bf16_f32 v230, v24, v25\n"
"v_cvt_pk_bf16_f32 v231, v26, v27\n"
"buffer_store_dwordx4 v[228:231], v254, s[36:39], s88 offen offset:2048 sc1\n"
"ds_read_b128 a[108:111], v244 offset:11264\n"
"v_pk_mul_f32 v[12:13], v[12:13], v[242:243] op_sel_hi:[1,0]\n"
"v_pk_mul_f32 v[14:15], v[14:15], v[242:243] op_sel_hi:[1,0]\n"
"v_pk_mul_f32 v[28:29], v[28:29], v[242:243] op_sel_hi:[1,0]\n"
"v_pk_mul_f32 v[30:31], v[30:31], v[242:243] op_sel_hi:[1,0]\n"
"v_cvt_pk_bf16_f32 v232, v12, v13\n"
"v_cvt_pk_bf16_f32 v233, v14, v15\n"
"v_cvt_pk_bf16_f32 v234, v28, v29\n"
"v_cvt_pk_bf16_f32 v235, v30, v31\n"
"buffer_store_dwordx4 v[232:235], v254, s[36:39], s88 offen offset:3072 sc1\n"
"ds_read_b128 a[112:115], v244 offset:12288\n"
"v_pk_mul_f32 v[32:33], v[32:33], v[236:237] op_sel_hi:[1,0]\n"
"v_pk_mul_f32 v[34:35], v[34:35], v[236:237] op_sel_hi:[1,0]\n"
"v_pk_mul_f32 v[48:49], v[48:49], v[236:237] op_sel_hi:[1,0]\n"
"v_pk_mul_f32 v[50:51], v[50:51], v[236:237] op_sel_hi:[1,0]\n"
"v_cvt_pk_bf16_f32 v220, v32, v33\n"
"v_cvt_pk_bf16_f32 v221, v34, v35\n"
"v_cvt_pk_bf16_f32 v222, v48, v49\n"
"v_cvt_pk_bf16_f32 v223, v50, v51\n"
"buffer_store_dwordx4 v[220:223], v254, s[36:39], s74 offen sc1\n"
"ds_read_b128 a[116:119], v244 offset:13312\n"
"v_pk_mul_f32 v[36:37], v[36:37], v[238:239] op_sel_hi:[1,0]\n"
"v_pk_mul_f32 v[38:39], v[38:39], v[238:239] op_sel_hi:[1,0]\n"
"v_pk_mul_f32 v[52:53], v[52:53], v[238:239] op_sel_hi:[1,0]\n"
"v_pk_mul_f32 v[54:55], v[54:55], v[238:239] op_sel_hi:[1,0]\n"
"v_cvt_pk_bf16_f32 v224, v36, v37\n"
"v_cvt_pk_bf16_f32 v225, v38, v39\n"
"v_cvt_pk_bf16_f32 v226, v52, v53\n"
"v_cvt_pk_bf16_f32 v227, v54, v55\n"
"buffer_store_dwordx4 v[224:227], v254, s[36:39], s74 offen offset:1024 sc1\n"
"ds_read_b128 a[120:123], v244 offset:14336\n"
"v_pk_mul_f32 v[40:41], v[40:41], v[240:241] op_sel_hi:[1,0]\n"
"v_pk_mul_f32 v[42:43], v[42:43], v[240:241] op_sel_hi:[1,0]\n"
"v_pk_mul_f32 v[56:57], v[56:57], v[240:241] op_sel_hi:[1,0]\n"
"v_pk_mul_f32 v[58:59], v[58:59], v[240:241] op_sel_hi:[1,0]\n"
"v_cvt_pk_bf16_f32 v228, v40, v41\n"
"v_cvt_pk_bf16_f32 v229, v42, v43\n"
"v_cvt_pk_bf16_f32 v230, v56, v57\n"
"v_cvt_pk_bf16_f32 v231, v58, v59\n"
"buffer_store_dwordx4 v[228:231], v254, s[36:39], s74 offen offset:2048 sc1\n"
"ds_read_b128 a[124:127], v244 offset:15360\n"
"v_pk_mul_f32 v[44:45], v[44:45], v[242:243] op_sel_hi:[1,0]\n"
"v_pk_mul_f32 v[46:47], v[46:47], v[242:243] op_sel_hi:[1,0]\n"
"v_pk_mul_f32 v[60:61], v[60:61], v[242:243] op_sel_hi:[1,0]\n"
"v_pk_mul_f32 v[62:63], v[62:63], v[242:243] op_sel_hi:[1,0]\n"
"v_cvt_pk_bf16_f32 v232, v44, v45\n"
"v_cvt_pk_bf16_f32 v233, v46, v47\n"
"v_cvt_pk_bf16_f32 v234, v60, v61\n"
"v_cvt_pk_bf16_f32 v235, v62, v63\n"
"buffer_store_dwordx4 v[232:235], v254, s[36:39], s74 offen offset:3072 sc1\n"
"s_nop 3\n"
"s_waitcnt vmcnt(8) lgkmcnt(0)\n"
"s_barrier\n"
"s_mov_b64 exec, s[34:35]\n"
"buffer_store_dwordx2 v[250:251], v249, s[92:95], s53 offen sc1\n"
"s_mov_b64 exec, -1\n"
"s_add_u32 s62, s62, 1\n"
"v_xor_b32 v192, 0x8000, v192\n"
"v_add_u32 v192, 0x4000, v192\n"
"v_add_u32 v193, 0x5000, v193\n"
"v_add_u32 v194, 0x3000, v194\n"
"v_xor_b32 v193, 0x8000, v193\n"
"v_xor_b32 v194, 0x8000, v194\n"
"s_lshl_b32 s12, s11, 3\n"
"s_sub_u32 s77, 0x7c, s12\n"
"s_lshl_b32 s12, s8, 2\n"
"s_sub_u32 s77, s77, s12\n"
"s_cmp_eq_u32 s8, 0\n"
"s_cbranch_scc1 .Llb0_16_%=\n"
"s_cmp_eq_u32 s62, 17\n"
"s_cbranch_scc1 .Llzero_5048_%=\n"
"s_xor_b32 s85, s85, 0x8000\n"
"s_mov_b32 m0, s85\n"
"s_nop 0\n"
"buffer_load_dwordx4 v254, s[96:99], s67 offen sc1 lds\n"
"v_mov_b32 v160, v128\n"
"v_mov_b32 v161, v128\n"
"v_mov_b32 v162, v128\n"
"v_mov_b32 v163, v128\n"
"v_mov_b32 v164, v129\n"
"v_mov_b32 v165, v129\n"
"v_mov_b32 v166, v129\n"
"v_mov_b32 v167, v129\n"
"v_mov_b32 v168, v130\n"
"v_mov_b32 v169, v130\n"
"v_mov_b32 v170, v130\n"
"v_mov_b32 v171, v130\n"
"v_mov_b32 v172, v131\n"
"v_mov_b32 v173, v131\n"
"s_nop 0\n"
"buffer_load_dwordx4 v254, s[96:99], s67 offen offset:1024 sc1 lds\n"
"v_mov_b32 v174, v131\n"
"v_mov_b32 v175, v131\n"
"v_accvgpr_read_b32 v204, a0\n"
"v_accvgpr_read_b32 v205, a1\n"
"v_accvgpr_read_b32 v206, a2\n"
"v_accvgpr_read_b32 v207, a3\n"
"v_accvgpr_read_b32 v208, a16\n"
"v_accvgpr_read_b32 v209, a17\n"
"v_accvgpr_read_b32 v210, a18\n"
"v_accvgpr_read_b32 v211, a19\n"
"v_pk_mul_f32 v[204:205], v[204:205], v[236:237] op_sel_hi:[1,0]\n"
"v_pk_mul_f32 v[206:207], v[206:207], v[236:237] op_sel_hi:[1,0]\n"
"v_pk_mul_f32 v[208:209], v[208:209], v[236:237] op_sel_hi:[1,0]\n"
"v_pk_mul_f32 v[210:211], v[210:211], v[236:237] op_sel_hi:[1,0]\n"
"s_nop 0\n"
"buffer_load_dwordx4 v254, s[96:99], s67 offen offset:2048 sc1 lds\n"
"v_cvt_pk_bf16_f32 v220, v204, v205\n"
"v_cvt_pk_bf16_f32 v221, v206, v207\n"
"v_cvt_pk_bf16_f32 v222, v208, v209\n"
"v_cvt_pk_bf16_f32 v223, v210, v211\n"
"buffer_store_dwordx4 v[220:223], v254, s[36:39], s75 offen sc1\n"
"v_accvgpr_read_b32 v212, a4\n"
"v_accvgpr_read_b32 v213, a5\n"
"v_accvgpr_read_b32 v214, a6\n"
"v_accvgpr_read_b32 v215, a7\n"
"v_accvgpr_read_b32 v216, a20\n"
"v_accvgpr_read_b32 v217, a21\n"
"v_accvgpr_read_b32 v218, a22\n"
"v_accvgpr_read_b32 v219, a23\n"
"v_pk_mul_f32 v[212:213], v[212:213], v[238:239] op_sel_hi:[1,0]\n"
"s_nop 0\n"
"buffer_load_dwordx4 v254, s[96:99], s67 offen offset:3072 sc1 lds\n"
"v_pk_mul_f32 v[214:215], v[214:215], v[238:239] op_sel_hi:[1,0]\n"
"v_pk_mul_f32 v[216:217], v[216:217], v[238:239] op_sel_hi:[1,0]\n"
"v_pk_mul_f32 v[218:219], v[218:219], v[238:239] op_sel_hi:[1,0]\n"
"v_cvt_pk_bf16_f32 v224, v212, v213\n"
"v_cvt_pk_bf16_f32 v225, v214, v215\n"
"v_cvt_pk_bf16_f32 v226, v216, v217\n"
"v_cvt_pk_bf16_f32 v227, v218, v219\n"
"buffer_store_dwordx4 v[224:227], v254, s[36:39], s75 offen offset:1024 sc1\n"
"v_accvgpr_read_b32 v204, a8\n"
"v_accvgpr_read_b32 v205, a9\n"
"v_accvgpr_read_b32 v206, a10\n"
"v_accvgpr_read_b32 v207, a11\n"
"v_accvgpr_read_b32 v208, a24\n"
"v_accvgpr_read_b32 v209, a25\n"
"s_add_u32 s70, s67, 0x4000\n"
"s_add_u32 m0, s85, 0x4000\n"
"s_nop 0\n"
"buffer_load_dwordx4 v254, s[96:99], s70 offen sc1 lds\n"
"v_accvgpr_read_b32 v210, a26\n"
"v_accvgpr_read_b32 v211, a27\n"
"v_pk_mul_f32 v[204:205], v[204:205], v[240:241] op_sel_hi:[1,0]\n"
"v_pk_mul_f32 v[206:207], v[206:207], v[240:241] op_sel_hi:[1,0]\n"
"v_pk_mul_f32 v[208:209], v[208:209], v[240:241] op_sel_hi:[1,0]\n"
"v_pk_mul_f32 v[210:211], v[210:211], v[240:241] op_sel_hi:[1,0]\n"
"v_cvt_pk_bf16_f32 v228, v204, v205\n"
"v_cvt_pk_bf16_f32 v229, v206, v207\n"
"v_cvt_pk_bf16_f32 v230, v208, v209\n"
"v_cvt_pk_bf16_f32 v231, v210, v211\n"
"buffer_store_dwordx4 v[228:231], v254, s[36:39], s75 offen offset:2048 sc1\n"
"v_accvgpr_read_b32 v212, a12\n"
"v_accvgpr_read_b32 v213, a13\n"
"v_accvgpr_read_b32 v214, a14\n"
"s_nop 0\n"
"buffer_load_dwordx4 v254, s[96:99], s70 offen offset:1024 sc1 lds\n"
"v_accvgpr_read_b32 v215, a15\n"
"v_accvgpr_read_b32 v216, a28\n"
"v_accvgpr_read_b32 v217, a29\n"
"v_accvgpr_read_b32 v218, a30\n"
"v_accvgpr_read_b32 v219, a31\n"
"v_pk_mul_f32 v[212:213], v[212:213], v[242:243] op_sel_hi:[1,0]\n"
"v_pk_mul_f32 v[214:215], v[214:215], v[242:243] op_sel_hi:[1,0]\n"
"v_pk_mul_f32 v[216:217], v[216:217], v[242:243] op_sel_hi:[1,0]\n"
"v_pk_mul_f32 v[218:219], v[218:219], v[242:243] op_sel_hi:[1,0]\n"
"v_cvt_pk_bf16_f32 v232, v212, v213\n"
"v_cvt_pk_bf16_f32 v233, v214, v215\n"
"v_cvt_pk_bf16_f32 v234, v216, v217\n"
"v_cvt_pk_bf16_f32 v235, v218, v219\n"
"buffer_store_dwordx4 v[232:235], v254, s[36:39], s75 offen offset:3072 sc1\n"
"s_nop 0\n"
"buffer_load_dwordx4 v254, s[96:99], s70 offen offset:2048 sc1 lds\n"
"v_accvgpr_read_b32 v204, a32\n"
"v_accvgpr_read_b32 v205, a33\n"
"v_accvgpr_read_b32 v206, a34\n"
"v_accvgpr_read_b32 v207, a35\n"
"v_accvgpr_read_b32 v208, a48\n"
"v_accvgpr_read_b32 v209, a49\n"
"v_accvgpr_read_b32 v210, a50\n"
"v_accvgpr_read_b32 v211, a51\n"
"v_pk_mul_f32 v[204:205], v[204:205], v[236:237] op_sel_hi:[1,0]\n"
"v_pk_mul_f32 v[206:207], v[206:207], v[236:237] op_sel_hi:[1,0]\n"
"v_pk_mul_f32 v[208:209], v[208:209], v[236:237] op_sel_hi:[1,0]\n"
"v_pk_mul_f32 v[210:211], v[210:211], v[236:237] op_sel_hi:[1,0]\n"
"v_cvt_pk_bf16_f32 v220, v204, v205\n"
"v_cvt_pk_bf16_f32 v221, v206, v207\n"
"v_cvt_pk_bf16_f32 v222, v208, v209\n"
"v_cvt_pk_bf16_f32 v223, v210, v211\n"
"buffer_store_dwordx4 v[220:223], v254, s[36:39], s76 offen sc1\n"
"v_accvgpr_read_b32 v212, a36\n"
"v_accvgpr_read_b32 v213, a37\n"
"v_accvgpr_read_b32 v214, a38\n"
"v_accvgpr_read_b32 v215, a39\n"
"v_accvgpr_read_b32 v216, a52\n"
"v_accvgpr_read_b32 v217, a53\n"
"v_accvgpr_read_b32 v218, a54\n"
"v_accvgpr_read_b32 v219, a55\n"
"v_pk_mul_f32 v[212:213], v[212:213], v[238:239] op_sel_hi:[1,0]\n"
"v_pk_mul_f32 v[214:215], v[214:215], v[238:239] op_sel_hi:[1,0]\n"
"v_pk_mul_f32 v[216:217], v[216:217], v[238:239] op_sel_hi:[1,0]\n"
"v_pk_mul_f32 v[218:219], v[218:219], v[238:239] op_sel_hi:[1,0]\n"
"v_cvt_pk_bf16_f32 v224, v212, v213\n"
"v_cvt_pk_bf16_f32 v225, v214, v215\n"
"v_cvt_pk_bf16_f32 v226, v216, v217\n"
"v_cvt_pk_bf16_f32 v227, v218, v219\n"
"buffer_store_dwordx4 v[224:227], v254, s[36:39], s76 offen offset:1024 sc1\n"
"v_accvgpr_read_b32 v204, a40\n"
"v_accvgpr_read_b32 v205, a41\n"
"v_accvgpr_read_b32 v206, a42\n"
"v_accvgpr_read_b32 v207, a43\n"
"v_accvgpr_read_b32 v208, a56\n"
"v_accvgpr_read_b32 v209, a57\n"
"v_accvgpr_read_b32 v210, a58\n"
"v_accvgpr_read_b32 v211, a59\n"
"v_pk_mul_f32 v[204:205], v[204:205], v[240:241] op_sel_hi:[1,0]\n"
"v_pk_mul_f32 v[206:207], v[206:207], v[240:241] op_sel_hi:[1,0]\n"
"v_pk_mul_f32 v[208:209], v[208:209], v[240:241] op_sel_hi:[1,0]\n"
"v_pk_mul_f32 v[210:211], v[210:211], v[240:241] op_sel_hi:[1,0]\n"
"v_cvt_pk_bf16_f32 v228, v204, v205\n"
"v_cvt_pk_bf16_f32 v229, v206, v207\n"
"v_cvt_pk_bf16_f32 v230, v208, v209\n"
"v_cvt_pk_bf16_f32 v231, v210, v211\n"
"buffer_store_dwordx4 v[228:231], v254, s[36:39], s76 offen offset:2048 sc1\n"
"v_accvgpr_read_b32 v212, a44\n"
"v_accvgpr_read_b32 v213, a45\n"
"v_accvgpr_read_b32 v214, a46\n"
"v_accvgpr_read_b32 v215, a47\n"
"v_accvgpr_read_b32 v216, a60\n"
"v_accvgpr_read_b32 v217, a61\n"
"v_accvgpr_read_b32 v218, a62\n"
"v_accvgpr_read_b32 v219, a63\n"
"v_pk_mul_f32 v[212:213], v[212:213], v[242:243] op_sel_hi:[1,0]\n"
"v_pk_mul_f32 v[214:215], v[214:215], v[242:243] op_sel_hi:[1,0]\n"
"v_pk_mul_f32 v[216:217], v[216:217], v[242:243] op_sel_hi:[1,0]\n"
"v_pk_mul_f32 v[218:219], v[218:219], v[242:243] op_sel_hi:[1,0]\n"
"v_cvt_pk_bf16_f32 v232, v212, v213\n"
"v_cvt_pk_bf16_f32 v233, v214, v215\n"
"v_cvt_pk_bf16_f32 v234, v216, v217\n"
"v_cvt_pk_bf16_f32 v235, v218, v219\n"
"buffer_store_dwordx4 v[232:235], v254, s[36:39], s76 offen offset:3072 sc1\n"
"ds_read_b128 a[144:147], v192\n"
"ds_read_b128 a[148:151], v192 offset:1024\n"
"ds_read_b128 a[152:155], v192 offset:2048\n"
"ds_read_b128 a[156:159], v192 offset:3072\n"
"ds_read_b128 a[160:163], v192 offset:4096\n"
"ds_read_b128 a[164:167], v192 offset:5120\n"
"ds_read_b128 a[168:171], v192 offset:6144\n"
"ds_read_b128 a[172:175], v192 offset:7168\n"
"ds_read_b128 a[176:179], v193\n"
"ds_read_b128 a[208:211], v193 offset:8192\n"
"ds_read_b128 a[180:183], v193 offset:1024\n"
"ds_read_b128 a[212:215], v193 offset:9216\n"
"s_waitcnt lgkmcnt(8)\n"
"v_mfma_f32_16x16x32_bf16 v[64:67], a[144:147], a[64:67], v[160:163]\n"
"v_mfma_f32_16x16x32_bf16 v[64:67], a[148:151], a[80:83], v[64:67]\n"
"v_mfma_f32_16x16x32_bf16 v[64:67], a[152:155], a[96:99], v[64:67]\n"
"buffer_load_dwordx4 v254, s[96:99], s70 offen offset:3072 sc1 lds\n"
"v_mfma_f32_16x16x32_bf16 v[64:67], a[156:159], a[112:115], v[64:67]\n"
"v_mfma_f32_16x16x32_bf16 v[68:71], a[156:159], a[116:119], v[164:167]\n"
"v_mfma_f32_16x16x32_bf16 v[68:71], a[152:155], a[100:103], v[68:71]\n"
"v_mfma_f32_16x16x32_bf16 v[68:71], a[148:151], a[84:87], v[68:71]\n"
"s_add_u32 s67, s67, 0x8000\n"
"s_xor_b32 s84, s84, 0x8000\n"
"s_mov_b32 m0, s84\n"
"v_mfma_f32_16x16x32_bf16 v[68:71], a[144:147], a[68:71], v[68:71]\n"
"buffer_load_dwordx4 v254, s[28:31], s67 offen sc1 lds\n"
"v_mfma_f32_16x16x32_bf16 v[72:75], a[144:147], a[72:75], v[168:171]\n"
"v_mfma_f32_16x16x32_bf16 v[72:75], a[148:151], a[88:91], v[72:75]\n"
"v_mfma_f32_16x16x32_bf16 v[72:75], a[152:155], a[104:107], v[72:75]\n"
"v_mfma_f32_16x16x32_bf16 v[72:75], a[156:159], a[120:123], v[72:75]\n"
"buffer_load_dwordx4 v254, s[28:31], s67 offen offset:1024 sc1 lds\n"
"v_mfma_f32_16x16x32_bf16 v[76:79], a[156:159], a[124:127], v[172:175]\n"
"v_mfma_f32_16x16x32_bf16 v[76:79], a[152:155], a[108:111], v[76:79]\n"
"v_mfma_f32_16x16x32_bf16 v[76:79], a[148:151], a[92:95], v[76:79]\n"
"v_mfma_f32_16x16x32_bf16 v[76:79], a[144:147], a[76:79], v[76:79]\n"
"buffer_load_dwordx4 v254, s[28:31], s67 offen offset:2048 sc1 lds\n"
"s_waitcnt lgkmcnt(4)\n"
"v_mfma_f32_16x16x32_bf16 v[80:83], a[160:163], a[64:67], v[160:163]\n"
"ds_read_b128 a[144:147], v192 offset:8192\n"
"ds_read_b128 a[148:151], v192 offset:9216\n"
"v_mfma_f32_16x16x32_bf16 v[80:83], a[164:167], a[80:83], v[80:83]\n"
"v_mfma_f32_16x16x32_bf16 v[80:83], a[168:171], a[96:99], v[80:83]\n"
"ds_read_b128 a[152:155], v192 offset:10240\n"
"ds_read_b128 a[156:159], v192 offset:11264\n"
"v_mfma_f32_16x16x32_bf16 v[80:83], a[172:175], a[112:115], v[80:83]\n"
"buffer_load_dwordx4 v254, s[28:31], s67 offen offset:3072 sc1 lds\n"
"v_mfma_f32_16x16x32_bf16 v[84:87], a[172:175], a[116:119], v[164:167]\n"
"v_mfma_f32_16x16x32_bf16 v[84:87], a[168:171], a[100:103], v[84:87]\n"
"v_exp_f32 v64, v64\n"
"v_mfma_f32_16x16x32_bf16 v[84:87], a[164:167], a[84:87], v[84:87]\n"
"v_exp_f32 v65, v65\n"
"v_mfma_f32_16x16x32_bf16 v[84:87], a[160:163], a[68:71], v[84:87]\n"
"s_add_u32 s69, s67, 0x4000\n"
"s_add_u32 m0, s84, 0x4000\n"
"v_mfma_f32_16x16x32_bf16 v[88:91], a[160:163], a[72:75], v[168:171]\n"
"buffer_load_dwordx4 v254, s[28:31], s69 offen sc1 lds\n"
"v_mfma_f32_16x16x32_bf16 v[88:91], a[164:167], a[88:91], v[88:91]\n"
"v_exp_f32 v66, v66\n"
"v_mfma_f32_16x16x32_bf16 v[88:91], a[168:171], a[104:107], v[88:91]\n"
"v_cvt_pk_bf16_f32 v128, v64, v65\n"
"v_mfma_f32_16x16x32_bf16 v[88:91], a[172:175], a[120:123], v[88:91]\n"
"v_exp_f32 v67, v67\n"
"v_mfma_f32_16x16x32_bf16 v[92:95], a[172:175], a[124:127], v[172:175]\n"
"buffer_load_dwordx4 v254, s[28:31], s69 offen offset:1024 sc1 lds\n"
"v_mfma_f32_16x16x32_bf16 v[92:95], a[168:171], a[108:111], v[92:95]\n"
"v_exp_f32 v80, v80\n"
"v_mfma_f32_16x16x32_bf16 v[92:95], a[164:167], a[92:95], v[92:95]\n"
"v_cvt_pk_bf16_f32 v129, v66, v67\n"
"v_mfma_f32_16x16x32_bf16 v[92:95], a[160:163], a[76:79], v[92:95]\n"
"v_exp_f32 v81, v81\n"
"s_waitcnt lgkmcnt(0)\n"
"v_mfma_f32_16x16x32_bf16 v[96:99], a[144:147], a[64:67], v[160:163]\n"
"ds_read_b128 a[160:163], v192 offset:12288\n"
"ds_read_b128 a[164:167], v192 offset:13312\n"
"buffer_load_dwordx4 v254, s[28:31], s69 offen offset:2048 sc1 lds\n"
"v_mfma_f32_16x16x32_bf16 v[96:99], a[148:151], a[80:83], v[96:99]\n"
"v_exp_f32 v82, v82\n"
"v_mfma_f32_16x16x32_bf16 v[96:99], a[152:155], a[96:99], v[96:99]\n"
"ds_read_b128 a[168:171], v192 offset:14336\n"
"ds_read_b128 a[172:175], v192 offset:15360\n"
"v_mfma_f32_16x16x32_bf16 v[96:99], a[156:159], a[112:115], v[96:99]\n"
"v_cvt_pk_bf16_f32 v130, v80, v81\n"
"v_mfma_f32_16x16x32_bf16 v[100:103], a[156:159], a[116:119], v[164:167]\n"
"buffer_load_dwordx4 v254, s[28:31], s69 offen offset:3072 sc1 lds\n"
"v_mfma_f32_16x16x32_bf16 v[100:103], a[152:155], a[100:103], v[100:103]\n"
"v_exp_f32 v83, v83\n"
"v_mfma_f32_16x16x32_bf16 v[100:103], a[148:151], a[84:87], v[100:103]\n"
"v_exp_f32 v68, v68\n"
"v_mfma_f32_16x16x32_bf16 v[100:103], a[144:147], a[68:71], v[100:103]\n"
"v_cvt_pk_bf16_f32 v131, v82, v83\n"
"v_mfma_f32_16x16x32_bf16 v[104:107], a[144:147], a[72:75], v[168:171]\n"
"v_exp_f32 v69, v69\n"
"v_mfma_f32_16x16x32_bf16 v[104:107], a[148:151], a[88:91], v[104:107]\n"
"v_exp_f32 v70, v70\n"
"v_mfma_f32_16x16x32_bf16 v[104:107], a[152:155], a[104:107], v[104:107]\n"
"v_cvt_pk_bf16_f32 v132, v68, v69\n"
"v_mfma_f32_16x16x32_bf16 v[104:107], a[156:159], a[120:123], v[104:107]\n"
"v_exp_f32 v71, v71\n"
"v_mfma_f32_16x16x32_bf16 v[108:111], a[156:159], a[124:127], v[172:175]\n"
"v_exp_f32 v84, v84\n"
"v_mfma_f32_16x16x32_bf16 v[108:111], a[152:155], a[108:111], v[108:111]\n"
"v_cvt_pk_bf16_f32 v133, v70, v71\n"
"v_mfma_f32_16x16x32_bf16 v[108:111], a[148:151], a[92:95], v[108:111]\n"
"v_exp_f32 v85, v85\n"
"v_mfma_f32_16x16x32_bf16 v[108:111], a[144:147], a[76:79], v[108:111]\n"
"v_exp_f32 v86, v86\n"
"s_waitcnt lgkmcnt(0)\n"
"v_mfma_f32_16x16x32_bf16 v[112:115], a[160:163], a[64:67], v[160:163]\n"
"v_cvt_pk_bf16_f32 v134, v84, v85\n"
"v_mfma_f32_16x16x32_bf16 v[112:115], a[164:167], a[80:83], v[112:115]\n"
"v_exp_f32 v87, v87\n"
"v_mfma_f32_16x16x32_bf16 v[112:115], a[168:171], a[96:99], v[112:115]\n"
"v_exp_f32 v72, v72\n"
"v_mfma_f32_16x16x32_bf16 v[112:115], a[172:175], a[112:115], v[112:115]\n"
"v_cvt_pk_bf16_f32 v135, v86, v87\n"
"v_mfma_f32_16x16x32_bf16 v[116:119], a[172:175], a[116:119], v[164:167]\n"
"v_exp_f32 v73, v73\n"
"v_mfma_f32_16x16x32_bf16 v[116:119], a[168:171], a[100:103], v[116:119]\n"
"v_exp_f32 v74, v74\n"
"v_mfma_f32_16x16x32_bf16 v[116:119], a[164:167], a[84:87], v[116:119]\n"
"v_cvt_pk_bf16_f32 v136, v72, v73\n"
"v_mfma_f32_16x16x32_bf16 v[116:119], a[160:163], a[68:71], v[116:119]\n"
"v_exp_f32 v75, v75\n"
"v_mfma_f32_16x16x32_bf16 v[120:123], a[160:163], a[72:75], v[168:171]\n"
"v_exp_f32 v88, v88\n"
"v_mfma_f32_16x16x32_bf16 v[120:123], a[164:167], a[88:91], v[120:123]\n"
"v_cvt_pk_bf16_f32 v137, v74, v75\n"
"v_mfma_f32_16x16x32_bf16 v[120:123], a[168:171], a[104:107], v[120:123]\n"
"v_exp_f32 v89, v89\n"
"v_mfma_f32_16x16x32_bf16 v[120:123], a[172:175], a[120:123], v[120:123]\n"
"v_exp_f32 v90, v90\n"
"v_mfma_f32_16x16x32_bf16 v[124:127], a[172:175], a[124:127], v[172:175]\n"
"v_cvt_pk_bf16_f32 v138, v88, v89\n"
"v_mfma_f32_16x16x32_bf16 v[124:127], a[168:171], a[108:111], v[124:127]\n"
"v_exp_f32 v91, v91\n"
"v_mfma_f32_16x16x32_bf16 v[124:127], a[164:167], a[92:95], v[124:127]\n"
"v_exp_f32 v76, v76\n"
"v_mfma_f32_16x16x32_bf16 v[124:127], a[160:163], a[76:79], v[124:127]\n"
"v_cvt_pk_bf16_f32 v139, v90, v91\n"
"s_nop 1\n"
"v_exp_f32 v77, v77\n"
"v_exp_f32 v78, v78\n"
"v_exp_f32 v79, v79\n"
"v_exp_f32 v92, v92\n"
"v_exp_f32 v93, v93\n"
"v_exp_f32 v94, v94\n"
"v_exp_f32 v95, v95\n"
"v_exp_f32 v96, v96\n"
"v_exp_f32 v97, v97\n"
"v_exp_f32 v98, v98\n"
"v_cvt_pk_bf16_f32 v144, v96, v97\n"
"v_exp_f32 v99, v99\n"
"v_exp_f32 v112, v112\n"
"v_cvt_pk_bf16_f32 v145, v98, v99\n"
"v_exp_f32 v113, v113\n"
"v_exp_f32 v114, v114\n"
"v_cvt_pk_bf16_f32 v146, v112, v113\n"
"v_exp_f32 v115, v115\n"
"v_exp_f32 v100, v100\n"
"v_cvt_pk_bf16_f32 v147, v114, v115\n"
"v_exp_f32 v101, v101\n"
"v_exp_f32 v102, v102\n"
"s_waitcnt vmcnt(0) lgkmcnt(0)\n"
"s_barrier\n"
"v_xor_b32 v192, 0x8000, v192\n"
"v_xor_b32 v193, 0x8000, v193\n"
"v_xor_b32 v194, 0x8000, v194\n"
"ds_read_b128 a[144:147], v192\n"
"v_cvt_pk_bf16_f32 v148, v100, v101\n"
"v_exp_f32 v103, v103\n"
"ds_read_b128 a[148:151], v192 offset:1024\n"
"v_exp_f32 v116, v116\n"
"v_cvt_pk_bf16_f32 v149, v102, v103\n"
"ds_read_b128 a[152:155], v192 offset:2048\n"
"v_exp_f32 v117, v117\n"
"v_exp_f32 v118, v118\n"
"ds_read_b128 a[156:159], v192 offset:3072\n"
"v_cvt_pk_bf16_f32 v150, v116, v117\n"
"v_exp_f32 v119, v119\n"
"ds_read_b128 a[160:163], v192 offset:4096\n"
"v_exp_f32 v104, v104\n"
"v_cvt_pk_bf16_f32 v151, v118, v119\n"
"ds_read_b128 a[164:167], v192 offset:5120\n"
"v_exp_f32 v105, v105\n"
"v_exp_f32 v106, v106\n"
"ds_read_b128 a[168:171], v192 offset:6144\n"
"v_cvt_pk_bf16_f32 v152, v104, v105\n"
"v_exp_f32 v107, v107\n"
"ds_read_b128 a[172:175], v192 offset:7168\n"
"v_exp_f32 v120, v120\n"
"v_cvt_pk_bf16_f32 v153, v106, v107\n"
"ds_read_b128 a[176:179], v193\n"
"v_exp_f32 v121, v121\n"
"v_exp_f32 v122, v122\n"
"ds_read_b128 a[208:211], v193 offset:8192\n"
"v_cvt_pk_bf16_f32 v154, v120, v121\n"
"v_exp_f32 v123, v123\n"
"ds_read_b128 a[180:183], v193 offset:1024\n"
"v_exp_f32 v108, v108\n"
"v_cvt_pk_bf16_f32 v155, v122, v123\n"
"ds_read_b128 a[212:215], v193 offset:9216\n"
"v_exp_f32 v109, v109\n"
"v_exp_f32 v110, v110\n"
"v_exp_f32 v111, v111\n"
"v_exp_f32 v124, v124\n"
"v_exp_f32 v125, v125\n"
"v_exp_f32 v126, v126\n"
"v_exp_f32 v127, v127\n"
"s_nop 0\n"
"s_add_u32 s62, s62, 1\n"
"s_cmp_eq_u32 s62, 16\n"
"s_cbranch_scc1 .Lfullvz_24_%=\n"
"s_nop 0\n"
".Lfull2z_22_%=:\n"
"s_waitcnt lgkmcnt(8)\n"
"v_mfma_f32_16x16x32_bf16 v[64:67], a[144:147], a[64:67], v[160:163]\n"
"v_cvt_pk_bf16_f32 v140, v76, v77\n"
"v_cvt_pk_bf16_f32 v141, v78, v79\n"
"v_mfma_f32_16x16x32_bf16 v[64:67], a[148:151], a[80:83], v[64:67]\n"
"s_xor_b32 s85, s85, 0x8000\n"
"v_mfma_f32_16x16x32_bf16 v[64:67], a[152:155], a[96:99], v[64:67]\n"
"v_cvt_pk_bf16_f32 v142, v92, v93\n"
"v_cvt_pk_bf16_f32 v143, v94, v95\n"
"v_mfma_f32_16x16x32_bf16 v[64:67], a[156:159], a[112:115], v[64:67]\n"
"s_mov_b32 m0, s85\n"
"v_mfma_f32_16x16x32_bf16 v[68:71], a[156:159], a[116:119], v[164:167]\n"
"v_cvt_pk_bf16_f32 v156, v108, v109\n"
"v_cvt_pk_bf16_f32 v157, v110, v111\n"
"v_mfma_f32_16x16x32_bf16 v[68:71], a[152:155], a[100:103], v[68:71]\n"
"buffer_load_dwordx4 v254, s[96:99], s67 offen sc1 lds\n"
"v_mfma_f32_16x16x32_bf16 v[68:71], a[148:151], a[84:87], v[68:71]\n"
"v_cvt_pk_bf16_f32 v158, v124, v125\n"
"v_cvt_pk_bf16_f32 v159, v126, v127\n"
"v_mfma_f32_16x16x32_bf16 v[68:71], a[144:147], a[68:71], v[68:71]\n"
"buffer_load_dwordx4 v254, s[96:99], s67 offen offset:1024 sc1 lds\n"
"v_mfma_f32_16x16x32_bf16 v[72:75], a[144:147], a[72:75], v[168:171]\n"
"ds_read_b128 a[184:187], v193 offset:2048\n"
"ds_read_b128 a[216:219], v193 offset:10240\n"
"v_mfma_f32_16x16x32_bf16 v[72:75], a[148:151], a[88:91], v[72:75]\n"
"v_mfma_f32_16x16x32_bf16 v[72:75], a[152:155], a[104:107], v[72:75]\n"
"buffer_load_dwordx4 v254, s[96:99], s67 offen offset:2048 sc1 lds\n"
"v_mfma_f32_16x16x32_bf16 v[72:75], a[156:159], a[120:123], v[72:75]\n"
"ds_read_b128 a[188:191], v193 offset:3072\n"
"ds_read_b128 a[220:223], v193 offset:11264\n"
"v_mfma_f32_16x16x32_bf16 v[76:79], a[156:159], a[124:127], v[172:175]\n"
"v_mfma_f32_16x16x32_bf16 v[76:79], a[152:155], a[108:111], v[76:79]\n"
"ds_read_b128 a[192:195], v194 offset:4096\n"
"v_mfma_f32_16x16x32_bf16 v[76:79], a[148:151], a[92:95], v[76:79]\n"
"ds_read_b128 a[224:227], v194 offset:12288\n"
"s_waitcnt lgkmcnt(10)\n"
"v_mfma_f32_16x16x32_bf16 v[76:79], a[144:147], a[76:79], v[76:79]\n"
"buffer_load_dwordx4 v254, s[96:99], s67 offen offset:3072 sc1 lds\n"
"v_mfma_f32_16x16x32_bf16 v[80:83], a[160:163], a[64:67], v[160:163]\n"
"ds_read_b128 a[144:147], v192 offset:8192\n"
"ds_read_b128 a[148:151], v192 offset:9216\n"
"v_mfma_f32_16x16x32_bf16 v[80:83], a[164:167], a[80:83], v[80:83]\n"
"s_add_u32 s70, s67, 0x4000\n"
"v_mfma_f32_16x16x32_bf16 v[80:83], a[168:171], a[96:99], v[80:83]\n"
"ds_read_b128 a[152:155], v192 offset:10240\n"
"ds_read_b128 a[156:159], v192 offset:11264\n"
"v_mfma_f32_16x16x32_bf16 v[80:83], a[172:175], a[112:115], v[80:83]\n"
"s_add_u32 m0, s85, 0x4000\n"
"v_mfma_f32_16x16x32_bf16 v[84:87], a[172:175], a[116:119], v[164:167]\n"
"buffer_load_dwordx4 v254, s[96:99], s70 offen sc1 lds\n"
"v_mfma_f32_16x16x32_bf16 v[84:87], a[168:171], a[100:103], v[84:87]\n"
"v_exp_f32 v64, v64\n"
"v_mfma_f32_16x16x32_bf16 v[84:87], a[164:167], a[84:87], v[84:87]\n"
"buffer_load_dwordx4 v254, s[96:99], s70 offen offset:1024 sc1 lds\n"
"v_mfma_f32_16x16x32_bf16 v[84:87], a[160:163], a[68:71], v[84:87]\n"
"v_exp_f32 v65, v65\n"
"v_mfma_f32_16x16x32_bf16 v[88:91], a[160:163], a[72:75], v[168:171]\n"
"ds_read_b128 a[196:199], v194 offset:5120\n"
"ds_read_b128 a[228:231], v194 offset:13312\n"
"v_mfma_f32_16x16x32_bf16 v[88:91], a[164:167], a[88:91], v[88:91]\n"
"v_exp_f32 v66, v66\n"
"v_mfma_f32_16x16x32_bf16 v[88:91], a[168:171], a[104:107], v[88:91]\n"
"buffer_load_dwordx4 v254, s[96:99], s70 offen offset:2048 sc1 lds\n"
"v_mfma_f32_16x16x32_bf16 v[88:91], a[172:175], a[120:123], v[88:91]\n"
"v_exp_f32 v67, v67\n"
"v_mfma_f32_16x16x32_bf16 v[92:95], a[172:175], a[124:127], v[172:175]\n"
"ds_read_b128 a[200:203], v194 offset:6144\n"
"ds_read_b128 a[232:235], v194 offset:14336\n"
"v_mfma_f32_16x16x32_bf16 v[92:95], a[168:171], a[108:111], v[92:95]\n"
"v_exp_f32 v80, v80\n"
"v_mfma_f32_16x16x32_bf16 v[92:95], a[164:167], a[92:95], v[92:95]\n"
"ds_read_b128 a[204:207], v194 offset:7168\n"
"s_waitcnt lgkmcnt(5)\n"
"v_mfma_f32_16x16x32_bf16 v[92:95], a[160:163], a[76:79], v[92:95]\n"
"v_exp_f32 v81, v81\n"
"v_mfma_f32_16x16x32_bf16 v[96:99], a[144:147], a[64:67], v[160:163]\n"
"ds_read_b128 a[160:163], v192 offset:12288\n"
"ds_read_b128 a[164:167], v192 offset:13312\n"
"v_mfma_f32_16x16x32_bf16 v[96:99], a[148:151], a[80:83], v[96:99]\n"
"v_exp_f32 v82, v82\n"
"v_mfma_f32_16x16x32_bf16 v[96:99], a[152:155], a[96:99], v[96:99]\n"
"ds_read_b128 a[168:171], v192 offset:14336\n"
"ds_read_b128 a[172:175], v192 offset:15360\n"
"v_mfma_f32_16x16x32_bf16 v[96:99], a[156:159], a[112:115], v[96:99]\n"
"v_exp_f32 v83, v83\n"
"v_mfma_f32_16x16x32_bf16 v[100:103], a[156:159], a[116:119], v[164:167]\n"
"buffer_load_dwordx4 v254, s[96:99], s70 offen offset:3072 sc1 lds\n"
"v_mfma_f32_16x16x32_bf16 v[100:103], a[152:155], a[100:103], v[100:103]\n"
"v_exp_f32 v68, v68\n"
"v_mfma_f32_16x16x32_bf16 v[100:103], a[148:151], a[84:87], v[100:103]\n"
"s_add_u32 s67, s67, 0x8000\n"
"s_xor_b32 s84, s84, 0x8000\n"
"v_mfma_f32_16x16x32_bf16 v[100:103], a[144:147], a[68:71], v[100:103]\n"
"v_exp_f32 v69, v69\n"
"v_mfma_f32_16x16x32_bf16 v[104:107], a[144:147], a[72:75], v[168:171]\n"
"s_mov_b32 m0, s84\n"
"ds_read_b128 a[236:239], v194 offset:15360\n"
"v_mfma_f32_16x16x32_bf16 v[104:107], a[148:151], a[88:91], v[104:107]\n"
"v_exp_f32 v70, v70\n"
"v_mfma_f32_16x16x32_bf16 v[104:107], a[152:155], a[104:107], v[104:107]\n"
"buffer_load_dwordx4 v254, s[28:31], s67 offen sc1 lds\n"
"v_mfma_f32_16x16x32_bf16 v[104:107], a[156:159], a[120:123], v[104:107]\n"
"v_exp_f32 v71, v71\n"
"v_mfma_f32_16x16x32_bf16 v[108:111], a[156:159], a[124:127], v[172:175]\n"
"buffer_load_dwordx4 v254, s[28:31], s67 offen offset:1024 sc1 lds\n"
"v_mfma_f32_16x16x32_bf16 v[108:111], a[152:155], a[108:111], v[108:111]\n"
"v_exp_f32 v84, v84\n"
"v_mfma_f32_16x16x32_bf16 v[108:111], a[148:151], a[92:95], v[108:111]\n"
"s_waitcnt lgkmcnt(1)\n"
"v_mfma_f32_16x16x32_bf16 v[108:111], a[144:147], a[76:79], v[108:111]\n"
"v_exp_f32 v85, v85\n"
"v_mfma_f32_16x16x32_bf16 v[112:115], a[160:163], a[64:67], v[160:163]\n"
"buffer_load_dwordx4 v254, s[28:31], s67 offen offset:2048 sc1 lds\n"
"v_mfma_f32_16x16x32_bf16 v[112:115], a[164:167], a[80:83], v[112:115]\n"
"v_exp_f32 v86, v86\n"
"v_mfma_f32_16x16x32_bf16 v[112:115], a[168:171], a[96:99], v[112:115]\n"
"v_mfma_f32_16x16x32_bf16 v[112:115], a[172:175], a[112:115], v[112:115]\n"
"v_exp_f32 v87, v87\n"
"v_mfma_f32_16x16x32_bf16 v[116:119], a[172:175], a[116:119], v[164:167]\n"
"buffer_load_dwordx4 v254, s[28:31], s67 offen offset:3072 sc1 lds\n"
"v_mfma_f32_16x16x32_bf16 v[116:119], a[168:171], a[100:103], v[116:119]\n"
"v_exp_f32 v72, v72\n"
"v_mfma_f32_16x16x32_bf16 v[116:119], a[164:167], a[84:87], v[116:119]\n"
"s_add_u32 s69, s67, 0x4000\n"
"s_add_u32 m0, s84, 0x4000\n"
"v_mfma_f32_16x16x32_bf16 v[116:119], a[160:163], a[68:71], v[116:119]\n"
"v_exp_f32 v73, v73\n"
"v_mfma_f32_16x16x32_bf16 v[120:123], a[160:163], a[72:75], v[168:171]\n"
"buffer_load_dwordx4 v254, s[28:31], s69 offen sc1 lds\n"
"v_mfma_f32_16x16x32_bf16 v[120:123], a[164:167], a[88:91], v[120:123]\n"
"v_exp_f32 v74, v74\n"
"v_mfma_f32_16x16x32_bf16 v[120:123], a[168:171], a[104:107], v[120:123]\n"
"v_mfma_f32_16x16x32_bf16 v[120:123], a[172:175], a[120:123], v[120:123]\n"
"v_exp_f32 v75, v75\n"
"v_mfma_f32_16x16x32_bf16 v[124:127], a[172:175], a[124:127], v[172:175]\n"
"buffer_load_dwordx4 v254, s[28:31], s69 offen offset:1024 sc1 lds\n"
"v_mfma_f32_16x16x32_bf16 v[124:127], a[168:171], a[108:111], v[124:127]\n"
"v_exp_f32 v88, v88\n"
"v_mfma_f32_16x16x32_bf16 v[124:127], a[164:167], a[92:95], v[124:127]\n"
"v_mfma_f32_16x16x32_bf16 v[124:127], a[160:163], a[76:79], v[124:127]\n"
"v_exp_f32 v89, v89\n"
"v_mfma_f32_16x16x32_bf16 v[0:3], a[176:179], v[128:131], 0\n"
"buffer_load_dwordx4 v254, s[28:31], s69 offen offset:2048 sc1 lds\n"
"v_mfma_f32_16x16x32_bf16 v[0:3], a[208:211], v[144:147], v[0:3]\n"
"v_exp_f32 v90, v90\n"
"v_mfma_f32_16x16x32_bf16 v[16:19], a[212:215], v[144:147], 0\n"
"buffer_load_dwordx4 v254, s[28:31], s69 offen offset:3072 sc1 lds\n"
"v_mfma_f32_16x16x32_bf16 v[16:19], a[180:183], v[128:131], v[16:19]\n"
"v_exp_f32 v91, v91\n"
"v_mfma_f32_16x16x32_bf16 v[32:35], a[184:187], v[128:131], 0\n"
"global_load_dword a255, v248, s[4:5]\n"
"v_mfma_f32_16x16x32_bf16 v[32:35], a[216:219], v[144:147], v[32:35]\n"
"v_exp_f32 v76, v76\n"
"v_mfma_f32_16x16x32_bf16 v[48:51], a[220:223], v[144:147], 0\n"
"s_waitcnt lgkmcnt(0)\n"
"v_mfma_f32_16x16x32_bf16 v[48:51], a[188:191], v[128:131], v[48:51]\n"
"v_exp_f32 v77, v77\n"
"v_mfma_f32_16x16x32_bf16 a[0:3], a[192:195], v[128:131], 0\n"
"v_mfma_f32_16x16x32_bf16 a[0:3], a[224:227], v[144:147], a[0:3]\n"
"v_exp_f32 v78, v78\n"
"v_mfma_f32_16x16x32_bf16 a[16:19], a[228:231], v[144:147], 0\n"
"v_mfma_f32_16x16x32_bf16 a[16:19], a[196:199], v[128:131], a[16:19]\n"
"v_exp_f32 v79, v79\n"
"v_mfma_f32_16x16x32_bf16 a[32:35], a[200:203], v[128:131], 0\n"
"v_exp_f32 v92, v92\n"
"v_mfma_f32_16x16x32_bf16 a[32:35], a[232:235], v[144:147], a[32:35]\n"
"v_exp_f32 v93, v93\n"
"v_mfma_f32_16x16x32_bf16 a[48:51], a[236:239], v[144:147], 0\n"
"v_exp_f32 v94, v94\n"
"v_mfma_f32_16x16x32_bf16 a[48:51], a[204:207], v[128:131], a[48:51]\n"
"v_exp_f32 v95, v95\n"
"v_mfma_f32_16x16x32_bf16 a[128:131], v[200:203], v[128:131], 0\n"
"v_exp_f32 v96, v96\n"
"v_mfma_f32_16x16x32_bf16 a[128:131], v[200:203], v[144:147], a[128:131]\n"
"v_exp_f32 v97, v97\n"
"v_mfma_f32_16x16x32_bf16 v[4:7], a[176:179], v[132:135], 0\n"
"v_cvt_pk_bf16_f32 v128, v64, v65\n"
"v_cvt_pk_bf16_f32 v129, v66, v67\n"
"v_mfma_f32_16x16x32_bf16 v[4:7], a[208:211], v[148:151], v[4:7]\n"
"v_exp_f32 v98, v98\n"
"v_mfma_f32_16x16x32_bf16 v[20:23], a[212:215], v[148:151], 0\n"
"v_cvt_pk_bf16_f32 v130, v80, v81\n"
"v_cvt_pk_bf16_f32 v131, v82, v83\n"
"v_mfma_f32_16x16x32_bf16 v[20:23], a[180:183], v[132:135], v[20:23]\n"
"v_exp_f32 v99, v99\n"
"v_mfma_f32_16x16x32_bf16 v[36:39], a[184:187], v[132:135], 0\n"
"v_exp_f32 v112, v112\n"
"v_mfma_f32_16x16x32_bf16 v[36:39], a[216:219], v[148:151], v[36:39]\n"
"v_exp_f32 v113, v113\n"
"v_mfma_f32_16x16x32_bf16 v[52:55], a[220:223], v[148:151], 0\n"
"v_exp_f32 v114, v114\n"
"v_mfma_f32_16x16x32_bf16 v[52:55], a[188:191], v[132:135], v[52:55]\n"
"v_exp_f32 v115, v115\n"
"v_mfma_f32_16x16x32_bf16 a[4:7], a[192:195], v[132:135], 0\n"
"v_exp_f32 v100, v100\n"
"v_mfma_f32_16x16x32_bf16 a[4:7], a[224:227], v[148:151], a[4:7]\n"
"v_exp_f32 v101, v101\n"
"v_mfma_f32_16x16x32_bf16 a[20:23], a[228:231], v[148:151], 0\n"
"v_cvt_pk_bf16_f32 v144, v96, v97\n"
"v_cvt_pk_bf16_f32 v145, v98, v99\n"
"v_mfma_f32_16x16x32_bf16 a[20:23], a[196:199], v[132:135], a[20:23]\n"
"v_exp_f32 v102, v102\n"
"v_mfma_f32_16x16x32_bf16 a[36:39], a[200:203], v[132:135], 0\n"
"v_cvt_pk_bf16_f32 v146, v112, v113\n"
"v_cvt_pk_bf16_f32 v147, v114, v115\n"
"v_mfma_f32_16x16x32_bf16 a[36:39], a[232:235], v[148:151], a[36:39]\n"
"v_exp_f32 v103, v103\n"
"v_mfma_f32_16x16x32_bf16 a[52:55], a[236:239], v[148:151], 0\n"
"v_exp_f32 v116, v116\n"
"v_mfma_f32_16x16x32_bf16 a[52:55], a[204:207], v[132:135], a[52:55]\n"
"v_exp_f32 v117, v117\n"
"v_mfma_f32_16x16x32_bf16 a[132:135], v[200:203], v[132:135], 0\n"
"v_mfma_f32_16x16x32_bf16 a[132:135], v[200:203], v[148:151], a[132:135]\n"
"v_exp_f32 v118, v118\n"
"v_mfma_f32_16x16x32_bf16 v[8:11], a[176:179], v[136:139], 0\n"
"v_cvt_pk_bf16_f32 v132, v68, v69\n"
"v_cvt_pk_bf16_f32 v133, v70, v71\n"
"v_mfma_f32_16x16x32_bf16 v[8:11], a[208:211], v[152:155], v[8:11]\n"
"v_exp_f32 v119, v119\n"
"v_mfma_f32_16x16x32_bf16 v[24:27], a[212:215], v[152:155], 0\n"
"v_cvt_pk_bf16_f32 v134, v84, v85\n"
"v_cvt_pk_bf16_f32 v135, v86, v87\n"
"v_mfma_f32_16x16x32_bf16 v[24:27], a[180:183], v[136:139], v[24:27]\n"
"v_exp_f32 v104, v104\n"
"v_mfma_f32_16x16x32_bf16 v[40:43], a[184:187], v[136:139], 0\n"
"v_cvt_pk_bf16_f32 v148, v100, v101\n"
"v_cvt_pk_bf16_f32 v149, v102, v103\n"
"v_mfma_f32_16x16x32_bf16 v[40:43], a[216:219], v[152:155], v[40:43]\n"
"v_exp_f32 v105, v105\n"
"v_mfma_f32_16x16x32_bf16 v[56:59], a[220:223], v[152:155], 0\n"
"v_mfma_f32_16x16x32_bf16 v[56:59], a[188:191], v[136:139], v[56:59]\n"
"v_mfma_f32_16x16x32_bf16 a[8:11], a[192:195], v[136:139], 0\n"
"s_waitcnt vmcnt(1) lgkmcnt(0)\n"
"s_barrier\n"
"v_mfma_f32_16x16x32_bf16 a[8:11], a[224:227], v[152:155], a[8:11]\n"
"v_exp_f32 v106, v106\n"
"v_mfma_f32_16x16x32_bf16 a[24:27], a[228:231], v[152:155], 0\n"
"v_xor_b32 v192, 0x8000, v192\n"
"v_xor_b32 v193, 0x8000, v193\n"
"v_mfma_f32_16x16x32_bf16 a[24:27], a[196:199], v[136:139], a[24:27]\n"
"v_exp_f32 v107, v107\n"
"v_mfma_f32_16x16x32_bf16 a[40:43], a[200:203], v[136:139], 0\n"
"ds_read_b128 a[144:147], v192\n"
"ds_read_b128 a[148:151], v192 offset:1024\n"
"v_mfma_f32_16x16x32_bf16 a[40:43], a[232:235], v[152:155], a[40:43]\n"
"v_exp_f32 v120, v120\n"
"v_mfma_f32_16x16x32_bf16 a[56:59], a[236:239], v[152:155], 0\n"
"ds_read_b128 a[152:155], v192 offset:2048\n"
"ds_read_b128 a[156:159], v192 offset:3072\n"
"v_mfma_f32_16x16x32_bf16 a[56:59], a[204:207], v[136:139], a[56:59]\n"
"v_exp_f32 v121, v121\n"
"v_mfma_f32_16x16x32_bf16 a[136:139], v[200:203], v[136:139], 0\n"
"ds_read_b128 a[160:163], v192 offset:4096\n"
"ds_read_b128 a[164:167], v192 offset:5120\n"
"v_mfma_f32_16x16x32_bf16 a[136:139], v[200:203], v[152:155], a[136:139]\n"
"v_exp_f32 v122, v122\n"
"v_mfma_f32_16x16x32_bf16 v[12:15], a[176:179], v[140:143], 0\n"
"ds_read_b128 a[168:171], v192 offset:6144\n"
"ds_read_b128 a[172:175], v192 offset:7168\n"
"v_mfma_f32_16x16x32_bf16 v[12:15], a[208:211], v[156:159], v[12:15]\n"
"v_exp_f32 v123, v123\n"
"v_mfma_f32_16x16x32_bf16 v[28:31], a[212:215], v[156:159], 0\n"
"v_cvt_pk_bf16_f32 v136, v72, v73\n"
"v_cvt_pk_bf16_f32 v137, v74, v75\n"
"v_mfma_f32_16x16x32_bf16 v[28:31], a[180:183], v[140:143], v[28:31]\n"
"v_exp_f32 v108, v108\n"
"v_mfma_f32_16x16x32_bf16 v[44:47], a[184:187], v[140:143], 0\n"
"ds_read_b128 a[176:179], v193\n"
"ds_read_b128 a[208:211], v193 offset:8192\n"
"v_mfma_f32_16x16x32_bf16 v[44:47], a[216:219], v[156:159], v[44:47]\n"
"v_exp_f32 v109, v109\n"
"v_mfma_f32_16x16x32_bf16 v[60:63], a[220:223], v[156:159], 0\n"
"ds_read_b128 a[180:183], v193 offset:1024\n"
"ds_read_b128 a[212:215], v193 offset:9216\n"
"v_mfma_f32_16x16x32_bf16 v[60:63], a[188:191], v[140:143], v[60:63]\n"
"v_exp_f32 v110, v110\n"
"v_mfma_f32_16x16x32_bf16 a[12:15], a[192:195], v[140:143], 0\n"
"v_cvt_pk_bf16_f32 v138, v88, v89\n"
"v_cvt_pk_bf16_f32 v139, v90, v91\n"
"v_mfma_f32_16x16x32_bf16 a[12:15], a[224:227], v[156:159], a[12:15]\n"
"v_exp_f32 v111, v111\n"
"v_mfma_f32_16x16x32_bf16 a[28:31], a[228:231], v[156:159], 0\n"
"v_cvt_pk_bf16_f32 v150, v116, v117\n"
"v_cvt_pk_bf16_f32 v151, v118, v119\n"
"v_mfma_f32_16x16x32_bf16 a[28:31], a[196:199], v[140:143], a[28:31]\n"
"v_exp_f32 v124, v124\n"
"v_mfma_f32_16x16x32_bf16 a[44:47], a[200:203], v[140:143], 0\n"
"v_cvt_pk_bf16_f32 v152, v104, v105\n"
"v_cvt_pk_bf16_f32 v153, v106, v107\n"
"v_mfma_f32_16x16x32_bf16 a[44:47], a[232:235], v[156:159], a[44:47]\n"
"v_exp_f32 v125, v125\n"
"v_mfma_f32_16x16x32_bf16 a[60:63], a[236:239], v[156:159], 0\n"
"v_xor_b32 v194, 0x8000, v194\n"
"v_cvt_pk_bf16_f32 v154, v120, v121\n"
"v_mfma_f32_16x16x32_bf16 a[60:63], a[204:207], v[140:143], a[60:63]\n"
"v_exp_f32 v126, v126\n"
"v_mfma_f32_16x16x32_bf16 a[140:143], v[200:203], v[140:143], 0\n"
"s_add_u32 s62, s62, 1\n"
"v_cvt_pk_bf16_f32 v155, v122, v123\n"
"v_mfma_f32_16x16x32_bf16 a[140:143], v[200:203], v[156:159], a[140:143]\n"
"v_exp_f32 v127, v127\n"
"s_branch .Lph2_45_%=\n"
".Lfullvz_24_%=:\n"
"s_waitcnt lgkmcnt(8)\n"
"v_mov_b32 v208, 0x21000\n"
"v_mfma_f32_16x16x32_bf16 v[64:67], a[144:147], a[64:67], v[160:163]\n"
"v_cvt_pk_bf16_f32 v140, v76, v77\n"
"v_cvt_pk_bf16_f32 v141, v78, v79\n"
"v_mfma_f32_16x16x32_bf16 v[64:67], a[148:151], a[80:83], v[64:67]\n"
"s_xor_b32 s85, s85, 0x8000\n"
"v_mfma_f32_16x16x32_bf16 v[64:67], a[152:155], a[96:99], v[64:67]\n"
"v_cvt_pk_bf16_f32 v142, v92, v93\n"
"v_cvt_pk_bf16_f32 v143, v94, v95\n"
"v_mfma_f32_16x16x32_bf16 v[64:67], a[156:159], a[112:115], v[64:67]\n"
"s_mov_b32 m0, s85\n"
"v_mfma_f32_16x16x32_bf16 v[68:71], a[156:159], a[116:119], v[164:167]\n"
"v_cvt_pk_bf16_f32 v156, v108, v109\n"
"v_cvt_pk_bf16_f32 v157, v110, v111\n"
"v_mfma_f32_16x16x32_bf16 v[68:71], a[152:155], a[100:103], v[68:71]\n"
"buffer_load_dwordx4 v254, s[96:99], s67 offen sc1 lds\n"
"v_mfma_f32_16x16x32_bf16 v[68:71], a[148:151], a[84:87], v[68:71]\n"
"v_cvt_pk_bf16_f32 v158, v124, v125\n"
"v_cvt_pk_bf16_f32 v159, v126, v127\n"
"v_mfma_f32_16x16x32_bf16 v[68:71], a[144:147], a[68:71], v[68:71]\n"
"v_mfma_f32_16x16x32_bf16 v[72:75], a[144:147], a[72:75], v[168:171]\n"
"buffer_load_dwordx4 v254, s[96:99], s67 offen offset:1024 sc1 lds\n"
"v_mfma_f32_16x16x32_bf16 v[72:75], a[148:151], a[88:91], v[72:75]\n"
"ds_read_b128 a[184:187], v193 offset:2048\n"
"ds_read_b128 a[216:219], v193 offset:10240\n"
"v_mfma_f32_16x16x32_bf16 v[72:75], a[152:155], a[104:107], v[72:75]\n"
"v_mfma_f32_16x16x32_bf16 v[72:75], a[156:159], a[120:123], v[72:75]\n"
"ds_read_b128 a[188:191], v193 offset:3072\n"
"v_mfma_f32_16x16x32_bf16 v[76:79], a[156:159], a[124:127], v[172:175]\n"
"ds_read_b128 a[220:223], v193 offset:11264\n"
"v_mfma_f32_16x16x32_bf16 v[76:79], a[152:155], a[108:111], v[76:79]\n"
"ds_read_b128 a[192:195], v194 offset:4096\n"
"v_mfma_f32_16x16x32_bf16 v[76:79], a[148:151], a[92:95], v[76:79]\n"
"ds_read_b128 a[224:227], v194 offset:12288\n"
"s_waitcnt lgkmcnt(10)\n"
"v_mfma_f32_16x16x32_bf16 v[76:79], a[144:147], a[76:79], v[76:79]\n"
"buffer_load_dwordx4 v254, s[96:99], s67 offen offset:2048 sc1 lds\n"
"v_mfma_f32_16x16x32_bf16 v[80:83], a[160:163], a[64:67], v[160:163]\n"
"ds_read_b128 a[144:147], v192 offset:8192\n"
"ds_read_b128 a[148:151], v192 offset:9216\n"
"v_mfma_f32_16x16x32_bf16 v[80:83], a[164:167], a[80:83], v[80:83]\n"
"v_mfma_f32_16x16x32_bf16 v[80:83], a[168:171], a[96:99], v[80:83]\n"
"ds_read_b128 a[152:155], v192 offset:10240\n"
"ds_read_b128 a[156:159], v192 offset:11264\n"
"v_mfma_f32_16x16x32_bf16 v[80:83], a[172:175], a[112:115], v[80:83]\n"
"v_mfma_f32_16x16x32_bf16 v[84:87], a[172:175], a[116:119], v[164:167]\n"
"buffer_load_dwordx4 v254, s[96:99], s67 offen offset:3072 sc1 lds\n"
"v_mfma_f32_16x16x32_bf16 v[84:87], a[168:171], a[100:103], v[84:87]\n"
"v_exp_f32 v64, v64\n"
"v_mfma_f32_16x16x32_bf16 v[84:87], a[164:167], a[84:87], v[84:87]\n"
"s_add_u32 s70, s67, 0x4000\n"
"s_add_u32 m0, s85, 0x4000\n"
"v_mfma_f32_16x16x32_bf16 v[84:87], a[160:163], a[68:71], v[84:87]\n"
"v_exp_f32 v65, v65\n"
"v_mfma_f32_16x16x32_bf16 v[88:91], a[160:163], a[72:75], v[168:171]\n"
"ds_read_b128 a[196:199], v194 offset:5120\n"
"ds_read_b128 a[228:231], v194 offset:13312\n"
"v_mfma_f32_16x16x32_bf16 v[88:91], a[164:167], a[88:91], v[88:91]\n"
"v_exp_f32 v66, v66\n"
"v_mfma_f32_16x16x32_bf16 v[88:91], a[168:171], a[104:107], v[88:91]\n"
"buffer_load_dwordx4 v254, s[96:99], s70 offen sc1 lds\n"
"v_mfma_f32_16x16x32_bf16 v[88:91], a[172:175], a[120:123], v[88:91]\n"
"v_exp_f32 v67, v67\n"
"v_mfma_f32_16x16x32_bf16 v[92:95], a[172:175], a[124:127], v[172:175]\n"
"ds_read_b128 a[200:203], v194 offset:6144\n"
"ds_read_b128 a[232:235], v194 offset:14336\n"
"v_mfma_f32_16x16x32_bf16 v[92:95], a[168:171], a[108:111], v[92:95]\n"
"v_exp_f32 v80, v80\n"
"v_mfma_f32_16x16x32_bf16 v[92:95], a[164:167], a[92:95], v[92:95]\n"
"ds_read_b128 a[204:207], v194 offset:7168\n"
"s_waitcnt lgkmcnt(5)\n"
"v_mfma_f32_16x16x32_bf16 v[92:95], a[160:163], a[76:79], v[92:95]\n"
"v_exp_f32 v81, v81\n"
"v_mfma_f32_16x16x32_bf16 v[96:99], a[144:147], a[64:67], v[160:163]\n"
"ds_read_b128 a[160:163], v192 offset:12288\n"
"ds_read_b128 a[164:167], v192 offset:13312\n"
"v_mfma_f32_16x16x32_bf16 v[96:99], a[148:151], a[80:83], v[96:99]\n"
"v_exp_f32 v82, v82\n"
"v_mfma_f32_16x16x32_bf16 v[96:99], a[152:155], a[96:99], v[96:99]\n"
"ds_read_b128 a[168:171], v192 offset:14336\n"
"ds_read_b128 a[172:175], v192 offset:15360\n"
"v_mfma_f32_16x16x32_bf16 v[96:99], a[156:159], a[112:115], v[96:99]\n"
"v_exp_f32 v83, v83\n"
"v_mfma_f32_16x16x32_bf16 v[100:103], a[156:159], a[116:119], v[164:167]\n"
"buffer_load_dwordx4 v254, s[96:99], s70 offen offset:1024 sc1 lds\n"
"v_mfma_f32_16x16x32_bf16 v[100:103], a[152:155], a[100:103], v[100:103]\n"
"v_exp_f32 v68, v68\n"
"v_mfma_f32_16x16x32_bf16 v[100:103], a[148:151], a[84:87], v[100:103]\n"
"buffer_load_dwordx4 v254, s[96:99], s70 offen offset:2048 sc1 lds\n"
"v_mfma_f32_16x16x32_bf16 v[100:103], a[144:147], a[68:71], v[100:103]\n"
"v_exp_f32 v69, v69\n"
"v_mfma_f32_16x16x32_bf16 v[104:107], a[144:147], a[72:75], v[168:171]\n"
"ds_read_b128 a[236:239], v194 offset:15360\n"
"v_mfma_f32_16x16x32_bf16 v[104:107], a[148:151], a[88:91], v[104:107]\n"
"v_exp_f32 v70, v70\n"
"v_mfma_f32_16x16x32_bf16 v[104:107], a[152:155], a[104:107], v[104:107]\n"
"v_mfma_f32_16x16x32_bf16 v[104:107], a[156:159], a[120:123], v[104:107]\n"
"v_exp_f32 v71, v71\n"
"v_mfma_f32_16x16x32_bf16 v[108:111], a[156:159], a[124:127], v[172:175]\n"
"buffer_load_dwordx4 v254, s[96:99], s70 offen offset:3072 sc1 lds\n"
"v_mfma_f32_16x16x32_bf16 v[108:111], a[152:155], a[108:111], v[108:111]\n"
"v_exp_f32 v84, v84\n"
"v_mfma_f32_16x16x32_bf16 v[108:111], a[148:151], a[92:95], v[108:111]\n"
"global_load_dword a255, v248, s[4:5]\n"
"s_waitcnt lgkmcnt(1)\n"
"v_mfma_f32_16x16x32_bf16 v[108:111], a[144:147], a[76:79], v[108:111]\n"
"v_exp_f32 v85, v85\n"
"v_mfma_f32_16x16x32_bf16 v[112:115], a[160:163], a[64:67], v[160:163]\n"
"v_mfma_f32_16x16x32_bf16 v[112:115], a[164:167], a[80:83], v[112:115]\n"
"v_exp_f32 v86, v86\n"
"v_mfma_f32_16x16x32_bf16 v[112:115], a[168:171], a[96:99], v[112:115]\n"
"v_mfma_f32_16x16x32_bf16 v[112:115], a[172:175], a[112:115], v[112:115]\n"
"v_exp_f32 v87, v87\n"
"v_mfma_f32_16x16x32_bf16 v[116:119], a[172:175], a[116:119], v[164:167]\n"
"v_mfma_f32_16x16x32_bf16 v[116:119], a[168:171], a[100:103], v[116:119]\n"
"v_exp_f32 v72, v72\n"
"v_mfma_f32_16x16x32_bf16 v[116:119], a[164:167], a[84:87], v[116:119]\n"
"v_mfma_f32_16x16x32_bf16 v[116:119], a[160:163], a[68:71], v[116:119]\n"
"v_exp_f32 v73, v73\n"
"v_mfma_f32_16x16x32_bf16 v[120:123], a[160:163], a[72:75], v[168:171]\n"
"v_mfma_f32_16x16x32_bf16 v[120:123], a[164:167], a[88:91], v[120:123]\n"
"v_exp_f32 v74, v74\n"
"v_mfma_f32_16x16x32_bf16 v[120:123], a[168:171], a[104:107], v[120:123]\n"
"v_mfma_f32_16x16x32_bf16 v[120:123], a[172:175], a[120:123], v[120:123]\n"
"v_exp_f32 v75, v75\n"
"v_mfma_f32_16x16x32_bf16 v[124:127], a[172:175], a[124:127], v[172:175]\n"
"v_mfma_f32_16x16x32_bf16 v[124:127], a[168:171], a[108:111], v[124:127]\n"
"v_exp_f32 v88, v88\n"
"v_mfma_f32_16x16x32_bf16 v[124:127], a[164:167], a[92:95], v[124:127]\n"
"v_mfma_f32_16x16x32_bf16 v[124:127], a[160:163], a[76:79], v[124:127]\n"
"v_exp_f32 v89, v89\n"
"v_mfma_f32_16x16x32_bf16 v[0:3], a[176:179], v[128:131], 0\n"
"v_mfma_f32_16x16x32_bf16 v[0:3], a[208:211], v[144:147], v[0:3]\n"
"v_exp_f32 v90, v90\n"
"v_mfma_f32_16x16x32_bf16 v[16:19], a[212:215], v[144:147], 0\n"
"v_mfma_f32_16x16x32_bf16 v[16:19], a[180:183], v[128:131], v[16:19]\n"
"v_exp_f32 v91, v91\n"
"v_mfma_f32_16x16x32_bf16 v[32:35], a[184:187], v[128:131], 0\n"
"v_mfma_f32_16x16x32_bf16 v[32:35], a[216:219], v[144:147], v[32:35]\n"
"v_exp_f32 v76, v76\n"
"v_mfma_f32_16x16x32_bf16 v[48:51], a[220:223], v[144:147], 0\n"
"s_waitcnt lgkmcnt(0)\n"
"v_mfma_f32_16x16x32_bf16 v[48:51], a[188:191], v[128:131], v[48:51]\n"
"v_exp_f32 v77, v77\n"
"v_mfma_f32_16x16x32_bf16 a[0:3], a[192:195], v[128:131], 0\n"
"v_mfma_f32_16x16x32_bf16 a[0:3], a[224:227], v[144:147], a[0:3]\n"
"v_exp_f32 v78, v78\n"
"v_mfma_f32_16x16x32_bf16 a[16:19], a[228:231], v[144:147], 0\n"
"v_mfma_f32_16x16x32_bf16 a[16:19], a[196:199], v[128:131], a[16:19]\n"
"v_exp_f32 v79, v79\n"
"v_mfma_f32_16x16x32_bf16 a[32:35], a[200:203], v[128:131], 0\n"
"v_exp_f32 v92, v92\n"
"v_mfma_f32_16x16x32_bf16 a[32:35], a[232:235], v[144:147], a[32:35]\n"
"v_exp_f32 v93, v93\n"
"v_mfma_f32_16x16x32_bf16 a[48:51], a[236:239], v[144:147], 0\n"
"v_exp_f32 v94, v94\n"
"v_mfma_f32_16x16x32_bf16 a[48:51], a[204:207], v[128:131], a[48:51]\n"
"v_exp_f32 v95, v95\n"
"v_mfma_f32_16x16x32_bf16 a[128:131], v[200:203], v[128:131], 0\n"
"v_exp_f32 v96, v96\n"
"v_mfma_f32_16x16x32_bf16 a[128:131], v[200:203], v[144:147], a[128:131]\n"
"v_exp_f32 v97, v97\n"
"v_mfma_f32_16x16x32_bf16 v[4:7], a[176:179], v[132:135], 0\n"
"v_cvt_pk_bf16_f32 v128, v64, v65\n"
"v_cvt_pk_bf16_f32 v129, v66, v67\n"
"v_mfma_f32_16x16x32_bf16 v[4:7], a[208:211], v[148:151], v[4:7]\n"
"v_exp_f32 v98, v98\n"
"v_mfma_f32_16x16x32_bf16 v[20:23], a[212:215], v[148:151], 0\n"
"v_cvt_pk_bf16_f32 v130, v80, v81\n"
"v_cvt_pk_bf16_f32 v131, v82, v83\n"
"v_mfma_f32_16x16x32_bf16 v[20:23], a[180:183], v[132:135], v[20:23]\n"
"v_exp_f32 v99, v99\n"
"v_mfma_f32_16x16x32_bf16 v[36:39], a[184:187], v[132:135], 0\n"
"v_exp_f32 v112, v112\n"
"v_mfma_f32_16x16x32_bf16 v[36:39], a[216:219], v[148:151], v[36:39]\n"
"v_exp_f32 v113, v113\n"
"v_mfma_f32_16x16x32_bf16 v[52:55], a[220:223], v[148:151], 0\n"
"v_exp_f32 v114, v114\n"
"v_mfma_f32_16x16x32_bf16 v[52:55], a[188:191], v[132:135], v[52:55]\n"
"v_exp_f32 v115, v115\n"
"v_mfma_f32_16x16x32_bf16 a[4:7], a[192:195], v[132:135], 0\n"
"v_exp_f32 v100, v100\n"
"v_mfma_f32_16x16x32_bf16 a[4:7], a[224:227], v[148:151], a[4:7]\n"
"v_exp_f32 v101, v101\n"
"v_mfma_f32_16x16x32_bf16 a[20:23], a[228:231], v[148:151], 0\n"
"v_cvt_pk_bf16_f32 v144, v96, v97\n"
"v_cvt_pk_bf16_f32 v145, v98, v99\n"
"v_mfma_f32_16x16x32_bf16 a[20:23], a[196:199], v[132:135], a[20:23]\n"
"v_exp_f32 v102, v102\n"
"v_mfma_f32_16x16x32_bf16 a[36:39], a[200:203], v[132:135], 0\n"
"v_cvt_pk_bf16_f32 v146, v112, v113\n"
"v_cvt_pk_bf16_f32 v147, v114, v115\n"
"v_mfma_f32_16x16x32_bf16 a[36:39], a[232:235], v[148:151], a[36:39]\n"
"v_exp_f32 v103, v103\n"
"ds_read_b128 v[204:207], v208\n"
"s_waitcnt lgkmcnt(0)\n"
"v_or_b32 v204, v204, v206\n"
"s_nop 0\n"
"v_readfirstlane_b32 s12, v204\n"
"s_nop 1\n"
"s_cmp_eq_u32 s12, 0\n"
"s_cselect_b32 s12, s54, 0x40000000\n"
"s_mov_b64 exec, s[34:35]\n"
"buffer_store_dwordx2 v[250:251], v249, s[92:95], s12 offen sc1\n"
"s_mov_b64 exec, -1\n"
"v_mfma_f32_16x16x32_bf16 a[52:55], a[236:239], v[148:151], 0\n"
"v_exp_f32 v116, v116\n"
"v_mfma_f32_16x16x32_bf16 a[52:55], a[204:207], v[132:135], a[52:55]\n"
"v_exp_f32 v117, v117\n"
"v_mfma_f32_16x16x32_bf16 a[132:135], v[200:203], v[132:135], 0\n"
"v_mfma_f32_16x16x32_bf16 a[132:135], v[200:203], v[148:151], a[132:135]\n"
"v_exp_f32 v118, v118\n"
"v_mfma_f32_16x16x32_bf16 v[8:11], a[176:179], v[136:139], 0\n"
"v_cvt_pk_bf16_f32 v132, v68, v69\n"
"v_cvt_pk_bf16_f32 v133, v70, v71\n"
"v_mfma_f32_16x16x32_bf16 v[8:11], a[208:211], v[152:155], v[8:11]\n"
"v_exp_f32 v119, v119\n"
"v_mfma_f32_16x16x32_bf16 v[24:27], a[212:215], v[152:155], 0\n"
"v_cvt_pk_bf16_f32 v134, v84, v85\n"
"v_cvt_pk_bf16_f32 v135, v86, v87\n"
"v_mfma_f32_16x16x32_bf16 v[24:27], a[180:183], v[136:139], v[24:27]\n"
"v_exp_f32 v104, v104\n"
"v_mfma_f32_16x16x32_bf16 v[40:43], a[184:187], v[136:139], 0\n"
"v_cvt_pk_bf16_f32 v148, v100, v101\n"
"v_cvt_pk_bf16_f32 v149, v102, v103\n"
"v_mfma_f32_16x16x32_bf16 v[40:43], a[216:219], v[152:155], v[40:43]\n"
"v_exp_f32 v105, v105\n"
"v_mfma_f32_16x16x32_bf16 v[56:59], a[220:223], v[152:155], 0\n"
"v_mfma_f32_16x16x32_bf16 v[56:59], a[188:191], v[136:139], v[56:59]\n"
"v_mfma_f32_16x16x32_bf16 a[8:11], a[192:195], v[136:139], 0\n"
"s_waitcnt vmcnt(2) lgkmcnt(0)\n"
"s_barrier\n"
"v_mfma_f32_16x16x32_bf16 a[8:11], a[224:227], v[152:155], a[8:11]\n"
"v_exp_f32 v106, v106\n"
"v_mfma_f32_16x16x32_bf16 a[24:27], a[228:231], v[152:155], 0\n"
"v_xor_b32 v192, 0x8000, v192\n"
"v_xor_b32 v193, 0x8000, v193\n"
"v_mfma_f32_16x16x32_bf16 a[24:27], a[196:199], v[136:139], a[24:27]\n"
"v_exp_f32 v107, v107\n"
"v_mfma_f32_16x16x32_bf16 a[40:43], a[200:203], v[136:139], 0\n"
"ds_read_b128 a[144:147], v192\n"
"ds_read_b128 a[148:151], v192 offset:1024\n"
"v_mfma_f32_16x16x32_bf16 a[40:43], a[232:235], v[152:155], a[40:43]\n"
"v_exp_f32 v120, v120\n"
"v_mfma_f32_16x16x32_bf16 a[56:59], a[236:239], v[152:155], 0\n"
"ds_read_b128 a[152:155], v192 offset:2048\n"
"ds_read_b128 a[156:159], v192 offset:3072\n"
"v_mfma_f32_16x16x32_bf16 a[56:59], a[204:207], v[136:139], a[56:59]\n"
"v_exp_f32 v121, v121\n"
"v_mfma_f32_16x16x32_bf16 a[136:139], v[200:203], v[136:139], 0\n"
"ds_read_b128 a[160:163], v192 offset:4096\n"
"ds_read_b128 a[164:167], v192 offset:5120\n"
"v_mfma_f32_16x16x32_bf16 a[136:139], v[200:203], v[152:155], a[136:139]\n"
"v_exp_f32 v122, v122\n"
"v_mfma_f32_16x16x32_bf16 v[12:15], a[176:179], v[140:143], 0\n"
"ds_read_b128 a[168:171], v192 offset:6144\n"
"ds_read_b128 a[172:175], v192 offset:7168\n"
"v_mfma_f32_16x16x32_bf16 v[12:15], a[208:211], v[156:159], v[12:15]\n"
"v_exp_f32 v123, v123\n"
"v_mfma_f32_16x16x32_bf16 v[28:31], a[212:215], v[156:159], 0\n"
"v_cvt_pk_bf16_f32 v136, v72, v73\n"
"v_cvt_pk_bf16_f32 v137, v74, v75\n"
"v_mfma_f32_16x16x32_bf16 v[28:31], a[180:183], v[140:143], v[28:31]\n"
"v_exp_f32 v108, v108\n"
"v_mfma_f32_16x16x32_bf16 v[44:47], a[184:187], v[140:143], 0\n"
"ds_read_b128 a[176:179], v193\n"
"ds_read_b128 a[208:211], v193 offset:8192\n"
"v_mfma_f32_16x16x32_bf16 v[44:47], a[216:219], v[156:159], v[44:47]\n"
"v_exp_f32 v109, v109\n"
"v_mfma_f32_16x16x32_bf16 v[60:63], a[220:223], v[156:159], 0\n"
"ds_read_b128 a[180:183], v193 offset:1024\n"
"ds_read_b128 a[212:215], v193 offset:9216\n"
"v_mfma_f32_16x16x32_bf16 v[60:63], a[188:191], v[140:143], v[60:63]\n"
"v_exp_f32 v110, v110\n"
"v_mfma_f32_16x16x32_bf16 a[12:15], a[192:195], v[140:143], 0\n"
"v_cvt_pk_bf16_f32 v138, v88, v89\n"
"v_cvt_pk_bf16_f32 v139, v90, v91\n"
"v_mfma_f32_16x16x32_bf16 a[12:15], a[224:227], v[156:159], a[12:15]\n"
"v_exp_f32 v111, v111\n"
"v_mfma_f32_16x16x32_bf16 a[28:31], a[228:231], v[156:159], 0\n"
"v_cvt_pk_bf16_f32 v150, v116, v117\n"
"v_cvt_pk_bf16_f32 v151, v118, v119\n"
"v_mfma_f32_16x16x32_bf16 a[28:31], a[196:199], v[140:143], a[28:31]\n"
"v_exp_f32 v124, v124\n"
"v_mfma_f32_16x16x32_bf16 a[44:47], a[200:203], v[140:143], 0\n"
"v_cvt_pk_bf16_f32 v152, v104, v105\n"
"v_cvt_pk_bf16_f32 v153, v106, v107\n"
"v_mfma_f32_16x16x32_bf16 a[44:47], a[232:235], v[156:159], a[44:47]\n"
"v_exp_f32 v125, v125\n"
"v_mfma_f32_16x16x32_bf16 a[60:63], a[236:239], v[156:159], 0\n"
"v_xor_b32 v194, 0x8000, v194\n"
"v_cvt_pk_bf16_f32 v154, v120, v121\n"
"v_mfma_f32_16x16x32_bf16 a[60:63], a[204:207], v[140:143], a[60:63]\n"
"v_exp_f32 v126, v126\n"
"v_mfma_f32_16x16x32_bf16 a[140:143], v[200:203], v[140:143], 0\n"
"s_add_u32 s62, s62, 1\n"
"v_cvt_pk_bf16_f32 v155, v122, v123\n"
"v_mfma_f32_16x16x32_bf16 a[140:143], v[200:203], v[156:159], a[140:143]\n"
"v_exp_f32 v127, v127\n"
"s_branch .Ldrain_36_%=\n"
".Llb0_16_%=:\n"
"s_xor_b32 s85, s85, 0x8000\n"
"s_mov_b32 m0, s85\n"
"s_nop 0\n"
"buffer_load_dwordx4 v254, s[96:99], s67 offen sc1 lds\n"
"v_mov_b32 v160, v128\n"
"v_mov_b32 v161, v128\n"
"v_mov_b32 v162, v128\n"
"v_mov_b32 v163, v128\n"
"v_mov_b32 v164, v129\n"
"v_mov_b32 v165, v129\n"
"v_mov_b32 v166, v129\n"
"v_mov_b32 v167, v129\n"
"v_mov_b32 v168, v130\n"
"v_mov_b32 v169, v130\n"
"v_mov_b32 v170, v130\n"
"v_mov_b32 v171, v130\n"
"v_mov_b32 v172, v131\n"
"v_mov_b32 v173, v131\n"
"s_nop 0\n"
"buffer_load_dwordx4 v254, s[96:99], s67 offen offset:1024 sc1 lds\n"
"v_mov_b32 v174, v131\n"
"v_mov_b32 v175, v131\n"
"v_cndmask_b32 v176, v128, v196, s[42:43]\n"
"v_cndmask_b32 v177, v128, v196, s[44:45]\n"
"v_cndmask_b32 v178, v128, v196, s[46:47]\n"
"v_cndmask_b32 v179, v128, v196, s[48:49]\n"
"v_cndmask_b32 v180, v129, v196, s[42:43]\n"
"v_cndmask_b32 v181, v129, v196, s[44:45]\n"
"v_cndmask_b32 v182, v129, v196, s[46:47]\n"
"v_cndmask_b32 v183, v129, v196, s[48:49]\n"
"v_cndmask_b32 v184, v130, v196, s[42:43]\n"
"v_cndmask_b32 v185, v130, v196, s[44:45]\n"
"v_cndmask_b32 v186, v130, v196, s[46:47]\n"
"v_cndmask_b32 v187, v130, v196, s[48:49]\n"
"s_nop 0\n"
"buffer_load_dwordx4 v254, s[96:99], s67 offen offset:2048 sc1 lds\n"
"v_cndmask_b32 v188, v131, v196, s[42:43]\n"
"v_cndmask_b32 v189, v131, v196, s[44:45]\n"
"v_cndmask_b32 v190, v131, v196, s[46:47]\n"
"v_cndmask_b32 v191, v131, v196, s[48:49]\n"
"v_accvgpr_read_b32 v204, a0\n"
"v_accvgpr_read_b32 v205, a1\n"
"v_accvgpr_read_b32 v206, a2\n"
"v_accvgpr_read_b32 v207, a3\n"
"v_accvgpr_read_b32 v208, a16\n"
"v_accvgpr_read_b32 v209, a17\n"
"v_accvgpr_read_b32 v210, a18\n"
"v_accvgpr_read_b32 v211, a19\n"
"v_pk_mul_f32 v[204:205], v[204:205], v[236:237] op_sel_hi:[1,0]\n"
"v_pk_mul_f32 v[206:207], v[206:207], v[236:237] op_sel_hi:[1,0]\n"
"s_nop 0\n"
"buffer_load_dwordx4 v254, s[96:99], s67 offen offset:3072 sc1 lds\n"
"v_pk_mul_f32 v[208:209], v[208:209], v[236:237] op_sel_hi:[1,0]\n"
"v_pk_mul_f32 v[210:211], v[210:211], v[236:237] op_sel_hi:[1,0]\n"
"v_cvt_pk_bf16_f32 v220, v204, v205\n"
"v_cvt_pk_bf16_f32 v221, v206, v207\n"
"v_cvt_pk_bf16_f32 v222, v208, v209\n"
"v_cvt_pk_bf16_f32 v223, v210, v211\n"
"buffer_store_dwordx4 v[220:223], v254, s[36:39], s75 offen sc1\n"
"v_accvgpr_read_b32 v212, a4\n"
"v_accvgpr_read_b32 v213, a5\n"
"v_accvgpr_read_b32 v214, a6\n"
"v_accvgpr_read_b32 v215, a7\n"
"v_accvgpr_read_b32 v216, a20\n"
"v_accvgpr_read_b32 v217, a21\n"
"v_accvgpr_read_b32 v218, a22\n"
"s_add_u32 s70, s67, 0x4000\n"
"s_add_u32 m0, s85, 0x4000\n"
"s_nop 0\n"
"buffer_load_dwordx4 v254, s[96:99], s70 offen sc1 lds\n"
"v_accvgpr_read_b32 v219, a23\n"
"v_pk_mul_f32 v[212:213], v[212:213], v[238:239] op_sel_hi:[1,0]\n"
"v_pk_mul_f32 v[214:215], v[214:215], v[238:239] op_sel_hi:[1,0]\n"
"v_pk_mul_f32 v[216:217], v[216:217], v[238:239] op_sel_hi:[1,0]\n"
"v_pk_mul_f32 v[218:219], v[218:219], v[238:239] op_sel_hi:[1,0]\n"
"v_cvt_pk_bf16_f32 v224, v212, v213\n"
"v_cvt_pk_bf16_f32 v225, v214, v215\n"
"v_cvt_pk_bf16_f32 v226, v216, v217\n"
"v_cvt_pk_bf16_f32 v227, v218, v219\n"
"buffer_store_dwordx4 v[224:227], v254, s[36:39], s75 offen offset:1024 sc1\n"
"v_accvgpr_read_b32 v204, a8\n"
"v_accvgpr_read_b32 v205, a9\n"
"v_accvgpr_read_b32 v206, a10\n"
"v_accvgpr_read_b32 v207, a11\n"
"s_nop 0\n"
"buffer_load_dwordx4 v254, s[96:99], s70 offen offset:1024 sc1 lds\n"
"v_accvgpr_read_b32 v208, a24\n"
"v_accvgpr_read_b32 v209, a25\n"
"v_accvgpr_read_b32 v210, a26\n"
"v_accvgpr_read_b32 v211, a27\n"
"v_pk_mul_f32 v[204:205], v[204:205], v[240:241] op_sel_hi:[1,0]\n"
"v_pk_mul_f32 v[206:207], v[206:207], v[240:241] op_sel_hi:[1,0]\n"
"v_pk_mul_f32 v[208:209], v[208:209], v[240:241] op_sel_hi:[1,0]\n"
"v_pk_mul_f32 v[210:211], v[210:211], v[240:241] op_sel_hi:[1,0]\n"
"v_cvt_pk_bf16_f32 v228, v204, v205\n"
"v_cvt_pk_bf16_f32 v229, v206, v207\n"
"v_cvt_pk_bf16_f32 v230, v208, v209\n"
"v_cvt_pk_bf16_f32 v231, v210, v211\n"
"buffer_store_dwordx4 v[228:231], v254, s[36:39], s75 offen offset:2048 sc1\n"
"v_accvgpr_read_b32 v212, a12\n"
"s_nop 0\n"
"buffer_load_dwordx4 v254, s[96:99], s70 offen offset:2048 sc1 lds\n"
"v_accvgpr_read_b32 v213, a13\n"
"v_accvgpr_read_b32 v214, a14\n"
"v_accvgpr_read_b32 v215, a15\n"
"v_accvgpr_read_b32 v216, a28\n"
"v_accvgpr_read_b32 v217, a29\n"
"v_accvgpr_read_b32 v218, a30\n"
"v_accvgpr_read_b32 v219, a31\n"
"v_pk_mul_f32 v[212:213], v[212:213], v[242:243] op_sel_hi:[1,0]\n"
"v_pk_mul_f32 v[214:215], v[214:215], v[242:243] op_sel_hi:[1,0]\n"
"v_pk_mul_f32 v[216:217], v[216:217], v[242:243] op_sel_hi:[1,0]\n"
"v_pk_mul_f32 v[218:219], v[218:219], v[242:243] op_sel_hi:[1,0]\n"
"v_cvt_pk_bf16_f32 v232, v212, v213\n"
"v_cvt_pk_bf16_f32 v233, v214, v215\n"
"v_cvt_pk_bf16_f32 v234, v216, v217\n"
"v_cvt_pk_bf16_f32 v235, v218, v219\n"
"buffer_store_dwordx4 v[232:235], v254, s[36:39], s75 offen offset:3072 sc1\n"
"v_accvgpr_read_b32 v204, a32\n"
"v_accvgpr_read_b32 v205, a33\n"
"v_accvgpr_read_b32 v206, a34\n"
"v_accvgpr_read_b32 v207, a35\n"
"v_accvgpr_read_b32 v208, a48\n"
"v_accvgpr_read_b32 v209, a49\n"
"v_accvgpr_read_b32 v210, a50\n"
"v_accvgpr_read_b32 v211, a51\n"
"v_pk_mul_f32 v[204:205], v[204:205], v[236:237] op_sel_hi:[1,0]\n"
"v_pk_mul_f32 v[206:207], v[206:207], v[236:237] op_sel_hi:[1,0]\n"
"v_pk_mul_f32 v[208:209], v[208:209], v[236:237] op_sel_hi:[1,0]\n"
"v_pk_mul_f32 v[210:211], v[210:211], v[236:237] op_sel_hi:[1,0]\n"
"v_cvt_pk_bf16_f32 v220, v204, v205\n"
"v_cvt_pk_bf16_f32 v221, v206, v207\n"
"v_cvt_pk_bf16_f32 v222, v208, v209\n"
"v_cvt_pk_bf16_f32 v223, v210, v211\n"
"buffer_store_dwordx4 v[220:223], v254, s[36:39], s76 offen sc1\n"
"v_accvgpr_read_b32 v212, a36\n"
"v_accvgpr_read_b32 v213, a37\n"
"v_accvgpr_read_b32 v214, a38\n"
"v_accvgpr_read_b32 v215, a39\n"
"v_accvgpr_read_b32 v216, a52\n"
"v_accvgpr_read_b32 v217, a53\n"
"v_accvgpr_read_b32 v218, a54\n"
"v_accvgpr_read_b32 v219, a55\n"
"v_pk_mul_f32 v[212:213], v[212:213], v[238:239] op_sel_hi:[1,0]\n"
"v_pk_mul_f32 v[214:215], v[214:215], v[238:239] op_sel_hi:[1,0]\n"
"v_pk_mul_f32 v[216:217], v[216:217], v[238:239] op_sel_hi:[1,0]\n"
"v_pk_mul_f32 v[218:219], v[218:219], v[238:239] op_sel_hi:[1,0]\n"
"v_cvt_pk_bf16_f32 v224, v212, v213\n"
"v_cvt_pk_bf16_f32 v225, v214, v215\n"
"v_cvt_pk_bf16_f32 v226, v216, v217\n"
"v_cvt_pk_bf16_f32 v227, v218, v219\n"
"buffer_store_dwordx4 v[224:227], v254, s[36:39], s76 offen offset:1024 sc1\n"
"v_accvgpr_read_b32 v204, a40\n"
"v_accvgpr_read_b32 v205, a41\n"
"v_accvgpr_read_b32 v206, a42\n"
"v_accvgpr_read_b32 v207, a43\n"
"v_accvgpr_read_b32 v208, a56\n"
"v_accvgpr_read_b32 v209, a57\n"
"v_accvgpr_read_b32 v210, a58\n"
"v_accvgpr_read_b32 v211, a59\n"
"v_pk_mul_f32 v[204:205], v[204:205], v[240:241] op_sel_hi:[1,0]\n"
"v_pk_mul_f32 v[206:207], v[206:207], v[240:241] op_sel_hi:[1,0]\n"
"v_pk_mul_f32 v[208:209], v[208:209], v[240:241] op_sel_hi:[1,0]\n"
"v_pk_mul_f32 v[210:211], v[210:211], v[240:241] op_sel_hi:[1,0]\n"
"v_cvt_pk_bf16_f32 v228, v204, v205\n"
"v_cvt_pk_bf16_f32 v229, v206, v207\n"
"v_cvt_pk_bf16_f32 v230, v208, v209\n"
"v_cvt_pk_bf16_f32 v231, v210, v211\n"
"buffer_store_dwordx4 v[228:231], v254, s[36:39], s76 offen offset:2048 sc1\n"
"v_accvgpr_read_b32 v212, a44\n"
"v_accvgpr_read_b32 v213, a45\n"
"v_accvgpr_read_b32 v214, a46\n"
"v_accvgpr_read_b32 v215, a47\n"
"v_accvgpr_read_b32 v216, a60\n"
"v_accvgpr_read_b32 v217, a61\n"
"v_accvgpr_read_b32 v218, a62\n"
"v_accvgpr_read_b32 v219, a63\n"
"v_pk_mul_f32 v[212:213], v[212:213], v[242:243] op_sel_hi:[1,0]\n"
"v_pk_mul_f32 v[214:215], v[214:215], v[242:243] op_sel_hi:[1,0]\n"
"v_pk_mul_f32 v[216:217], v[216:217], v[242:243] op_sel_hi:[1,0]\n"
"v_pk_mul_f32 v[218:219], v[218:219], v[242:243] op_sel_hi:[1,0]\n"
"v_cvt_pk_bf16_f32 v232, v212, v213\n"
"v_cvt_pk_bf16_f32 v233, v214, v215\n"
"v_cvt_pk_bf16_f32 v234, v216, v217\n"
"v_cvt_pk_bf16_f32 v235, v218, v219\n"
"buffer_store_dwordx4 v[232:235], v254, s[36:39], s76 offen offset:3072 sc1\n"
"ds_read_b128 a[144:147], v192\n"
"ds_read_b128 a[148:151], v192 offset:1024\n"
"ds_read_b128 a[152:155], v192 offset:2048\n"
"ds_read_b128 a[156:159], v192 offset:3072\n"
"ds_read_b128 a[160:163], v192 offset:4096\n"
"ds_read_b128 a[164:167], v192 offset:5120\n"
"ds_read_b128 a[168:171], v192 offset:6144\n"
"ds_read_b128 a[172:175], v192 offset:7168\n"
"ds_read_b128 a[176:179], v193\n"
"ds_read_b128 a[208:211], v193 offset:8192\n"
"ds_read_b128 a[180:183], v193 offset:1024\n"
"ds_read_b128 a[212:215], v193 offset:9216\n"
"s_waitcnt lgkmcnt(8)\n"
"v_mfma_f32_16x16x32_bf16 v[64:67], a[144:147], a[64:67], v[176:179]\n"
"v_mfma_f32_16x16x32_bf16 v[64:67], a[148:151], a[80:83], v[64:67]\n"
"v_mfma_f32_16x16x32_bf16 v[64:67], a[152:155], a[96:99], v[64:67]\n"
"buffer_load_dwordx4 v254, s[96:99], s70 offen offset:3072 sc1 lds\n"
"v_mfma_f32_16x16x32_bf16 v[64:67], a[156:159], a[112:115], v[64:67]\n"
"v_mfma_f32_16x16x32_bf16 v[68:71], a[156:159], a[116:119], v[164:167]\n"
"v_mfma_f32_16x16x32_bf16 v[68:71], a[152:155], a[100:103], v[68:71]\n"
"v_mfma_f32_16x16x32_bf16 v[68:71], a[148:151], a[84:87], v[68:71]\n"
"s_lshl_b32 s89, s66, 14\n"
"s_add_u32 s67, s73, s89\n"
"s_xor_b32 s84, s84, 0x8000\n"
"s_mov_b32 m0, s84\n"
"v_mfma_f32_16x16x32_bf16 v[68:71], a[144:147], a[68:71], v[68:71]\n"
"buffer_load_dwordx4 v254, s[28:31], s67 offen sc1 lds\n"
"v_mfma_f32_16x16x32_bf16 v[72:75], a[144:147], a[72:75], v[168:171]\n"
"v_mfma_f32_16x16x32_bf16 v[72:75], a[148:151], a[88:91], v[72:75]\n"
"v_mfma_f32_16x16x32_bf16 v[72:75], a[152:155], a[104:107], v[72:75]\n"
"v_mfma_f32_16x16x32_bf16 v[72:75], a[156:159], a[120:123], v[72:75]\n"
"buffer_load_dwordx4 v254, s[28:31], s67 offen offset:1024 sc1 lds\n"
"v_mfma_f32_16x16x32_bf16 v[76:79], a[156:159], a[124:127], v[172:175]\n"
"v_mfma_f32_16x16x32_bf16 v[76:79], a[152:155], a[108:111], v[76:79]\n"
"v_mfma_f32_16x16x32_bf16 v[76:79], a[148:151], a[92:95], v[76:79]\n"
"v_mfma_f32_16x16x32_bf16 v[76:79], a[144:147], a[76:79], v[76:79]\n"
"buffer_load_dwordx4 v254, s[28:31], s67 offen offset:2048 sc1 lds\n"
"s_waitcnt lgkmcnt(4)\n"
"s_nop 0\n"
"ds_read_b128 a[144:147], v192 offset:8192\n"
"ds_read_b128 a[148:151], v192 offset:9216\n"
"s_nop 0\n"
"s_nop 0\n"
"ds_read_b128 a[152:155], v192 offset:10240\n"
"ds_read_b128 a[156:159], v192 offset:11264\n"
"s_nop 0\n"
"buffer_load_dwordx4 v254, s[28:31], s67 offen offset:3072 sc1 lds\n"
"v_mfma_f32_16x16x32_bf16 v[84:87], a[172:175], a[116:119], v[180:183]\n"
"v_mfma_f32_16x16x32_bf16 v[84:87], a[168:171], a[100:103], v[84:87]\n"
"v_exp_f32 v64, v64\n"
"v_mfma_f32_16x16x32_bf16 v[84:87], a[164:167], a[84:87], v[84:87]\n"
"v_exp_f32 v65, v65\n"
"v_mfma_f32_16x16x32_bf16 v[84:87], a[160:163], a[68:71], v[84:87]\n"
"s_add_u32 s69, s67, 0x4000\n"
"s_add_u32 m0, s84, 0x4000\n"
"v_mfma_f32_16x16x32_bf16 v[88:91], a[160:163], a[72:75], v[168:171]\n"
"buffer_load_dwordx4 v254, s[28:31], s69 offen sc1 lds\n"
"v_mfma_f32_16x16x32_bf16 v[88:91], a[164:167], a[88:91], v[88:91]\n"
"v_exp_f32 v66, v66\n"
"v_mfma_f32_16x16x32_bf16 v[88:91], a[168:171], a[104:107], v[88:91]\n"
"v_cvt_pk_bf16_f32 v128, v64, v65\n"
"v_mfma_f32_16x16x32_bf16 v[88:91], a[172:175], a[120:123], v[88:91]\n"
"v_exp_f32 v67, v67\n"
"v_mfma_f32_16x16x32_bf16 v[92:95], a[172:175], a[124:127], v[172:175]\n"
"buffer_load_dwordx4 v254, s[28:31], s69 offen offset:1024 sc1 lds\n"
"v_mfma_f32_16x16x32_bf16 v[92:95], a[168:171], a[108:111], v[92:95]\n"
"v_cvt_pk_bf16_f32 v129, v66, v67\n"
"v_mov_b32 v130, 0\n"
"v_mfma_f32_16x16x32_bf16 v[92:95], a[164:167], a[92:95], v[92:95]\n"
"v_exp_f32 v68, v68\n"
"v_mfma_f32_16x16x32_bf16 v[92:95], a[160:163], a[76:79], v[92:95]\n"
"v_mov_b32 v131, 0\n"
"s_waitcnt lgkmcnt(0)\n"
"s_nop 0\n"
"ds_read_b128 a[160:163], v192 offset:12288\n"
"ds_read_b128 a[164:167], v192 offset:13312\n"
"buffer_load_dwordx4 v254, s[28:31], s69 offen offset:2048 sc1 lds\n"
"s_nop 0\n"
"v_exp_f32 v69, v69\n"
"s_nop 0\n"
"ds_read_b128 a[168:171], v192 offset:14336\n"
"ds_read_b128 a[172:175], v192 offset:15360\n"
"s_nop 0\n"
"v_exp_f32 v70, v70\n"
"s_nop 0\n"
"buffer_load_dwordx4 v254, s[28:31], s69 offen offset:3072 sc1 lds\n"
"s_nop 0\n"
"v_cvt_pk_bf16_f32 v132, v68, v69\n"
"s_nop 0\n"
"v_exp_f32 v71, v71\n"
"s_nop 0\n"
"v_exp_f32 v84, v84\n"
"v_mfma_f32_16x16x32_bf16 v[104:107], a[144:147], a[72:75], v[184:187]\n"
"v_cvt_pk_bf16_f32 v133, v70, v71\n"
"v_mfma_f32_16x16x32_bf16 v[104:107], a[148:151], a[88:91], v[104:107]\n"
"v_exp_f32 v85, v85\n"
"v_mfma_f32_16x16x32_bf16 v[104:107], a[152:155], a[104:107], v[104:107]\n"
"v_exp_f32 v86, v86\n"
"v_mfma_f32_16x16x32_bf16 v[104:107], a[156:159], a[120:123], v[104:107]\n"
"v_cvt_pk_bf16_f32 v134, v84, v85\n"
"v_mfma_f32_16x16x32_bf16 v[108:111], a[156:159], a[124:127], v[172:175]\n"
"v_exp_f32 v87, v87\n"
"v_mfma_f32_16x16x32_bf16 v[108:111], a[152:155], a[108:111], v[108:111]\n"
"v_exp_f32 v72, v72\n"
"v_mfma_f32_16x16x32_bf16 v[108:111], a[148:151], a[92:95], v[108:111]\n"
"v_cvt_pk_bf16_f32 v135, v86, v87\n"
"v_mfma_f32_16x16x32_bf16 v[108:111], a[144:147], a[76:79], v[108:111]\n"
"v_exp_f32 v73, v73\n"
"s_waitcnt lgkmcnt(0)\n"
"s_nop 0\n"
"v_exp_f32 v74, v74\n"
"s_nop 0\n"
"v_cvt_pk_bf16_f32 v136, v72, v73\n"
"s_nop 0\n"
"v_exp_f32 v75, v75\n"
"s_nop 0\n"
"v_exp_f32 v88, v88\n"
"s_nop 0\n"
"v_cvt_pk_bf16_f32 v137, v74, v75\n"
"s_nop 0\n"
"v_exp_f32 v89, v89\n"
"s_nop 0\n"
"v_exp_f32 v90, v90\n"
"s_nop 0\n"
"v_cvt_pk_bf16_f32 v138, v88, v89\n"
"s_nop 0\n"
"v_exp_f32 v91, v91\n"
"s_nop 0\n"
"v_exp_f32 v76, v76\n"
"s_nop 0\n"
"v_cvt_pk_bf16_f32 v139, v90, v91\n"
"s_nop 0\n"
"v_exp_f32 v77, v77\n"
"v_mfma_f32_16x16x32_bf16 v[124:127], a[172:175], a[124:127], v[188:191]\n"
"v_exp_f32 v78, v78\n"
"v_mfma_f32_16x16x32_bf16 v[124:127], a[168:171], a[108:111], v[124:127]\n"
"v_exp_f32 v79, v79\n"
"v_mfma_f32_16x16x32_bf16 v[124:127], a[164:167], a[92:95], v[124:127]\n"
"v_exp_f32 v92, v92\n"
"v_mfma_f32_16x16x32_bf16 v[124:127], a[160:163], a[76:79], v[124:127]\n"
"v_exp_f32 v93, v93\n"
"s_nop 1\n"
"v_exp_f32 v94, v94\n"
"v_exp_f32 v95, v95\n"
"v_mov_b32 v144, 0\n"
"v_mov_b32 v145, 0\n"
"v_mov_b32 v146, 0\n"
"v_mov_b32 v147, 0\n"
"s_waitcnt vmcnt(0) lgkmcnt(0)\n"
"s_barrier\n"
"v_xor_b32 v192, 0x8000, v192\n"
"v_xor_b32 v193, 0x8000, v193\n"
"v_xor_b32 v194, 0x8000, v194\n"
"ds_read_b128 a[144:147], v192\n"
"v_mov_b32 v148, 0\n"
"ds_read_b128 a[148:151], v192 offset:1024\n"
"v_mov_b32 v149, 0\n"
"ds_read_b128 a[152:155], v192 offset:2048\n"
"ds_read_b128 a[156:159], v192 offset:3072\n"
"v_mov_b32 v150, 0\n"
"ds_read_b128 a[160:163], v192 offset:4096\n"
"v_exp_f32 v104, v104\n"
"v_mov_b32 v151, 0\n"
"ds_read_b128 a[164:167], v192 offset:5120\n"
"v_exp_f32 v105, v105\n"
"v_exp_f32 v106, v106\n"
"ds_read_b128 a[168:171], v192 offset:6144\n"
"v_cvt_pk_bf16_f32 v152, v104, v105\n"
"v_exp_f32 v107, v107\n"
"ds_read_b128 a[172:175], v192 offset:7168\n"
"v_cvt_pk_bf16_f32 v153, v106, v107\n"
"ds_read_b128 a[176:179], v193\n"
"ds_read_b128 a[208:211], v193 offset:8192\n"
"v_mov_b32 v154, 0\n"
"ds_read_b128 a[180:183], v193 offset:1024\n"
"v_exp_f32 v108, v108\n"
"v_mov_b32 v155, 0\n"
"ds_read_b128 a[212:215], v193 offset:9216\n"
"v_exp_f32 v109, v109\n"
"v_exp_f32 v110, v110\n"
"v_exp_f32 v111, v111\n"
"v_exp_f32 v124, v124\n"
"v_exp_f32 v125, v125\n"
"v_exp_f32 v126, v126\n"
"v_exp_f32 v127, v127\n"
"s_nop 0\n"
"s_add_u32 s62, s62, 1\n"
"s_cmp_lt_u32 s62, 16\n"
"s_cbranch_scc1 .Lfull2z_22_%=\n"
"s_nop 0\n"
"s_waitcnt lgkmcnt(2)\n"
"v_mfma_f32_16x16x32_bf16 v[0:3], a[176:179], v[128:131], 0\n"
"ds_read_b128 a[184:187], v193 offset:2048\n"
"ds_read_b128 a[216:219], v193 offset:10240\n"
"s_lshr_b32 s62, s9, 1\n"
"s_and_b32 s62, s62, 3\n"
"s_and_b32 s63, s9, 1\n"
"s_lshl_b32 s63, s63, 3\n"
"v_mfma_f32_16x16x32_bf16 v[0:3], a[208:211], v[144:147], v[0:3]\n"
"ds_read_b128 a[188:191], v193 offset:3072\n"
"ds_read_b128 a[220:223], v193 offset:11264\n"
"s_lshr_b32 s12, s9, 5\n"
"s_add_u32 s63, s63, s12\n"
"s_lshr_b32 s64, s9, 3\n"
"s_and_b32 s64, s64, 3\n"
"s_waitcnt lgkmcnt(4)\n"
"v_mfma_f32_16x16x32_bf16 v[16:19], a[212:215], v[144:147], 0\n"
"ds_read_b128 a[192:195], v194 offset:4096\n"
"ds_read_b128 a[224:227], v194 offset:12288\n"
"s_lshr_b32 s12, s63, 1\n"
"s_lshl_b32 s13, s12, 8\n"
"s_add_u32 s71, s13, 0x1800\n"
"s_lshl_b32 s14, s62, 6\n"
"v_mfma_f32_16x16x32_bf16 v[16:19], a[180:183], v[128:131], v[16:19]\n"
"ds_read_b128 a[196:199], v194 offset:5120\n"
"ds_read_b128 a[228:231], v194 offset:13312\n"
"s_add_u32 s13, s13, s14\n"
"s_add_u32 s81, s13, 0x2000\n"
"s_lshl_b32 s13, s13, 2\n"
"s_add_u32 s72, s13, 0x4000\n"
"s_waitcnt lgkmcnt(6)\n"
"v_mfma_f32_16x16x32_bf16 v[32:35], a[184:187], v[128:131], 0\n"
"ds_read_b128 a[200:203], v194 offset:6144\n"
"ds_read_b128 a[232:235], v194 offset:14336\n"
"v_and_b32 v228, 31, v255\n"
"v_lshlrev_b32 v228, 3, v228\n"
"v_and_b32 v229, 56, v228\n"
"s_mov_b32 s24, s56\n"
"v_mfma_f32_16x16x32_bf16 v[32:35], a[216:219], v[144:147], v[32:35]\n"
"ds_read_b128 a[204:207], v194 offset:7168\n"
"ds_read_b128 a[236:239], v194 offset:15360\n"
"s_and_b32 s25, s57, 0xffff\n"
"s_mov_b32 s26, 0x158000\n"
"s_mov_b32 s27, 0x20000\n"
"v_lshrrev_b32 v208, 4, v255\n"
"s_waitcnt lgkmcnt(8)\n"
"v_mfma_f32_16x16x32_bf16 v[48:51], a[220:223], v[144:147], 0\n"
"v_cvt_pk_bf16_f32 v140, v76, v77\n"
"v_cvt_pk_bf16_f32 v141, v78, v79\n"
"v_and_b32 v209, 15, v255\n"
"s_lshl_b32 s13, s8, 5\n"
"v_add_u32 v204, s13, v208\n"
"v_lshlrev_b32 v204, 10, v204\n"
"v_mfma_f32_16x16x32_bf16 v[48:51], a[188:191], v[128:131], v[48:51]\n"
"v_cvt_pk_bf16_f32 v142, v92, v93\n"
"v_cvt_pk_bf16_f32 v143, v94, v95\n"
"s_lshl_b32 s13, s64, 8\n"
"v_add_u32 v204, s13, v204\n"
"v_lshlrev_b32 v207, 1, v208\n"
"v_xor_b32 v206, v209, v207\n"
"s_waitcnt lgkmcnt(6)\n"
"v_mfma_f32_16x16x32_bf16 a[0:3], a[192:195], v[128:131], 0\n"
"v_cvt_pk_bf16_f32 v156, v108, v109\n"
"v_cvt_pk_bf16_f32 v157, v110, v111\n"
"v_lshl_add_u32 v216, v206, 4, v204\n"
"v_xor_b32 v206, 8, v206\n"
"v_lshl_add_u32 v217, v206, 4, v204\n"
"v_add_u32 v217, 0xc00, v217\n"
"v_mfma_f32_16x16x32_bf16 a[0:3], a[224:227], v[144:147], a[0:3]\n"
"v_cvt_pk_bf16_f32 v158, v124, v125\n"
"v_cvt_pk_bf16_f32 v159, v126, v127\n"
"v_add_u32 v218, 0x1800, v216\n"
"v_add_u32 v219, 0x1800, v217\n"
"s_lshl_b32 s67, s8, 13\n"
"s_mul_i32 s65, s62, 0x58000\n"
"s_waitcnt lgkmcnt(4)\n"
"v_mfma_f32_16x16x32_bf16 a[16:19], a[228:231], v[144:147], 0\n"
"s_lshl_b32 s66, s62, 20\n"
"s_lshl_b32 s13, s63, 2\n"
"s_add_u32 s13, s13, s8\n"
"s_lshl_b32 s14, s12, 4\n"
"v_mfma_f32_16x16x32_bf16 a[16:19], a[196:199], v[128:131], a[16:19]\n"
"s_sub_u32 s14, 0x78, s14\n"
"s_add_u32 s14, s14, s13\n"
"s_lshl_b32 s13, s13, 9\n"
"s_lshl_b32 s14, s14, 9\n"
"s_waitcnt lgkmcnt(2)\n"
"v_mfma_f32_16x16x32_bf16 a[32:35], a[200:203], v[128:131], 0\n"
"v_add_u32 v212, s13, v247\n"
"v_add_u32 v213, s14, v247\n"
"v_lshlrev_b32 v214, 1, v212\n"
"v_lshlrev_b32 v215, 1, v213\n"
"v_mfma_f32_16x16x32_bf16 a[32:35], a[232:235], v[144:147], a[32:35]\n"
"s_mul_i32 s68, s62, 22\n"
"s_add_u32 s68, s68, 0x100\n"
"s_lshl_b32 s68, s68, 16\n"
"s_lshl_b32 s69, s62, 22\n"
"s_waitcnt lgkmcnt(0)\n"
"v_mfma_f32_16x16x32_bf16 a[48:51], a[236:239], v[144:147], 0\n"
"s_mov_b32 s70, s69\n"
"s_lshl_b32 s13, s13, 5\n"
"s_lshl_b32 s14, s14, 5\n"
"s_sub_u32 s80, s14, s13\n"
"v_mfma_f32_16x16x32_bf16 a[48:51], a[204:207], v[128:131], a[48:51]\n"
"s_lshl_b32 s14, s64, 8\n"
"s_add_u32 s13, s13, s14\n"
"s_lshl_b32 s14, s62, 6\n"
"s_add_u32 s13, s13, s14\n"
"v_mfma_f32_16x16x32_bf16 a[128:131], v[200:203], v[128:131], 0\n"
"v_lshlrev_b32 v204, 10, v209\n"
"v_lshl_add_u32 v204, v208, 3, v204\n"
"v_add_u32 v234, s13, v204\n"
"s_lshl_b32 s12, s8, 14\n"
"v_mfma_f32_16x16x32_bf16 a[128:131], v[200:203], v[144:147], a[128:131]\n"
"v_add_u32 v244, s12, v254\n"
"v_mfma_f32_16x16x32_bf16 v[4:7], a[176:179], v[132:135], 0\n"
"buffer_load_dwordx2 v[230:231], v228, s[92:95], s71 offen sc1\n"
"v_mfma_f32_16x16x32_bf16 v[4:7], a[208:211], v[148:151], v[4:7]\n"
"s_add_u32 s82, s65, 0\n"
"s_add_u32 s83, s82, 0x4000\n"
"s_add_u32 m0, s67, 0x18000\n"
"v_mfma_f32_16x16x32_bf16 v[20:23], a[212:215], v[148:151], 0\n"
"buffer_load_dwordx4 v216, s[24:27], s82 offen lds\n"
"v_mfma_f32_16x16x32_bf16 v[20:23], a[180:183], v[132:135], v[20:23]\n"
"v_mfma_f32_16x16x32_bf16 v[36:39], a[184:187], v[132:135], 0\n"
"buffer_load_dwordx4 v217, s[24:27], s82 offen offset:1024 lds\n"
"ds_write_b128 v244, a[0:3]\n"
"v_mfma_f32_16x16x32_bf16 v[36:39], a[216:219], v[148:151], v[36:39]\n"
"v_mfma_f32_16x16x32_bf16 v[52:55], a[220:223], v[148:151], 0\n"
"v_mfma_f32_16x16x32_bf16 v[52:55], a[188:191], v[132:135], v[52:55]\n"
"buffer_load_dwordx4 v218, s[24:27], s82 offen offset:2048 lds\n"
"v_mfma_f32_16x16x32_bf16 a[4:7], a[192:195], v[132:135], 0\n"
"ds_write_b128 v244, a[16:19] offset:4096\n"
"v_mfma_f32_16x16x32_bf16 a[4:7], a[224:227], v[148:151], a[4:7]\n"
"buffer_load_dwordx4 v219, s[24:27], s82 offen offset:3072 lds\n"
"v_mfma_f32_16x16x32_bf16 a[20:23], a[228:231], v[148:151], 0\n"
"v_mfma_f32_16x16x32_bf16 a[20:23], a[196:199], v[132:135], a[20:23]\n"
"s_add_u32 m0, s67, 0x19000\n"
"v_mfma_f32_16x16x32_bf16 a[36:39], a[200:203], v[132:135], 0\n"
"buffer_load_dwordx4 v216, s[24:27], s83 offen lds\n"
"ds_write_b128 v244, a[32:35] offset:8192\n"
"v_mfma_f32_16x16x32_bf16 a[36:39], a[232:235], v[148:151], a[36:39]\n"
"v_mfma_f32_16x16x32_bf16 a[52:55], a[236:239], v[148:151], 0\n"
"buffer_load_dwordx4 v217, s[24:27], s83 offen offset:1024 lds\n"
"v_mfma_f32_16x16x32_bf16 a[52:55], a[204:207], v[132:135], a[52:55]\n"
"v_mfma_f32_16x16x32_bf16 a[132:135], v[200:203], v[132:135], 0\n"
"ds_write_b128 v244, a[48:51] offset:12288\n"
"v_mfma_f32_16x16x32_bf16 a[132:135], v[200:203], v[148:151], a[132:135]\n"
"buffer_load_dwordx4 v218, s[24:27], s83 offen offset:2048 lds\n"
"v_mfma_f32_16x16x32_bf16 v[8:11], a[176:179], v[136:139], 0\n"
"v_mfma_f32_16x16x32_bf16 v[8:11], a[208:211], v[152:155], v[8:11]\n"
"buffer_load_dwordx4 v219, s[24:27], s83 offen offset:3072 lds\n"
"v_mfma_f32_16x16x32_bf16 v[24:27], a[212:215], v[152:155], 0\n"
"v_mfma_f32_16x16x32_bf16 v[24:27], a[180:183], v[136:139], v[24:27]\n"
"v_mfma_f32_16x16x32_bf16 v[40:43], a[184:187], v[136:139], 0\n"
"ds_write_b128 v244, a[4:7] offset:1024\n"
"v_mfma_f32_16x16x32_bf16 v[40:43], a[216:219], v[152:155], v[40:43]\n"
"s_waitcnt vmcnt(8)\n"
"v_cmp_eq_u64 vcc, v[230:231], v[250:251]\n"
"s_nop 1\n"
"s_cmp_eq_u64 vcc, exec\n"
"s_cbranch_scc1 .Lpok_5049_%=\n"
"s_mov_b32 s15, 0\n"
".Lploop_5050_%=:\n"
"s_sleep 4\n"
"buffer_load_dwordx2 v[230:231], v228, s[92:95], s71 offen sc1\n"
"s_waitcnt vmcnt(0)\n"
"v_cmp_eq_u64 vcc, v[230:231], v[250:251]\n"
"s_add_u32 s15, s15, 1\n"
"s_nop 0\n"
"s_cmp_eq_u64 vcc, exec\n"
"s_cbranch_scc1 .Lpok_5049_%=\n"
"s_cmp_lt_u32 s15, 0x20000\n"
"s_cbranch_scc1 .Lploop_5050_%=\n"
"s_mov_b32 s15, 0x80\n"
"s_mov_b64 exec, 1\n"
"buffer_store_dwordx2 v[250:251], v249, s[92:95], s15 offen sc1\n"
"s_mov_b64 exec, -1\n"
".Lpok_5049_%=:\n"
"v_mfma_f32_16x16x32_bf16 v[56:59], a[220:223], v[152:155], 0\n"
"v_mfma_f32_16x16x32_bf16 v[56:59], a[188:191], v[136:139], v[56:59]\n"
"buffer_load_dwordx2 a[64:65], v212, s[36:39], s68 offen sc1\n"
"v_mfma_f32_16x16x32_bf16 a[8:11], a[192:195], v[136:139], 0\n"
"ds_write_b128 v244, a[20:23] offset:5120\n"
"v_mfma_f32_16x16x32_bf16 a[8:11], a[224:227], v[152:155], a[8:11]\n"
"buffer_load_dwordx2 a[68:69], v213, s[36:39], s68 offen sc1\n"
"s_add_u32 s68, s68, 0x10000\n"
"v_mfma_f32_16x16x32_bf16 a[24:27], a[228:231], v[152:155], 0\n"
"v_mfma_f32_16x16x32_bf16 a[24:27], a[196:199], v[136:139], a[24:27]\n"
"buffer_load_dwordx2 a[66:67], v212, s[36:39], s68 offen sc1\n"
"v_mfma_f32_16x16x32_bf16 a[40:43], a[200:203], v[136:139], 0\n"
"ds_write_b128 v244, a[36:39] offset:9216\n"
"v_mfma_f32_16x16x32_bf16 a[40:43], a[232:235], v[152:155], a[40:43]\n"
"buffer_load_dwordx2 a[70:71], v213, s[36:39], s68 offen sc1\n"
"s_add_u32 s68, s68, 0x10000\n"
"v_mfma_f32_16x16x32_bf16 a[56:59], a[236:239], v[152:155], 0\n"
"v_mfma_f32_16x16x32_bf16 a[56:59], a[204:207], v[136:139], a[56:59]\n"
"buffer_load_dwordx2 a[72:73], v212, s[36:39], s68 offen sc1\n"
"v_mfma_f32_16x16x32_bf16 a[136:139], v[200:203], v[136:139], 0\n"
"ds_write_b128 v244, a[52:55] offset:13312\n"
"v_mfma_f32_16x16x32_bf16 a[136:139], v[200:203], v[152:155], a[136:139]\n"
"buffer_load_dwordx2 a[76:77], v213, s[36:39], s68 offen sc1\n"
"s_add_u32 s68, s68, 0x10000\n"
"v_mfma_f32_16x16x32_bf16 v[12:15], a[176:179], v[140:143], 0\n"
"v_mfma_f32_16x16x32_bf16 v[12:15], a[208:211], v[156:159], v[12:15]\n"
"buffer_load_dwordx2 a[74:75], v212, s[36:39], s68 offen sc1\n"
"v_mfma_f32_16x16x32_bf16 v[28:31], a[212:215], v[156:159], 0\n"
"v_mfma_f32_16x16x32_bf16 v[28:31], a[180:183], v[140:143], v[28:31]\n"
"buffer_load_dwordx2 a[78:79], v213, s[36:39], s68 offen sc1\n"
"s_add_u32 s68, s68, 0x10000\n"
"v_mfma_f32_16x16x32_bf16 v[44:47], a[184:187], v[140:143], 0\n"
"ds_write_b128 v244, a[8:11] offset:2048\n"
"v_mfma_f32_16x16x32_bf16 v[44:47], a[216:219], v[156:159], v[44:47]\n"
"buffer_load_dwordx2 a[80:81], v212, s[36:39], s68 offen sc1\n"
"v_mfma_f32_16x16x32_bf16 v[60:63], a[220:223], v[156:159], 0\n"
"v_mfma_f32_16x16x32_bf16 v[60:63], a[188:191], v[140:143], v[60:63]\n"
"buffer_load_dwordx2 a[84:85], v213, s[36:39], s68 offen sc1\n"
"s_add_u32 s68, s68, 0x10000\n"
"v_mfma_f32_16x16x32_bf16 a[12:15], a[192:195], v[140:143], 0\n"
"ds_write_b128 v244, a[24:27] offset:6144\n"
"v_mfma_f32_16x16x32_bf16 a[12:15], a[224:227], v[156:159], a[12:15]\n"
"buffer_load_dwordx2 a[82:83], v212, s[36:39], s68 offen sc1\n"
"v_mfma_f32_16x16x32_bf16 a[28:31], a[228:231], v[156:159], 0\n"
"v_mfma_f32_16x16x32_bf16 a[28:31], a[196:199], v[140:143], a[28:31]\n"
"buffer_load_dwordx2 a[86:87], v213, s[36:39], s68 offen sc1\n"
"s_add_u32 s68, s68, 0x10000\n"
"v_mfma_f32_16x16x32_bf16 a[44:47], a[200:203], v[140:143], 0\n"
"ds_write_b128 v244, a[40:43] offset:10240\n"
"v_mfma_f32_16x16x32_bf16 a[44:47], a[232:235], v[156:159], a[44:47]\n"
"buffer_load_dwordx2 a[88:89], v212, s[36:39], s68 offen sc1\n"
"v_mfma_f32_16x16x32_bf16 a[60:63], a[236:239], v[156:159], 0\n"
"v_mfma_f32_16x16x32_bf16 a[60:63], a[204:207], v[140:143], a[60:63]\n"
"buffer_load_dwordx2 a[92:93], v213, s[36:39], s68 offen sc1\n"
"s_add_u32 s68, s68, 0x10000\n"
"v_mfma_f32_16x16x32_bf16 a[140:143], v[200:203], v[140:143], 0\n"
"ds_write_b128 v244, a[56:59] offset:14336\n"
"v_mfma_f32_16x16x32_bf16 a[140:143], v[200:203], v[156:159], a[140:143]\n"
"buffer_load_dwordx2 a[90:91], v212, s[36:39], s68 offen sc1\n"
"buffer_load_dwordx2 a[94:95], v213, s[36:39], s68 offen sc1\n"
"s_add_u32 s68, s68, 0x10000\n"
"s_branch .Lmerge_38_%=\n"
".Llzero_5048_%=:\n"
"v_accvgpr_read_b32 v204, a0\n"
"v_accvgpr_read_b32 v205, a1\n"
"v_accvgpr_read_b32 v206, a2\n"
"v_accvgpr_read_b32 v207, a3\n"
"v_accvgpr_read_b32 v208, a16\n"
"v_accvgpr_read_b32 v209, a17\n"
"v_accvgpr_read_b32 v210, a18\n"
"v_accvgpr_read_b32 v211, a19\n"
"v_pk_mul_f32 v[204:205], v[204:205], v[236:237] op_sel_hi:[1,0]\n"
"v_pk_mul_f32 v[206:207], v[206:207], v[236:237] op_sel_hi:[1,0]\n"
"v_pk_mul_f32 v[208:209], v[208:209], v[236:237] op_sel_hi:[1,0]\n"
"v_pk_mul_f32 v[210:211], v[210:211], v[236:237] op_sel_hi:[1,0]\n"
"v_cvt_pk_bf16_f32 v220, v204, v205\n"
"v_cvt_pk_bf16_f32 v221, v206, v207\n"
"v_cvt_pk_bf16_f32 v222, v208, v209\n"
"v_cvt_pk_bf16_f32 v223, v210, v211\n"
"buffer_store_dwordx4 v[220:223], v254, s[36:39], s75 offen sc1\n"
"v_accvgpr_read_b32 v212, a4\n"
"v_accvgpr_read_b32 v213, a5\n"
"v_accvgpr_read_b32 v214, a6\n"
"v_accvgpr_read_b32 v215, a7\n"
"v_accvgpr_read_b32 v216, a20\n"
"v_accvgpr_read_b32 v217, a21\n"
"v_accvgpr_read_b32 v218, a22\n"
"v_accvgpr_read_b32 v219, a23\n"
"v_pk_mul_f32 v[212:213], v[212:213], v[238:239] op_sel_hi:[1,0]\n"
"v_pk_mul_f32 v[214:215], v[214:215], v[238:239] op_sel_hi:[1,0]\n"
"v_pk_mul_f32 v[216:217], v[216:217], v[238:239] op_sel_hi:[1,0]\n"
"v_pk_mul_f32 v[218:219], v[218:219], v[238:239] op_sel_hi:[1,0]\n"
"v_cvt_pk_bf16_f32 v224, v212, v213\n"
"v_cvt_pk_bf16_f32 v225, v214, v215\n"
"v_cvt_pk_bf16_f32 v226, v216, v217\n"
"v_cvt_pk_bf16_f32 v227, v218, v219\n"
"buffer_store_dwordx4 v[224:227], v254, s[36:39], s75 offen offset:1024 sc1\n"
"v_accvgpr_read_b32 v204, a8\n"
"v_accvgpr_read_b32 v205, a9\n"
"v_accvgpr_read_b32 v206, a10\n"
"v_accvgpr_read_b32 v207, a11\n"
"v_accvgpr_read_b32 v208, a24\n"
"v_accvgpr_read_b32 v209, a25\n"
"v_accvgpr_read_b32 v210, a26\n"
"v_accvgpr_read_b32 v211, a27\n"
"v_pk_mul_f32 v[204:205], v[204:205], v[240:241] op_sel_hi:[1,0]\n"
"v_pk_mul_f32 v[206:207], v[206:207], v[240:241] op_sel_hi:[1,0]\n"
"v_pk_mul_f32 v[208:209], v[208:209], v[240:241] op_sel_hi:[1,0]\n"
"v_pk_mul_f32 v[210:211], v[210:211], v[240:241] op_sel_hi:[1,0]\n"
"v_cvt_pk_bf16_f32 v228, v204, v205\n"
"v_cvt_pk_bf16_f32 v229, v206, v207\n"
"v_cvt_pk_bf16_f32 v230, v208, v209\n"
"v_cvt_pk_bf16_f32 v231, v210, v211\n"
"buffer_store_dwordx4 v[228:231], v254, s[36:39], s75 offen offset:2048 sc1\n"
"v_accvgpr_read_b32 v212, a12\n"
"v_accvgpr_read_b32 v213, a13\n"
"v_accvgpr_read_b32 v214, a14\n"
"v_accvgpr_read_b32 v215, a15\n"
"v_accvgpr_read_b32 v216, a28\n"
"v_accvgpr_read_b32 v217, a29\n"
"v_accvgpr_read_b32 v218, a30\n"
"v_accvgpr_read_b32 v219, a31\n"
"v_pk_mul_f32 v[212:213], v[212:213], v[242:243] op_sel_hi:[1,0]\n"
"v_pk_mul_f32 v[214:215], v[214:215], v[242:243] op_sel_hi:[1,0]\n"
"v_pk_mul_f32 v[216:217], v[216:217], v[242:243] op_sel_hi:[1,0]\n"
"v_pk_mul_f32 v[218:219], v[218:219], v[242:243] op_sel_hi:[1,0]\n"
"v_cvt_pk_bf16_f32 v232, v212, v213\n"
"v_cvt_pk_bf16_f32 v233, v214, v215\n"
"v_cvt_pk_bf16_f32 v234, v216, v217\n"
"v_cvt_pk_bf16_f32 v235, v218, v219\n"
"buffer_store_dwordx4 v[232:235], v254, s[36:39], s75 offen offset:3072 sc1\n"
"v_accvgpr_read_b32 v204, a32\n"
"v_accvgpr_read_b32 v205, a33\n"
"v_accvgpr_read_b32 v206, a34\n"
"v_accvgpr_read_b32 v207, a35\n"
"v_accvgpr_read_b32 v208, a48\n"
"v_accvgpr_read_b32 v209, a49\n"
"v_accvgpr_read_b32 v210, a50\n"
"v_accvgpr_read_b32 v211, a51\n"
"v_pk_mul_f32 v[204:205], v[204:205], v[236:237] op_sel_hi:[1,0]\n"
"v_pk_mul_f32 v[206:207], v[206:207], v[236:237] op_sel_hi:[1,0]\n"
"v_pk_mul_f32 v[208:209], v[208:209], v[236:237] op_sel_hi:[1,0]\n"
"v_pk_mul_f32 v[210:211], v[210:211], v[236:237] op_sel_hi:[1,0]\n"
"v_cvt_pk_bf16_f32 v220, v204, v205\n"
"v_cvt_pk_bf16_f32 v221, v206, v207\n"
"v_cvt_pk_bf16_f32 v222, v208, v209\n"
"v_cvt_pk_bf16_f32 v223, v210, v211\n"
"buffer_store_dwordx4 v[220:223], v254, s[36:39], s76 offen sc1\n"
"v_accvgpr_read_b32 v212, a36\n"
"v_accvgpr_read_b32 v213, a37\n"
"v_accvgpr_read_b32 v214, a38\n"
"v_accvgpr_read_b32 v215, a39\n"
"v_accvgpr_read_b32 v216, a52\n"
"v_accvgpr_read_b32 v217, a53\n"
"v_accvgpr_read_b32 v218, a54\n"
"v_accvgpr_read_b32 v219, a55\n"
"v_pk_mul_f32 v[212:213], v[212:213], v[238:239] op_sel_hi:[1,0]\n"
"v_pk_mul_f32 v[214:215], v[214:215], v[238:239] op_sel_hi:[1,0]\n"
"v_pk_mul_f32 v[216:217], v[216:217], v[238:239] op_sel_hi:[1,0]\n"
"v_pk_mul_f32 v[218:219], v[218:219], v[238:239] op_sel_hi:[1,0]\n"
"v_cvt_pk_bf16_f32 v224, v212, v213\n"
"v_cvt_pk_bf16_f32 v225, v214, v215\n"
"v_cvt_pk_bf16_f32 v226, v216, v217\n"
"v_cvt_pk_bf16_f32 v227, v218, v219\n"
"buffer_store_dwordx4 v[224:227], v254, s[36:39], s76 offen offset:1024 sc1\n"
"v_accvgpr_read_b32 v204, a40\n"
"v_accvgpr_read_b32 v205, a41\n"
"v_accvgpr_read_b32 v206, a42\n"
"v_accvgpr_read_b32 v207, a43\n"
"v_accvgpr_read_b32 v208, a56\n"
"v_accvgpr_read_b32 v209, a57\n"
"v_accvgpr_read_b32 v210, a58\n"
"v_accvgpr_read_b32 v211, a59\n"
"v_pk_mul_f32 v[204:205], v[204:205], v[240:241] op_sel_hi:[1,0]\n"
"v_pk_mul_f32 v[206:207], v[206:207], v[240:241] op_sel_hi:[1,0]\n"
"v_pk_mul_f32 v[208:209], v[208:209], v[240:241] op_sel_hi:[1,0]\n"
"v_pk_mul_f32 v[210:211], v[210:211], v[240:241] op_sel_hi:[1,0]\n"
"v_cvt_pk_bf16_f32 v228, v204, v205\n"
"v_cvt_pk_bf16_f32 v229, v206, v207\n"
"v_cvt_pk_bf16_f32 v230, v208, v209\n"
"v_cvt_pk_bf16_f32 v231, v210, v211\n"
"buffer_store_dwordx4 v[228:231], v254, s[36:39], s76 offen offset:2048 sc1\n"
"v_accvgpr_read_b32 v212, a44\n"
"v_accvgpr_read_b32 v213, a45\n"
"v_accvgpr_read_b32 v214, a46\n"
"v_accvgpr_read_b32 v215, a47\n"
"v_accvgpr_read_b32 v216, a60\n"
"v_accvgpr_read_b32 v217, a61\n"
"v_accvgpr_read_b32 v218, a62\n"
"v_accvgpr_read_b32 v219, a63\n"
"v_pk_mul_f32 v[212:213], v[212:213], v[242:243] op_sel_hi:[1,0]\n"
"v_pk_mul_f32 v[214:215], v[214:215], v[242:243] op_sel_hi:[1,0]\n"
"v_pk_mul_f32 v[216:217], v[216:217], v[242:243] op_sel_hi:[1,0]\n"
"v_pk_mul_f32 v[218:219], v[218:219], v[242:243] op_sel_hi:[1,0]\n"
"v_cvt_pk_bf16_f32 v232, v212, v213\n"
"v_cvt_pk_bf16_f32 v233, v214, v215\n"
"v_cvt_pk_bf16_f32 v234, v216, v217\n"
"v_cvt_pk_bf16_f32 v235, v218, v219\n"
"buffer_store_dwordx4 v[232:235], v254, s[36:39], s76 offen offset:3072 sc1\n"
"s_lshr_b32 s62, s9, 1\n"
"s_and_b32 s62, s62, 3\n"
"s_and_b32 s63, s9, 1\n"
"s_lshl_b32 s63, s63, 3\n"
"s_lshr_b32 s12, s9, 5\n"
"s_add_u32 s63, s63, s12\n"
"s_lshr_b32 s64, s9, 3\n"
"s_and_b32 s64, s64, 3\n"
"s_lshr_b32 s12, s63, 1\n"
"s_lshl_b32 s13, s12, 8\n"
"s_add_u32 s71, s13, 0x1800\n"
"s_lshl_b32 s14, s62, 6\n"
"s_add_u32 s13, s13, s14\n"
"s_add_u32 s81, s13, 0x2000\n"
"s_lshl_b32 s13, s13, 2\n"
"s_add_u32 s72, s13, 0x4000\n"
"v_and_b32 v228, 31, v255\n"
"v_lshlrev_b32 v228, 3, v228\n"
"v_and_b32 v229, 56, v228\n"
"s_mov_b32 s24, s56\n"
"s_and_b32 s25, s57, 0xffff\n"
"s_mov_b32 s26, 0x158000\n"
"s_mov_b32 s27, 0x20000\n"
"v_lshrrev_b32 v208, 4, v255\n"
"v_and_b32 v209, 15, v255\n"
"s_lshl_b32 s13, s8, 5\n"
"v_add_u32 v204, s13, v208\n"
"v_lshlrev_b32 v204, 10, v204\n"
"s_lshl_b32 s13, s64, 8\n"
"v_add_u32 v204, s13, v204\n"
"v_lshlrev_b32 v207, 1, v208\n"
"v_xor_b32 v206, v209, v207\n"
"v_lshl_add_u32 v216, v206, 4, v204\n"
"v_xor_b32 v206, 8, v206\n"
"v_lshl_add_u32 v217, v206, 4, v204\n"
"v_add_u32 v217, 0xc00, v217\n"
"v_add_u32 v218, 0x1800, v216\n"
"v_add_u32 v219, 0x1800, v217\n"
"s_lshl_b32 s67, s8, 13\n"
"s_mul_i32 s65, s62, 0x58000\n"
"s_lshl_b32 s66, s62, 20\n"
"s_lshl_b32 s13, s63, 2\n"
"s_add_u32 s13, s13, s8\n"
"s_lshl_b32 s14, s12, 4\n"
"s_sub_u32 s14, 0x78, s14\n"
"s_add_u32 s14, s14, s13\n"
"s_lshl_b32 s13, s13, 9\n"
"s_lshl_b32 s14, s14, 9\n"
"v_add_u32 v212, s13, v247\n"
"v_add_u32 v213, s14, v247\n"
"v_lshlrev_b32 v214, 1, v212\n"
"v_lshlrev_b32 v215, 1, v213\n"
"s_mul_i32 s68, s62, 22\n"
"s_add_u32 s68, s68, 0x100\n"
"s_lshl_b32 s68, s68, 16\n"
"s_lshl_b32 s69, s62, 22\n"
"s_mov_b32 s70, s69\n"
"s_lshl_b32 s13, s13, 5\n"
"s_lshl_b32 s14, s14, 5\n"
"s_sub_u32 s80, s14, s13\n"
"s_lshl_b32 s14, s64, 8\n"
"s_add_u32 s13, s13, s14\n"
"s_lshl_b32 s14, s62, 6\n"
"s_add_u32 s13, s13, s14\n"
"v_lshlrev_b32 v204, 10, v209\n"
"v_lshl_add_u32 v204, v208, 3, v204\n"
"v_add_u32 v234, s13, v204\n"
"s_lshl_b32 s12, s8, 14\n"
"v_add_u32 v244, s12, v254\n"
"buffer_load_dwordx2 v[230:231], v228, s[92:95], s71 offen sc1\n"
"s_add_u32 s82, s65, 0\n"
"s_add_u32 s83, s82, 0x4000\n"
"s_add_u32 m0, s67, 0x18000\n"
"s_nop 0\n"
"buffer_load_dwordx4 v216, s[24:27], s82 offen lds\n"
"buffer_load_dwordx4 v217, s[24:27], s82 offen offset:1024 lds\n"
"buffer_load_dwordx4 v218, s[24:27], s82 offen offset:2048 lds\n"
"buffer_load_dwordx4 v219, s[24:27], s82 offen offset:3072 lds\n"
"s_add_u32 m0, s67, 0x19000\n"
"s_nop 0\n"
"buffer_load_dwordx4 v216, s[24:27], s83 offen lds\n"
"buffer_load_dwordx4 v217, s[24:27], s83 offen offset:1024 lds\n"
"buffer_load_dwordx4 v218, s[24:27], s83 offen offset:2048 lds\n"
"buffer_load_dwordx4 v219, s[24:27], s83 offen offset:3072 lds\n"
"s_waitcnt vmcnt(0)\n"
"v_cmp_eq_u64 vcc, v[230:231], v[250:251]\n"
"s_nop 1\n"
"s_cmp_eq_u64 vcc, exec\n"
"s_cbranch_scc1 .Lpok_5051_%=\n"
"s_mov_b32 s15, 0\n"
".Lploop_5052_%=:\n"
"s_sleep 4\n"
"buffer_load_dwordx2 v[230:231], v228, s[92:95], s71 offen sc1\n"
"s_waitcnt vmcnt(0)\n"
"v_cmp_eq_u64 vcc, v[230:231], v[250:251]\n"
"s_add_u32 s15, s15, 1\n"
"s_nop 0\n"
"s_cmp_eq_u64 vcc, exec\n"
"s_cbranch_scc1 .Lpok_5051_%=\n"
"s_cmp_lt_u32 s15, 0x20000\n"
"s_cbranch_scc1 .Lploop_5052_%=\n"
"s_mov_b32 s15, 0x80\n"
"s_mov_b64 exec, 1\n"
"buffer_store_dwordx2 v[250:251], v249, s[92:95], s15 offen sc1\n"
"s_mov_b64 exec, -1\n"
".Lpok_5051_%=:\n"
"buffer_load_dwordx2 a[64:65], v212, s[36:39], s68 offen sc1\n"
"buffer_load_dwordx2 a[68:69], v213, s[36:39], s68 offen sc1\n"
"s_add_u32 s68, s68, 0x10000\n"
"buffer_load_dwordx2 a[66:67], v212, s[36:39], s68 offen sc1\n"
"buffer_load_dwordx2 a[70:71], v213, s[36:39], s68 offen sc1\n"
"s_add_u32 s68, s68, 0x10000\n"
"buffer_load_dwordx2 a[72:73], v212, s[36:39], s68 offen sc1\n"
"buffer_load_dwordx2 a[76:77], v213, s[36:39], s68 offen sc1\n"
"s_add_u32 s68, s68, 0x10000\n"
"buffer_load_dwordx2 a[74:75], v212, s[36:39], s68 offen sc1\n"
"buffer_load_dwordx2 a[78:79], v213, s[36:39], s68 offen sc1\n"
"s_add_u32 s68, s68, 0x10000\n"
"buffer_load_dwordx2 a[80:81], v212, s[36:39], s68 offen sc1\n"
"buffer_load_dwordx2 a[84:85], v213, s[36:39], s68 offen sc1\n"
"s_add_u32 s68, s68, 0x10000\n"
"buffer_load_dwordx2 a[82:83], v212, s[36:39], s68 offen sc1\n"
"buffer_load_dwordx2 a[86:87], v213, s[36:39], s68 offen sc1\n"
"s_add_u32 s68, s68, 0x10000\n"
"buffer_load_dwordx2 a[88:89], v212, s[36:39], s68 offen sc1\n"
"buffer_load_dwordx2 a[92:93], v213, s[36:39], s68 offen sc1\n"
"s_add_u32 s68, s68, 0x10000\n"
"buffer_load_dwordx2 a[90:91], v212, s[36:39], s68 offen sc1\n"
"buffer_load_dwordx2 a[94:95], v213, s[36:39], s68 offen sc1\n"
"s_add_u32 s68, s68, 0x10000\n"
"v_mov_b32 v0, 0\n"
"v_mov_b32 v1, 0\n"
"v_mov_b32 v2, 0\n"
"v_mov_b32 v3, 0\n"
"v_mov_b32 v4, 0\n"
"v_mov_b32 v5, 0\n"
"v_mov_b32 v6, 0\n"
"v_mov_b32 v7, 0\n"
"v_mov_b32 v8, 0\n"
"v_mov_b32 v9, 0\n"
"v_mov_b32 v10, 0\n"
"v_mov_b32 v11, 0\n"
"v_mov_b32 v12, 0\n"
"v_mov_b32 v13, 0\n"
"v_mov_b32 v14, 0\n"
"v_mov_b32 v15, 0\n"
"v_mov_b32 v16, 0\n"
"v_mov_b32 v17, 0\n"
"v_mov_b32 v18, 0\n"
"v_mov_b32 v19, 0\n"
"v_mov_b32 v20, 0\n"
"v_mov_b32 v21, 0\n"
"v_mov_b32 v22, 0\n"
"v_mov_b32 v23, 0\n"
"v_mov_b32 v24, 0\n"
"v_mov_b32 v25, 0\n"
"v_mov_b32 v26, 0\n"
"v_mov_b32 v27, 0\n"
"v_mov_b32 v28, 0\n"
"v_mov_b32 v29, 0\n"
"v_mov_b32 v30, 0\n"
"v_mov_b32 v31, 0\n"
"v_mov_b32 v32, 0\n"
"v_mov_b32 v33, 0\n"
"v_mov_b32 v34, 0\n"
"v_mov_b32 v35, 0\n"
"v_mov_b32 v36, 0\n"
"v_mov_b32 v37, 0\n"
"v_mov_b32 v38, 0\n"
"v_mov_b32 v39, 0\n"
"v_mov_b32 v40, 0\n"
"v_mov_b32 v41, 0\n"
"v_mov_b32 v42, 0\n"
"v_mov_b32 v43, 0\n"
"v_mov_b32 v44, 0\n"
"v_mov_b32 v45, 0\n"
"v_mov_b32 v46, 0\n"
"v_mov_b32 v47, 0\n"
"v_mov_b32 v48, 0\n"
"v_mov_b32 v49, 0\n"
"v_mov_b32 v50, 0\n"
"v_mov_b32 v51, 0\n"
"v_mov_b32 v52, 0\n"
"v_mov_b32 v53, 0\n"
"v_mov_b32 v54, 0\n"
"v_mov_b32 v55, 0\n"
"v_mov_b32 v56, 0\n"
"v_mov_b32 v57, 0\n"
"v_mov_b32 v58, 0\n"
"v_mov_b32 v59, 0\n"
"v_mov_b32 v60, 0\n"
"v_mov_b32 v61, 0\n"
"v_mov_b32 v62, 0\n"
"v_mov_b32 v63, 0\n"
"v_accvgpr_write_b32 a0, 0\n"
"v_accvgpr_write_b32 a1, 0\n"
"v_accvgpr_write_b32 a2, 0\n"
"v_accvgpr_write_b32 a3, 0\n"
"v_accvgpr_write_b32 a4, 0\n"
"v_accvgpr_write_b32 a5, 0\n"
"v_accvgpr_write_b32 a6, 0\n"
"v_accvgpr_write_b32 a7, 0\n"
"v_accvgpr_write_b32 a8, 0\n"
"v_accvgpr_write_b32 a9, 0\n"
"v_accvgpr_write_b32 a10, 0\n"
"v_accvgpr_write_b32 a11, 0\n"
"v_accvgpr_write_b32 a12, 0\n"
"v_accvgpr_write_b32 a13, 0\n"
"v_accvgpr_write_b32 a14, 0\n"
"v_accvgpr_write_b32 a15, 0\n"
"v_accvgpr_write_b32 a16, 0\n"
"v_accvgpr_write_b32 a17, 0\n"
"v_accvgpr_write_b32 a18, 0\n"
"v_accvgpr_write_b32 a19, 0\n"
"v_accvgpr_write_b32 a20, 0\n"
"v_accvgpr_write_b32 a21, 0\n"
"v_accvgpr_write_b32 a22, 0\n"
"v_accvgpr_write_b32 a23, 0\n"
"v_accvgpr_write_b32 a24, 0\n"
"v_accvgpr_write_b32 a25, 0\n"
"v_accvgpr_write_b32 a26, 0\n"
"v_accvgpr_write_b32 a27, 0\n"
"v_accvgpr_write_b32 a28, 0\n"
"v_accvgpr_write_b32 a29, 0\n"
"v_accvgpr_write_b32 a30, 0\n"
"v_accvgpr_write_b32 a31, 0\n"
"v_accvgpr_write_b32 a32, 0\n"
"v_accvgpr_write_b32 a33, 0\n"
"v_accvgpr_write_b32 a34, 0\n"
"v_accvgpr_write_b32 a35, 0\n"
"v_accvgpr_write_b32 a36, 0\n"
"v_accvgpr_write_b32 a37, 0\n"
"v_accvgpr_write_b32 a38, 0\n"
"v_accvgpr_write_b32 a39, 0\n"
"v_accvgpr_write_b32 a40, 0\n"
"v_accvgpr_write_b32 a41, 0\n"
"v_accvgpr_write_b32 a42, 0\n"
"v_accvgpr_write_b32 a43, 0\n"
"v_accvgpr_write_b32 a44, 0\n"
"v_accvgpr_write_b32 a45, 0\n"
"v_accvgpr_write_b32 a46, 0\n"
"v_accvgpr_write_b32 a47, 0\n"
"v_accvgpr_write_b32 a48, 0\n"
"v_accvgpr_write_b32 a49, 0\n"
"v_accvgpr_write_b32 a50, 0\n"
"v_accvgpr_write_b32 a51, 0\n"
"v_accvgpr_write_b32 a52, 0\n"
"v_accvgpr_write_b32 a53, 0\n"
"v_accvgpr_write_b32 a54, 0\n"
"v_accvgpr_write_b32 a55, 0\n"
"v_accvgpr_write_b32 a56, 0\n"
"v_accvgpr_write_b32 a57, 0\n"
"v_accvgpr_write_b32 a58, 0\n"
"v_accvgpr_write_b32 a59, 0\n"
"v_accvgpr_write_b32 a60, 0\n"
"v_accvgpr_write_b32 a61, 0\n"
"v_accvgpr_write_b32 a62, 0\n"
"v_accvgpr_write_b32 a63, 0\n"
"v_accvgpr_write_b32 a128, 0\n"
"v_accvgpr_write_b32 a129, 0\n"
"v_accvgpr_write_b32 a130, 0\n"
"v_accvgpr_write_b32 a131, 0\n"
"v_accvgpr_write_b32 a132, 0\n"
"v_accvgpr_write_b32 a133, 0\n"
"v_accvgpr_write_b32 a134, 0\n"
"v_accvgpr_write_b32 a135, 0\n"
"v_accvgpr_write_b32 a136, 0\n"
"v_accvgpr_write_b32 a137, 0\n"
"v_accvgpr_write_b32 a138, 0\n"
"v_accvgpr_write_b32 a139, 0\n"
"v_accvgpr_write_b32 a140, 0\n"
"v_accvgpr_write_b32 a141, 0\n"
"v_accvgpr_write_b32 a142, 0\n"
"v_accvgpr_write_b32 a143, 0\n"
"ds_write_b128 v244, a[0:3]\n"
"ds_write_b128 v244, a[16:19] offset:4096\n"
"ds_write_b128 v244, a[32:35] offset:8192\n"
"ds_write_b128 v244, a[48:51] offset:12288\n"
"ds_write_b128 v244, a[4:7] offset:1024\n"
"ds_write_b128 v244, a[20:23] offset:5120\n"
"ds_write_b128 v244, a[36:39] offset:9216\n"
"ds_write_b128 v244, a[52:55] offset:13312\n"
"ds_write_b128 v244, a[8:11] offset:2048\n"
"ds_write_b128 v244, a[24:27] offset:6144\n"
"ds_write_b128 v244, a[40:43] offset:10240\n"
"ds_write_b128 v244, a[56:59] offset:14336\n"
"s_branch .Lmerge_38_%=\n"
".Lslow_48_%=:\n"
"s_waitcnt vmcnt(0) lgkmcnt(0)\n"
"s_barrier\n"
"s_load_dwordx2 s[62:63], %[ka], 16\n"
"s_waitcnt lgkmcnt(0)\n"
"s_mov_b32 s16, s62\n"
"s_and_b32 s17, s63, 0xffff\n"
"s_mov_b32 s18, 0x400000\n"
"s_mov_b32 s19, 0x20000\n"
"s_add_u32 s24, s60, 0x100000\n"
"s_addc_u32 s25, s61, 0\n"
"s_and_b32 s25, s25, 0xffff\n"
"s_mov_b32 s26, 0x200000\n"
"s_mov_b32 s27, 0x20000\n"
"s_add_u32 s28, s60, 0x400000\n"
"s_addc_u32 s29, s61, 0\n"
"s_and_b32 s29, s29, 0xffff\n"
"s_mov_b32 s30, 0x1000000\n"
"s_mov_b32 s31, 0x20000\n"
"s_lshl_b32 s12, s11, 3\n"
"s_sub_u32 s13, 0x78, s12\n"
"s_cmp_lt_u32 s8, 2\n"
"s_cselect_b32 s77, s12, s13\n"
"s_and_b32 s12, s8, 1\n"
"s_lshl_b32 s12, s12, 2\n"
"s_add_u32 s77, s77, s12\n"
"s_lshl_b32 s80, s77, 10\n"
"v_lshrrev_b32 v212, 4, v255\n"
"v_and_b32 v213, 15, v255\n"
"v_lshrrev_b32 v214, 2, v213\n"
"v_and_b32 v215, 3, v255\n"
"s_lshl_b32 s12, s8, 5\n"
"v_add_u32 v208, s12, v212\n"
"v_lshlrev_b32 v208, 13, v208\n"
"s_lshl_b32 s13, s10, 8\n"
"v_add_u32 v208, s13, v208\n"
"v_lshlrev_b32 v211, 1, v212\n"
"v_xor_b32 v210, v213, v211\n"
"v_lshl_add_u32 v188, v210, 4, v208\n"
"v_add_u32 v189, 0x7c00, v188\n"
"v_xor_b32 v210, 8, v210\n"
"v_lshl_add_u32 v190, v210, 4, v208\n"
"v_add_u32 v191, 0x17400, v190\n"
"v_add_u32 v190, 0xf800, v190\n"
"v_lshrrev_b32 v208, 3, v255\n"
"v_add_u32 v208, s12, v208\n"
"v_mul_u32_u24 v208, 0xac0, v208\n"
"s_mul_i32 s13, s10, 0x60\n"
"v_add_u32 v208, s13, v208\n"
"v_and_b32 v210, 7, v255\n"
"v_and_b32 v211, 1, v212\n"
"v_lshlrev_b32 v211, 1, v211\n"
"v_xor_b32 v210, v210, v211\n"
"v_lshl_add_u32 v192, v210, 4, v208\n"
"v_add_u32 v209, 2, v210\n"
"v_and_b32 v209, 8, v209\n"
"v_lshl_add_u32 v192, v209, 27, v192\n"
"v_xor_b32 v210, 4, v210\n"
"v_lshl_add_u32 v193, v210, 4, v208\n"
"v_add_u32 v193, 0x5200, v193\n"
"v_add_u32 v209, 2, v210\n"
"v_and_b32 v209, 8, v209\n"
"v_lshl_add_u32 v193, v209, 27, v193\n"
"v_add_u32 v194, 0xa400, v192\n"
"v_add_u32 v195, 0xa400, v193\n"
"s_mov_b32 s74, 0\n"
"s_mov_b32 s75, 0x20000\n"
"s_mov_b32 s76, 0\n"
"s_lshl_b32 s78, s8, 13\n"
"s_lshl_b32 s79, s8, 12\n"
"s_add_u32 s79, s79, 0x20000\n"
"v_lshl_add_u32 v208, v212, 3, v214\n"
"v_lshlrev_b32 v209, 8, v208\n"
"v_lshl_add_u32 v209, v215, 3, v209\n"
"v_and_b32 v210, 1, v212\n"
"v_lshl_or_b32 v210, v210, 2, v214\n"
"v_xor_b32 v211, 0, v210\n"
"v_lshl_add_u32 v196, v211, 5, v209\n"
"v_xor_b32 v211, 1, v210\n"
"v_lshl_add_u32 v197, v211, 5, v209\n"
"v_xor_b32 v211, 2, v210\n"
"v_lshl_add_u32 v198, v211, 5, v209\n"
"v_xor_b32 v211, 3, v210\n"
"v_lshl_add_u32 v199, v211, 5, v209\n"
"v_xor_b32 v211, 4, v210\n"
"v_lshl_add_u32 v200, v211, 5, v209\n"
"v_xor_b32 v211, 5, v210\n"
"v_lshl_add_u32 v201, v211, 5, v209\n"
"v_xor_b32 v211, 6, v210\n"
"v_lshl_add_u32 v202, v211, 5, v209\n"
"v_xor_b32 v211, 7, v210\n"
"v_lshl_add_u32 v203, v211, 5, v209\n"
"v_lshlrev_b32 v209, 7, v208\n"
"v_lshl_add_u32 v209, v215, 3, v209\n"
"v_add_u32 v209, 0x20000, v209\n"
"v_and_b32 v210, 1, v212\n"
"v_lshlrev_b32 v210, 1, v210\n"
"v_lshrrev_b32 v211, 1, v214\n"
"v_or_b32 v210, v210, v211\n"
"v_xor_b32 v211, 0, v210\n"
"v_lshl_add_u32 v204, v211, 5, v209\n"
"v_xor_b32 v211, 1, v210\n"
"v_lshl_add_u32 v205, v211, 5, v209\n"
"v_xor_b32 v211, 2, v210\n"
"v_lshl_add_u32 v206, v211, 5, v209\n"
"v_and_b32 v207, 7, v255\n"
"v_lshlrev_b32 v207, 3, v207\n"
"v_mov_b32 v0, 0\n"
"v_mov_b32 v1, 0\n"
"v_mov_b32 v2, 0\n"
"v_mov_b32 v3, 0\n"
"v_mov_b32 v4, 0\n"
"v_mov_b32 v5, 0\n"
"v_mov_b32 v6, 0\n"
"v_mov_b32 v7, 0\n"
"v_mov_b32 v8, 0\n"
"v_mov_b32 v9, 0\n"
"v_mov_b32 v10, 0\n"
"v_mov_b32 v11, 0\n"
"v_mov_b32 v12, 0\n"
"v_mov_b32 v13, 0\n"
"v_mov_b32 v14, 0\n"
"v_mov_b32 v15, 0\n"
"v_mov_b32 v16, 0\n"
"v_mov_b32 v17, 0\n"
"v_mov_b32 v18, 0\n"
"v_mov_b32 v19, 0\n"
"v_mov_b32 v20, 0\n"
"v_mov_b32 v21, 0\n"
"v_mov_b32 v22, 0\n"
"v_mov_b32 v23, 0\n"
"v_mov_b32 v24, 0\n"
"v_mov_b32 v25, 0\n"
"v_mov_b32 v26, 0\n"
"v_mov_b32 v27, 0\n"
"v_mov_b32 v28, 0\n"
"v_mov_b32 v29, 0\n"
"v_mov_b32 v30, 0\n"
"v_mov_b32 v31, 0\n"
"v_mov_b32 v32, 0\n"
"v_mov_b32 v33, 0\n"
"v_mov_b32 v34, 0\n"
"v_mov_b32 v35, 0\n"
"v_mov_b32 v36, 0\n"
"v_mov_b32 v37, 0\n"
"v_mov_b32 v38, 0\n"
"v_mov_b32 v39, 0\n"
"v_mov_b32 v40, 0\n"
"v_mov_b32 v41, 0\n"
"v_mov_b32 v42, 0\n"
"v_mov_b32 v43, 0\n"
"v_mov_b32 v44, 0\n"
"v_mov_b32 v45, 0\n"
"v_mov_b32 v46, 0\n"
"v_mov_b32 v47, 0\n"
"v_mov_b32 v48, 0\n"
"v_mov_b32 v49, 0\n"
"v_mov_b32 v50, 0\n"
"v_mov_b32 v51, 0\n"
"v_mov_b32 v52, 0\n"
"v_mov_b32 v53, 0\n"
"v_mov_b32 v54, 0\n"
"v_mov_b32 v55, 0\n"
"v_mov_b32 v56, 0\n"
"v_mov_b32 v57, 0\n"
"v_mov_b32 v58, 0\n"
"v_mov_b32 v59, 0\n"
"v_mov_b32 v60, 0\n"
"v_mov_b32 v61, 0\n"
"v_mov_b32 v62, 0\n"
"v_mov_b32 v63, 0\n"
"v_mov_b32 v64, 0\n"
"v_mov_b32 v65, 0\n"
"v_mov_b32 v66, 0\n"
"v_mov_b32 v67, 0\n"
"v_mov_b32 v68, 0\n"
"v_mov_b32 v69, 0\n"
"v_mov_b32 v70, 0\n"
"v_mov_b32 v71, 0\n"
"v_mov_b32 v72, 0\n"
"v_mov_b32 v73, 0\n"
"v_mov_b32 v74, 0\n"
"v_mov_b32 v75, 0\n"
"v_mov_b32 v76, 0\n"
"v_mov_b32 v77, 0\n"
"v_mov_b32 v78, 0\n"
"v_mov_b32 v79, 0\n"
"v_mov_b32 v80, 0\n"
"v_mov_b32 v81, 0\n"
"v_mov_b32 v82, 0\n"
"v_mov_b32 v83, 0\n"
"v_mov_b32 v84, 0\n"
"v_mov_b32 v85, 0\n"
"v_mov_b32 v86, 0\n"
"v_mov_b32 v87, 0\n"
"v_mov_b32 v88, 0\n"
"v_mov_b32 v89, 0\n"
"v_mov_b32 v90, 0\n"
"v_mov_b32 v91, 0\n"
"v_mov_b32 v92, 0\n"
"v_mov_b32 v93, 0\n"
"v_mov_b32 v94, 0\n"
"v_mov_b32 v95, 0\n"
"v_mov_b32 v96, 0\n"
"v_mov_b32 v97, 0\n"
"v_mov_b32 v98, 0\n"
"v_mov_b32 v99, 0\n"
"v_mov_b32 v100, 0\n"
"v_mov_b32 v101, 0\n"
"v_mov_b32 v102, 0\n"
"v_mov_b32 v103, 0\n"
"v_mov_b32 v104, 0\n"
"v_mov_b32 v105, 0\n"
"v_mov_b32 v106, 0\n"
"v_mov_b32 v107, 0\n"
"v_mov_b32 v108, 0\n"
"v_mov_b32 v109, 0\n"
"v_mov_b32 v110, 0\n"
"v_mov_b32 v111, 0\n"
"v_mov_b32 v112, 0\n"
"v_mov_b32 v113, 0\n"
"v_mov_b32 v114, 0\n"
"v_mov_b32 v115, 0\n"
"v_mov_b32 v116, 0\n"
"v_mov_b32 v117, 0\n"
"v_mov_b32 v118, 0\n"
"v_mov_b32 v119, 0\n"
"v_mov_b32 v120, 0\n"
"v_mov_b32 v121, 0\n"
"v_mov_b32 v122, 0\n"
"v_mov_b32 v123, 0\n"
"v_mov_b32 v124, 0\n"
"v_mov_b32 v125, 0\n"
"v_mov_b32 v126, 0\n"
"v_mov_b32 v127, 0\n"
"s_mov_b32 s62, 0\n"
".Lslowq_57_%=:\n"
"s_add_u32 m0, s78, 0\n"
"s_nop 0\n"
"buffer_load_dwordx4 v188, s[16:19], s74 offen lds\n"
"buffer_load_dwordx4 v189, s[16:19], s74 offen offset:1024 lds\n"
"buffer_load_dwordx4 v190, s[16:19], s74 offen offset:2048 lds\n"
"buffer_load_dwordx4 v191, s[16:19], s74 offen offset:3072 lds\n"
"s_add_u32 m0, s78, 0x1000\n"
"s_nop 0\n"
"buffer_load_dwordx4 v188, s[16:19], s75 offen lds\n"
"buffer_load_dwordx4 v189, s[16:19], s75 offen offset:1024 lds\n"
"buffer_load_dwordx4 v190, s[16:19], s75 offen offset:2048 lds\n"
"buffer_load_dwordx4 v191, s[16:19], s75 offen offset:3072 lds\n"
"s_add_u32 s74, s74, 0x100000\n"
"s_add_u32 s75, s75, 0x100000\n"
"buffer_load_dwordx4 a[0:3], v254, s[24:27], s80 offen sc1\n"
"buffer_load_dwordx4 a[4:7], v254, s[24:27], s80 offen offset:1024 sc1\n"
"buffer_load_dwordx4 a[8:11], v254, s[24:27], s80 offen offset:2048 sc1\n"
"buffer_load_dwordx4 a[12:15], v254, s[24:27], s80 offen offset:3072 sc1\n"
"s_add_u32 s80, s80, 0x20000\n"
"buffer_load_dwordx4 a[16:19], v254, s[24:27], s80 offen sc1\n"
"buffer_load_dwordx4 a[20:23], v254, s[24:27], s80 offen offset:1024 sc1\n"
"buffer_load_dwordx4 a[24:27], v254, s[24:27], s80 offen offset:2048 sc1\n"
"buffer_load_dwordx4 a[28:31], v254, s[24:27], s80 offen offset:3072 sc1\n"
"s_add_u32 s80, s80, 0x20000\n"
"buffer_load_dwordx4 a[32:35], v254, s[24:27], s80 offen sc1\n"
"buffer_load_dwordx4 a[36:39], v254, s[24:27], s80 offen offset:1024 sc1\n"
"buffer_load_dwordx4 a[40:43], v254, s[24:27], s80 offen offset:2048 sc1\n"
"buffer_load_dwordx4 a[44:47], v254, s[24:27], s80 offen offset:3072 sc1\n"
"s_add_u32 s80, s80, 0x20000\n"
"buffer_load_dwordx4 a[48:51], v254, s[24:27], s80 offen sc1\n"
"buffer_load_dwordx4 a[52:55], v254, s[24:27], s80 offen offset:1024 sc1\n"
"buffer_load_dwordx4 a[56:59], v254, s[24:27], s80 offen offset:2048 sc1\n"
"buffer_load_dwordx4 a[60:63], v254, s[24:27], s80 offen offset:3072 sc1\n"
"s_add_u32 s80, s80, 0x20000\n"
"s_waitcnt vmcnt(0)\n"
"s_barrier\n"
"ds_read_b64_tr_b16 a[128:129], v196\n"
"ds_read_b64_tr_b16 a[130:131], v196 offset:1024\n"
"ds_read_b64_tr_b16 a[132:133], v197\n"
"ds_read_b64_tr_b16 a[134:135], v197 offset:1024\n"
"ds_read_b64_tr_b16 a[136:137], v198\n"
"ds_read_b64_tr_b16 a[138:139], v198 offset:1024\n"
"ds_read_b64_tr_b16 a[140:141], v199\n"
"ds_read_b64_tr_b16 a[142:143], v199 offset:1024\n"
"s_waitcnt lgkmcnt(0)\n"
"ds_read_b64_tr_b16 a[144:145], v200\n"
"ds_read_b64_tr_b16 a[146:147], v200 offset:1024\n"
"ds_read_b64_tr_b16 a[148:149], v201\n"
"ds_read_b64_tr_b16 a[150:151], v201 offset:1024\n"
"ds_read_b64_tr_b16 a[152:153], v202\n"
"ds_read_b64_tr_b16 a[154:155], v202 offset:1024\n"
"ds_read_b64_tr_b16 a[156:157], v203\n"
"ds_read_b64_tr_b16 a[158:159], v203 offset:1024\n"
"s_waitcnt lgkmcnt(0)\n"
"v_mfma_f32_16x16x32_bf16 v[0:3], a[128:131], a[0:3], v[0:3]\n"
"v_mfma_f32_16x16x32_bf16 v[8:11], a[128:131], a[4:7], v[8:11]\n"
"v_mfma_f32_16x16x32_bf16 v[16:19], a[128:131], a[8:11], v[16:19]\n"
"v_mfma_f32_16x16x32_bf16 v[24:27], a[128:131], a[12:15], v[24:27]\n"
"v_mfma_f32_16x16x32_bf16 v[4:7], a[132:135], a[0:3], v[4:7]\n"
"v_mfma_f32_16x16x32_bf16 v[12:15], a[132:135], a[4:7], v[12:15]\n"
"v_mfma_f32_16x16x32_bf16 v[20:23], a[132:135], a[8:11], v[20:23]\n"
"v_mfma_f32_16x16x32_bf16 v[28:31], a[132:135], a[12:15], v[28:31]\n"
"v_mfma_f32_16x16x32_bf16 v[32:35], a[136:139], a[0:3], v[32:35]\n"
"v_mfma_f32_16x16x32_bf16 v[40:43], a[136:139], a[4:7], v[40:43]\n"
"v_mfma_f32_16x16x32_bf16 v[48:51], a[136:139], a[8:11], v[48:51]\n"
"v_mfma_f32_16x16x32_bf16 v[56:59], a[136:139], a[12:15], v[56:59]\n"
"v_mfma_f32_16x16x32_bf16 v[36:39], a[140:143], a[0:3], v[36:39]\n"
"v_mfma_f32_16x16x32_bf16 v[44:47], a[140:143], a[4:7], v[44:47]\n"
"v_mfma_f32_16x16x32_bf16 v[52:55], a[140:143], a[8:11], v[52:55]\n"
"v_mfma_f32_16x16x32_bf16 v[60:63], a[140:143], a[12:15], v[60:63]\n"
"v_mfma_f32_16x16x32_bf16 v[64:67], a[144:147], a[0:3], v[64:67]\n"
"v_mfma_f32_16x16x32_bf16 v[72:75], a[144:147], a[4:7], v[72:75]\n"
"v_mfma_f32_16x16x32_bf16 v[80:83], a[144:147], a[8:11], v[80:83]\n"
"v_mfma_f32_16x16x32_bf16 v[88:91], a[144:147], a[12:15], v[88:91]\n"
"v_mfma_f32_16x16x32_bf16 v[68:71], a[148:151], a[0:3], v[68:71]\n"
"v_mfma_f32_16x16x32_bf16 v[76:79], a[148:151], a[4:7], v[76:79]\n"
"v_mfma_f32_16x16x32_bf16 v[84:87], a[148:151], a[8:11], v[84:87]\n"
"v_mfma_f32_16x16x32_bf16 v[92:95], a[148:151], a[12:15], v[92:95]\n"
"v_mfma_f32_16x16x32_bf16 v[96:99], a[152:155], a[0:3], v[96:99]\n"
"v_mfma_f32_16x16x32_bf16 v[104:107], a[152:155], a[4:7], v[104:107]\n"
"v_mfma_f32_16x16x32_bf16 v[112:115], a[152:155], a[8:11], v[112:115]\n"
"v_mfma_f32_16x16x32_bf16 v[120:123], a[152:155], a[12:15], v[120:123]\n"
"v_mfma_f32_16x16x32_bf16 v[100:103], a[156:159], a[0:3], v[100:103]\n"
"v_mfma_f32_16x16x32_bf16 v[108:111], a[156:159], a[4:7], v[108:111]\n"
"v_mfma_f32_16x16x32_bf16 v[116:119], a[156:159], a[8:11], v[116:119]\n"
"v_mfma_f32_16x16x32_bf16 v[124:127], a[156:159], a[12:15], v[124:127]\n"
"ds_read_b64_tr_b16 a[128:129], v196 offset:8192\n"
"ds_read_b64_tr_b16 a[130:131], v196 offset:9216\n"
"ds_read_b64_tr_b16 a[132:133], v197 offset:8192\n"
"ds_read_b64_tr_b16 a[134:135], v197 offset:9216\n"
"ds_read_b64_tr_b16 a[136:137], v198 offset:8192\n"
"ds_read_b64_tr_b16 a[138:139], v198 offset:9216\n"
"ds_read_b64_tr_b16 a[140:141], v199 offset:8192\n"
"ds_read_b64_tr_b16 a[142:143], v199 offset:9216\n"
"s_waitcnt lgkmcnt(0)\n"
"ds_read_b64_tr_b16 a[144:145], v200 offset:8192\n"
"ds_read_b64_tr_b16 a[146:147], v200 offset:9216\n"
"ds_read_b64_tr_b16 a[148:149], v201 offset:8192\n"
"ds_read_b64_tr_b16 a[150:151], v201 offset:9216\n"
"ds_read_b64_tr_b16 a[152:153], v202 offset:8192\n"
"ds_read_b64_tr_b16 a[154:155], v202 offset:9216\n"
"ds_read_b64_tr_b16 a[156:157], v203 offset:8192\n"
"ds_read_b64_tr_b16 a[158:159], v203 offset:9216\n"
"s_waitcnt lgkmcnt(0)\n"
"v_mfma_f32_16x16x32_bf16 v[0:3], a[128:131], a[16:19], v[0:3]\n"
"v_mfma_f32_16x16x32_bf16 v[8:11], a[128:131], a[20:23], v[8:11]\n"
"v_mfma_f32_16x16x32_bf16 v[16:19], a[128:131], a[24:27], v[16:19]\n"
"v_mfma_f32_16x16x32_bf16 v[24:27], a[128:131], a[28:31], v[24:27]\n"
"v_mfma_f32_16x16x32_bf16 v[4:7], a[132:135], a[16:19], v[4:7]\n"
"v_mfma_f32_16x16x32_bf16 v[12:15], a[132:135], a[20:23], v[12:15]\n"
"v_mfma_f32_16x16x32_bf16 v[20:23], a[132:135], a[24:27], v[20:23]\n"
"v_mfma_f32_16x16x32_bf16 v[28:31], a[132:135], a[28:31], v[28:31]\n"
"v_mfma_f32_16x16x32_bf16 v[32:35], a[136:139], a[16:19], v[32:35]\n"
"v_mfma_f32_16x16x32_bf16 v[40:43], a[136:139], a[20:23], v[40:43]\n"
"v_mfma_f32_16x16x32_bf16 v[48:51], a[136:139], a[24:27], v[48:51]\n"
"v_mfma_f32_16x16x32_bf16 v[56:59], a[136:139], a[28:31], v[56:59]\n"
"v_mfma_f32_16x16x32_bf16 v[36:39], a[140:143], a[16:19], v[36:39]\n"
"v_mfma_f32_16x16x32_bf16 v[44:47], a[140:143], a[20:23], v[44:47]\n"
"v_mfma_f32_16x16x32_bf16 v[52:55], a[140:143], a[24:27], v[52:55]\n"
"v_mfma_f32_16x16x32_bf16 v[60:63], a[140:143], a[28:31], v[60:63]\n"
"v_mfma_f32_16x16x32_bf16 v[64:67], a[144:147], a[16:19], v[64:67]\n"
"v_mfma_f32_16x16x32_bf16 v[72:75], a[144:147], a[20:23], v[72:75]\n"
"v_mfma_f32_16x16x32_bf16 v[80:83], a[144:147], a[24:27], v[80:83]\n"
"v_mfma_f32_16x16x32_bf16 v[88:91], a[144:147], a[28:31], v[88:91]\n"
"v_mfma_f32_16x16x32_bf16 v[68:71], a[148:151], a[16:19], v[68:71]\n"
"v_mfma_f32_16x16x32_bf16 v[76:79], a[148:151], a[20:23], v[76:79]\n"
"v_mfma_f32_16x16x32_bf16 v[84:87], a[148:151], a[24:27], v[84:87]\n"
"v_mfma_f32_16x16x32_bf16 v[92:95], a[148:151], a[28:31], v[92:95]\n"
"v_mfma_f32_16x16x32_bf16 v[96:99], a[152:155], a[16:19], v[96:99]\n"
"v_mfma_f32_16x16x32_bf16 v[104:107], a[152:155], a[20:23], v[104:107]\n"
"v_mfma_f32_16x16x32_bf16 v[112:115], a[152:155], a[24:27], v[112:115]\n"
"v_mfma_f32_16x16x32_bf16 v[120:123], a[152:155], a[28:31], v[120:123]\n"
"v_mfma_f32_16x16x32_bf16 v[100:103], a[156:159], a[16:19], v[100:103]\n"
"v_mfma_f32_16x16x32_bf16 v[108:111], a[156:159], a[20:23], v[108:111]\n"
"v_mfma_f32_16x16x32_bf16 v[116:119], a[156:159], a[24:27], v[116:119]\n"
"v_mfma_f32_16x16x32_bf16 v[124:127], a[156:159], a[28:31], v[124:127]\n"
"ds_read_b64_tr_b16 a[128:129], v196 offset:16384\n"
"ds_read_b64_tr_b16 a[130:131], v196 offset:17408\n"
"ds_read_b64_tr_b16 a[132:133], v197 offset:16384\n"
"ds_read_b64_tr_b16 a[134:135], v197 offset:17408\n"
"ds_read_b64_tr_b16 a[136:137], v198 offset:16384\n"
"ds_read_b64_tr_b16 a[138:139], v198 offset:17408\n"
"ds_read_b64_tr_b16 a[140:141], v199 offset:16384\n"
"ds_read_b64_tr_b16 a[142:143], v199 offset:17408\n"
"s_waitcnt lgkmcnt(0)\n"
"ds_read_b64_tr_b16 a[144:145], v200 offset:16384\n"
"ds_read_b64_tr_b16 a[146:147], v200 offset:17408\n"
"ds_read_b64_tr_b16 a[148:149], v201 offset:16384\n"
"ds_read_b64_tr_b16 a[150:151], v201 offset:17408\n"
"ds_read_b64_tr_b16 a[152:153], v202 offset:16384\n"
"ds_read_b64_tr_b16 a[154:155], v202 offset:17408\n"
"ds_read_b64_tr_b16 a[156:157], v203 offset:16384\n"
"ds_read_b64_tr_b16 a[158:159], v203 offset:17408\n"
"s_waitcnt lgkmcnt(0)\n"
"v_mfma_f32_16x16x32_bf16 v[0:3], a[128:131], a[32:35], v[0:3]\n"
"v_mfma_f32_16x16x32_bf16 v[8:11], a[128:131], a[36:39], v[8:11]\n"
"v_mfma_f32_16x16x32_bf16 v[16:19], a[128:131], a[40:43], v[16:19]\n"
"v_mfma_f32_16x16x32_bf16 v[24:27], a[128:131], a[44:47], v[24:27]\n"
"v_mfma_f32_16x16x32_bf16 v[4:7], a[132:135], a[32:35], v[4:7]\n"
"v_mfma_f32_16x16x32_bf16 v[12:15], a[132:135], a[36:39], v[12:15]\n"
"v_mfma_f32_16x16x32_bf16 v[20:23], a[132:135], a[40:43], v[20:23]\n"
"v_mfma_f32_16x16x32_bf16 v[28:31], a[132:135], a[44:47], v[28:31]\n"
"v_mfma_f32_16x16x32_bf16 v[32:35], a[136:139], a[32:35], v[32:35]\n"
"v_mfma_f32_16x16x32_bf16 v[40:43], a[136:139], a[36:39], v[40:43]\n"
"v_mfma_f32_16x16x32_bf16 v[48:51], a[136:139], a[40:43], v[48:51]\n"
"v_mfma_f32_16x16x32_bf16 v[56:59], a[136:139], a[44:47], v[56:59]\n"
"v_mfma_f32_16x16x32_bf16 v[36:39], a[140:143], a[32:35], v[36:39]\n"
"v_mfma_f32_16x16x32_bf16 v[44:47], a[140:143], a[36:39], v[44:47]\n"
"v_mfma_f32_16x16x32_bf16 v[52:55], a[140:143], a[40:43], v[52:55]\n"
"v_mfma_f32_16x16x32_bf16 v[60:63], a[140:143], a[44:47], v[60:63]\n"
"v_mfma_f32_16x16x32_bf16 v[64:67], a[144:147], a[32:35], v[64:67]\n"
"v_mfma_f32_16x16x32_bf16 v[72:75], a[144:147], a[36:39], v[72:75]\n"
"v_mfma_f32_16x16x32_bf16 v[80:83], a[144:147], a[40:43], v[80:83]\n"
"v_mfma_f32_16x16x32_bf16 v[88:91], a[144:147], a[44:47], v[88:91]\n"
"v_mfma_f32_16x16x32_bf16 v[68:71], a[148:151], a[32:35], v[68:71]\n"
"v_mfma_f32_16x16x32_bf16 v[76:79], a[148:151], a[36:39], v[76:79]\n"
"v_mfma_f32_16x16x32_bf16 v[84:87], a[148:151], a[40:43], v[84:87]\n"
"v_mfma_f32_16x16x32_bf16 v[92:95], a[148:151], a[44:47], v[92:95]\n"
"v_mfma_f32_16x16x32_bf16 v[96:99], a[152:155], a[32:35], v[96:99]\n"
"v_mfma_f32_16x16x32_bf16 v[104:107], a[152:155], a[36:39], v[104:107]\n"
"v_mfma_f32_16x16x32_bf16 v[112:115], a[152:155], a[40:43], v[112:115]\n"
"v_mfma_f32_16x16x32_bf16 v[120:123], a[152:155], a[44:47], v[120:123]\n"
"v_mfma_f32_16x16x32_bf16 v[100:103], a[156:159], a[32:35], v[100:103]\n"
"v_mfma_f32_16x16x32_bf16 v[108:111], a[156:159], a[36:39], v[108:111]\n"
"v_mfma_f32_16x16x32_bf16 v[116:119], a[156:159], a[40:43], v[116:119]\n"
"v_mfma_f32_16x16x32_bf16 v[124:127], a[156:159], a[44:47], v[124:127]\n"
"ds_read_b64_tr_b16 a[128:129], v196 offset:24576\n"
"ds_read_b64_tr_b16 a[130:131], v196 offset:25600\n"
"ds_read_b64_tr_b16 a[132:133], v197 offset:24576\n"
"ds_read_b64_tr_b16 a[134:135], v197 offset:25600\n"
"ds_read_b64_tr_b16 a[136:137], v198 offset:24576\n"
"ds_read_b64_tr_b16 a[138:139], v198 offset:25600\n"
"ds_read_b64_tr_b16 a[140:141], v199 offset:24576\n"
"ds_read_b64_tr_b16 a[142:143], v199 offset:25600\n"
"s_waitcnt lgkmcnt(0)\n"
"ds_read_b64_tr_b16 a[144:145], v200 offset:24576\n"
"ds_read_b64_tr_b16 a[146:147], v200 offset:25600\n"
"ds_read_b64_tr_b16 a[148:149], v201 offset:24576\n"
"ds_read_b64_tr_b16 a[150:151], v201 offset:25600\n"
"ds_read_b64_tr_b16 a[152:153], v202 offset:24576\n"
"ds_read_b64_tr_b16 a[154:155], v202 offset:25600\n"
"ds_read_b64_tr_b16 a[156:157], v203 offset:24576\n"
"ds_read_b64_tr_b16 a[158:159], v203 offset:25600\n"
"s_waitcnt lgkmcnt(0)\n"
"v_mfma_f32_16x16x32_bf16 v[0:3], a[128:131], a[48:51], v[0:3]\n"
"v_mfma_f32_16x16x32_bf16 v[8:11], a[128:131], a[52:55], v[8:11]\n"
"v_mfma_f32_16x16x32_bf16 v[16:19], a[128:131], a[56:59], v[16:19]\n"
"v_mfma_f32_16x16x32_bf16 v[24:27], a[128:131], a[60:63], v[24:27]\n"
"v_mfma_f32_16x16x32_bf16 v[4:7], a[132:135], a[48:51], v[4:7]\n"
"v_mfma_f32_16x16x32_bf16 v[12:15], a[132:135], a[52:55], v[12:15]\n"
"v_mfma_f32_16x16x32_bf16 v[20:23], a[132:135], a[56:59], v[20:23]\n"
"v_mfma_f32_16x16x32_bf16 v[28:31], a[132:135], a[60:63], v[28:31]\n"
"v_mfma_f32_16x16x32_bf16 v[32:35], a[136:139], a[48:51], v[32:35]\n"
"v_mfma_f32_16x16x32_bf16 v[40:43], a[136:139], a[52:55], v[40:43]\n"
"v_mfma_f32_16x16x32_bf16 v[48:51], a[136:139], a[56:59], v[48:51]\n"
"v_mfma_f32_16x16x32_bf16 v[56:59], a[136:139], a[60:63], v[56:59]\n"
"v_mfma_f32_16x16x32_bf16 v[36:39], a[140:143], a[48:51], v[36:39]\n"
"v_mfma_f32_16x16x32_bf16 v[44:47], a[140:143], a[52:55], v[44:47]\n"
"v_mfma_f32_16x16x32_bf16 v[52:55], a[140:143], a[56:59], v[52:55]\n"
"v_mfma_f32_16x16x32_bf16 v[60:63], a[140:143], a[60:63], v[60:63]\n"
"v_mfma_f32_16x16x32_bf16 v[64:67], a[144:147], a[48:51], v[64:67]\n"
"v_mfma_f32_16x16x32_bf16 v[72:75], a[144:147], a[52:55], v[72:75]\n"
"v_mfma_f32_16x16x32_bf16 v[80:83], a[144:147], a[56:59], v[80:83]\n"
"v_mfma_f32_16x16x32_bf16 v[88:91], a[144:147], a[60:63], v[88:91]\n"
"v_mfma_f32_16x16x32_bf16 v[68:71], a[148:151], a[48:51], v[68:71]\n"
"v_mfma_f32_16x16x32_bf16 v[76:79], a[148:151], a[52:55], v[76:79]\n"
"v_mfma_f32_16x16x32_bf16 v[84:87], a[148:151], a[56:59], v[84:87]\n"
"v_mfma_f32_16x16x32_bf16 v[92:95], a[148:151], a[60:63], v[92:95]\n"
"v_mfma_f32_16x16x32_bf16 v[96:99], a[152:155], a[48:51], v[96:99]\n"
"v_mfma_f32_16x16x32_bf16 v[104:107], a[152:155], a[52:55], v[104:107]\n"
"v_mfma_f32_16x16x32_bf16 v[112:115], a[152:155], a[56:59], v[112:115]\n"
"v_mfma_f32_16x16x32_bf16 v[120:123], a[152:155], a[60:63], v[120:123]\n"
"v_mfma_f32_16x16x32_bf16 v[100:103], a[156:159], a[48:51], v[100:103]\n"
"v_mfma_f32_16x16x32_bf16 v[108:111], a[156:159], a[52:55], v[108:111]\n"
"v_mfma_f32_16x16x32_bf16 v[116:119], a[156:159], a[56:59], v[116:119]\n"
"v_mfma_f32_16x16x32_bf16 v[124:127], a[156:159], a[60:63], v[124:127]\n"
"s_barrier\n"
"s_add_u32 s62, s62, 1\n"
"s_cmp_lt_u32 s62, 4\n"
"s_cbranch_scc1 .Lslowq_57_%=\n"
"s_nop 7\n"
"v_cvt_pk_bf16_f32 v0, v0, v1\n"
"v_cvt_pk_bf16_f32 v1, v2, v3\n"
"v_cvt_pk_bf16_f32 v2, v4, v5\n"
"v_cvt_pk_bf16_f32 v3, v6, v7\n"
"v_accvgpr_write_b32 a64, v0\n"
"v_accvgpr_write_b32 a65, v1\n"
"v_accvgpr_write_b32 a66, v2\n"
"v_accvgpr_write_b32 a67, v3\n"
"v_cvt_pk_bf16_f32 v8, v8, v9\n"
"v_cvt_pk_bf16_f32 v9, v10, v11\n"
"v_cvt_pk_bf16_f32 v10, v12, v13\n"
"v_cvt_pk_bf16_f32 v11, v14, v15\n"
"v_accvgpr_write_b32 a68, v8\n"
"v_accvgpr_write_b32 a69, v9\n"
"v_accvgpr_write_b32 a70, v10\n"
"v_accvgpr_write_b32 a71, v11\n"
"v_cvt_pk_bf16_f32 v16, v16, v17\n"
"v_cvt_pk_bf16_f32 v17, v18, v19\n"
"v_cvt_pk_bf16_f32 v18, v20, v21\n"
"v_cvt_pk_bf16_f32 v19, v22, v23\n"
"v_accvgpr_write_b32 a72, v16\n"
"v_accvgpr_write_b32 a73, v17\n"
"v_accvgpr_write_b32 a74, v18\n"
"v_accvgpr_write_b32 a75, v19\n"
"v_cvt_pk_bf16_f32 v24, v24, v25\n"
"v_cvt_pk_bf16_f32 v25, v26, v27\n"
"v_cvt_pk_bf16_f32 v26, v28, v29\n"
"v_cvt_pk_bf16_f32 v27, v30, v31\n"
"v_accvgpr_write_b32 a76, v24\n"
"v_accvgpr_write_b32 a77, v25\n"
"v_accvgpr_write_b32 a78, v26\n"
"v_accvgpr_write_b32 a79, v27\n"
"v_cvt_pk_bf16_f32 v32, v32, v33\n"
"v_cvt_pk_bf16_f32 v33, v34, v35\n"
"v_cvt_pk_bf16_f32 v34, v36, v37\n"
"v_cvt_pk_bf16_f32 v35, v38, v39\n"
"v_accvgpr_write_b32 a80, v32\n"
"v_accvgpr_write_b32 a81, v33\n"
"v_accvgpr_write_b32 a82, v34\n"
"v_accvgpr_write_b32 a83, v35\n"
"v_cvt_pk_bf16_f32 v40, v40, v41\n"
"v_cvt_pk_bf16_f32 v41, v42, v43\n"
"v_cvt_pk_bf16_f32 v42, v44, v45\n"
"v_cvt_pk_bf16_f32 v43, v46, v47\n"
"v_accvgpr_write_b32 a84, v40\n"
"v_accvgpr_write_b32 a85, v41\n"
"v_accvgpr_write_b32 a86, v42\n"
"v_accvgpr_write_b32 a87, v43\n"
"v_cvt_pk_bf16_f32 v48, v48, v49\n"
"v_cvt_pk_bf16_f32 v49, v50, v51\n"
"v_cvt_pk_bf16_f32 v50, v52, v53\n"
"v_cvt_pk_bf16_f32 v51, v54, v55\n"
"v_accvgpr_write_b32 a88, v48\n"
"v_accvgpr_write_b32 a89, v49\n"
"v_accvgpr_write_b32 a90, v50\n"
"v_accvgpr_write_b32 a91, v51\n"
"v_cvt_pk_bf16_f32 v56, v56, v57\n"
"v_cvt_pk_bf16_f32 v57, v58, v59\n"
"v_cvt_pk_bf16_f32 v58, v60, v61\n"
"v_cvt_pk_bf16_f32 v59, v62, v63\n"
"v_accvgpr_write_b32 a92, v56\n"
"v_accvgpr_write_b32 a93, v57\n"
"v_accvgpr_write_b32 a94, v58\n"
"v_accvgpr_write_b32 a95, v59\n"
"v_cvt_pk_bf16_f32 v64, v64, v65\n"
"v_cvt_pk_bf16_f32 v65, v66, v67\n"
"v_cvt_pk_bf16_f32 v66, v68, v69\n"
"v_cvt_pk_bf16_f32 v67, v70, v71\n"
"v_accvgpr_write_b32 a96, v64\n"
"v_accvgpr_write_b32 a97, v65\n"
"v_accvgpr_write_b32 a98, v66\n"
"v_accvgpr_write_b32 a99, v67\n"
"v_cvt_pk_bf16_f32 v72, v72, v73\n"
"v_cvt_pk_bf16_f32 v73, v74, v75\n"
"v_cvt_pk_bf16_f32 v74, v76, v77\n"
"v_cvt_pk_bf16_f32 v75, v78, v79\n"
"v_accvgpr_write_b32 a100, v72\n"
"v_accvgpr_write_b32 a101, v73\n"
"v_accvgpr_write_b32 a102, v74\n"
"v_accvgpr_write_b32 a103, v75\n"
"v_cvt_pk_bf16_f32 v80, v80, v81\n"
"v_cvt_pk_bf16_f32 v81, v82, v83\n"
"v_cvt_pk_bf16_f32 v82, v84, v85\n"
"v_cvt_pk_bf16_f32 v83, v86, v87\n"
"v_accvgpr_write_b32 a104, v80\n"
"v_accvgpr_write_b32 a105, v81\n"
"v_accvgpr_write_b32 a106, v82\n"
"v_accvgpr_write_b32 a107, v83\n"
"v_cvt_pk_bf16_f32 v88, v88, v89\n"
"v_cvt_pk_bf16_f32 v89, v90, v91\n"
"v_cvt_pk_bf16_f32 v90, v92, v93\n"
"v_cvt_pk_bf16_f32 v91, v94, v95\n"
"v_accvgpr_write_b32 a108, v88\n"
"v_accvgpr_write_b32 a109, v89\n"
"v_accvgpr_write_b32 a110, v90\n"
"v_accvgpr_write_b32 a111, v91\n"
"v_cvt_pk_bf16_f32 v96, v96, v97\n"
"v_cvt_pk_bf16_f32 v97, v98, v99\n"
"v_cvt_pk_bf16_f32 v98, v100, v101\n"
"v_cvt_pk_bf16_f32 v99, v102, v103\n"
"v_accvgpr_write_b32 a112, v96\n"
"v_accvgpr_write_b32 a113, v97\n"
"v_accvgpr_write_b32 a114, v98\n"
"v_accvgpr_write_b32 a115, v99\n"
"v_cvt_pk_bf16_f32 v104, v104, v105\n"
"v_cvt_pk_bf16_f32 v105, v106, v107\n"
"v_cvt_pk_bf16_f32 v106, v108, v109\n"
"v_cvt_pk_bf16_f32 v107, v110, v111\n"
"v_accvgpr_write_b32 a116, v104\n"
"v_accvgpr_write_b32 a117, v105\n"
"v_accvgpr_write_b32 a118, v106\n"
"v_accvgpr_write_b32 a119, v107\n"
"v_cvt_pk_bf16_f32 v112, v112, v113\n"
"v_cvt_pk_bf16_f32 v113, v114, v115\n"
"v_cvt_pk_bf16_f32 v114, v116, v117\n"
"v_cvt_pk_bf16_f32 v115, v118, v119\n"
"v_accvgpr_write_b32 a120, v112\n"
"v_accvgpr_write_b32 a121, v113\n"
"v_accvgpr_write_b32 a122, v114\n"
"v_accvgpr_write_b32 a123, v115\n"
"v_cvt_pk_bf16_f32 v120, v120, v121\n"
"v_cvt_pk_bf16_f32 v121, v122, v123\n"
"v_cvt_pk_bf16_f32 v122, v124, v125\n"
"v_cvt_pk_bf16_f32 v123, v126, v127\n"
"v_accvgpr_write_b32 a124, v120\n"
"v_accvgpr_write_b32 a125, v121\n"
"v_accvgpr_write_b32 a126, v122\n"
"v_accvgpr_write_b32 a127, v123\n"
"v_mov_b32 v0, 0\n"
"v_mov_b32 v1, 0\n"
"v_mov_b32 v2, 0\n"
"v_mov_b32 v3, 0\n"
"v_mov_b32 v4, 0\n"
"v_mov_b32 v5, 0\n"
"v_mov_b32 v6, 0\n"
"v_mov_b32 v7, 0\n"
"v_mov_b32 v8, 0\n"
"v_mov_b32 v9, 0\n"
"v_mov_b32 v10, 0\n"
"v_mov_b32 v11, 0\n"
"v_mov_b32 v12, 0\n"
"v_mov_b32 v13, 0\n"
"v_mov_b32 v14, 0\n"
"v_mov_b32 v15, 0\n"
"v_mov_b32 v16, 0\n"
"v_mov_b32 v17, 0\n"
"v_mov_b32 v18, 0\n"
"v_mov_b32 v19, 0\n"
"v_mov_b32 v20, 0\n"
"v_mov_b32 v21, 0\n"
"v_mov_b32 v22, 0\n"
"v_mov_b32 v23, 0\n"
"v_mov_b32 v24, 0\n"
"v_mov_b32 v25, 0\n"
"v_mov_b32 v26, 0\n"
"v_mov_b32 v27, 0\n"
"v_mov_b32 v28, 0\n"
"v_mov_b32 v29, 0\n"
"v_mov_b32 v30, 0\n"
"v_mov_b32 v31, 0\n"
"v_mov_b32 v32, 0\n"
"v_mov_b32 v33, 0\n"
"v_mov_b32 v34, 0\n"
"v_mov_b32 v35, 0\n"
"v_mov_b32 v36, 0\n"
"v_mov_b32 v37, 0\n"
"v_mov_b32 v38, 0\n"
"v_mov_b32 v39, 0\n"
"v_mov_b32 v40, 0\n"
"v_mov_b32 v41, 0\n"
"v_mov_b32 v42, 0\n"
"v_mov_b32 v43, 0\n"
"v_mov_b32 v44, 0\n"
"v_mov_b32 v45, 0\n"
"v_mov_b32 v46, 0\n"
"v_mov_b32 v47, 0\n"
"v_mov_b32 v48, 0\n"
"v_mov_b32 v49, 0\n"
"v_mov_b32 v50, 0\n"
"v_mov_b32 v51, 0\n"
"v_mov_b32 v52, 0\n"
"v_mov_b32 v53, 0\n"
"v_mov_b32 v54, 0\n"
"v_mov_b32 v55, 0\n"
"v_mov_b32 v56, 0\n"
"v_mov_b32 v57, 0\n"
"v_mov_b32 v58, 0\n"
"v_mov_b32 v59, 0\n"
"v_mov_b32 v60, 0\n"
"v_mov_b32 v61, 0\n"
"v_mov_b32 v62, 0\n"
"v_mov_b32 v63, 0\n"
"v_mov_b32 v64, 0\n"
"v_mov_b32 v65, 0\n"
"v_mov_b32 v66, 0\n"
"v_mov_b32 v67, 0\n"
"v_mov_b32 v68, 0\n"
"v_mov_b32 v69, 0\n"
"v_mov_b32 v70, 0\n"
"v_mov_b32 v71, 0\n"
"v_mov_b32 v72, 0\n"
"v_mov_b32 v73, 0\n"
"v_mov_b32 v74, 0\n"
"v_mov_b32 v75, 0\n"
"v_mov_b32 v76, 0\n"
"v_mov_b32 v77, 0\n"
"v_mov_b32 v78, 0\n"
"v_mov_b32 v79, 0\n"
"v_mov_b32 v80, 0\n"
"v_mov_b32 v81, 0\n"
"v_mov_b32 v82, 0\n"
"v_mov_b32 v83, 0\n"
"v_mov_b32 v84, 0\n"
"v_mov_b32 v85, 0\n"
"v_mov_b32 v86, 0\n"
"v_mov_b32 v87, 0\n"
"v_mov_b32 v88, 0\n"
"v_mov_b32 v89, 0\n"
"v_mov_b32 v90, 0\n"
"v_mov_b32 v91, 0\n"
"v_mov_b32 v92, 0\n"
"v_mov_b32 v93, 0\n"
"v_mov_b32 v94, 0\n"
"v_mov_b32 v95, 0\n"
"v_mov_b32 v96, 0\n"
"v_mov_b32 v97, 0\n"
"v_mov_b32 v98, 0\n"
"v_mov_b32 v99, 0\n"
"v_mov_b32 v100, 0\n"
"v_mov_b32 v101, 0\n"
"v_mov_b32 v102, 0\n"
"v_mov_b32 v103, 0\n"
"v_mov_b32 v104, 0\n"
"v_mov_b32 v105, 0\n"
"v_mov_b32 v106, 0\n"
"v_mov_b32 v107, 0\n"
"v_mov_b32 v108, 0\n"
"v_mov_b32 v109, 0\n"
"v_mov_b32 v110, 0\n"
"v_mov_b32 v111, 0\n"
"v_mov_b32 v112, 0\n"
"v_mov_b32 v113, 0\n"
"v_mov_b32 v114, 0\n"
"v_mov_b32 v115, 0\n"
"v_mov_b32 v116, 0\n"
"v_mov_b32 v117, 0\n"
"v_mov_b32 v118, 0\n"
"v_mov_b32 v119, 0\n"
"v_mov_b32 v120, 0\n"
"v_mov_b32 v121, 0\n"
"v_mov_b32 v122, 0\n"
"v_mov_b32 v123, 0\n"
"v_mov_b32 v124, 0\n"
"v_mov_b32 v125, 0\n"
"v_mov_b32 v126, 0\n"
"v_mov_b32 v127, 0\n"
"v_mov_b32 v176, 0\n"
"v_mov_b32 v177, 0\n"
"v_mov_b32 v178, 0\n"
"v_mov_b32 v179, 0\n"
"v_mov_b32 v204, 0xf149f2ca\n"
"v_mov_b32 v180, 0\n"
"v_mov_b32 v181, 0\n"
"v_mov_b32 v182, 0\n"
"v_mov_b32 v183, 0\n"
"v_mov_b32 v205, 0xf149f2ca\n"
"v_mov_b32 v184, 0\n"
"v_mov_b32 v185, 0\n"
"v_mov_b32 v186, 0\n"
"v_mov_b32 v187, 0\n"
"v_mov_b32 v206, 0xf149f2ca\n"
"v_mov_b32 v188, 0\n"
"v_mov_b32 v189, 0\n"
"v_mov_b32 v190, 0\n"
"v_mov_b32 v191, 0\n"
"v_mov_b32 v207, 0xf149f2ca\n"
"v_mov_b32 v196, 0xff800000\n"
"v_mov_b32 v200, 0x3f803f80\n"
"v_mov_b32 v197, 0xff800000\n"
"v_mov_b32 v201, 0x3f803f80\n"
"v_mov_b32 v198, 0xff800000\n"
"v_mov_b32 v202, 0x3f803f80\n"
"v_mov_b32 v199, 0xff800000\n"
"v_mov_b32 v203, 0x3f803f80\n"
"v_and_b32 v212, 15, v255\n"
"v_lshrrev_b32 v213, 4, v255\n"
"v_lshlrev_b32 v213, 2, v213\n"
"v_sub_u32 v212, v212, v213\n"
"s_lshl_b32 s12, s77, 4\n"
"v_add_u32 v224, s12, v212\n"
"s_add_u32 s12, s12, 16\n"
"v_add_u32 v225, s12, v212\n"
"s_add_u32 s12, s12, 16\n"
"v_add_u32 v226, s12, v212\n"
"s_add_u32 s12, s12, 16\n"
"v_add_u32 v227, s12, v212\n"
"s_add_u32 s12, s12, 16\n"
"s_lshl_b32 s63, s10, 19\n"
"s_mov_b32 s64, 0\n"
"s_lshr_b32 s65, s77, 1\n"
"s_add_u32 s65, s65, 2\n"
".Lslowa_58_%=:\n"
"s_add_u32 s14, s63, 0x1000\n"
"buffer_load_dwordx4 a[144:147], v254, s[28:31], s63 offen sc1\n"
"buffer_load_dwordx4 a[148:151], v254, s[28:31], s63 offen offset:1024 sc1\n"
"buffer_load_dwordx4 a[152:155], v254, s[28:31], s63 offen offset:2048 sc1\n"
"buffer_load_dwordx4 a[156:159], v254, s[28:31], s63 offen offset:3072 sc1\n"
"buffer_load_dwordx4 a[160:163], v254, s[28:31], s14 offen sc1\n"
"buffer_load_dwordx4 a[164:167], v254, s[28:31], s14 offen offset:1024 sc1\n"
"buffer_load_dwordx4 a[168:171], v254, s[28:31], s14 offen offset:2048 sc1\n"
"buffer_load_dwordx4 a[172:175], v254, s[28:31], s14 offen offset:3072 sc1\n"
"buffer_load_dwordx4 a[176:179], v254, s[96:99], s63 offen sc1\n"
"buffer_load_dwordx4 a[180:183], v254, s[96:99], s63 offen offset:1024 sc1\n"
"buffer_load_dwordx4 a[184:187], v254, s[96:99], s63 offen offset:2048 sc1\n"
"buffer_load_dwordx4 a[188:191], v254, s[96:99], s63 offen offset:3072 sc1\n"
"buffer_load_dwordx4 a[192:195], v254, s[96:99], s14 offen sc1\n"
"buffer_load_dwordx4 a[196:199], v254, s[96:99], s14 offen offset:1024 sc1\n"
"buffer_load_dwordx4 a[200:203], v254, s[96:99], s14 offen offset:2048 sc1\n"
"buffer_load_dwordx4 a[204:207], v254, s[96:99], s14 offen offset:3072 sc1\n"
"s_waitcnt vmcnt(0)\n"
"v_mfma_f32_16x16x32_bf16 v[128:131], a[144:147], a[64:67], 0\n"
"v_mfma_f32_16x16x32_bf16 v[132:135], a[144:147], a[68:71], 0\n"
"v_mfma_f32_16x16x32_bf16 v[136:139], a[144:147], a[72:75], 0\n"
"v_mfma_f32_16x16x32_bf16 v[140:143], a[144:147], a[76:79], 0\n"
"v_mfma_f32_16x16x32_bf16 v[128:131], a[148:151], a[80:83], v[128:131]\n"
"v_mfma_f32_16x16x32_bf16 v[132:135], a[148:151], a[84:87], v[132:135]\n"
"v_mfma_f32_16x16x32_bf16 v[136:139], a[148:151], a[88:91], v[136:139]\n"
"v_mfma_f32_16x16x32_bf16 v[140:143], a[148:151], a[92:95], v[140:143]\n"
"v_mfma_f32_16x16x32_bf16 v[128:131], a[152:155], a[96:99], v[128:131]\n"
"v_mfma_f32_16x16x32_bf16 v[132:135], a[152:155], a[100:103], v[132:135]\n"
"v_mfma_f32_16x16x32_bf16 v[136:139], a[152:155], a[104:107], v[136:139]\n"
"v_mfma_f32_16x16x32_bf16 v[140:143], a[152:155], a[108:111], v[140:143]\n"
"v_mfma_f32_16x16x32_bf16 v[128:131], a[156:159], a[112:115], v[128:131]\n"
"v_mfma_f32_16x16x32_bf16 v[132:135], a[156:159], a[116:119], v[132:135]\n"
"v_mfma_f32_16x16x32_bf16 v[136:139], a[156:159], a[120:123], v[136:139]\n"
"v_mfma_f32_16x16x32_bf16 v[140:143], a[156:159], a[124:127], v[140:143]\n"
"v_mfma_f32_16x16x32_bf16 v[144:147], a[160:163], a[64:67], 0\n"
"v_mfma_f32_16x16x32_bf16 v[148:151], a[160:163], a[68:71], 0\n"
"v_mfma_f32_16x16x32_bf16 v[152:155], a[160:163], a[72:75], 0\n"
"v_mfma_f32_16x16x32_bf16 v[156:159], a[160:163], a[76:79], 0\n"
"v_mfma_f32_16x16x32_bf16 v[144:147], a[164:167], a[80:83], v[144:147]\n"
"v_mfma_f32_16x16x32_bf16 v[148:151], a[164:167], a[84:87], v[148:151]\n"
"v_mfma_f32_16x16x32_bf16 v[152:155], a[164:167], a[88:91], v[152:155]\n"
"v_mfma_f32_16x16x32_bf16 v[156:159], a[164:167], a[92:95], v[156:159]\n"
"v_mfma_f32_16x16x32_bf16 v[144:147], a[168:171], a[96:99], v[144:147]\n"
"v_mfma_f32_16x16x32_bf16 v[148:151], a[168:171], a[100:103], v[148:151]\n"
"v_mfma_f32_16x16x32_bf16 v[152:155], a[168:171], a[104:107], v[152:155]\n"
"v_mfma_f32_16x16x32_bf16 v[156:159], a[168:171], a[108:111], v[156:159]\n"
"v_mfma_f32_16x16x32_bf16 v[144:147], a[172:175], a[112:115], v[144:147]\n"
"v_mfma_f32_16x16x32_bf16 v[148:151], a[172:175], a[116:119], v[148:151]\n"
"v_mfma_f32_16x16x32_bf16 v[152:155], a[172:175], a[120:123], v[152:155]\n"
"v_mfma_f32_16x16x32_bf16 v[156:159], a[172:175], a[124:127], v[156:159]\n"
"s_nop 7\n"
"s_add_u32 s12, s64, 0\n"
"v_cmp_gt_i32 vcc, s12, v224\n"
"s_nop 1\n"
"v_cndmask_b32 v128, v128, v196, vcc\n"
"v_cmp_gt_i32 vcc, s12, v225\n"
"s_nop 1\n"
"v_cndmask_b32 v132, v132, v196, vcc\n"
"v_cmp_gt_i32 vcc, s12, v226\n"
"s_nop 1\n"
"v_cndmask_b32 v136, v136, v196, vcc\n"
"v_cmp_gt_i32 vcc, s12, v227\n"
"s_nop 1\n"
"v_cndmask_b32 v140, v140, v196, vcc\n"
"s_add_u32 s12, s64, 1\n"
"v_cmp_gt_i32 vcc, s12, v224\n"
"s_nop 1\n"
"v_cndmask_b32 v129, v129, v196, vcc\n"
"v_cmp_gt_i32 vcc, s12, v225\n"
"s_nop 1\n"
"v_cndmask_b32 v133, v133, v196, vcc\n"
"v_cmp_gt_i32 vcc, s12, v226\n"
"s_nop 1\n"
"v_cndmask_b32 v137, v137, v196, vcc\n"
"v_cmp_gt_i32 vcc, s12, v227\n"
"s_nop 1\n"
"v_cndmask_b32 v141, v141, v196, vcc\n"
"s_add_u32 s12, s64, 2\n"
"v_cmp_gt_i32 vcc, s12, v224\n"
"s_nop 1\n"
"v_cndmask_b32 v130, v130, v196, vcc\n"
"v_cmp_gt_i32 vcc, s12, v225\n"
"s_nop 1\n"
"v_cndmask_b32 v134, v134, v196, vcc\n"
"v_cmp_gt_i32 vcc, s12, v226\n"
"s_nop 1\n"
"v_cndmask_b32 v138, v138, v196, vcc\n"
"v_cmp_gt_i32 vcc, s12, v227\n"
"s_nop 1\n"
"v_cndmask_b32 v142, v142, v196, vcc\n"
"s_add_u32 s12, s64, 3\n"
"v_cmp_gt_i32 vcc, s12, v224\n"
"s_nop 1\n"
"v_cndmask_b32 v131, v131, v196, vcc\n"
"v_cmp_gt_i32 vcc, s12, v225\n"
"s_nop 1\n"
"v_cndmask_b32 v135, v135, v196, vcc\n"
"v_cmp_gt_i32 vcc, s12, v226\n"
"s_nop 1\n"
"v_cndmask_b32 v139, v139, v196, vcc\n"
"v_cmp_gt_i32 vcc, s12, v227\n"
"s_nop 1\n"
"v_cndmask_b32 v143, v143, v196, vcc\n"
"s_add_u32 s12, s64, 16\n"
"v_cmp_gt_i32 vcc, s12, v224\n"
"s_nop 1\n"
"v_cndmask_b32 v144, v144, v196, vcc\n"
"v_cmp_gt_i32 vcc, s12, v225\n"
"s_nop 1\n"
"v_cndmask_b32 v148, v148, v196, vcc\n"
"v_cmp_gt_i32 vcc, s12, v226\n"
"s_nop 1\n"
"v_cndmask_b32 v152, v152, v196, vcc\n"
"v_cmp_gt_i32 vcc, s12, v227\n"
"s_nop 1\n"
"v_cndmask_b32 v156, v156, v196, vcc\n"
"s_add_u32 s12, s64, 17\n"
"v_cmp_gt_i32 vcc, s12, v224\n"
"s_nop 1\n"
"v_cndmask_b32 v145, v145, v196, vcc\n"
"v_cmp_gt_i32 vcc, s12, v225\n"
"s_nop 1\n"
"v_cndmask_b32 v149, v149, v196, vcc\n"
"v_cmp_gt_i32 vcc, s12, v226\n"
"s_nop 1\n"
"v_cndmask_b32 v153, v153, v196, vcc\n"
"v_cmp_gt_i32 vcc, s12, v227\n"
"s_nop 1\n"
"v_cndmask_b32 v157, v157, v196, vcc\n"
"s_add_u32 s12, s64, 18\n"
"v_cmp_gt_i32 vcc, s12, v224\n"
"s_nop 1\n"
"v_cndmask_b32 v146, v146, v196, vcc\n"
"v_cmp_gt_i32 vcc, s12, v225\n"
"s_nop 1\n"
"v_cndmask_b32 v150, v150, v196, vcc\n"
"v_cmp_gt_i32 vcc, s12, v226\n"
"s_nop 1\n"
"v_cndmask_b32 v154, v154, v196, vcc\n"
"v_cmp_gt_i32 vcc, s12, v227\n"
"s_nop 1\n"
"v_cndmask_b32 v158, v158, v196, vcc\n"
"s_add_u32 s12, s64, 19\n"
"v_cmp_gt_i32 vcc, s12, v224\n"
"s_nop 1\n"
"v_cndmask_b32 v147, v147, v196, vcc\n"
"v_cmp_gt_i32 vcc, s12, v225\n"
"s_nop 1\n"
"v_cndmask_b32 v151, v151, v196, vcc\n"
"v_cmp_gt_i32 vcc, s12, v226\n"
"s_nop 1\n"
"v_cndmask_b32 v155, v155, v196, vcc\n"
"v_cmp_gt_i32 vcc, s12, v227\n"
"s_nop 1\n"
"v_cndmask_b32 v159, v159, v196, vcc\n"
"v_max3_f32 v208, v128, v129, v204\n"
"v_max3_f32 v208, v130, v131, v208\n"
"v_max3_f32 v208, v144, v145, v208\n"
"v_max3_f32 v208, v146, v147, v208\n"
"v_max3_f32 v209, v132, v133, v205\n"
"v_max3_f32 v209, v134, v135, v209\n"
"v_max3_f32 v209, v148, v149, v209\n"
"v_max3_f32 v209, v150, v151, v209\n"
"v_max3_f32 v210, v136, v137, v206\n"
"v_max3_f32 v210, v138, v139, v210\n"
"v_max3_f32 v210, v152, v153, v210\n"
"v_max3_f32 v210, v154, v155, v210\n"
"v_max3_f32 v211, v140, v141, v207\n"
"v_max3_f32 v211, v142, v143, v211\n"
"v_max3_f32 v211, v156, v157, v211\n"
"v_max3_f32 v211, v158, v159, v211\n"
"v_mov_b32 v212, v208\n"
"v_mov_b32 v213, v209\n"
"v_mov_b32 v214, v210\n"
"v_mov_b32 v215, v211\n"
"s_nop 0\n"
"v_permlane16_swap_b32 v208, v212\n"
"v_permlane16_swap_b32 v209, v213\n"
"v_permlane16_swap_b32 v210, v214\n"
"v_permlane16_swap_b32 v211, v215\n"
"v_max_f32 v208, v208, v212\n"
"v_max_f32 v209, v209, v213\n"
"v_max_f32 v210, v210, v214\n"
"v_max_f32 v211, v211, v215\n"
"v_mov_b32 v212, v208\n"
"v_mov_b32 v213, v209\n"
"v_mov_b32 v214, v210\n"
"v_mov_b32 v215, v211\n"
"s_nop 0\n"
"v_permlane32_swap_b32 v208, v212\n"
"v_permlane32_swap_b32 v209, v213\n"
"v_permlane32_swap_b32 v210, v214\n"
"v_permlane32_swap_b32 v211, v215\n"
"v_max_f32 v208, v208, v212\n"
"v_max_f32 v209, v209, v213\n"
"v_max_f32 v210, v210, v214\n"
"v_max_f32 v211, v211, v215\n"
"v_sub_f32 v216, v204, v208\n"
"v_exp_f32 v216, v216\n"
"v_mov_b32 v204, v208\n"
"v_sub_f32 v128, v128, v208\n"
"v_sub_f32 v129, v129, v208\n"
"v_sub_f32 v130, v130, v208\n"
"v_sub_f32 v131, v131, v208\n"
"v_exp_f32 v128, v128\n"
"v_exp_f32 v129, v129\n"
"v_exp_f32 v130, v130\n"
"v_exp_f32 v131, v131\n"
"v_sub_f32 v144, v144, v208\n"
"v_sub_f32 v145, v145, v208\n"
"v_sub_f32 v146, v146, v208\n"
"v_sub_f32 v147, v147, v208\n"
"v_exp_f32 v144, v144\n"
"v_exp_f32 v145, v145\n"
"v_exp_f32 v146, v146\n"
"v_exp_f32 v147, v147\n"
"s_nop 0\n"
"v_cvt_pk_bf16_f32 v160, v128, v129\n"
"v_cvt_pk_bf16_f32 v161, v130, v131\n"
"v_cvt_pk_bf16_f32 v162, v144, v145\n"
"v_cvt_pk_bf16_f32 v163, v146, v147\n"
"v_pk_mul_f32 v[0:1], v[0:1], v[216:217] op_sel_hi:[1,0]\n"
"v_pk_mul_f32 v[2:3], v[2:3], v[216:217] op_sel_hi:[1,0]\n"
"v_pk_mul_f32 v[16:17], v[16:17], v[216:217] op_sel_hi:[1,0]\n"
"v_pk_mul_f32 v[18:19], v[18:19], v[216:217] op_sel_hi:[1,0]\n"
"v_pk_mul_f32 v[32:33], v[32:33], v[216:217] op_sel_hi:[1,0]\n"
"v_pk_mul_f32 v[34:35], v[34:35], v[216:217] op_sel_hi:[1,0]\n"
"v_pk_mul_f32 v[48:49], v[48:49], v[216:217] op_sel_hi:[1,0]\n"
"v_pk_mul_f32 v[50:51], v[50:51], v[216:217] op_sel_hi:[1,0]\n"
"v_pk_mul_f32 v[64:65], v[64:65], v[216:217] op_sel_hi:[1,0]\n"
"v_pk_mul_f32 v[66:67], v[66:67], v[216:217] op_sel_hi:[1,0]\n"
"v_pk_mul_f32 v[80:81], v[80:81], v[216:217] op_sel_hi:[1,0]\n"
"v_pk_mul_f32 v[82:83], v[82:83], v[216:217] op_sel_hi:[1,0]\n"
"v_pk_mul_f32 v[96:97], v[96:97], v[216:217] op_sel_hi:[1,0]\n"
"v_pk_mul_f32 v[98:99], v[98:99], v[216:217] op_sel_hi:[1,0]\n"
"v_pk_mul_f32 v[112:113], v[112:113], v[216:217] op_sel_hi:[1,0]\n"
"v_pk_mul_f32 v[114:115], v[114:115], v[216:217] op_sel_hi:[1,0]\n"
"v_pk_mul_f32 v[176:177], v[176:177], v[216:217] op_sel_hi:[1,0]\n"
"v_pk_mul_f32 v[178:179], v[178:179], v[216:217] op_sel_hi:[1,0]\n"
"v_sub_f32 v218, v205, v209\n"
"v_exp_f32 v218, v218\n"
"v_mov_b32 v205, v209\n"
"v_sub_f32 v132, v132, v209\n"
"v_sub_f32 v133, v133, v209\n"
"v_sub_f32 v134, v134, v209\n"
"v_sub_f32 v135, v135, v209\n"
"v_exp_f32 v132, v132\n"
"v_exp_f32 v133, v133\n"
"v_exp_f32 v134, v134\n"
"v_exp_f32 v135, v135\n"
"v_sub_f32 v148, v148, v209\n"
"v_sub_f32 v149, v149, v209\n"
"v_sub_f32 v150, v150, v209\n"
"v_sub_f32 v151, v151, v209\n"
"v_exp_f32 v148, v148\n"
"v_exp_f32 v149, v149\n"
"v_exp_f32 v150, v150\n"
"v_exp_f32 v151, v151\n"
"s_nop 0\n"
"v_cvt_pk_bf16_f32 v164, v132, v133\n"
"v_cvt_pk_bf16_f32 v165, v134, v135\n"
"v_cvt_pk_bf16_f32 v166, v148, v149\n"
"v_cvt_pk_bf16_f32 v167, v150, v151\n"
"v_pk_mul_f32 v[4:5], v[4:5], v[218:219] op_sel_hi:[1,0]\n"
"v_pk_mul_f32 v[6:7], v[6:7], v[218:219] op_sel_hi:[1,0]\n"
"v_pk_mul_f32 v[20:21], v[20:21], v[218:219] op_sel_hi:[1,0]\n"
"v_pk_mul_f32 v[22:23], v[22:23], v[218:219] op_sel_hi:[1,0]\n"
"v_pk_mul_f32 v[36:37], v[36:37], v[218:219] op_sel_hi:[1,0]\n"
"v_pk_mul_f32 v[38:39], v[38:39], v[218:219] op_sel_hi:[1,0]\n"
"v_pk_mul_f32 v[52:53], v[52:53], v[218:219] op_sel_hi:[1,0]\n"
"v_pk_mul_f32 v[54:55], v[54:55], v[218:219] op_sel_hi:[1,0]\n"
"v_pk_mul_f32 v[68:69], v[68:69], v[218:219] op_sel_hi:[1,0]\n"
"v_pk_mul_f32 v[70:71], v[70:71], v[218:219] op_sel_hi:[1,0]\n"
"v_pk_mul_f32 v[84:85], v[84:85], v[218:219] op_sel_hi:[1,0]\n"
"v_pk_mul_f32 v[86:87], v[86:87], v[218:219] op_sel_hi:[1,0]\n"
"v_pk_mul_f32 v[100:101], v[100:101], v[218:219] op_sel_hi:[1,0]\n"
"v_pk_mul_f32 v[102:103], v[102:103], v[218:219] op_sel_hi:[1,0]\n"
"v_pk_mul_f32 v[116:117], v[116:117], v[218:219] op_sel_hi:[1,0]\n"
"v_pk_mul_f32 v[118:119], v[118:119], v[218:219] op_sel_hi:[1,0]\n"
"v_pk_mul_f32 v[180:181], v[180:181], v[218:219] op_sel_hi:[1,0]\n"
"v_pk_mul_f32 v[182:183], v[182:183], v[218:219] op_sel_hi:[1,0]\n"
"v_sub_f32 v220, v206, v210\n"
"v_exp_f32 v220, v220\n"
"v_mov_b32 v206, v210\n"
"v_sub_f32 v136, v136, v210\n"
"v_sub_f32 v137, v137, v210\n"
"v_sub_f32 v138, v138, v210\n"
"v_sub_f32 v139, v139, v210\n"
"v_exp_f32 v136, v136\n"
"v_exp_f32 v137, v137\n"
"v_exp_f32 v138, v138\n"
"v_exp_f32 v139, v139\n"
"v_sub_f32 v152, v152, v210\n"
"v_sub_f32 v153, v153, v210\n"
"v_sub_f32 v154, v154, v210\n"
"v_sub_f32 v155, v155, v210\n"
"v_exp_f32 v152, v152\n"
"v_exp_f32 v153, v153\n"
"v_exp_f32 v154, v154\n"
"v_exp_f32 v155, v155\n"
"s_nop 0\n"
"v_cvt_pk_bf16_f32 v168, v136, v137\n"
"v_cvt_pk_bf16_f32 v169, v138, v139\n"
"v_cvt_pk_bf16_f32 v170, v152, v153\n"
"v_cvt_pk_bf16_f32 v171, v154, v155\n"
"v_pk_mul_f32 v[8:9], v[8:9], v[220:221] op_sel_hi:[1,0]\n"
"v_pk_mul_f32 v[10:11], v[10:11], v[220:221] op_sel_hi:[1,0]\n"
"v_pk_mul_f32 v[24:25], v[24:25], v[220:221] op_sel_hi:[1,0]\n"
"v_pk_mul_f32 v[26:27], v[26:27], v[220:221] op_sel_hi:[1,0]\n"
"v_pk_mul_f32 v[40:41], v[40:41], v[220:221] op_sel_hi:[1,0]\n"
"v_pk_mul_f32 v[42:43], v[42:43], v[220:221] op_sel_hi:[1,0]\n"
"v_pk_mul_f32 v[56:57], v[56:57], v[220:221] op_sel_hi:[1,0]\n"
"v_pk_mul_f32 v[58:59], v[58:59], v[220:221] op_sel_hi:[1,0]\n"
"v_pk_mul_f32 v[72:73], v[72:73], v[220:221] op_sel_hi:[1,0]\n"
"v_pk_mul_f32 v[74:75], v[74:75], v[220:221] op_sel_hi:[1,0]\n"
"v_pk_mul_f32 v[88:89], v[88:89], v[220:221] op_sel_hi:[1,0]\n"
"v_pk_mul_f32 v[90:91], v[90:91], v[220:221] op_sel_hi:[1,0]\n"
"v_pk_mul_f32 v[104:105], v[104:105], v[220:221] op_sel_hi:[1,0]\n"
"v_pk_mul_f32 v[106:107], v[106:107], v[220:221] op_sel_hi:[1,0]\n"
"v_pk_mul_f32 v[120:121], v[120:121], v[220:221] op_sel_hi:[1,0]\n"
"v_pk_mul_f32 v[122:123], v[122:123], v[220:221] op_sel_hi:[1,0]\n"
"v_pk_mul_f32 v[184:185], v[184:185], v[220:221] op_sel_hi:[1,0]\n"
"v_pk_mul_f32 v[186:187], v[186:187], v[220:221] op_sel_hi:[1,0]\n"
"v_sub_f32 v222, v207, v211\n"
"v_exp_f32 v222, v222\n"
"v_mov_b32 v207, v211\n"
"v_sub_f32 v140, v140, v211\n"
"v_sub_f32 v141, v141, v211\n"
"v_sub_f32 v142, v142, v211\n"
"v_sub_f32 v143, v143, v211\n"
"v_exp_f32 v140, v140\n"
"v_exp_f32 v141, v141\n"
"v_exp_f32 v142, v142\n"
"v_exp_f32 v143, v143\n"
"v_sub_f32 v156, v156, v211\n"
"v_sub_f32 v157, v157, v211\n"
"v_sub_f32 v158, v158, v211\n"
"v_sub_f32 v159, v159, v211\n"
"v_exp_f32 v156, v156\n"
"v_exp_f32 v157, v157\n"
"v_exp_f32 v158, v158\n"
"v_exp_f32 v159, v159\n"
"s_nop 0\n"
"v_cvt_pk_bf16_f32 v172, v140, v141\n"
"v_cvt_pk_bf16_f32 v173, v142, v143\n"
"v_cvt_pk_bf16_f32 v174, v156, v157\n"
"v_cvt_pk_bf16_f32 v175, v158, v159\n"
"v_pk_mul_f32 v[12:13], v[12:13], v[222:223] op_sel_hi:[1,0]\n"
"v_pk_mul_f32 v[14:15], v[14:15], v[222:223] op_sel_hi:[1,0]\n"
"v_pk_mul_f32 v[28:29], v[28:29], v[222:223] op_sel_hi:[1,0]\n"
"v_pk_mul_f32 v[30:31], v[30:31], v[222:223] op_sel_hi:[1,0]\n"
"v_pk_mul_f32 v[44:45], v[44:45], v[222:223] op_sel_hi:[1,0]\n"
"v_pk_mul_f32 v[46:47], v[46:47], v[222:223] op_sel_hi:[1,0]\n"
"v_pk_mul_f32 v[60:61], v[60:61], v[222:223] op_sel_hi:[1,0]\n"
"v_pk_mul_f32 v[62:63], v[62:63], v[222:223] op_sel_hi:[1,0]\n"
"v_pk_mul_f32 v[76:77], v[76:77], v[222:223] op_sel_hi:[1,0]\n"
"v_pk_mul_f32 v[78:79], v[78:79], v[222:223] op_sel_hi:[1,0]\n"
"v_pk_mul_f32 v[92:93], v[92:93], v[222:223] op_sel_hi:[1,0]\n"
"v_pk_mul_f32 v[94:95], v[94:95], v[222:223] op_sel_hi:[1,0]\n"
"v_pk_mul_f32 v[108:109], v[108:109], v[222:223] op_sel_hi:[1,0]\n"
"v_pk_mul_f32 v[110:111], v[110:111], v[222:223] op_sel_hi:[1,0]\n"
"v_pk_mul_f32 v[124:125], v[124:125], v[222:223] op_sel_hi:[1,0]\n"
"v_pk_mul_f32 v[126:127], v[126:127], v[222:223] op_sel_hi:[1,0]\n"
"v_pk_mul_f32 v[188:189], v[188:189], v[222:223] op_sel_hi:[1,0]\n"
"v_pk_mul_f32 v[190:191], v[190:191], v[222:223] op_sel_hi:[1,0]\n"
"s_nop 1\n"
"v_mfma_f32_16x16x32_bf16 v[0:3], a[176:179], v[160:163], v[0:3]\n"
"v_mfma_f32_16x16x32_bf16 v[4:7], a[176:179], v[164:167], v[4:7]\n"
"v_mfma_f32_16x16x32_bf16 v[8:11], a[176:179], v[168:171], v[8:11]\n"
"v_mfma_f32_16x16x32_bf16 v[12:15], a[176:179], v[172:175], v[12:15]\n"
"v_mfma_f32_16x16x32_bf16 v[16:19], a[180:183], v[160:163], v[16:19]\n"
"v_mfma_f32_16x16x32_bf16 v[20:23], a[180:183], v[164:167], v[20:23]\n"
"v_mfma_f32_16x16x32_bf16 v[24:27], a[180:183], v[168:171], v[24:27]\n"
"v_mfma_f32_16x16x32_bf16 v[28:31], a[180:183], v[172:175], v[28:31]\n"
"v_mfma_f32_16x16x32_bf16 v[32:35], a[184:187], v[160:163], v[32:35]\n"
"v_mfma_f32_16x16x32_bf16 v[36:39], a[184:187], v[164:167], v[36:39]\n"
"v_mfma_f32_16x16x32_bf16 v[40:43], a[184:187], v[168:171], v[40:43]\n"
"v_mfma_f32_16x16x32_bf16 v[44:47], a[184:187], v[172:175], v[44:47]\n"
"v_mfma_f32_16x16x32_bf16 v[48:51], a[188:191], v[160:163], v[48:51]\n"
"v_mfma_f32_16x16x32_bf16 v[52:55], a[188:191], v[164:167], v[52:55]\n"
"v_mfma_f32_16x16x32_bf16 v[56:59], a[188:191], v[168:171], v[56:59]\n"
"v_mfma_f32_16x16x32_bf16 v[60:63], a[188:191], v[172:175], v[60:63]\n"
"v_mfma_f32_16x16x32_bf16 v[64:67], a[192:195], v[160:163], v[64:67]\n"
"v_mfma_f32_16x16x32_bf16 v[68:71], a[192:195], v[164:167], v[68:71]\n"
"v_mfma_f32_16x16x32_bf16 v[72:75], a[192:195], v[168:171], v[72:75]\n"
"v_mfma_f32_16x16x32_bf16 v[76:79], a[192:195], v[172:175], v[76:79]\n"
"v_mfma_f32_16x16x32_bf16 v[80:83], a[196:199], v[160:163], v[80:83]\n"
"v_mfma_f32_16x16x32_bf16 v[84:87], a[196:199], v[164:167], v[84:87]\n"
"v_mfma_f32_16x16x32_bf16 v[88:91], a[196:199], v[168:171], v[88:91]\n"
"v_mfma_f32_16x16x32_bf16 v[92:95], a[196:199], v[172:175], v[92:95]\n"
"v_mfma_f32_16x16x32_bf16 v[96:99], a[200:203], v[160:163], v[96:99]\n"
"v_mfma_f32_16x16x32_bf16 v[100:103], a[200:203], v[164:167], v[100:103]\n"
"v_mfma_f32_16x16x32_bf16 v[104:107], a[200:203], v[168:171], v[104:107]\n"
"v_mfma_f32_16x16x32_bf16 v[108:111], a[200:203], v[172:175], v[108:111]\n"
"v_mfma_f32_16x16x32_bf16 v[112:115], a[204:207], v[160:163], v[112:115]\n"
"v_mfma_f32_16x16x32_bf16 v[116:119], a[204:207], v[164:167], v[116:119]\n"
"v_mfma_f32_16x16x32_bf16 v[120:123], a[204:207], v[168:171], v[120:123]\n"
"v_mfma_f32_16x16x32_bf16 v[124:127], a[204:207], v[172:175], v[124:127]\n"
"v_mfma_f32_16x16x32_bf16 v[176:179], v[200:203], v[160:163], v[176:179]\n"
"v_mfma_f32_16x16x32_bf16 v[180:183], v[200:203], v[164:167], v[180:183]\n"
"v_mfma_f32_16x16x32_bf16 v[184:187], v[200:203], v[168:171], v[184:187]\n"
"v_mfma_f32_16x16x32_bf16 v[188:191], v[200:203], v[172:175], v[188:191]\n"
"s_add_u32 s63, s63, 0x2000\n"
"s_add_u32 s64, s64, 32\n"
"s_sub_u32 s65, s65, 1\n"
"s_nop 7\n"
"s_cmp_lg_u32 s65, 0\n"
"s_cbranch_scc1 .Lslowa_58_%=\n"
"v_rcp_f32 v216, v176\n"
"v_rcp_f32 v218, v180\n"
"v_rcp_f32 v220, v184\n"
"v_rcp_f32 v222, v188\n"
"s_lshl_b32 s63, s10, 19\n"
"s_lshl_b32 s12, s77, 10\n"
"s_add_u32 s63, s63, s12\n"
"v_pk_mul_f32 v[0:1], v[0:1], v[216:217] op_sel_hi:[1,0]\n"
"v_pk_mul_f32 v[2:3], v[2:3], v[216:217] op_sel_hi:[1,0]\n"
"v_cvt_pk_bf16_f32 v128, v0, v1\n"
"v_cvt_pk_bf16_f32 v129, v2, v3\n"
"v_pk_mul_f32 v[16:17], v[16:17], v[216:217] op_sel_hi:[1,0]\n"
"v_pk_mul_f32 v[18:19], v[18:19], v[216:217] op_sel_hi:[1,0]\n"
"v_cvt_pk_bf16_f32 v130, v16, v17\n"
"v_cvt_pk_bf16_f32 v131, v18, v19\n"
"buffer_store_dwordx4 v[128:131], v254, s[36:39], s63 offen sc1\n"
"v_pk_mul_f32 v[4:5], v[4:5], v[218:219] op_sel_hi:[1,0]\n"
"v_pk_mul_f32 v[6:7], v[6:7], v[218:219] op_sel_hi:[1,0]\n"
"v_cvt_pk_bf16_f32 v132, v4, v5\n"
"v_cvt_pk_bf16_f32 v133, v6, v7\n"
"v_pk_mul_f32 v[20:21], v[20:21], v[218:219] op_sel_hi:[1,0]\n"
"v_pk_mul_f32 v[22:23], v[22:23], v[218:219] op_sel_hi:[1,0]\n"
"v_cvt_pk_bf16_f32 v134, v20, v21\n"
"v_cvt_pk_bf16_f32 v135, v22, v23\n"
"buffer_store_dwordx4 v[132:135], v254, s[36:39], s63 offen offset:1024 sc1\n"
"v_pk_mul_f32 v[8:9], v[8:9], v[220:221] op_sel_hi:[1,0]\n"
"v_pk_mul_f32 v[10:11], v[10:11], v[220:221] op_sel_hi:[1,0]\n"
"v_cvt_pk_bf16_f32 v136, v8, v9\n"
"v_cvt_pk_bf16_f32 v137, v10, v11\n"
"v_pk_mul_f32 v[24:25], v[24:25], v[220:221] op_sel_hi:[1,0]\n"
"v_pk_mul_f32 v[26:27], v[26:27], v[220:221] op_sel_hi:[1,0]\n"
"v_cvt_pk_bf16_f32 v138, v24, v25\n"
"v_cvt_pk_bf16_f32 v139, v26, v27\n"
"buffer_store_dwordx4 v[136:139], v254, s[36:39], s63 offen offset:2048 sc1\n"
"v_pk_mul_f32 v[12:13], v[12:13], v[222:223] op_sel_hi:[1,0]\n"
"v_pk_mul_f32 v[14:15], v[14:15], v[222:223] op_sel_hi:[1,0]\n"
"v_cvt_pk_bf16_f32 v140, v12, v13\n"
"v_cvt_pk_bf16_f32 v141, v14, v15\n"
"v_pk_mul_f32 v[28:29], v[28:29], v[222:223] op_sel_hi:[1,0]\n"
"v_pk_mul_f32 v[30:31], v[30:31], v[222:223] op_sel_hi:[1,0]\n"
"v_cvt_pk_bf16_f32 v142, v28, v29\n"
"v_cvt_pk_bf16_f32 v143, v30, v31\n"
"buffer_store_dwordx4 v[140:143], v254, s[36:39], s63 offen offset:3072 sc1\n"
"s_add_u32 s63, s63, 0x20000\n"
"v_pk_mul_f32 v[32:33], v[32:33], v[216:217] op_sel_hi:[1,0]\n"
"v_pk_mul_f32 v[34:35], v[34:35], v[216:217] op_sel_hi:[1,0]\n"
"v_cvt_pk_bf16_f32 v144, v32, v33\n"
"v_cvt_pk_bf16_f32 v145, v34, v35\n"
"v_pk_mul_f32 v[48:49], v[48:49], v[216:217] op_sel_hi:[1,0]\n"
"v_pk_mul_f32 v[50:51], v[50:51], v[216:217] op_sel_hi:[1,0]\n"
"v_cvt_pk_bf16_f32 v146, v48, v49\n"
"v_cvt_pk_bf16_f32 v147, v50, v51\n"
"buffer_store_dwordx4 v[144:147], v254, s[36:39], s63 offen sc1\n"
"v_pk_mul_f32 v[36:37], v[36:37], v[218:219] op_sel_hi:[1,0]\n"
"v_pk_mul_f32 v[38:39], v[38:39], v[218:219] op_sel_hi:[1,0]\n"
"v_cvt_pk_bf16_f32 v148, v36, v37\n"
"v_cvt_pk_bf16_f32 v149, v38, v39\n"
"v_pk_mul_f32 v[52:53], v[52:53], v[218:219] op_sel_hi:[1,0]\n"
"v_pk_mul_f32 v[54:55], v[54:55], v[218:219] op_sel_hi:[1,0]\n"
"v_cvt_pk_bf16_f32 v150, v52, v53\n"
"v_cvt_pk_bf16_f32 v151, v54, v55\n"
"buffer_store_dwordx4 v[148:151], v254, s[36:39], s63 offen offset:1024 sc1\n"
"v_pk_mul_f32 v[40:41], v[40:41], v[220:221] op_sel_hi:[1,0]\n"
"v_pk_mul_f32 v[42:43], v[42:43], v[220:221] op_sel_hi:[1,0]\n"
"v_cvt_pk_bf16_f32 v152, v40, v41\n"
"v_cvt_pk_bf16_f32 v153, v42, v43\n"
"v_pk_mul_f32 v[56:57], v[56:57], v[220:221] op_sel_hi:[1,0]\n"
"v_pk_mul_f32 v[58:59], v[58:59], v[220:221] op_sel_hi:[1,0]\n"
"v_cvt_pk_bf16_f32 v154, v56, v57\n"
"v_cvt_pk_bf16_f32 v155, v58, v59\n"
"buffer_store_dwordx4 v[152:155], v254, s[36:39], s63 offen offset:2048 sc1\n"
"v_pk_mul_f32 v[44:45], v[44:45], v[222:223] op_sel_hi:[1,0]\n"
"v_pk_mul_f32 v[46:47], v[46:47], v[222:223] op_sel_hi:[1,0]\n"
"v_cvt_pk_bf16_f32 v156, v44, v45\n"
"v_cvt_pk_bf16_f32 v157, v46, v47\n"
"v_pk_mul_f32 v[60:61], v[60:61], v[222:223] op_sel_hi:[1,0]\n"
"v_pk_mul_f32 v[62:63], v[62:63], v[222:223] op_sel_hi:[1,0]\n"
"v_cvt_pk_bf16_f32 v158, v60, v61\n"
"v_cvt_pk_bf16_f32 v159, v62, v63\n"
"buffer_store_dwordx4 v[156:159], v254, s[36:39], s63 offen offset:3072 sc1\n"
"s_add_u32 s63, s63, 0x20000\n"
"v_pk_mul_f32 v[64:65], v[64:65], v[216:217] op_sel_hi:[1,0]\n"
"v_pk_mul_f32 v[66:67], v[66:67], v[216:217] op_sel_hi:[1,0]\n"
"v_cvt_pk_bf16_f32 v160, v64, v65\n"
"v_cvt_pk_bf16_f32 v161, v66, v67\n"
"v_pk_mul_f32 v[80:81], v[80:81], v[216:217] op_sel_hi:[1,0]\n"
"v_pk_mul_f32 v[82:83], v[82:83], v[216:217] op_sel_hi:[1,0]\n"
"v_cvt_pk_bf16_f32 v162, v80, v81\n"
"v_cvt_pk_bf16_f32 v163, v82, v83\n"
"buffer_store_dwordx4 v[160:163], v254, s[36:39], s63 offen sc1\n"
"v_pk_mul_f32 v[68:69], v[68:69], v[218:219] op_sel_hi:[1,0]\n"
"v_pk_mul_f32 v[70:71], v[70:71], v[218:219] op_sel_hi:[1,0]\n"
"v_cvt_pk_bf16_f32 v164, v68, v69\n"
"v_cvt_pk_bf16_f32 v165, v70, v71\n"
"v_pk_mul_f32 v[84:85], v[84:85], v[218:219] op_sel_hi:[1,0]\n"
"v_pk_mul_f32 v[86:87], v[86:87], v[218:219] op_sel_hi:[1,0]\n"
"v_cvt_pk_bf16_f32 v166, v84, v85\n"
"v_cvt_pk_bf16_f32 v167, v86, v87\n"
"buffer_store_dwordx4 v[164:167], v254, s[36:39], s63 offen offset:1024 sc1\n"
"v_pk_mul_f32 v[72:73], v[72:73], v[220:221] op_sel_hi:[1,0]\n"
"v_pk_mul_f32 v[74:75], v[74:75], v[220:221] op_sel_hi:[1,0]\n"
"v_cvt_pk_bf16_f32 v168, v72, v73\n"
"v_cvt_pk_bf16_f32 v169, v74, v75\n"
"v_pk_mul_f32 v[88:89], v[88:89], v[220:221] op_sel_hi:[1,0]\n"
"v_pk_mul_f32 v[90:91], v[90:91], v[220:221] op_sel_hi:[1,0]\n"
"v_cvt_pk_bf16_f32 v170, v88, v89\n"
"v_cvt_pk_bf16_f32 v171, v90, v91\n"
"buffer_store_dwordx4 v[168:171], v254, s[36:39], s63 offen offset:2048 sc1\n"
"v_pk_mul_f32 v[76:77], v[76:77], v[222:223] op_sel_hi:[1,0]\n"
"v_pk_mul_f32 v[78:79], v[78:79], v[222:223] op_sel_hi:[1,0]\n"
"v_cvt_pk_bf16_f32 v172, v76, v77\n"
"v_cvt_pk_bf16_f32 v173, v78, v79\n"
"v_pk_mul_f32 v[92:93], v[92:93], v[222:223] op_sel_hi:[1,0]\n"
"v_pk_mul_f32 v[94:95], v[94:95], v[222:223] op_sel_hi:[1,0]\n"
"v_cvt_pk_bf16_f32 v174, v92, v93\n"
"v_cvt_pk_bf16_f32 v175, v94, v95\n"
"buffer_store_dwordx4 v[172:175], v254, s[36:39], s63 offen offset:3072 sc1\n"
"s_add_u32 s63, s63, 0x20000\n"
"v_pk_mul_f32 v[96:97], v[96:97], v[216:217] op_sel_hi:[1,0]\n"
"v_pk_mul_f32 v[98:99], v[98:99], v[216:217] op_sel_hi:[1,0]\n"
"v_cvt_pk_bf16_f32 v228, v96, v97\n"
"v_cvt_pk_bf16_f32 v229, v98, v99\n"
"v_pk_mul_f32 v[112:113], v[112:113], v[216:217] op_sel_hi:[1,0]\n"
"v_pk_mul_f32 v[114:115], v[114:115], v[216:217] op_sel_hi:[1,0]\n"
"v_cvt_pk_bf16_f32 v230, v112, v113\n"
"v_cvt_pk_bf16_f32 v231, v114, v115\n"
"buffer_store_dwordx4 v[228:231], v254, s[36:39], s63 offen sc1\n"
"v_pk_mul_f32 v[100:101], v[100:101], v[218:219] op_sel_hi:[1,0]\n"
"v_pk_mul_f32 v[102:103], v[102:103], v[218:219] op_sel_hi:[1,0]\n"
"v_cvt_pk_bf16_f32 v232, v100, v101\n"
"v_cvt_pk_bf16_f32 v233, v102, v103\n"
"v_pk_mul_f32 v[116:117], v[116:117], v[218:219] op_sel_hi:[1,0]\n"
"v_pk_mul_f32 v[118:119], v[118:119], v[218:219] op_sel_hi:[1,0]\n"
"v_cvt_pk_bf16_f32 v234, v116, v117\n"
"v_cvt_pk_bf16_f32 v235, v118, v119\n"
"buffer_store_dwordx4 v[232:235], v254, s[36:39], s63 offen offset:1024 sc1\n"
"v_pk_mul_f32 v[104:105], v[104:105], v[220:221] op_sel_hi:[1,0]\n"
"v_pk_mul_f32 v[106:107], v[106:107], v[220:221] op_sel_hi:[1,0]\n"
"v_cvt_pk_bf16_f32 v236, v104, v105\n"
"v_cvt_pk_bf16_f32 v237, v106, v107\n"
"v_pk_mul_f32 v[120:121], v[120:121], v[220:221] op_sel_hi:[1,0]\n"
"v_pk_mul_f32 v[122:123], v[122:123], v[220:221] op_sel_hi:[1,0]\n"
"v_cvt_pk_bf16_f32 v238, v120, v121\n"
"v_cvt_pk_bf16_f32 v239, v122, v123\n"
"buffer_store_dwordx4 v[236:239], v254, s[36:39], s63 offen offset:2048 sc1\n"
"v_pk_mul_f32 v[108:109], v[108:109], v[222:223] op_sel_hi:[1,0]\n"
"v_pk_mul_f32 v[110:111], v[110:111], v[222:223] op_sel_hi:[1,0]\n"
"v_cvt_pk_bf16_f32 v240, v108, v109\n"
"v_cvt_pk_bf16_f32 v241, v110, v111\n"
"v_pk_mul_f32 v[124:125], v[124:125], v[222:223] op_sel_hi:[1,0]\n"
"v_pk_mul_f32 v[126:127], v[126:127], v[222:223] op_sel_hi:[1,0]\n"
"v_cvt_pk_bf16_f32 v242, v124, v125\n"
"v_cvt_pk_bf16_f32 v243, v126, v127\n"
"buffer_store_dwordx4 v[240:243], v254, s[36:39], s63 offen offset:3072 sc1\n"
"s_add_u32 s63, s63, 0x20000\n"
"s_waitcnt vmcnt(0)\n"
"s_barrier\n"
"s_mov_b64 exec, s[34:35]\n"
"buffer_store_dwordx2 v[250:251], v249, s[92:95], s54 offen sc1\n"
"s_mov_b64 exec, -1\n"
"s_lshr_b32 s62, s9, 1\n"
"s_and_b32 s62, s62, 3\n"
"s_and_b32 s63, s9, 1\n"
"s_lshl_b32 s63, s63, 3\n"
"s_lshr_b32 s12, s9, 5\n"
"s_add_u32 s63, s63, s12\n"
"s_lshr_b32 s64, s9, 3\n"
"s_and_b32 s64, s64, 3\n"
"s_lshr_b32 s12, s63, 1\n"
"s_lshl_b32 s13, s12, 8\n"
"s_add_u32 s71, s13, 0x1800\n"
"s_lshl_b32 s14, s62, 6\n"
"s_add_u32 s13, s13, s14\n"
"s_add_u32 s81, s13, 0x2000\n"
"s_lshl_b32 s13, s13, 2\n"
"s_add_u32 s72, s13, 0x4000\n"
"v_and_b32 v228, 31, v255\n"
"v_lshlrev_b32 v228, 3, v228\n"
"v_and_b32 v229, 56, v228\n"
"s_mov_b32 s24, s56\n"
"s_and_b32 s25, s57, 0xffff\n"
"s_mov_b32 s26, 0x158000\n"
"s_mov_b32 s27, 0x20000\n"
"v_lshrrev_b32 v208, 4, v255\n"
"v_and_b32 v209, 15, v255\n"
"s_lshl_b32 s13, s8, 5\n"
"v_add_u32 v204, s13, v208\n"
"v_lshlrev_b32 v204, 10, v204\n"
"s_lshl_b32 s13, s64, 8\n"
"v_add_u32 v204, s13, v204\n"
"v_lshlrev_b32 v207, 1, v208\n"
"v_xor_b32 v206, v209, v207\n"
"v_lshl_add_u32 v216, v206, 4, v204\n"
"v_xor_b32 v206, 8, v206\n"
"v_lshl_add_u32 v217, v206, 4, v204\n"
"v_add_u32 v217, 0xc00, v217\n"
"v_add_u32 v218, 0x1800, v216\n"
"v_add_u32 v219, 0x1800, v217\n"
"s_lshl_b32 s67, s8, 13\n"
"s_mul_i32 s65, s62, 0x58000\n"
"s_lshl_b32 s66, s62, 20\n"
"s_lshl_b32 s13, s63, 2\n"
"s_add_u32 s13, s13, s8\n"
"s_lshl_b32 s14, s12, 4\n"
"s_sub_u32 s14, 0x78, s14\n"
"s_add_u32 s14, s14, s13\n"
"s_lshl_b32 s13, s13, 9\n"
"s_lshl_b32 s14, s14, 9\n"
"v_add_u32 v212, s13, v247\n"
"v_add_u32 v213, s14, v247\n"
"v_lshlrev_b32 v214, 1, v212\n"
"v_lshlrev_b32 v215, 1, v213\n"
"s_mul_i32 s68, s62, 22\n"
"s_add_u32 s68, s68, 0x100\n"
"s_lshl_b32 s68, s68, 16\n"
"s_lshl_b32 s69, s62, 22\n"
"s_mov_b32 s70, s69\n"
"s_lshl_b32 s13, s13, 5\n"
"s_lshl_b32 s14, s14, 5\n"
"s_sub_u32 s80, s14, s13\n"
"s_lshl_b32 s14, s64, 8\n"
"s_add_u32 s13, s13, s14\n"
"s_lshl_b32 s14, s62, 6\n"
"s_add_u32 s13, s13, s14\n"
"v_lshlrev_b32 v204, 10, v209\n"
"v_lshl_add_u32 v204, v208, 3, v204\n"
"v_add_u32 v234, s13, v204\n"
"s_sub_u32 s20, s50, 0x18000\n"
"s_subb_u32 s21, s51, 0\n"
"s_and_b32 s21, s21, 0xffff\n"
"s_mov_b32 s22, 0x418000\n"
"s_mov_b32 s23, 0x20000\n"
"v_lshrrev_b32 v208, 4, v255\n"
"v_bfe_u32 v209, v255, 2, 2\n"
"v_and_b32 v210, 3, v255\n"
"v_lshl_add_u32 v204, v208, 2, v209\n"
"v_lshlrev_b32 v205, 8, v204\n"
"v_lshl_add_u32 v205, v210, 3, v205\n"
"v_add_u32 v205, 0x18000, v205\n"
"v_and_b32 v206, 7, v204\n"
"s_lshl_b32 s12, s62, 1\n"
"s_xor_b32 s12, s12, 6\n"
"v_xor_b32 v206, s12, v206\n"
"v_xor_b32 v207, 0, v206\n"
"v_lshl_add_u32 v220, v207, 5, v205\n"
"v_xor_b32 v207, 1, v206\n"
"v_lshl_add_u32 v221, v207, 5, v205\n"
"v_xor_b32 v207, 2, v206\n"
"v_lshl_add_u32 v222, v207, 5, v205\n"
"v_xor_b32 v207, 3, v206\n"
"v_lshl_add_u32 v223, v207, 5, v205\n"
"v_xor_b32 v207, 4, v206\n"
"v_lshl_add_u32 v224, v207, 5, v205\n"
"v_xor_b32 v207, 5, v206\n"
"v_lshl_add_u32 v225, v207, 5, v205\n"
"v_xor_b32 v207, 6, v206\n"
"v_lshl_add_u32 v226, v207, 5, v205\n"
"v_xor_b32 v207, 7, v206\n"
"v_lshl_add_u32 v227, v207, 5, v205\n"
"s_lshr_b32 s12, s9, 5\n"
"s_lshl_b32 s12, s12, 4\n"
"s_lshl_b32 s13, s8, 2\n"
"s_add_u32 s12, s12, s13\n"
"v_add_u32 v204, s12, v210\n"
"v_lshlrev_b32 v204, 10, v204\n"
"v_bfe_u32 v205, v255, 2, 1\n"
"v_lshl_add_u32 v204, v205, 7, v204\n"
"s_lshl_b32 s12, s64, 8\n"
"v_add_u32 v200, s12, v204\n"
"v_lshrrev_b32 v204, 3, v255\n"
"v_lshlrev_b32 v204, 19, v204\n"
"v_and_b32 v205, 7, v255\n"
"v_lshl_add_u32 v204, v205, 7, v204\n"
"s_lshl_b32 s12, s64, 17\n"
"v_add_u32 v204, s12, v204\n"
"v_sub_u32 v204, v204, v254\n"
"v_add_u32 v201, v204, v214\n"
"v_add_u32 v202, v204, v215\n"
"s_lshl_b32 s89, s62, 22\n"
"s_add_u32 s28, s60, 0x3c00000\n"
"s_addc_u32 s29, s61, 0\n"
"s_and_b32 s29, s29, 0xffff\n"
"s_mov_b32 s30, 0x800000\n"
"s_mov_b32 s31, 0x20000\n"
"s_lshl_b32 s73, s11, 5\n"
"s_add_u32 s73, s73, s10\n"
"s_lshl_b32 s73, s73, 2\n"
"s_add_u32 s73, s73, s8\n"
"s_lshl_b32 s73, s73, 3\n"
"s_add_u32 s73, s73, 0x4000\n"
"buffer_load_dwordx2 a[64:65], v212, s[36:39], s68 offen sc1\n"
"buffer_load_dwordx2 a[68:69], v213, s[36:39], s68 offen sc1\n"
"s_add_u32 s68, s68, 0x10000\n"
"buffer_load_dwordx2 a[66:67], v212, s[36:39], s68 offen sc1\n"
"buffer_load_dwordx2 a[70:71], v213, s[36:39], s68 offen sc1\n"
"s_add_u32 s68, s68, 0x10000\n"
"buffer_load_dwordx2 a[72:73], v212, s[36:39], s68 offen sc1\n"
"buffer_load_dwordx2 a[76:77], v213, s[36:39], s68 offen sc1\n"
"s_add_u32 s68, s68, 0x10000\n"
"buffer_load_dwordx2 a[74:75], v212, s[36:39], s68 offen sc1\n"
"buffer_load_dwordx2 a[78:79], v213, s[36:39], s68 offen sc1\n"
"s_add_u32 s68, s68, 0x10000\n"
"buffer_load_dwordx2 a[80:81], v212, s[36:39], s68 offen sc1\n"
"buffer_load_dwordx2 a[84:85], v213, s[36:39], s68 offen sc1\n"
"s_add_u32 s68, s68, 0x10000\n"
"buffer_load_dwordx2 a[82:83], v212, s[36:39], s68 offen sc1\n"
"buffer_load_dwordx2 a[86:87], v213, s[36:39], s68 offen sc1\n"
"s_add_u32 s68, s68, 0x10000\n"
"buffer_load_dwordx2 a[88:89], v212, s[36:39], s68 offen sc1\n"
"buffer_load_dwordx2 a[92:93], v213, s[36:39], s68 offen sc1\n"
"s_add_u32 s68, s68, 0x10000\n"
"buffer_load_dwordx2 a[90:91], v212, s[36:39], s68 offen sc1\n"
"buffer_load_dwordx2 a[94:95], v213, s[36:39], s68 offen sc1\n"
"s_add_u32 s68, s68, 0x10000\n"
"buffer_load_dwordx2 a[96:97], v212, s[36:39], s68 offen sc1\n"
"buffer_load_dwordx2 a[100:101], v213, s[36:39], s68 offen sc1\n"
"s_add_u32 s68, s68, 0x10000\n"
"buffer_load_dwordx2 a[98:99], v212, s[36:39], s68 offen sc1\n"
"buffer_load_dwordx2 a[102:103], v213, s[36:39], s68 offen sc1\n"
"s_add_u32 s68, s68, 0x10000\n"
"buffer_load_dwordx2 a[104:105], v212, s[36:39], s68 offen sc1\n"
"buffer_load_dwordx2 a[108:109], v213, s[36:39], s68 offen sc1\n"
"s_add_u32 s68, s68, 0x10000\n"
"buffer_load_dwordx2 a[106:107], v212, s[36:39], s68 offen sc1\n"
"buffer_load_dwordx2 a[110:111], v213, s[36:39], s68 offen sc1\n"
"s_add_u32 s68, s68, 0x10000\n"
"s_waitcnt vmcnt(0)\n"
"s_branch .Lback_49_%=\n"
".Lend_50_%=:\n"
    :
    : [ka] "s"(__builtin_amdgcn_kernarg_segment_ptr()), [wid] "s"(wid), [blk] "s"(blockIdx.x)
    : "v0", "v1", "v2", "v3", "v4", "v5", "v6", "v7", "v8", "v9", "v10", "v11", "v12", "v13", "v14", "v15", "v16", "v17", "v18", "v19", "v20", "v21", "v22", "v23", "v24", "v25", "v26", "v27", "v28", "v29", "v30", "v31", "v32", "v33", "v34", "v35", "v36", "v37", "v38", "v39", "v40", "v41", "v42", "v43", "v44", "v45", "v46", "v47", "v48", "v49", "v50", "v51", "v52", "v53", "v54", "v55", "v56", "v57", "v58", "v59", "v60", "v61", "v62", "v63", "v64", "v65", "v66", "v67", "v68", "v69", "v70", "v71", "v72", "v73", "v74", "v75", "v76", "v77", "v78", "v79", "v80", "v81", "v82", "v83", "v84", "v85", "v86", "v87", "v88", "v89", "v90", "v91", "v92", "v93", "v94", "v95", "v96", "v97", "v98", "v99", "v100", "v101", "v102", "v103", "v104", "v105", "v106", "v107", "v108", "v109", "v110", "v111", "v112", "v113", "v114", "v115", "v116", "v117", "v118", "v119", "v120", "v121", "v122", "v123", "v124", "v125", "v126", "v127", "v128", "v129", "v130", "v131", "v132", "v133", "v134", "v135", "v136", "v137", "v138", "v139", "v140", "v141", "v142", "v143", "v144", "v145", "v146", "v147", "v148", "v149", "v150", "v151", "v152", "v153", "v154", "v155", "v156", "v157", "v158", "v159", "v160", "v161", "v162", "v163", "v164", "v165", "v166", "v167", "v168", "v169", "v170", "v171", "v172", "v173", "v174", "v175", "v176", "v177", "v178", "v179", "v180", "v181", "v182", "v183", "v184", "v185", "v186", "v187", "v188", "v189", "v190", "v191", "v192", "v193", "v194", "v195", "v196", "v197", "v198", "v199", "v200", "v201", "v202", "v203", "v204", "v205", "v206", "v207", "v208", "v209", "v210", "v211", "v212", "v213", "v214", "v215", "v216", "v217", "v218", "v219", "v220", "v221", "v222", "v223", "v224", "v225", "v226", "v227", "v228", "v229", "v230", "v231", "v232", "v233", "v234", "v235", "v236", "v237", "v238", "v239", "v240", "v241", "v242", "v243", "v244", "v245", "v246", "v247", "v248", "v249", "v250", "v251", "v252", "v253", "v254", "v255", "a0", "a1", "a2", "a3", "a4", "a5", "a6", "a7", "a8", "a9", "a10", "a11", "a12", "a13", "a14", "a15", "a16", "a17", "a18", "a19", "a20", "a21", "a22", "a23", "a24", "a25", "a26", "a27", "a28", "a29", "a30", "a31", "a32", "a33", "a34", "a35", "a36", "a37", "a38", "a39", "a40", "a41", "a42", "a43", "a44", "a45", "a46", "a47", "a48", "a49", "a50", "a51", "a52", "a53", "a54", "a55", "a56", "a57", "a58", "a59", "a60", "a61", "a62", "a63", "a64", "a65", "a66", "a67", "a68", "a69", "a70", "a71", "a72", "a73", "a74", "a75", "a76", "a77", "a78", "a79", "a80", "a81", "a82", "a83", "a84", "a85", "a86", "a87", "a88", "a89", "a90", "a91", "a92", "a93", "a94", "a95", "a96", "a97", "a98", "a99", "a100", "a101", "a102", "a103", "a104", "a105", "a106", "a107", "a108", "a109", "a110", "a111", "a112", "a113", "a114", "a115", "a116", "a117", "a118", "a119", "a120", "a121", "a122", "a123", "a124", "a125", "a126", "a127", "a128", "a129", "a130", "a131", "a132", "a133", "a134", "a135", "a136", "a137", "a138", "a139", "a140", "a141", "a142", "a143", "a144", "a145", "a146", "a147", "a148", "a149", "a150", "a151", "a152", "a153", "a154", "a155", "a156", "a157", "a158", "a159", "a160", "a161", "a162", "a163", "a164", "a165", "a166", "a167", "a168", "a169", "a170", "a171", "a172", "a173", "a174", "a175", "a176", "a177", "a178", "a179", "a180", "a181", "a182", "a183", "a184", "a185", "a186", "a187", "a188", "a189", "a190", "a191", "a192", "a193", "a194", "a195", "a196", "a197", "a198", "a199", "a200", "a201", "a202", "a203", "a204", "a205", "a206", "a207", "a208", "a209", "a210", "a211", "a212", "a213", "a214", "a215", "a216", "a217", "a218", "a219", "a220", "a221", "a222", "a223", "a224", "a225", "a226", "a227", "a228", "a229", "a230", "a231", "a232", "a233", "a234", "a235", "a236", "a237", "a238", "a239", "a240", "a241", "a242", "a243", "a244", "a245", "a246", "a247", "a248", "a249", "a250", "a251", "a252", "a253", "a254", "a255", "s4", "s5", "s6", "s7", "s8", "s9", "s10", "s11", "s12", "s13", "s14", "s15", "s16", "s17", "s18", "s19", "s20", "s21", "s22", "s23", "s24", "s25", "s26", "s27", "s28", "s29", "s30", "s31", "s34", "s35", "s36", "s37", "s38", "s39", "s40", "s41", "s42", "s43", "s44", "s45", "s46", "s47", "s48", "s49", "s50", "s51", "s52", "s53", "s54", "s55", "s56", "s57", "s58", "s59", "s60", "s61", "s62", "s63", "s64", "s65", "s66", "s67", "s68", "s69", "s70", "s71", "s72", "s73", "s74", "s75", "s76", "s77", "s78", "s79", "s80", "s81", "s82", "s83", "s84", "s85", "s86", "s87", "s88", "s89", "s90", "s91", "s92", "s93", "s94", "s95", "s96", "s97", "s98", "s99", "vcc", "scc", "memory");
}

extern "C" void kernel_launch(void* const* d_in, const int* in_sizes, int n_in, void* d_out, int out_size, void* d_ws, size_t ws_size, hipStream_t stream) {
  (void)in_sizes; (void)n_in; (void)out_size; (void)ws_size;
  (void)hipFuncSetAttribute((const void*)fused_mla_ffn, hipFuncAttributeMaxDynamicSharedMemorySize, 163840);
  fused_mla_ffn<<<256, 256, 163840, stream>>>(d_in[0], d_in[1], d_in[2], d_in[3], d_in[4], d_in[5], d_in[7], d_in[8], d_in[9], d_out, (char*)d_ws + 0);
}
```
